```python
import torch

def _softmax(x, axis=-1):
    return torch.softmax(x, dim=axis)
def _gelu(x):
    return torch.nn.functional.gelu(x, approximate="tanh")
def _silu(x):
    return torch.nn.functional.silu(x)
def _sigmoid(x):
    return torch.sigmoid(x)
def _layernorm(x, axis=-1, eps=1e-5):
    m = x.mean(dim=axis, keepdim=True)
    v = x.var(dim=axis, keepdim=True, unbiased=False)
    return (x - m) / torch.sqrt(v + eps)
def _scatter_add(tgt, idx, src, axis=0):
    return tgt.clone().index_add_(axis, idx, src)
def _segment_sum(src, seg, n):
    out = torch.zeros((n,) + src.shape[1:], dtype=src.dtype)
    return out.index_add_(0, seg, src)
def _pad(x, pw):
    flat = tuple(v for pair in reversed(pw) for v in pair)
    return torch.nn.functional.pad(x, flat)

kernel_name = 'interleave_layers__grouped_kv_mqa__sliding_block__prompt'

def setup_inputs():

    _w = (lambda a: (lambda t: t.to(torch.bfloat16) if t.is_floating_point() else t)(torch.as_tensor(a)))
    def _A_setup():
        B, S, D, H = 1, 2048, 512, 32
        Dh = D // H
        return {
            "x": _w((lambda *s: torch.randn(*s, dtype=torch.bfloat16) * 0.05)(B, S, D)),
            "wq": _w((lambda *s: torch.randn(*s, dtype=torch.bfloat16) * 0.05)(D, H * Dh)),
            "wk": _w((lambda *s: torch.randn(*s, dtype=torch.bfloat16) * 0.05)(D, Dh)),
            "wv": _w((lambda *s: torch.randn(*s, dtype=torch.bfloat16) * 0.05)(D, Dh)),
            "wo": _w((lambda *s: torch.randn(*s, dtype=torch.bfloat16) * 0.05)(H * Dh, D)),
        }
    _A_in = _A_setup()

    def _B_setup():
        B, S, D = 1, 2048, 512
        return {
            "x": _w((lambda *s: torch.randn(*s, dtype=torch.bfloat16) * 0.05)(B, S, D)),
            "wqkv": _w((lambda *s: torch.randn(*s, dtype=torch.bfloat16) * 0.05)(D, 3 * D)),
            "wo": _w((lambda *s: torch.randn(*s, dtype=torch.bfloat16) * 0.05)(D, D)),
        }
    _B_in = _B_setup()

    return {
        "A_x": _A_in["x"],
        "A_wq": _A_in["wq"],
        "A_wk": _A_in["wk"],
        "A_wv": _A_in["wv"],
        "A_wo": _A_in["wo"],
        "B_x": _B_in["x"],
        "B_wqkv": _B_in["wqkv"],
        "B_wo": _B_in["wo"],
    }

def reference(A_x, A_wq, A_wk, A_wv, A_wo, B_x, B_wqkv, B_wo):
    def _A_ref(x, wq, wk, wv, wo):
        B, S, D = x.shape
        H = 32
        Dh = D // H
        q = (lambda x, *d: x.permute(*d))((x @ wq).reshape(B, S, H, Dh), 0, 2, 1, 3)
        k = x @ wk
        v = x @ wv
        scores = torch.einsum("bhqd,bkd->bhqk", q, k) * (Dh ** -0.5)
        p = _softmax(scores, axis=-1)
        o = torch.einsum("bhqk,bkd->bhqd", p, v)
        return (lambda x, *d: x.permute(*d))(o, 0, 2, 1, 3).reshape(B, S, H * Dh) @ wo

    def _B_ref(x, wqkv, wo):
        B, S, D = x.shape
        H = 32; Dh = D // H; W = 256
        qkv = (lambda x, *d: x.permute(*d))((x @ wqkv).reshape(B, S, 3, H, Dh), 2, 0, 3, 1, 4)
        q, k, v = qkv[0], qkv[1], qkv[2]
        scores = torch.einsum("bhqd,bhkd->bhqk", q, k) * (Dh ** -0.5)
        idx = torch.arange(S)
        mask = (idx[:, None] - idx[None, :] >= 0) & (idx[:, None] - idx[None, :] < W)
        scores = torch.where(mask[None, None], scores, -1e9)
        p = _softmax(scores, axis=-1)
        o = torch.einsum("bhqk,bhkd->bhqd", p, v)
        return (lambda x, *d: x.permute(*d))(o, 0, 2, 1, 3).reshape(B, S, D) @ wo

    x = A_x
    x = x + _A_ref(_layernorm(x, axis=-1), A_wq, A_wk, A_wv, A_wo)
    x = x + _B_ref(_layernorm(x, axis=-1), B_wqkv, B_wo)
    x = x + _A_ref(_layernorm(x, axis=-1), A_wq, A_wk, A_wv, A_wo)
    x = x + _B_ref(_layernorm(x, axis=-1), B_wqkv, B_wo)
    return x
```

```cpp
#include <hip/hip_runtime.h>
#include <hip/hip_bf16.h>
#define NEMPTY 0
typedef unsigned short u16;
__device__ __forceinline__ float b2f(u16 b) { return __uint_as_float((unsigned)b << 16); }
__device__ __forceinline__ u16 f2b(float f) { unsigned u = __float_as_uint(f); return (u16)((u + 0x7FFF + ((u >> 16) & 1)) >> 16); }
using bf16x8 = __attribute__((ext_vector_type(8))) short;
using f32x4 = __attribute__((ext_vector_type(4))) float;
__global__ void ln_k(const u16* x, u16* xn) {
    int row = blockIdx.x * 4 + (threadIdx.x >> 6), lane = threadIdx.x & 63;
    float v[8]; float s = 0;
    for (int i = 0; i < 8; ++i) { v[i] = b2f(x[row * 512 + lane * 8 + i]); s += v[i]; }
    for (int o = 32; o; o >>= 1) s += __shfl_xor(s, o);
    float m = s / 512; float q = 0; for (int i = 0; i < 8; ++i) q += (v[i] - m) * (v[i] - m);
    for (int o = 32; o; o >>= 1) q += __shfl_xor(q, o);
    float r = rsqrtf(q / 512 + 1e-5f);
    for (int i = 0; i < 8; ++i) xn[row * 512 + lane * 8 + i] = f2b((v[i] - m) * r);
}
__global__ void gemm_k(const u16* A, const u16* W, int N, u16* C, int ldc, const u16* res) {
    int wave = blockIdx.x * 4 + (threadIdx.x >> 6), lane = threadIdx.x & 63;
    int nt = N / 16; int tm = wave / nt, tn = wave % nt; if (tm >= 128) return;
    f32x4 acc = {0, 0, 0, 0};
    for (int k0 = 0; k0 < 512; k0 += 32) {
        bf16x8 a = *(const bf16x8*)(A + (tm * 16 + (lane & 15)) * 512 + k0 + (lane >> 4) * 8);
        bf16x8 b; for (int j = 0; j < 8; ++j) b[j] = W[(k0 + (lane >> 4) * 8 + j) * N + tn * 16 + (lane & 15)];
        acc = __builtin_amdgcn_mfma_f32_16x16x32_bf16(a, b, acc, 0, 0, 0);
    }
    for (int r = 0; r < 4; ++r) { int row = tm * 16 + (lane >> 4) * 4 + r, col = tn * 16 + (lane & 15); float v = acc[r]; if (res) v = b2f(f2b(v)) + b2f(res[row * ldc + col]); C[row * ldc + col] = f2b(v); }
}
__global__ void attn_k(const u16* Q, int ldq, const u16* K, const u16* V, int ldk, int hs, int W, u16* O) {
    int idx = blockIdx.x * blockDim.x + threadIdx.x; int h = idx / 2048, t = idx % 2048;
    float q[16], o[16]; for (int d = 0; d < 16; ++d) { q[d] = b2f(Q[t * ldq + h * 16 + d]) * 0.25f; o[d] = 0; }
    int j0 = W ? max(0, t - W + 1) : 0, j1 = W ? t + 1 : 2048; float m = -1e30f, l = 0;
    for (int j = j0; j < j1; ++j) { float s = 0; for (int d = 0; d < 16; ++d) s += q[d] * b2f(K[j * ldk + h * hs + d]);
        float mn = fmaxf(m, s), a = __expf(m - mn), p = __expf(s - mn); l = l * a + p; for (int d = 0; d < 16; ++d) o[d] = o[d] * a + p * b2f(V[j * ldk + h * hs + d]); m = mn; }
    for (int d = 0; d < 16; ++d) O[t * 512 + h * 16 + d] = f2b(o[d] / l);
}
__global__ void empty_k(int* p) { if (p == nullptr) __builtin_trap(); }
extern "C" void kernel_launch(void* const* d_in, const int* in_sizes, int n_in, void* d_out, int out_size, void* d_ws, size_t ws_size, hipStream_t stream) {
    const u16 *x0 = (const u16*)d_in[0], *wq = (const u16*)d_in[1], *wk = (const u16*)d_in[2], *wv = (const u16*)d_in[3], *woA = (const u16*)d_in[4], *wqkv = (const u16*)d_in[6], *woB = (const u16*)d_in[7];
    u16* ws = (u16*)d_ws; u16 *xn = ws, *qkv = ws + (1 << 20), *kv = ws + 5 * (1 << 20), *o = ws + 6 * (1 << 20), *xa = ws + 7 * (1 << 20), *xb = ws + 8 * (1 << 20);
    const u16* x = x0;
    for (int l = 0; l < 2; ++l) {
        ln_k<<<512, 256, 0, stream>>>(x, xn);
        gemm_k<<<128 * 32 / 4, 256, 0, stream>>>(xn, wq, 512, qkv, 512, nullptr);
        gemm_k<<<128 / 4, 256, 0, stream>>>(xn, wk, 16, kv, 32, nullptr);
        gemm_k<<<128 / 4, 256, 0, stream>>>(xn, wv, 16, kv + 16, 32, nullptr);
        attn_k<<<65536 / 64, 64, 0, stream>>>(qkv, 512, kv, kv + 16, 32, 0, 0, o);
        gemm_k<<<128 * 32 / 4, 256, 0, stream>>>(o, woA, 512, xa, 512, x);
        ln_k<<<512, 256, 0, stream>>>(xa, xn);
        gemm_k<<<128 * 96 / 4, 256, 0, stream>>>(xn, wqkv, 1536, qkv, 1536, nullptr);
        attn_k<<<65536 / 64, 64, 0, stream>>>(qkv, 1536, qkv + 512, qkv + 1024, 1536, 16, 256, o);
        u16* dst = l == 0 ? xb : (u16*)d_out;
        gemm_k<<<128 * 32 / 4, 256, 0, stream>>>(o, woB, 512, dst, 512, xa);
        x = xb;
    }
    for (int i = 0; i < NEMPTY; ++i) empty_k<<<256, 256, 0, stream>>>((int*)d_ws);
}
```

```cpp
#include <hip/hip_runtime.h>
#include <stdint.h>

typedef unsigned short u16;
typedef unsigned u32;
typedef unsigned long long u64;
typedef u32 u32x4 __attribute__((ext_vector_type(4)));
typedef u32 u32x8 __attribute__((ext_vector_type(8)));
typedef u32 u32x16 __attribute__((ext_vector_type(16)));
typedef int i32x4 __attribute__((ext_vector_type(4)));

constexpr u32 OFF_F3 = 0x10000, OFF_CS = 0x20000;
constexpr u32 OFF_KVA = 0x40000, KVA_SZ = 0x28000, OFF_NB = 0xC0000, OFF_WT = 0x100000;
constexpr u32 OFF_O = 0x500000, OFF_ST = 0x1500000, OFF_KVB = 0x1800000, WS_USED = 0x2000000;
constexpr u32 L_AT0 = 0, L_WQ = 65536, L_KVS = 131072, L_QS = 147456, L_SM = 155648, L_SR = 156672, L_MASK = 158720, L_CT = 162816;
struct Params { const u16 *x, *wq, *wk, *wv, *woA, *wqkv, *woB; u16* out; char* ws; };

__device__ __forceinline__ u32 rfl(u32 v) { return (u32)__builtin_amdgcn_readfirstlane((int)v); }
__device__ __forceinline__ i32x4 mksrd(const void* p, u32 bytes) { return i32x4{(int)(u32)(uintptr_t)p, (int)(((uintptr_t)p >> 32) & 0xffff), (int)bytes, 0x00020000}; }
#define L128(a) (*(__attribute__((address_space(3))) u32x4*)(uintptr_t)(a))
#define AGC "a0", "a1", "a2", "a3", "a4", "a5", "a6", "a7", "a8", "a9", "a10", "a11", "a12", "a13", "a14", "a15", "a16", "a17", "a18", "a19", "a20", "a21", "a22", "a23", "a24", "a25", "a26", "a27", "a28", "a29", "a30", "a31", "a32", "a33", "a34", "a35", "a36", "a37", "a38", "a39", "a40", "a41", "a42", "a43", "a44", "a45", "a46", "a47", "a48", "a49", "a50", "a51", "a52", "a53", "a54", "a55", "a56", "a57", "a58", "a59", "a60", "a61", "a62", "a63", "a64", "a65", "a66", "a67", "a68", "a69", "a70", "a71", "a72", "a73", "a74", "a75", "a76", "a77", "a78", "a79", "a80", "a81", "a82", "a83", "a84", "a85", "a86", "a87", "a88", "a89", "a90", "a91", "a92", "a93", "a94", "a95", "a96", "a97", "a98", "a99", "a100", "a101", "a102", "a103", "a104", "a105", "a106", "a107", "a108", "a109", "a110", "a111", "a112", "a113", "a114", "a115", "a116", "a117", "a118", "a119", "a120", "a121", "a122", "a123", "a124", "a125", "a126", "a127", "a128", "a129", "a130", "a131", "a132", "a133", "a134", "a135", "a136", "a137", "a138", "a139", "a140", "a141", "a142", "a143", "a144", "a145", "a146", "a147", "a148", "a149", "a150", "a151", "a152", "a153", "a154", "a155", "a156", "a157", "a158", "a159", "a160", "a161", "a162", "a163", "a164", "a165", "a166", "a167", "a168", "a169", "a170", "a171", "a172", "a173", "a174", "a175", "a176", "a177", "a178", "a179", "a180", "a181", "a182", "a183", "a184", "a185", "a186", "a187", "a188", "a189", "a190", "a191", "a192", "a193", "a194", "a195", "a196", "a197", "a198", "a199", "a200", "a201", "a202", "a203", "a204", "a205", "a206", "a207", "a208", "a209", "a210", "a211", "a212", "a213", "a214", "a215", "a216", "a217", "a218", "a219", "a220", "a221", "a222", "a223", "a224", "a225", "a226", "a227", "a228", "a229", "a230", "a231", "a232", "a233", "a234", "a235", "a236", "a237", "a238", "a239", "a240", "a241", "a242", "a243", "a244", "a245", "a246", "a247", "a248", "a249", "a250", "a251", "a252", "a253", "a254", "a255"
#define STMP "s53","s54","s55","s56","s58","s59","s60","s61","s62","s63","vcc","scc","memory"

__global__ void __launch_bounds__(256, 1) fused_abab(Params P) {
    __shared__ __attribute__((aligned(8192))) char lds_[163840];
    const u32 lb = (u32)(uintptr_t)lds_;
    { u64 pc; asm volatile("s_getpc_b64 %0" : "=s"(pc));
      asm volatile("global_load_dword a3, %0, %1" :: "v"((blockIdx.x >> 3) * 2048 + (threadIdx.x & 15) * 128), "s"(pc) : "memory", AGC); }
    const u32 b = blockIdx.x, lane = threadIdx.x & 63, w = rfl(threadIdx.x >> 6);
    const u32 rr = b >> 3, t = 4 * (b & 7) + (rr >> 3), c = rr & 7, wg = t * 8 + c, v16 = lane * 16;
    const i32x4 srw = mksrd(P.ws, WS_USED);
    const i32x4 sx = mksrd(P.x, 2048 * 512 * 2), skv = mksrd(c < 4 ? P.wk : P.wv, 512 * 16 * 2);
    asm volatile("buffer_load_dwordx2 a[0:1], off, %0, 0 sc1" :: "s"(srw) : "memory", AGC);
    const i32x4 sq = mksrd(P.wq, 512 * 512 * 2);
    const u32 xg = (64 * t + 16 * w) * 1024, xl = lb + L_AT0 + w * 16384, qp = (16 * w + 16 * t) & 63, qg = qp * 8192 + c * 128, ql = lb + L_WQ + qp * 1024;
    const u32 wv0 = ((((lane & 7) >> 1) ^ ((lane >> 4) & 1)) * 32) + (lane & 1) * 16;
    const u32 wva = (lane >> 3) * 1024 + wv0, wvb = wva ^ 64;
#define PIECE_X(i) do { u32 tv_, ts_; asm volatile("s_add_u32 m0, %2, %c6\n\tv_xor_b32 %0, %c7, %3\n\ts_add_u32 %1, %4, %c6\n\tbuffer_load_dwordx4 %0, %5, %1 offen lds" \
        : "=&v"(tv_), "=&s"(ts_) : "s"(xl), "v"(v16), "s"(xg), "s"(sx), "i"((i) * 1024), "i"((i) * 16) : "memory", "scc"); } while (0)
#define PIECE_K(i) do { u32 ts_; asm volatile("s_add_u32 m0, %1, %c5\n\ts_add_u32 %0, %2, %c5\n\tbuffer_load_dwordx4 %3, %4, %0 offen lds" \
        : "=&s"(ts_) : "s"(lb + L_KVS + w * 4096), "s"(w * 4096), "v"(v16), "s"(skv), "i"((i) * 1024) : "memory", "scc"); } while (0)
#define PIECE_Q(i) do { u32 ts_; if ((i) < 6) asm volatile("s_add_u32 m0, %1, %c5\n\ts_add_u32 %0, %2, %c6\n\tbuffer_load_dwordx4 %3, %4, %0 offen lds" \
        : "=&s"(ts_) : "s"(ql), "s"(qg), "v"(((i) & 1) ? wvb : wva), "s"(sq), "i"((i) * 1024), "i"((i) * 8192) : "memory", "scc"); } while (0)
#define SEL(c_, a_, b_) ({ const u32 x_ = (a_), y_ = (b_); (c_) ? x_ : y_; })
    PIECE_X(0); PIECE_X(1); PIECE_X(2); PIECE_X(3); PIECE_X(4); PIECE_X(5); PIECE_X(6); PIECE_X(7); PIECE_K(0); PIECE_K(1);
    const u32 l15 = lane & 15, g = lane >> 4, l31 = lane & 31, hh = lane >> 5, sw23 = (l15 & 3) | ((l15 & 4) << 1) | ((l15 & 8) >> 1);
    const u32 Tc = c & 3, kw = (Tc + 1) & 3, head = 4 * c + w, j = 2 * t + (Tc >> 1);
    u32x16 cb0 = {v16, hh * 1024 + l31 * 16, lb + (2 * w + hh) * 1024 + l31 * 16, lb + g * 1024 + l15 * 16, lb + 65536 + g * 1024 + l15 * 16,
                  SEL((lane < 32) & ((lane >> 2) != c) & ((lane & 1) == (w >> 1)), lane * 128, 0x7F000000u), 2 * g * 512 + (16 * w + l15) * 8, lb + L_SM + (16 * w + l15) * 4,
                  lb + L_SM + l15 * 4, lb + L_SM + g * 16, lb + L_SR + w * 512 + lane * 8, lb + L_SR + (16 * w + l15) * 8, 1024 + hh * 256 + l15 * 16,
                  1024 + (g & 1) * 512 + (g >> 1) * 256 + l15 * 16, (l31 == 16 || l31 == 20) ? 0x3F803F80u : 0u, lane * 128};
    asm volatile("" : "+{v[0:15]}"(cb0));
    PIECE_K(2); PIECE_K(3); PIECE_X(8); PIECE_X(9);
    u32x8 cb1 = {SEL((lane < 8) & (t + (lane >> 1) >= 4), ((t + (lane >> 1) - 4) * 32 + head) * 128 + (lane & 1) * 4, 0x7F000000u), (l15 == 0 || l15 == 4) ? 0x3F803F80u : 0u,
                 j * 1024 + (g & 1) * 8 + SEL(c < 4, hh * 512 + (Tc & 1) * 256 + l15 * 16, 65536 + (Tc & 1) * 512 + hh * 256 + sw23 * 16),
                 lb + L_QS + 2 * w * 1024 + v16, lb + L_QS + v16, (64 * t + l31) * 1024 + (64 * c + 16 * w + 8 * hh) * 2, 65536 + hh * 256 + l15 * 16, 0u};
    asm volatile("" : "+{v[224:231]}"(cb1));
    PIECE_X(10); PIECE_X(11); PIECE_X(12); PIECE_X(13);
    const u32 pb = wg < 160 ? wg : wg - 96, pg = pb >> 2, pq = pb & 3;
    const bool pmid = pg >= 8 && pg < 32; const u32 pld = pmid ? 3072 : 1024;
    const u32 tra = 8 * g + (l15 >> 2), trp = (l15 & 3) * 8;
    u32x16 pcv;
#pragma unroll
    for (int i = 0; i < 4; ++i) pcv[i] = lb + L_AT0 + l15 * 1024 + (((4 * i) ^ (l15 & 12)) + (g ^ (l15 & 3))) * 16;
    pcv[4] = lb + L_KVS + tra * 32 + trp; pcv[5] = lb + L_WQ + tra * 128 + (w ^ (((l15 >> 3) & 1) | ((g & 1) << 1))) * 32 + trp;
    pcv[6] = wva; pcv[7] = (lane >> 3) * pld + wv0; pcv[8] = lb + L_AT0 + l15 * 1024 + (((8 * c + 2 * w + (g >> 1)) ^ l15) * 16) + (g & 1) * 8;
    asm volatile("" : "+{v[96:111]}"(pcv));
    PIECE_X(14); PIECE_X(15);
    const i32x4 sp = mksrd(pg < 8 ? P.woA : (pmid ? P.wqkv : P.woB), 512 * pld);
    const u32 pco = (pmid ? pg - 8 : pg & 7) * 128 + 128 * pq * pld;
    PIECE_Q(0); PIECE_Q(1); PIECE_Q(2); PIECE_Q(3);
    { u32x4 mk;
#pragma unroll
      for (int r = 0; r < 4; ++r) mk[r] = (r + 8 * w + 4 * hh <= l31) ? 0u : 0xFF800000u;
      L128(lb + L_MASK + w * 1024 + v16) = mk;
      u32 f2p = SEL(lane < 54, (lane - 37) * 8 + (c & 4), wg); f2p = SEL(lane < 37, (lane - 20) * 8 + c, f2p); f2p = SEL(lane < 20, (lane >> 2) * 32 + 4 * c + (lane & 3), f2p);
      u32 f3p = SEL((lane < 12) & (t + lane >= 12), (t + lane - 12) * 8 + c, wg); f3p = SEL(lane < 8, t * 8 + lane, f3p);
      L128(lb + L_CT + v16) = u32x4{j * 1024 + SEL(c < 4, hh * 512 + (Tc & 1) * 256 + l31 * 8, 65536 + (Tc & 1) * 512 + lane * 8),
                                    f2p * 128, f3p * 128, SEL(c < 4, (l15 & 3) * 64 + g * 16, g * 64 + l15 * 4)}; }
    PIECE_Q(4); PIECE_Q(5); PIECE_Q(6); PIECE_Q(7);
    u32x16 sb; u32x8 sb2;
    asm volatile("s_mov_b32 s64, %1\n\ts_mov_b32 s65, %2\n\ts_mov_b32 s66, %3\n\ts_mov_b32 s67, %4\n\ts_mov_b32 s68, %5\n\ts_mov_b32 s69, %6\n\ts_mov_b32 s70, %7\n\ts_mov_b32 s71, %8\n\ts_mov_b32 s72, %9\n\ts_mov_b32 s73, %10\n\ts_mov_b32 s74, %11\n\ts_mov_b32 s75, %12\n\ts_mov_b32 s76, %13\n\ts_mov_b32 s77, %14\n\ts_mov_b32 s78, %15\n\ts_mov_b32 s79, %16\n\t" : "=&{s[64:79]}"(sb)
        : "s"(rfl(OFF_O + t * 65536 + (8 * c + 2 * w) * 1024)), "s"(rfl(OFF_O + t * 65536)), "s"(rfl(OFF_ST + t * 4096)), "s"(rfl(OFF_ST + t * 4096 + c * 512 + w * 128)), "s"(rfl(OFF_KVB + (2 * t * 32 + head) * 2048)), "s"(rfl(t)), "s"(rfl(OFF_NB)),
          "s"(rfl(OFF_NB + (t * 32 + head) * 128)), "s"(rfl(OFF_WT + head * 16384)), "s"(rfl(OFF_CS + head * 256)), "s"(rfl(OFF_KVA + KVA_SZ)), "s"(rfl(OFF_F3)), "s"(rfl(wg * 128 + w * 4)),
          "s"(rfl((8 * c + 8 + w) * 1024)), "s"(rfl(lb + (8 + w) * 1024)), "s"(rfl(4 * c + 4)));
    PIECE_Q(8); PIECE_Q(9); PIECE_Q(10); PIECE_Q(11);
    asm volatile("s_mov_b32 s80, %1\n\ts_mov_b32 s81, %2\n\ts_mov_b32 s82, %3\n\ts_mov_b32 s83, %4\n\ts_mov_b32 s84, %5\n\ts_mov_b32 s85, %6\n\ts_mov_b32 s86, %7\n\ts_mov_b32 s87, %8\n\t" : "=&{s[80:87]}"(sb2)
        : "s"(rfl((rr & 15) * 1024)), "s"(rfl(w * 16384)), "s"(rfl(w == kw ? ~0u : 0u)), "s"(rfl(2 * c * 1024)), "s"(rfl(OFF_WT + (192 + (c >> 2)) * 16384)), "s"(rfl(OFF_CS + (192 + (c >> 2)) * 256)), "s"(rfl(c)), "s"(rfl(0u)));
    {
        const i32x4 sro = mksrd(P.out, 2048 * 512 * 2);
        asm volatile(
        "; ---- prologue: statistics, K/V tile\n"
        "s_nop 4\n"
        "s_lshr_b32 s38, s81, 14\n"
        "s_lshl_b32 s34, s69, 12\n"
        "s_add_u32 s34, s34, 4259840\n"
        "s_lshl_b32 s54, s86, 2\n"
        "s_lshl_b32 s30, 15, s54\n"
        "s_not_b32 s30, s30\n"
        "s_lshr_b32 s54, s38, 1\n"
        "s_lshl_b32 s54, 0x55555555, s54\n"
        "s_and_b32 s30, s30, s54\n"
        "s_mov_b32 s31, 0\n"
        "v_mov_b32 v116, 0x3f803f80\n"
        "v_mov_b32 v117, 0x3f803f80\n"
        "v_mov_b32 v118, 0x3f803f80\n"
        "v_mov_b32 v119, 0x3f803f80\n"
        "v_xor_b32 v105, 64, v102\n"
        "v_accvgpr_write_b32 a128, v14\n"
        "v_accvgpr_write_b32 a129, v14\n"
        "v_accvgpr_write_b32 a130, v14\n"
        "v_accvgpr_write_b32 a131, v14\n"
        "v_accvgpr_write_b32 a132, v14\n"
        "v_accvgpr_write_b32 a133, v14\n"
        "v_accvgpr_write_b32 a134, v14\n"
        "v_accvgpr_write_b32 a135, v14\n"
        "v_accvgpr_write_b32 a136, v14\n"
        "v_accvgpr_write_b32 a137, v14\n"
        "v_accvgpr_write_b32 a138, v14\n"
        "v_accvgpr_write_b32 a139, v14\n"
        "v_accvgpr_write_b32 a140, v14\n"
        "v_accvgpr_write_b32 a141, v14\n"
        "v_accvgpr_write_b32 a142, v14\n"
        "v_accvgpr_write_b32 a143, v14\n"
        "s_waitcnt vmcnt(6) lgkmcnt(0)\n"
        "s_barrier\n"
        "v_accvgpr_read_b32 v120, a0\n"
        "v_accvgpr_read_b32 v121, a1\n"
        "v_readfirstlane_b32 s47, v120\n"
        "v_readfirstlane_b32 s48, v121\n"
        "s_nop 2\n"
        "s_add_u32 s50, s47, 2\n"
        "s_add_u32 s51, s47, 3\n"
        "s_add_u32 s37, s47, 1\n"
        "s_mov_b32 s36, s48\n"
        "s_add_u32 s47, s47, 16\n"
        "s_addc_u32 s48, s48, 0\n"
        "s_lshl_b32 s55, s38, 4\n"
        "s_lshl_b32 s54, s69, 4\n"
        "s_add_u32 s54, s54, s55\n"
        "s_and_b32 s54, s54, 63\n"
        "s_lshl_b32 s62, s54, 13\n"
        "s_add_u32 s62, s62, s40\n"
        "s_lshl_b32 s63, s54, 10\n"
        "s_add_u32 s63, s63, s49\n"
        "s_add_u32 s63, s63, 65536\n"
        "s_cmp_eq_u32 s43, 0\n"
        "s_cbranch_scc1 Lpkw_%=\n"
        "s_cmp_eq_u32 s43, 1\n"
        "s_cbranch_scc1 Lpr1_%=\n"
        "s_lshl_b32 s54, s38, 14\n"
        "v_add_u32 v112, s54, v96\n"
        "v_add_u32 v113, s54, v97\n"
        "v_add_u32 v114, s54, v98\n"
        "v_add_u32 v115, s54, v99\n"
        "ds_read_b128 v[128:131], v112 offset:0\n"
        "ds_read_b128 v[132:135], v113 offset:0\n"
        "ds_read_b128 v[136:139], v114 offset:0\n"
        "ds_read_b128 v[140:143], v115 offset:0\n"
        "ds_read_b128 v[144:147], v112 offset:256\n"
        "ds_read_b128 v[148:151], v113 offset:256\n"
        "s_nop 1\n"
        "s_waitcnt lgkmcnt(5)\n"
        "v_mfma_f32_16x16x32_bf16 v[160:163], v[128:131], v[128:131], 0\n"
        "v_mfma_f32_16x16x32_bf16 v[176:179], v[116:119], v[128:131], 0\n"
        "s_add_u32 m0, s63, 6144\n"
        "s_add_u32 s55, s62, 49152\n"
        "buffer_load_dwordx4 v102, %[sq], s55 offen lds\n"
        "ds_read_b128 v[152:155], v114 offset:256\n"
        "s_waitcnt lgkmcnt(5)\n"
        "v_mfma_f32_16x16x32_bf16 v[160:163], v[132:135], v[132:135], v[160:163]\n"
        "v_mfma_f32_16x16x32_bf16 v[176:179], v[116:119], v[132:135], v[176:179]\n"
        "s_add_u32 m0, s63, 7168\n"
        "s_add_u32 s55, s62, 57344\n"
        "buffer_load_dwordx4 v105, %[sq], s55 offen lds\n"
        "ds_read_b128 v[156:159], v115 offset:256\n"
        "s_waitcnt lgkmcnt(5)\n"
        "v_mfma_f32_16x16x32_bf16 v[160:163], v[136:139], v[136:139], v[160:163]\n"
        "v_mfma_f32_16x16x32_bf16 v[176:179], v[116:119], v[136:139], v[176:179]\n"
        "ds_read_b128 v[128:131], v112 offset:512\n"
        "s_waitcnt lgkmcnt(5)\n"
        "v_mfma_f32_16x16x32_bf16 v[160:163], v[140:143], v[140:143], v[160:163]\n"
        "v_mfma_f32_16x16x32_bf16 v[176:179], v[116:119], v[140:143], v[176:179]\n"
        "s_add_u32 m0, s63, 8192\n"
        "s_add_u32 s55, s62, 65536\n"
        "buffer_load_dwordx4 v102, %[sq], s55 offen lds\n"
        "ds_read_b128 v[132:135], v113 offset:512\n"
        "s_waitcnt lgkmcnt(5)\n"
        "v_mfma_f32_16x16x32_bf16 v[160:163], v[144:147], v[144:147], v[160:163]\n"
        "v_mfma_f32_16x16x32_bf16 v[176:179], v[116:119], v[144:147], v[176:179]\n"
        "s_add_u32 m0, s63, 9216\n"
        "s_add_u32 s55, s62, 73728\n"
        "buffer_load_dwordx4 v105, %[sq], s55 offen lds\n"
        "ds_read_b128 v[136:139], v114 offset:512\n"
        "s_waitcnt lgkmcnt(5)\n"
        "v_mfma_f32_16x16x32_bf16 v[160:163], v[148:151], v[148:151], v[160:163]\n"
        "v_mfma_f32_16x16x32_bf16 v[176:179], v[116:119], v[148:151], v[176:179]\n"
        "ds_read_b128 v[140:143], v115 offset:512\n"
        "s_waitcnt lgkmcnt(5)\n"
        "v_mfma_f32_16x16x32_bf16 v[160:163], v[152:155], v[152:155], v[160:163]\n"
        "v_mfma_f32_16x16x32_bf16 v[176:179], v[116:119], v[152:155], v[176:179]\n"
        "s_add_u32 m0, s63, 10240\n"
        "s_add_u32 s55, s62, 81920\n"
        "buffer_load_dwordx4 v102, %[sq], s55 offen lds\n"
        "ds_read_b128 v[144:147], v112 offset:768\n"
        "s_waitcnt lgkmcnt(5)\n"
        "v_mfma_f32_16x16x32_bf16 v[160:163], v[156:159], v[156:159], v[160:163]\n"
        "v_mfma_f32_16x16x32_bf16 v[176:179], v[116:119], v[156:159], v[176:179]\n"
        "ds_read_b128 v[148:151], v113 offset:768\n"
        "s_waitcnt lgkmcnt(5)\n"
        "v_mfma_f32_16x16x32_bf16 v[160:163], v[128:131], v[128:131], v[160:163]\n"
        "v_mfma_f32_16x16x32_bf16 v[176:179], v[116:119], v[128:131], v[176:179]\n"
        "s_add_u32 m0, s63, 11264\n"
        "s_add_u32 s55, s62, 90112\n"
        "buffer_load_dwordx4 v105, %[sq], s55 offen lds\n"
        "ds_read_b128 v[152:155], v114 offset:768\n"
        "s_waitcnt lgkmcnt(5)\n"
        "v_mfma_f32_16x16x32_bf16 v[160:163], v[132:135], v[132:135], v[160:163]\n"
        "v_mfma_f32_16x16x32_bf16 v[176:179], v[116:119], v[132:135], v[176:179]\n"
        "s_add_u32 m0, s63, 12288\n"
        "s_add_u32 s55, s62, 98304\n"
        "buffer_load_dwordx4 v102, %[sq], s55 offen lds\n"
        "ds_read_b128 v[156:159], v115 offset:768\n"
        "s_waitcnt lgkmcnt(5)\n"
        "v_mfma_f32_16x16x32_bf16 v[160:163], v[136:139], v[136:139], v[160:163]\n"
        "v_mfma_f32_16x16x32_bf16 v[176:179], v[116:119], v[136:139], v[176:179]\n"
        "s_waitcnt lgkmcnt(4)\n"
        "v_mfma_f32_16x16x32_bf16 v[160:163], v[140:143], v[140:143], v[160:163]\n"
        "v_mfma_f32_16x16x32_bf16 v[176:179], v[116:119], v[140:143], v[176:179]\n"
        "s_add_u32 m0, s63, 13312\n"
        "s_add_u32 s55, s62, 106496\n"
        "buffer_load_dwordx4 v105, %[sq], s55 offen lds\n"
        "s_waitcnt lgkmcnt(3)\n"
        "v_mfma_f32_16x16x32_bf16 v[160:163], v[144:147], v[144:147], v[160:163]\n"
        "v_mfma_f32_16x16x32_bf16 v[176:179], v[116:119], v[144:147], v[176:179]\n"
        "s_add_u32 m0, s63, 14336\n"
        "s_add_u32 s55, s62, 114688\n"
        "buffer_load_dwordx4 v102, %[sq], s55 offen lds\n"
        "s_waitcnt lgkmcnt(2)\n"
        "v_mfma_f32_16x16x32_bf16 v[160:163], v[148:151], v[148:151], v[160:163]\n"
        "v_mfma_f32_16x16x32_bf16 v[176:179], v[116:119], v[148:151], v[176:179]\n"
        "s_waitcnt lgkmcnt(1)\n"
        "v_mfma_f32_16x16x32_bf16 v[160:163], v[152:155], v[152:155], v[160:163]\n"
        "v_mfma_f32_16x16x32_bf16 v[176:179], v[116:119], v[152:155], v[176:179]\n"
        "s_add_u32 m0, s63, 15360\n"
        "s_add_u32 s55, s62, 122880\n"
        "buffer_load_dwordx4 v105, %[sq], s55 offen lds\n"
        "s_waitcnt lgkmcnt(0)\n"
        "v_mfma_f32_16x16x32_bf16 v[160:163], v[156:159], v[156:159], v[160:163]\n"
        "v_mfma_f32_16x16x32_bf16 v[176:179], v[116:119], v[156:159], v[176:179]\n"
        "s_lshl_b32 s54, s38, 6\n"
        "v_add_u32 v168, s54, v8\n"
        "s_mov_b32 vcc_lo, 0x22222222\n"
        "s_mov_b32 vcc_hi, 0x22222222\n"
        "s_nop 2\n"
        "v_cndmask_b32 v160, v160, v161, vcc\n"
        "s_mov_b32 vcc_lo, 0x44444444\n"
        "s_mov_b32 vcc_hi, 0x44444444\n"
        "v_cndmask_b32 v160, v160, v162, vcc\n"
        "s_mov_b32 vcc_lo, 0x88888888\n"
        "s_mov_b32 vcc_hi, 0x88888888\n"
        "v_cndmask_b32 v160, v160, v163, vcc\n"
        "v_mul_f32 v176, 0x3b000000, v176\n"
        "v_mul_f32 v160, 0x3b000000, v160\n"
        "v_fma_f32 v160, -v176, v176, v160\n"
        "v_and_b32 v160, 0x7fffffff, v160\n"
        "v_add_f32 v160, 0x3727c5ac, v160\n"
        "v_rsq_f32 v160, v160\n"
        "s_mov_b32 exec_lo, 0x00f0000f\n"
        "s_mov_b32 exec_hi, 0xf0000f00\n"
        "ds_write2_b32 v168, v176, v160 offset1:64\n"
        "s_mov_b64 exec, -1\n"
        "s_waitcnt vmcnt(0) lgkmcnt(0)\n"
        "s_barrier\n"
        "s_and_b32 s55, s43, 1\n"
        "s_lshl_b32 s61, s55, 13\n"
        "s_add_u32 s61, s61, s49\n"
        "s_add_u32 s61, s61, 131072\n"
        "s_mul_i32 s55, s55, s42\n"
        "s_lshl_b32 s55, s55, 3\n"
        "s_add_u32 s60, s55, s41\n"
        "v_xor_b32 v105, 64, v103\n"
        "s_add_u32 m0, s61, 0\n"
        "s_nop 0\n"
        "buffer_load_dwordx4 v103, %[sp], s60 offen lds\n"
        "s_add_u32 s60, s60, s42\n"
        "s_add_u32 m0, s61, 1024\n"
        "s_nop 0\n"
        "buffer_load_dwordx4 v105, %[sp], s60 offen lds\n"
        "s_add_u32 s60, s60, s42\n"
        "s_add_u32 m0, s61, 2048\n"
        "s_nop 0\n"
        "buffer_load_dwordx4 v103, %[sp], s60 offen lds\n"
        "s_add_u32 s60, s60, s42\n"
        "s_add_u32 m0, s61, 3072\n"
        "s_nop 0\n"
        "buffer_load_dwordx4 v105, %[sp], s60 offen lds\n"
        "s_add_u32 s60, s60, s42\n"
        "s_add_u32 m0, s61, 4096\n"
        "s_nop 0\n"
        "buffer_load_dwordx4 v103, %[sp], s60 offen lds\n"
        "s_add_u32 s60, s60, s42\n"
        "s_add_u32 m0, s61, 5120\n"
        "s_nop 0\n"
        "buffer_load_dwordx4 v105, %[sp], s60 offen lds\n"
        "s_add_u32 s60, s60, s42\n"
        "s_add_u32 m0, s61, 6144\n"
        "s_nop 0\n"
        "buffer_load_dwordx4 v103, %[sp], s60 offen lds\n"
        "s_add_u32 s60, s60, s42\n"
        "s_add_u32 m0, s61, 7168\n"
        "s_nop 0\n"
        "buffer_load_dwordx4 v105, %[sp], s60 offen lds\n"
        "s_add_u32 s60, s60, s42\n"
        "s_branch Lpq_%=\n"
        "; ---- prologue: statistics of two token tiles\n"
        "Lpr1_%=:\n"
        "s_add_u32 s60, s38, 3\n"
        "s_and_b32 s60, s60, 3\n"
        "s_lshl_b32 s54, s38, 14\n"
        "v_add_u32 v112, s54, v96\n"
        "v_add_u32 v113, s54, v97\n"
        "v_add_u32 v114, s54, v98\n"
        "v_add_u32 v115, s54, v99\n"
        "s_lshl_b32 s54, s60, 14\n"
        "v_add_u32 v124, s54, v96\n"
        "v_add_u32 v125, s54, v97\n"
        "v_add_u32 v126, s54, v98\n"
        "v_add_u32 v127, s54, v99\n"
        "ds_read_b128 v[128:131], v112 offset:0\n"
        "ds_read_b128 v[132:135], v124 offset:0\n"
        "ds_read_b128 v[136:139], v113 offset:0\n"
        "ds_read_b128 v[140:143], v125 offset:0\n"
        "ds_read_b128 v[144:147], v114 offset:0\n"
        "ds_read_b128 v[148:151], v126 offset:0\n"
        "ds_read_b128 v[152:155], v115 offset:0\n"
        "ds_read_b128 v[156:159], v127 offset:0\n"
        "s_waitcnt lgkmcnt(7)\n"
        "v_mfma_f32_16x16x32_bf16 v[160:163], v[128:131], v[128:131], 0\n"
        "v_mfma_f32_16x16x32_bf16 v[176:179], v[116:119], v[128:131], 0\n"
        "s_add_u32 m0, s63, 6144\n"
        "s_add_u32 s55, s62, 49152\n"
        "buffer_load_dwordx4 v102, %[sq], s55 offen lds\n"
        "ds_read_b128 v[128:131], v112 offset:256\n"
        "s_waitcnt lgkmcnt(7)\n"
        "v_mfma_f32_16x16x32_bf16 v[164:167], v[132:135], v[132:135], 0\n"
        "v_mfma_f32_16x16x32_bf16 v[180:183], v[116:119], v[132:135], 0\n"
        "s_add_u32 m0, s63, 7168\n"
        "s_add_u32 s55, s62, 57344\n"
        "buffer_load_dwordx4 v105, %[sq], s55 offen lds\n"
        "ds_read_b128 v[132:135], v124 offset:256\n"
        "s_waitcnt lgkmcnt(7)\n"
        "v_mfma_f32_16x16x32_bf16 v[160:163], v[136:139], v[136:139], v[160:163]\n"
        "v_mfma_f32_16x16x32_bf16 v[176:179], v[116:119], v[136:139], v[176:179]\n"
        "ds_read_b128 v[136:139], v113 offset:256\n"
        "s_waitcnt lgkmcnt(7)\n"
        "v_mfma_f32_16x16x32_bf16 v[164:167], v[140:143], v[140:143], v[164:167]\n"
        "v_mfma_f32_16x16x32_bf16 v[180:183], v[116:119], v[140:143], v[180:183]\n"
        "s_add_u32 m0, s63, 8192\n"
        "s_add_u32 s55, s62, 65536\n"
        "buffer_load_dwordx4 v102, %[sq], s55 offen lds\n"
        "ds_read_b128 v[140:143], v125 offset:256\n"
        "s_waitcnt lgkmcnt(7)\n"
        "v_mfma_f32_16x16x32_bf16 v[160:163], v[144:147], v[144:147], v[160:163]\n"
        "v_mfma_f32_16x16x32_bf16 v[176:179], v[116:119], v[144:147], v[176:179]\n"
        "s_add_u32 m0, s63, 9216\n"
        "s_add_u32 s55, s62, 73728\n"
        "buffer_load_dwordx4 v105, %[sq], s55 offen lds\n"
        "ds_read_b128 v[144:147], v114 offset:256\n"
        "s_waitcnt lgkmcnt(7)\n"
        "v_mfma_f32_16x16x32_bf16 v[164:167], v[148:151], v[148:151], v[164:167]\n"
        "v_mfma_f32_16x16x32_bf16 v[180:183], v[116:119], v[148:151], v[180:183]\n"
        "ds_read_b128 v[148:151], v126 offset:256\n"
        "s_waitcnt lgkmcnt(7)\n"
        "v_mfma_f32_16x16x32_bf16 v[160:163], v[152:155], v[152:155], v[160:163]\n"
        "v_mfma_f32_16x16x32_bf16 v[176:179], v[116:119], v[152:155], v[176:179]\n"
        "s_add_u32 m0, s63, 10240\n"
        "s_add_u32 s55, s62, 81920\n"
        "buffer_load_dwordx4 v102, %[sq], s55 offen lds\n"
        "ds_read_b128 v[152:155], v115 offset:256\n"
        "s_waitcnt lgkmcnt(7)\n"
        "v_mfma_f32_16x16x32_bf16 v[164:167], v[156:159], v[156:159], v[164:167]\n"
        "v_mfma_f32_16x16x32_bf16 v[180:183], v[116:119], v[156:159], v[180:183]\n"
        "ds_read_b128 v[156:159], v127 offset:256\n"
        "s_waitcnt lgkmcnt(7)\n"
        "v_mfma_f32_16x16x32_bf16 v[160:163], v[128:131], v[128:131], v[160:163]\n"
        "v_mfma_f32_16x16x32_bf16 v[176:179], v[116:119], v[128:131], v[176:179]\n"
        "s_add_u32 m0, s63, 11264\n"
        "s_add_u32 s55, s62, 90112\n"
        "buffer_load_dwordx4 v105, %[sq], s55 offen lds\n"
        "ds_read_b128 v[128:131], v112 offset:512\n"
        "s_waitcnt lgkmcnt(7)\n"
        "v_mfma_f32_16x16x32_bf16 v[164:167], v[132:135], v[132:135], v[164:167]\n"
        "v_mfma_f32_16x16x32_bf16 v[180:183], v[116:119], v[132:135], v[180:183]\n"
        "s_add_u32 m0, s63, 12288\n"
        "s_add_u32 s55, s62, 98304\n"
        "buffer_load_dwordx4 v102, %[sq], s55 offen lds\n"
        "ds_read_b128 v[132:135], v124 offset:512\n"
        "s_waitcnt lgkmcnt(7)\n"
        "v_mfma_f32_16x16x32_bf16 v[160:163], v[136:139], v[136:139], v[160:163]\n"
        "v_mfma_f32_16x16x32_bf16 v[176:179], v[116:119], v[136:139], v[176:179]\n"
        "ds_read_b128 v[136:139], v113 offset:512\n"
        "s_waitcnt lgkmcnt(7)\n"
        "v_mfma_f32_16x16x32_bf16 v[164:167], v[140:143], v[140:143], v[164:167]\n"
        "v_mfma_f32_16x16x32_bf16 v[180:183], v[116:119], v[140:143], v[180:183]\n"
        "s_add_u32 m0, s63, 13312\n"
        "s_add_u32 s55, s62, 106496\n"
        "buffer_load_dwordx4 v105, %[sq], s55 offen lds\n"
        "ds_read_b128 v[140:143], v125 offset:512\n"
        "s_waitcnt lgkmcnt(7)\n"
        "v_mfma_f32_16x16x32_bf16 v[160:163], v[144:147], v[144:147], v[160:163]\n"
        "v_mfma_f32_16x16x32_bf16 v[176:179], v[116:119], v[144:147], v[176:179]\n"
        "s_add_u32 m0, s63, 14336\n"
        "s_add_u32 s55, s62, 114688\n"
        "buffer_load_dwordx4 v102, %[sq], s55 offen lds\n"
        "ds_read_b128 v[144:147], v114 offset:512\n"
        "s_waitcnt lgkmcnt(7)\n"
        "v_mfma_f32_16x16x32_bf16 v[164:167], v[148:151], v[148:151], v[164:167]\n"
        "v_mfma_f32_16x16x32_bf16 v[180:183], v[116:119], v[148:151], v[180:183]\n"
        "ds_read_b128 v[148:151], v126 offset:512\n"
        "s_waitcnt lgkmcnt(7)\n"
        "v_mfma_f32_16x16x32_bf16 v[160:163], v[152:155], v[152:155], v[160:163]\n"
        "v_mfma_f32_16x16x32_bf16 v[176:179], v[116:119], v[152:155], v[176:179]\n"
        "s_add_u32 m0, s63, 15360\n"
        "s_add_u32 s55, s62, 122880\n"
        "buffer_load_dwordx4 v105, %[sq], s55 offen lds\n"
        "ds_read_b128 v[152:155], v115 offset:512\n"
        "s_waitcnt lgkmcnt(7)\n"
        "v_mfma_f32_16x16x32_bf16 v[164:167], v[156:159], v[156:159], v[164:167]\n"
        "v_mfma_f32_16x16x32_bf16 v[180:183], v[116:119], v[156:159], v[180:183]\n"
        "ds_read_b128 v[156:159], v127 offset:512\n"
        "s_waitcnt lgkmcnt(7)\n"
        "v_mfma_f32_16x16x32_bf16 v[160:163], v[128:131], v[128:131], v[160:163]\n"
        "v_mfma_f32_16x16x32_bf16 v[176:179], v[116:119], v[128:131], v[176:179]\n"
        "ds_read_b128 v[128:131], v112 offset:768\n"
        "s_waitcnt lgkmcnt(7)\n"
        "v_mfma_f32_16x16x32_bf16 v[164:167], v[132:135], v[132:135], v[164:167]\n"
        "v_mfma_f32_16x16x32_bf16 v[180:183], v[116:119], v[132:135], v[180:183]\n"
        "ds_read_b128 v[132:135], v124 offset:768\n"
        "s_waitcnt lgkmcnt(7)\n"
        "v_mfma_f32_16x16x32_bf16 v[160:163], v[136:139], v[136:139], v[160:163]\n"
        "v_mfma_f32_16x16x32_bf16 v[176:179], v[116:119], v[136:139], v[176:179]\n"
        "ds_read_b128 v[136:139], v113 offset:768\n"
        "s_waitcnt lgkmcnt(7)\n"
        "v_mfma_f32_16x16x32_bf16 v[164:167], v[140:143], v[140:143], v[164:167]\n"
        "v_mfma_f32_16x16x32_bf16 v[180:183], v[116:119], v[140:143], v[180:183]\n"
        "ds_read_b128 v[140:143], v125 offset:768\n"
        "s_waitcnt lgkmcnt(7)\n"
        "v_mfma_f32_16x16x32_bf16 v[160:163], v[144:147], v[144:147], v[160:163]\n"
        "v_mfma_f32_16x16x32_bf16 v[176:179], v[116:119], v[144:147], v[176:179]\n"
        "ds_read_b128 v[144:147], v114 offset:768\n"
        "s_waitcnt lgkmcnt(7)\n"
        "v_mfma_f32_16x16x32_bf16 v[164:167], v[148:151], v[148:151], v[164:167]\n"
        "v_mfma_f32_16x16x32_bf16 v[180:183], v[116:119], v[148:151], v[180:183]\n"
        "ds_read_b128 v[148:151], v126 offset:768\n"
        "s_waitcnt lgkmcnt(7)\n"
        "v_mfma_f32_16x16x32_bf16 v[160:163], v[152:155], v[152:155], v[160:163]\n"
        "v_mfma_f32_16x16x32_bf16 v[176:179], v[116:119], v[152:155], v[176:179]\n"
        "ds_read_b128 v[152:155], v115 offset:768\n"
        "s_waitcnt lgkmcnt(7)\n"
        "v_mfma_f32_16x16x32_bf16 v[164:167], v[156:159], v[156:159], v[164:167]\n"
        "v_mfma_f32_16x16x32_bf16 v[180:183], v[116:119], v[156:159], v[180:183]\n"
        "ds_read_b128 v[156:159], v127 offset:768\n"
        "s_waitcnt lgkmcnt(7)\n"
        "v_mfma_f32_16x16x32_bf16 v[160:163], v[128:131], v[128:131], v[160:163]\n"
        "v_mfma_f32_16x16x32_bf16 v[176:179], v[116:119], v[128:131], v[176:179]\n"
        "s_waitcnt lgkmcnt(6)\n"
        "v_mfma_f32_16x16x32_bf16 v[164:167], v[132:135], v[132:135], v[164:167]\n"
        "v_mfma_f32_16x16x32_bf16 v[180:183], v[116:119], v[132:135], v[180:183]\n"
        "s_waitcnt lgkmcnt(5)\n"
        "v_mfma_f32_16x16x32_bf16 v[160:163], v[136:139], v[136:139], v[160:163]\n"
        "v_mfma_f32_16x16x32_bf16 v[176:179], v[116:119], v[136:139], v[176:179]\n"
        "s_waitcnt lgkmcnt(4)\n"
        "v_mfma_f32_16x16x32_bf16 v[164:167], v[140:143], v[140:143], v[164:167]\n"
        "v_mfma_f32_16x16x32_bf16 v[180:183], v[116:119], v[140:143], v[180:183]\n"
        "s_waitcnt lgkmcnt(3)\n"
        "v_mfma_f32_16x16x32_bf16 v[160:163], v[144:147], v[144:147], v[160:163]\n"
        "v_mfma_f32_16x16x32_bf16 v[176:179], v[116:119], v[144:147], v[176:179]\n"
        "s_waitcnt lgkmcnt(2)\n"
        "v_mfma_f32_16x16x32_bf16 v[164:167], v[148:151], v[148:151], v[164:167]\n"
        "v_mfma_f32_16x16x32_bf16 v[180:183], v[116:119], v[148:151], v[180:183]\n"
        "s_waitcnt lgkmcnt(1)\n"
        "v_mfma_f32_16x16x32_bf16 v[160:163], v[152:155], v[152:155], v[160:163]\n"
        "v_mfma_f32_16x16x32_bf16 v[176:179], v[116:119], v[152:155], v[176:179]\n"
        "s_waitcnt lgkmcnt(0)\n"
        "v_mfma_f32_16x16x32_bf16 v[164:167], v[156:159], v[156:159], v[164:167]\n"
        "v_mfma_f32_16x16x32_bf16 v[180:183], v[116:119], v[156:159], v[180:183]\n"
        "s_lshl_b32 s54, s38, 6\n"
        "v_add_u32 v168, s54, v8\n"
        "s_mov_b32 vcc_lo, 0x22222222\n"
        "s_mov_b32 vcc_hi, 0x22222222\n"
        "v_cndmask_b32 v160, v160, v161, vcc\n"
        "s_mov_b32 vcc_lo, 0x44444444\n"
        "s_mov_b32 vcc_hi, 0x44444444\n"
        "v_cndmask_b32 v160, v160, v162, vcc\n"
        "s_mov_b32 vcc_lo, 0x88888888\n"
        "s_mov_b32 vcc_hi, 0x88888888\n"
        "v_cndmask_b32 v160, v160, v163, vcc\n"
        "v_mul_f32 v176, 0x3b000000, v176\n"
        "v_mul_f32 v160, 0x3b000000, v160\n"
        "v_fma_f32 v160, -v176, v176, v160\n"
        "v_and_b32 v160, 0x7fffffff, v160\n"
        "v_add_f32 v160, 0x3727c5ac, v160\n"
        "v_rsq_f32 v160, v160\n"
        "s_mov_b32 exec_lo, 0x00f0000f\n"
        "s_mov_b32 exec_hi, 0xf0000f00\n"
        "ds_write2_b32 v168, v176, v160 offset1:64\n"
        "s_mov_b64 exec, -1\n"
        "s_lshl_b32 s54, s60, 6\n"
        "v_add_u32 v169, s54, v8\n"
        "s_mov_b32 vcc_lo, 0x22222222\n"
        "s_mov_b32 vcc_hi, 0x22222222\n"
        "v_cndmask_b32 v164, v164, v165, vcc\n"
        "s_mov_b32 vcc_lo, 0x44444444\n"
        "s_mov_b32 vcc_hi, 0x44444444\n"
        "v_cndmask_b32 v164, v164, v166, vcc\n"
        "s_mov_b32 vcc_lo, 0x88888888\n"
        "s_mov_b32 vcc_hi, 0x88888888\n"
        "v_cndmask_b32 v164, v164, v167, vcc\n"
        "v_mul_f32 v180, 0x3b000000, v180\n"
        "v_mul_f32 v164, 0x3b000000, v164\n"
        "v_fma_f32 v164, -v180, v180, v164\n"
        "v_and_b32 v164, 0x7fffffff, v164\n"
        "v_add_f32 v164, 0x3727c5ac, v164\n"
        "v_rsq_f32 v164, v164\n"
        "s_mov_b32 exec_lo, 0x00f0000f\n"
        "s_mov_b32 exec_hi, 0xf0000f00\n"
        "ds_write2_b32 v169, v180, v164 offset1:64\n"
        "s_mov_b64 exec, -1\n"
        "s_waitcnt vmcnt(0) lgkmcnt(0)\n"
        "s_barrier\n"
        "s_branch Lpq_%=\n"
        "; ---- prologue: the K or V tile\n"
        "Lpkw_%=:\n"
        "s_and_b32 s60, s86, 3\n"
        "s_lshl_b32 s54, s60, 14\n"
        "v_add_u32 v112, s54, v96\n"
        "v_add_u32 v113, s54, v97\n"
        "v_add_u32 v114, s54, v98\n"
        "v_add_u32 v115, s54, v99\n"
        "s_and_b32 s54, s69, 15\n"
        "s_lshl_b32 s54, s54, 10\n"
        "v_add_u32 v122, s54, v100\n"
        "s_lshl_b32 s60, s60, 6\n"
        "s_cmp_lt_u32 s86, 4\n"
        "s_cbranch_scc0 Lpkv_%=\n"
        "ds_read_b64_tr_b16 v[32:33], v100 offset:0\n"
        "ds_read_b64_tr_b16 v[34:35], v100 offset:128\n"
        "ds_read_b128 v[128:131], v112 offset:0\n"
        "ds_read_b64_tr_b16 v[36:37], v100 offset:1024\n"
        "ds_read_b64_tr_b16 v[38:39], v100 offset:1152\n"
        "ds_read_b128 v[132:135], v113 offset:0\n"
        "ds_read_b64_tr_b16 v[40:41], v100 offset:2048\n"
        "ds_read_b64_tr_b16 v[42:43], v100 offset:2176\n"
        "ds_read_b128 v[136:139], v114 offset:0\n"
        "ds_read_b64_tr_b16 v[44:45], v100 offset:3072\n"
        "ds_read_b64_tr_b16 v[46:47], v100 offset:3200\n"
        "ds_read_b128 v[140:143], v115 offset:0\n"
        "s_nop 1\n"
        "s_waitcnt lgkmcnt(9)\n"
        "v_mfma_f32_16x16x32_bf16 v[160:163], v[32:35], v[128:131], 0\n"
        "v_mfma_f32_16x16x32_bf16 v[176:179], v[32:35], v[116:119], 0\n"
        "s_add_u32 m0, s63, 6144\n"
        "s_add_u32 s55, s62, 49152\n"
        "buffer_load_dwordx4 v102, %[sq], s55 offen lds\n"
        "ds_read_b64_tr_b16 v[48:49], v100 offset:4096\n"
        "ds_read_b64_tr_b16 v[50:51], v100 offset:4224\n"
        "ds_read_b128 v[144:147], v112 offset:256\n"
        "s_waitcnt lgkmcnt(9)\n"
        "v_mfma_f32_16x16x32_bf16 v[160:163], v[36:39], v[132:135], v[160:163]\n"
        "v_mfma_f32_16x16x32_bf16 v[176:179], v[36:39], v[116:119], v[176:179]\n"
        "s_add_u32 m0, s63, 7168\n"
        "s_add_u32 s55, s62, 57344\n"
        "buffer_load_dwordx4 v105, %[sq], s55 offen lds\n"
        "ds_read_b64_tr_b16 v[52:53], v100 offset:5120\n"
        "ds_read_b64_tr_b16 v[54:55], v100 offset:5248\n"
        "ds_read_b128 v[148:151], v113 offset:256\n"
        "s_waitcnt lgkmcnt(9)\n"
        "v_mfma_f32_16x16x32_bf16 v[160:163], v[40:43], v[136:139], v[160:163]\n"
        "v_mfma_f32_16x16x32_bf16 v[176:179], v[40:43], v[116:119], v[176:179]\n"
        "ds_read_b64_tr_b16 v[56:57], v100 offset:6144\n"
        "ds_read_b64_tr_b16 v[58:59], v100 offset:6272\n"
        "ds_read_b128 v[152:155], v114 offset:256\n"
        "s_waitcnt lgkmcnt(9)\n"
        "v_mfma_f32_16x16x32_bf16 v[160:163], v[44:47], v[140:143], v[160:163]\n"
        "v_mfma_f32_16x16x32_bf16 v[176:179], v[44:47], v[116:119], v[176:179]\n"
        "s_add_u32 m0, s63, 8192\n"
        "s_add_u32 s55, s62, 65536\n"
        "buffer_load_dwordx4 v102, %[sq], s55 offen lds\n"
        "ds_read_b64_tr_b16 v[60:61], v100 offset:7168\n"
        "ds_read_b64_tr_b16 v[62:63], v100 offset:7296\n"
        "ds_read_b128 v[156:159], v115 offset:256\n"
        "s_waitcnt lgkmcnt(9)\n"
        "v_mfma_f32_16x16x32_bf16 v[160:163], v[48:51], v[144:147], v[160:163]\n"
        "v_mfma_f32_16x16x32_bf16 v[176:179], v[48:51], v[116:119], v[176:179]\n"
        "s_add_u32 m0, s63, 9216\n"
        "s_add_u32 s55, s62, 73728\n"
        "buffer_load_dwordx4 v105, %[sq], s55 offen lds\n"
        "ds_read_b64_tr_b16 v[64:65], v100 offset:8192\n"
        "ds_read_b64_tr_b16 v[66:67], v100 offset:8320\n"
        "ds_read_b128 v[128:131], v112 offset:512\n"
        "s_waitcnt lgkmcnt(9)\n"
        "v_mfma_f32_16x16x32_bf16 v[160:163], v[52:55], v[148:151], v[160:163]\n"
        "v_mfma_f32_16x16x32_bf16 v[176:179], v[52:55], v[116:119], v[176:179]\n"
        "ds_read_b64_tr_b16 v[68:69], v100 offset:9216\n"
        "ds_read_b64_tr_b16 v[70:71], v100 offset:9344\n"
        "ds_read_b128 v[132:135], v113 offset:512\n"
        "s_waitcnt lgkmcnt(9)\n"
        "v_mfma_f32_16x16x32_bf16 v[160:163], v[56:59], v[152:155], v[160:163]\n"
        "v_mfma_f32_16x16x32_bf16 v[176:179], v[56:59], v[116:119], v[176:179]\n"
        "s_add_u32 m0, s63, 10240\n"
        "s_add_u32 s55, s62, 81920\n"
        "buffer_load_dwordx4 v102, %[sq], s55 offen lds\n"
        "ds_read_b64_tr_b16 v[72:73], v100 offset:10240\n"
        "ds_read_b64_tr_b16 v[74:75], v100 offset:10368\n"
        "ds_read_b128 v[136:139], v114 offset:512\n"
        "s_waitcnt lgkmcnt(9)\n"
        "v_mfma_f32_16x16x32_bf16 v[160:163], v[60:63], v[156:159], v[160:163]\n"
        "v_mfma_f32_16x16x32_bf16 v[176:179], v[60:63], v[116:119], v[176:179]\n"
        "ds_read_b64_tr_b16 v[76:77], v100 offset:11264\n"
        "ds_read_b64_tr_b16 v[78:79], v100 offset:11392\n"
        "ds_read_b128 v[140:143], v115 offset:512\n"
        "s_waitcnt lgkmcnt(9)\n"
        "v_mfma_f32_16x16x32_bf16 v[160:163], v[64:67], v[128:131], v[160:163]\n"
        "v_mfma_f32_16x16x32_bf16 v[176:179], v[64:67], v[116:119], v[176:179]\n"
        "s_add_u32 m0, s63, 11264\n"
        "s_add_u32 s55, s62, 90112\n"
        "buffer_load_dwordx4 v105, %[sq], s55 offen lds\n"
        "ds_read_b64_tr_b16 v[80:81], v100 offset:12288\n"
        "ds_read_b64_tr_b16 v[82:83], v100 offset:12416\n"
        "ds_read_b128 v[144:147], v112 offset:768\n"
        "s_waitcnt lgkmcnt(9)\n"
        "v_mfma_f32_16x16x32_bf16 v[160:163], v[68:71], v[132:135], v[160:163]\n"
        "v_mfma_f32_16x16x32_bf16 v[176:179], v[68:71], v[116:119], v[176:179]\n"
        "s_add_u32 m0, s63, 12288\n"
        "s_add_u32 s55, s62, 98304\n"
        "buffer_load_dwordx4 v102, %[sq], s55 offen lds\n"
        "ds_read_b64_tr_b16 v[84:85], v100 offset:13312\n"
        "ds_read_b64_tr_b16 v[86:87], v100 offset:13440\n"
        "ds_read_b128 v[148:151], v113 offset:768\n"
        "s_waitcnt lgkmcnt(9)\n"
        "v_mfma_f32_16x16x32_bf16 v[160:163], v[72:75], v[136:139], v[160:163]\n"
        "v_mfma_f32_16x16x32_bf16 v[176:179], v[72:75], v[116:119], v[176:179]\n"
        "ds_read_b64_tr_b16 v[88:89], v100 offset:14336\n"
        "ds_read_b64_tr_b16 v[90:91], v100 offset:14464\n"
        "ds_read_b128 v[152:155], v114 offset:768\n"
        "s_waitcnt lgkmcnt(9)\n"
        "v_mfma_f32_16x16x32_bf16 v[160:163], v[76:79], v[140:143], v[160:163]\n"
        "v_mfma_f32_16x16x32_bf16 v[176:179], v[76:79], v[116:119], v[176:179]\n"
        "s_add_u32 m0, s63, 13312\n"
        "s_add_u32 s55, s62, 106496\n"
        "buffer_load_dwordx4 v105, %[sq], s55 offen lds\n"
        "ds_read_b64_tr_b16 v[92:93], v100 offset:15360\n"
        "ds_read_b64_tr_b16 v[94:95], v100 offset:15488\n"
        "ds_read_b128 v[156:159], v115 offset:768\n"
        "s_waitcnt lgkmcnt(9)\n"
        "v_mfma_f32_16x16x32_bf16 v[160:163], v[80:83], v[144:147], v[160:163]\n"
        "v_mfma_f32_16x16x32_bf16 v[176:179], v[80:83], v[116:119], v[176:179]\n"
        "s_add_u32 m0, s63, 14336\n"
        "s_add_u32 s55, s62, 114688\n"
        "buffer_load_dwordx4 v102, %[sq], s55 offen lds\n"
        "s_waitcnt lgkmcnt(6)\n"
        "v_mfma_f32_16x16x32_bf16 v[160:163], v[84:87], v[148:151], v[160:163]\n"
        "v_mfma_f32_16x16x32_bf16 v[176:179], v[84:87], v[116:119], v[176:179]\n"
        "s_waitcnt lgkmcnt(3)\n"
        "v_mfma_f32_16x16x32_bf16 v[160:163], v[88:91], v[152:155], v[160:163]\n"
        "v_mfma_f32_16x16x32_bf16 v[176:179], v[88:91], v[116:119], v[176:179]\n"
        "s_add_u32 m0, s63, 15360\n"
        "s_add_u32 s55, s62, 122880\n"
        "buffer_load_dwordx4 v105, %[sq], s55 offen lds\n"
        "s_waitcnt lgkmcnt(0)\n"
        "v_mfma_f32_16x16x32_bf16 v[160:163], v[92:95], v[156:159], v[160:163]\n"
        "v_mfma_f32_16x16x32_bf16 v[176:179], v[92:95], v[116:119], v[176:179]\n"
        "ds_read_b64_tr_b16 v[124:125], v122\n"
        "ds_read_b64_tr_b16 v[126:127], v122 offset:128\n"
        "s_waitcnt vmcnt(0) lgkmcnt(0)\n"
        "s_barrier\n"
        "v_add_u32 v193, s60, v8\n"
        "ds_read2_b32 v[180:181], v193 offset1:64\n"
        "s_waitcnt lgkmcnt(0)\n"
        "s_nop 0\n"
        "v_pk_fma_f32 v[160:161], v[180:181], v[176:177], v[160:161] op_sel_hi:[0,1,1] neg_lo:[1,0,0] neg_hi:[1,0,0]\n"
        "v_pk_fma_f32 v[162:163], v[180:181], v[178:179], v[162:163] op_sel_hi:[0,1,1] neg_lo:[1,0,0] neg_hi:[1,0,0]\n"
        "v_pk_mul_f32 v[160:161], v[180:181], v[160:161] op_sel:[1,0] op_sel_hi:[1,1]\n"
        "v_pk_mul_f32 v[162:163], v[180:181], v[162:163] op_sel:[1,0] op_sel_hi:[1,1]\n"
        "v_pk_mul_f32 v[184:185], v[160:161], v[160:161]\n"
        "v_pk_fma_f32 v[184:185], v[162:163], v[162:163], v[184:185]\n"
        "v_add_f32 v184, v184, v185\n"
        "v_mov_b32 v185, v184\n"
        "s_nop 1\n"
        "v_permlane16_swap_b32 v184, v185\n"
        "v_add_f32 v184, v184, v185\n"
        "v_mov_b32 v185, v184\n"
        "s_nop 1\n"
        "v_permlane32_swap_b32 v184, v185\n"
        "v_add_f32 v184, v184, v185\n"
        "s_branch Lpkj_%=\n"
        "Lpkv_%=:\n"
        "ds_read_b64_tr_b16 v[32:33], v100 offset:0\n"
        "ds_read_b64_tr_b16 v[34:35], v100 offset:128\n"
        "ds_read_b128 v[128:131], v112 offset:0\n"
        "ds_read_b64_tr_b16 v[36:37], v100 offset:1024\n"
        "ds_read_b64_tr_b16 v[38:39], v100 offset:1152\n"
        "ds_read_b128 v[132:135], v113 offset:0\n"
        "ds_read_b64_tr_b16 v[40:41], v100 offset:2048\n"
        "ds_read_b64_tr_b16 v[42:43], v100 offset:2176\n"
        "ds_read_b128 v[136:139], v114 offset:0\n"
        "ds_read_b64_tr_b16 v[44:45], v100 offset:3072\n"
        "ds_read_b64_tr_b16 v[46:47], v100 offset:3200\n"
        "ds_read_b128 v[140:143], v115 offset:0\n"
        "s_nop 1\n"
        "s_waitcnt lgkmcnt(9)\n"
        "v_mfma_f32_16x16x32_bf16 v[160:163], v[128:131], v[32:35], 0\n"
        "v_mfma_f32_16x16x32_bf16 v[176:179], v[116:119], v[32:35], 0\n"
        "s_add_u32 m0, s63, 6144\n"
        "s_add_u32 s55, s62, 49152\n"
        "buffer_load_dwordx4 v102, %[sq], s55 offen lds\n"
        "ds_read_b64_tr_b16 v[48:49], v100 offset:4096\n"
        "ds_read_b64_tr_b16 v[50:51], v100 offset:4224\n"
        "ds_read_b128 v[144:147], v112 offset:256\n"
        "s_waitcnt lgkmcnt(9)\n"
        "v_mfma_f32_16x16x32_bf16 v[160:163], v[132:135], v[36:39], v[160:163]\n"
        "v_mfma_f32_16x16x32_bf16 v[176:179], v[116:119], v[36:39], v[176:179]\n"
        "s_add_u32 m0, s63, 7168\n"
        "s_add_u32 s55, s62, 57344\n"
        "buffer_load_dwordx4 v105, %[sq], s55 offen lds\n"
        "ds_read_b64_tr_b16 v[52:53], v100 offset:5120\n"
        "ds_read_b64_tr_b16 v[54:55], v100 offset:5248\n"
        "ds_read_b128 v[148:151], v113 offset:256\n"
        "s_waitcnt lgkmcnt(9)\n"
        "v_mfma_f32_16x16x32_bf16 v[160:163], v[136:139], v[40:43], v[160:163]\n"
        "v_mfma_f32_16x16x32_bf16 v[176:179], v[116:119], v[40:43], v[176:179]\n"
        "ds_read_b64_tr_b16 v[56:57], v100 offset:6144\n"
        "ds_read_b64_tr_b16 v[58:59], v100 offset:6272\n"
        "ds_read_b128 v[152:155], v114 offset:256\n"
        "s_waitcnt lgkmcnt(9)\n"
        "v_mfma_f32_16x16x32_bf16 v[160:163], v[140:143], v[44:47], v[160:163]\n"
        "v_mfma_f32_16x16x32_bf16 v[176:179], v[116:119], v[44:47], v[176:179]\n"
        "s_add_u32 m0, s63, 8192\n"
        "s_add_u32 s55, s62, 65536\n"
        "buffer_load_dwordx4 v102, %[sq], s55 offen lds\n"
        "ds_read_b64_tr_b16 v[60:61], v100 offset:7168\n"
        "ds_read_b64_tr_b16 v[62:63], v100 offset:7296\n"
        "ds_read_b128 v[156:159], v115 offset:256\n"
        "s_waitcnt lgkmcnt(9)\n"
        "v_mfma_f32_16x16x32_bf16 v[160:163], v[144:147], v[48:51], v[160:163]\n"
        "v_mfma_f32_16x16x32_bf16 v[176:179], v[116:119], v[48:51], v[176:179]\n"
        "s_add_u32 m0, s63, 9216\n"
        "s_add_u32 s55, s62, 73728\n"
        "buffer_load_dwordx4 v105, %[sq], s55 offen lds\n"
        "ds_read_b64_tr_b16 v[64:65], v100 offset:8192\n"
        "ds_read_b64_tr_b16 v[66:67], v100 offset:8320\n"
        "ds_read_b128 v[128:131], v112 offset:512\n"
        "s_waitcnt lgkmcnt(9)\n"
        "v_mfma_f32_16x16x32_bf16 v[160:163], v[148:151], v[52:55], v[160:163]\n"
        "v_mfma_f32_16x16x32_bf16 v[176:179], v[116:119], v[52:55], v[176:179]\n"
        "ds_read_b64_tr_b16 v[68:69], v100 offset:9216\n"
        "ds_read_b64_tr_b16 v[70:71], v100 offset:9344\n"
        "ds_read_b128 v[132:135], v113 offset:512\n"
        "s_waitcnt lgkmcnt(9)\n"
        "v_mfma_f32_16x16x32_bf16 v[160:163], v[152:155], v[56:59], v[160:163]\n"
        "v_mfma_f32_16x16x32_bf16 v[176:179], v[116:119], v[56:59], v[176:179]\n"
        "s_add_u32 m0, s63, 10240\n"
        "s_add_u32 s55, s62, 81920\n"
        "buffer_load_dwordx4 v102, %[sq], s55 offen lds\n"
        "ds_read_b64_tr_b16 v[72:73], v100 offset:10240\n"
        "ds_read_b64_tr_b16 v[74:75], v100 offset:10368\n"
        "ds_read_b128 v[136:139], v114 offset:512\n"
        "s_waitcnt lgkmcnt(9)\n"
        "v_mfma_f32_16x16x32_bf16 v[160:163], v[156:159], v[60:63], v[160:163]\n"
        "v_mfma_f32_16x16x32_bf16 v[176:179], v[116:119], v[60:63], v[176:179]\n"
        "ds_read_b64_tr_b16 v[76:77], v100 offset:11264\n"
        "ds_read_b64_tr_b16 v[78:79], v100 offset:11392\n"
        "ds_read_b128 v[140:143], v115 offset:512\n"
        "s_waitcnt lgkmcnt(9)\n"
        "v_mfma_f32_16x16x32_bf16 v[160:163], v[128:131], v[64:67], v[160:163]\n"
        "v_mfma_f32_16x16x32_bf16 v[176:179], v[116:119], v[64:67], v[176:179]\n"
        "s_add_u32 m0, s63, 11264\n"
        "s_add_u32 s55, s62, 90112\n"
        "buffer_load_dwordx4 v105, %[sq], s55 offen lds\n"
        "ds_read_b64_tr_b16 v[80:81], v100 offset:12288\n"
        "ds_read_b64_tr_b16 v[82:83], v100 offset:12416\n"
        "ds_read_b128 v[144:147], v112 offset:768\n"
        "s_waitcnt lgkmcnt(9)\n"
        "v_mfma_f32_16x16x32_bf16 v[160:163], v[132:135], v[68:71], v[160:163]\n"
        "v_mfma_f32_16x16x32_bf16 v[176:179], v[116:119], v[68:71], v[176:179]\n"
        "s_add_u32 m0, s63, 12288\n"
        "s_add_u32 s55, s62, 98304\n"
        "buffer_load_dwordx4 v102, %[sq], s55 offen lds\n"
        "ds_read_b64_tr_b16 v[84:85], v100 offset:13312\n"
        "ds_read_b64_tr_b16 v[86:87], v100 offset:13440\n"
        "ds_read_b128 v[148:151], v113 offset:768\n"
        "s_waitcnt lgkmcnt(9)\n"
        "v_mfma_f32_16x16x32_bf16 v[160:163], v[136:139], v[72:75], v[160:163]\n"
        "v_mfma_f32_16x16x32_bf16 v[176:179], v[116:119], v[72:75], v[176:179]\n"
        "ds_read_b64_tr_b16 v[88:89], v100 offset:14336\n"
        "ds_read_b64_tr_b16 v[90:91], v100 offset:14464\n"
        "ds_read_b128 v[152:155], v114 offset:768\n"
        "s_waitcnt lgkmcnt(9)\n"
        "v_mfma_f32_16x16x32_bf16 v[160:163], v[140:143], v[76:79], v[160:163]\n"
        "v_mfma_f32_16x16x32_bf16 v[176:179], v[116:119], v[76:79], v[176:179]\n"
        "s_add_u32 m0, s63, 13312\n"
        "s_add_u32 s55, s62, 106496\n"
        "buffer_load_dwordx4 v105, %[sq], s55 offen lds\n"
        "ds_read_b64_tr_b16 v[92:93], v100 offset:15360\n"
        "ds_read_b64_tr_b16 v[94:95], v100 offset:15488\n"
        "ds_read_b128 v[156:159], v115 offset:768\n"
        "s_waitcnt lgkmcnt(9)\n"
        "v_mfma_f32_16x16x32_bf16 v[160:163], v[144:147], v[80:83], v[160:163]\n"
        "v_mfma_f32_16x16x32_bf16 v[176:179], v[116:119], v[80:83], v[176:179]\n"
        "s_add_u32 m0, s63, 14336\n"
        "s_add_u32 s55, s62, 114688\n"
        "buffer_load_dwordx4 v102, %[sq], s55 offen lds\n"
        "s_waitcnt lgkmcnt(6)\n"
        "v_mfma_f32_16x16x32_bf16 v[160:163], v[148:151], v[84:87], v[160:163]\n"
        "v_mfma_f32_16x16x32_bf16 v[176:179], v[116:119], v[84:87], v[176:179]\n"
        "s_waitcnt lgkmcnt(3)\n"
        "v_mfma_f32_16x16x32_bf16 v[160:163], v[152:155], v[88:91], v[160:163]\n"
        "v_mfma_f32_16x16x32_bf16 v[176:179], v[116:119], v[88:91], v[176:179]\n"
        "s_add_u32 m0, s63, 15360\n"
        "s_add_u32 s55, s62, 122880\n"
        "buffer_load_dwordx4 v105, %[sq], s55 offen lds\n"
        "s_waitcnt lgkmcnt(0)\n"
        "v_mfma_f32_16x16x32_bf16 v[160:163], v[156:159], v[92:95], v[160:163]\n"
        "v_mfma_f32_16x16x32_bf16 v[176:179], v[116:119], v[92:95], v[176:179]\n"
        "ds_read_b64_tr_b16 v[124:125], v122\n"
        "ds_read_b64_tr_b16 v[126:127], v122 offset:128\n"
        "s_waitcnt vmcnt(0) lgkmcnt(0)\n"
        "s_barrier\n"
        "v_add_u32 v193, s60, v9\n"
        "ds_read_b128 v[180:183], v193\n"
        "ds_read_b128 v[188:191], v193 offset:256\n"
        "s_waitcnt lgkmcnt(0)\n"
        "v_pk_fma_f32 v[160:161], v[180:181], v[176:177], v[160:161] op_sel_hi:[1,0,1] neg_lo:[1,0,0] neg_hi:[1,0,0]\n"
        "v_pk_fma_f32 v[162:163], v[182:183], v[176:177], v[162:163] op_sel_hi:[1,0,1] neg_lo:[1,0,0] neg_hi:[1,0,0]\n"
        "v_pk_mul_f32 v[160:161], v[188:189], v[160:161]\n"
        "v_pk_mul_f32 v[162:163], v[190:191], v[162:163]\n"
        "v_max3_f32 v184, |v160|, |v161|, |v162|\n"
        "v_max_f32 v184, v184, |v163|\n"
        "Lpkj_%=:\n"
        "s_nop 1\n"
        "v_cvt_pk_bf16_f32 v186, v160, v161\n"
        "v_cvt_pk_bf16_f32 v187, v162, v163\n"
        "v_min_u32 v186, -2, v186\n"
        "v_min_u32 v187, -2, v187\n"
        "s_add_u32 s54, s74, 4294803456\n"
        "v_mov_b32 v188, v186\n"
        "v_mov_b32 v189, v187\n"
        "s_nop 0\n"
        "v_permlane16_swap_b32 v186, v188\n"
        "v_permlane16_swap_b32 v187, v189\n"
        "s_mov_b32 exec_lo, 0xffff\n"
        "s_mov_b32 exec_hi, 0xffff\n"
        "buffer_store_dwordx4 v[186:189], v226, %[srd], s54 offen sc1\n"
        "s_mov_b64 exec, -1\n"
        "v_max_u32_dpp v184, v184, v184 row_ror:8 row_mask:0xf bank_mask:0xf\n"
        "s_nop 1\n"
        "v_max_u32_dpp v184, v184, v184 row_ror:4 row_mask:0xf bank_mask:0xf\n"
        "s_nop 1\n"
        "v_max_u32_dpp v184, v184, v184 row_ror:2 row_mask:0xf bank_mask:0xf\n"
        "s_nop 1\n"
        "v_max_u32_dpp v184, v184, v184 row_ror:1 row_mask:0xf bank_mask:0xf\n"
        "v_mov_b32 v185, v184\n"
        "s_nop 1\n"
        "v_permlane16_swap_b32 v184, v185\n"
        "v_max_u32 v184, v184, v185\n"
        "v_mov_b32 v185, v184\n"
        "s_nop 1\n"
        "v_permlane32_swap_b32 v184, v185\n"
        "v_max_u32 v184, v184, v185\n"
        "v_min_u32 v108, -2, v184\n"
        "s_and_b32 s54, s86, 3\n"
        "s_cbranch_scc1 Lpkd_%=\n"
        "s_cmp_lt_u32 s69, 16\n"
        "s_cbranch_scc0 Lp67_%=\n"
        "s_lshl_b32 s54, s69, 10\n"
        "s_add_u32 s54, s54, s84\n"
        "buffer_store_dwordx4 v[124:127], v0, %[srd], s54 offen sc1\n"
        "s_branch Lpkd_%=\n"
        "Lp67_%=:\n"
        "s_cmp_eq_u32 s69, 16\n"
        "s_cbranch_scc0 Lpkd_%=\n"
        "v_lshrrev_b32 v190, 2, v0\n"
        "v_mov_b32 v191, 0\n"
        "s_mov_b32 exec_lo, 0xffff0000\n"
        "buffer_store_dword v191, v190, %[srd], s85 offen sc1\n"
        "s_mov_b64 exec, -1\n"
        "s_cmp_lt_u32 s86, 4\n"
        "s_cbranch_scc0 Lpkc_%=\n"
        "v_lshrrev_b32 v190, 4, v0\n"
        "s_mov_b32 exec_lo, 0x00010001\n"
        "s_mov_b32 exec_hi, 0x00010001\n"
        "buffer_store_dwordx4 v[176:179], v190, %[srd], s85 offen sc1\n"
        "s_mov_b64 exec, -1\n"
        "s_branch Lpkd_%=\n"
        "Lpkc_%=:\n"
        "s_mov_b64 exec, 0xffff\n"
        "buffer_store_dword v176, v190, %[srd], s85 offen sc1\n"
        "s_mov_b64 exec, -1\n"
        "Lpkd_%=:\n"
        "; ---- prologue: q GEMM, epilogue, wait for every K/V tile of this wave's keys\n"
        "Lpq_%=:\n"
        "s_and_b32 s54, s69, 15\n"
        "s_lshl_b32 s54, s54, 12\n"
        "v_add_u32 v122, s54, v101\n"
        "ds_read_b64_tr_b16 v[32:33], v101 offset:0\n"
        "ds_read_b64_tr_b16 v[34:35], v101 offset:512\n"
        "ds_read_b128 v[128:131], v96 offset:0\n"
        "ds_read_b128 v[132:135], v96 offset:16384\n"
        "ds_read_b128 v[136:139], v96 offset:32768\n"
        "ds_read_b128 v[140:143], v96 offset:49152\n"
        "ds_read_b64_tr_b16 v[36:37], v101 offset:4096\n"
        "ds_read_b64_tr_b16 v[38:39], v101 offset:4608\n"
        "ds_read_b128 v[144:147], v97 offset:0\n"
        "ds_read_b128 v[148:151], v97 offset:16384\n"
        "ds_read_b128 v[152:155], v97 offset:32768\n"
        "ds_read_b128 v[156:159], v97 offset:49152\n"
        "ds_read_b64_tr_b16 v[40:41], v101 offset:8192\n"
        "ds_read_b64_tr_b16 v[42:43], v101 offset:8704\n"
        "s_waitcnt lgkmcnt(10)\n"
        "v_mfma_f32_16x16x32_bf16 v[160:163], v[32:35], v[128:131], 0\n"
        "ds_read_b128 v[180:183], v98 offset:0\n"
        "v_mfma_f32_16x16x32_bf16 v[164:167], v[32:35], v[132:135], 0\n"
        "ds_read_b128 v[184:187], v98 offset:16384\n"
        "ds_read_b128 v[188:191], v98 offset:32768\n"
        "ds_read_b128 v[192:195], v98 offset:49152\n"
        "s_waitcnt lgkmcnt(12)\n"
        "v_mfma_f32_16x16x32_bf16 v[168:171], v[32:35], v[136:139], 0\n"
        "ds_read_b64_tr_b16 v[44:45], v101 offset:12288\n"
        "v_mfma_f32_16x16x32_bf16 v[172:175], v[32:35], v[140:143], 0\n"
        "ds_read_b64_tr_b16 v[46:47], v101 offset:12800\n"
        "v_mfma_f32_16x16x32_bf16 v[176:179], v[32:35], v[116:119], 0\n"
        "s_waitcnt lgkmcnt(10)\n"
        "v_mfma_f32_16x16x32_bf16 v[160:163], v[36:39], v[144:147], v[160:163]\n"
        "ds_read_b128 v[128:131], v99 offset:0\n"
        "v_mfma_f32_16x16x32_bf16 v[164:167], v[36:39], v[148:151], v[164:167]\n"
        "ds_read_b128 v[132:135], v99 offset:16384\n"
        "ds_read_b128 v[136:139], v99 offset:32768\n"
        "ds_read_b128 v[140:143], v99 offset:49152\n"
        "s_waitcnt lgkmcnt(12)\n"
        "v_mfma_f32_16x16x32_bf16 v[168:171], v[36:39], v[152:155], v[168:171]\n"
        "ds_read_b64_tr_b16 v[48:49], v101 offset:16384\n"
        "v_mfma_f32_16x16x32_bf16 v[172:175], v[36:39], v[156:159], v[172:175]\n"
        "ds_read_b64_tr_b16 v[50:51], v101 offset:16896\n"
        "v_mfma_f32_16x16x32_bf16 v[176:179], v[36:39], v[116:119], v[176:179]\n"
        "s_waitcnt lgkmcnt(10)\n"
        "v_mfma_f32_16x16x32_bf16 v[160:163], v[40:43], v[180:183], v[160:163]\n"
        "ds_read_b128 v[144:147], v96 offset:256\n"
        "v_mfma_f32_16x16x32_bf16 v[164:167], v[40:43], v[184:187], v[164:167]\n"
        "ds_read_b128 v[148:151], v96 offset:16640\n"
        "ds_read_b128 v[152:155], v96 offset:33024\n"
        "ds_read_b128 v[156:159], v96 offset:49408\n"
        "s_waitcnt lgkmcnt(12)\n"
        "v_mfma_f32_16x16x32_bf16 v[168:171], v[40:43], v[188:191], v[168:171]\n"
        "ds_read_b64_tr_b16 v[52:53], v101 offset:20480\n"
        "v_mfma_f32_16x16x32_bf16 v[172:175], v[40:43], v[192:195], v[172:175]\n"
        "ds_read_b64_tr_b16 v[54:55], v101 offset:20992\n"
        "v_mfma_f32_16x16x32_bf16 v[176:179], v[40:43], v[116:119], v[176:179]\n"
        "s_waitcnt lgkmcnt(10)\n"
        "v_mfma_f32_16x16x32_bf16 v[160:163], v[44:47], v[128:131], v[160:163]\n"
        "ds_read_b128 v[180:183], v97 offset:256\n"
        "v_mfma_f32_16x16x32_bf16 v[164:167], v[44:47], v[132:135], v[164:167]\n"
        "ds_read_b128 v[184:187], v97 offset:16640\n"
        "ds_read_b128 v[188:191], v97 offset:33024\n"
        "ds_read_b128 v[192:195], v97 offset:49408\n"
        "s_waitcnt lgkmcnt(12)\n"
        "v_mfma_f32_16x16x32_bf16 v[168:171], v[44:47], v[136:139], v[168:171]\n"
        "ds_read_b64_tr_b16 v[56:57], v101 offset:24576\n"
        "v_mfma_f32_16x16x32_bf16 v[172:175], v[44:47], v[140:143], v[172:175]\n"
        "ds_read_b64_tr_b16 v[58:59], v101 offset:25088\n"
        "v_mfma_f32_16x16x32_bf16 v[176:179], v[44:47], v[116:119], v[176:179]\n"
        "s_cmp_eq_u32 s43, 0\n"
        "s_cbranch_scc0 Lpf1_%=\n"
        "v_mov_b32 v109, 0\n"
        "v_mov_b32 v110, s37\n"
        "v_mov_b32 v111, s36\n"
        "s_and_b32 s54, s76, 0x7f80\n"
        "s_add_u32 s54, s54, s74\n"
        "s_add_u32 s54, s54, 4294934528\n"
        "s_waitcnt vmcnt(0)\n"
        "s_mov_b64 exec, 0xff\n"
        "buffer_store_dwordx4 v[108:111], v0, %[srd], s54 offen sc1\n"
        "s_mov_b64 exec, -1\n"
        "Lpf1_%=:\n"
        "s_waitcnt lgkmcnt(10)\n"
        "v_mfma_f32_16x16x32_bf16 v[160:163], v[48:51], v[144:147], v[160:163]\n"
        "ds_read_b128 v[128:131], v98 offset:256\n"
        "v_mfma_f32_16x16x32_bf16 v[164:167], v[48:51], v[148:151], v[164:167]\n"
        "ds_read_b128 v[132:135], v98 offset:16640\n"
        "ds_read_b128 v[136:139], v98 offset:33024\n"
        "ds_read_b128 v[140:143], v98 offset:49408\n"
        "s_waitcnt lgkmcnt(12)\n"
        "v_mfma_f32_16x16x32_bf16 v[168:171], v[48:51], v[152:155], v[168:171]\n"
        "ds_read_b64_tr_b16 v[60:61], v101 offset:28672\n"
        "v_mfma_f32_16x16x32_bf16 v[172:175], v[48:51], v[156:159], v[172:175]\n"
        "ds_read_b64_tr_b16 v[62:63], v101 offset:29184\n"
        "v_mfma_f32_16x16x32_bf16 v[176:179], v[48:51], v[116:119], v[176:179]\n"
        "s_waitcnt lgkmcnt(10)\n"
        "v_mfma_f32_16x16x32_bf16 v[160:163], v[52:55], v[180:183], v[160:163]\n"
        "ds_read_b128 v[144:147], v99 offset:256\n"
        "v_mfma_f32_16x16x32_bf16 v[164:167], v[52:55], v[184:187], v[164:167]\n"
        "ds_read_b128 v[148:151], v99 offset:16640\n"
        "ds_read_b128 v[152:155], v99 offset:33024\n"
        "ds_read_b128 v[156:159], v99 offset:49408\n"
        "s_waitcnt lgkmcnt(12)\n"
        "v_mfma_f32_16x16x32_bf16 v[168:171], v[52:55], v[188:191], v[168:171]\n"
        "ds_read_b64_tr_b16 v[64:65], v101 offset:32768\n"
        "v_mfma_f32_16x16x32_bf16 v[172:175], v[52:55], v[192:195], v[172:175]\n"
        "ds_read_b64_tr_b16 v[66:67], v101 offset:33280\n"
        "v_mfma_f32_16x16x32_bf16 v[176:179], v[52:55], v[116:119], v[176:179]\n"
        "s_waitcnt lgkmcnt(10)\n"
        "v_mfma_f32_16x16x32_bf16 v[160:163], v[56:59], v[128:131], v[160:163]\n"
        "ds_read_b128 v[180:183], v96 offset:512\n"
        "v_mfma_f32_16x16x32_bf16 v[164:167], v[56:59], v[132:135], v[164:167]\n"
        "ds_read_b128 v[184:187], v96 offset:16896\n"
        "ds_read_b128 v[188:191], v96 offset:33280\n"
        "ds_read_b128 v[192:195], v96 offset:49664\n"
        "s_waitcnt lgkmcnt(12)\n"
        "v_mfma_f32_16x16x32_bf16 v[168:171], v[56:59], v[136:139], v[168:171]\n"
        "ds_read_b64_tr_b16 v[68:69], v101 offset:36864\n"
        "v_mfma_f32_16x16x32_bf16 v[172:175], v[56:59], v[140:143], v[172:175]\n"
        "ds_read_b64_tr_b16 v[70:71], v101 offset:37376\n"
        "v_mfma_f32_16x16x32_bf16 v[176:179], v[56:59], v[116:119], v[176:179]\n"
        "s_waitcnt lgkmcnt(10)\n"
        "v_mfma_f32_16x16x32_bf16 v[160:163], v[60:63], v[144:147], v[160:163]\n"
        "ds_read_b128 v[128:131], v97 offset:512\n"
        "v_mfma_f32_16x16x32_bf16 v[164:167], v[60:63], v[148:151], v[164:167]\n"
        "ds_read_b128 v[132:135], v97 offset:16896\n"
        "ds_read_b128 v[136:139], v97 offset:33280\n"
        "ds_read_b128 v[140:143], v97 offset:49664\n"
        "s_waitcnt lgkmcnt(12)\n"
        "v_mfma_f32_16x16x32_bf16 v[168:171], v[60:63], v[152:155], v[168:171]\n"
        "ds_read_b64_tr_b16 v[72:73], v101 offset:40960\n"
        "v_mfma_f32_16x16x32_bf16 v[172:175], v[60:63], v[156:159], v[172:175]\n"
        "ds_read_b64_tr_b16 v[74:75], v101 offset:41472\n"
        "v_mfma_f32_16x16x32_bf16 v[176:179], v[60:63], v[116:119], v[176:179]\n"
        "s_waitcnt lgkmcnt(10)\n"
        "v_mfma_f32_16x16x32_bf16 v[160:163], v[64:67], v[180:183], v[160:163]\n"
        "ds_read_b128 v[144:147], v98 offset:512\n"
        "v_mfma_f32_16x16x32_bf16 v[164:167], v[64:67], v[184:187], v[164:167]\n"
        "ds_read_b128 v[148:151], v98 offset:16896\n"
        "ds_read_b128 v[152:155], v98 offset:33280\n"
        "ds_read_b128 v[156:159], v98 offset:49664\n"
        "s_waitcnt lgkmcnt(12)\n"
        "v_mfma_f32_16x16x32_bf16 v[168:171], v[64:67], v[188:191], v[168:171]\n"
        "ds_read_b64_tr_b16 v[76:77], v101 offset:45056\n"
        "v_mfma_f32_16x16x32_bf16 v[172:175], v[64:67], v[192:195], v[172:175]\n"
        "ds_read_b64_tr_b16 v[78:79], v101 offset:45568\n"
        "v_mfma_f32_16x16x32_bf16 v[176:179], v[64:67], v[116:119], v[176:179]\n"
        "s_waitcnt lgkmcnt(10)\n"
        "v_mfma_f32_16x16x32_bf16 v[160:163], v[68:71], v[128:131], v[160:163]\n"
        "ds_read_b128 v[180:183], v99 offset:512\n"
        "v_mfma_f32_16x16x32_bf16 v[164:167], v[68:71], v[132:135], v[164:167]\n"
        "ds_read_b128 v[184:187], v99 offset:16896\n"
        "ds_read_b128 v[188:191], v99 offset:33280\n"
        "ds_read_b128 v[192:195], v99 offset:49664\n"
        "s_waitcnt lgkmcnt(12)\n"
        "v_mfma_f32_16x16x32_bf16 v[168:171], v[68:71], v[136:139], v[168:171]\n"
        "ds_read_b64_tr_b16 v[80:81], v101 offset:49152\n"
        "v_mfma_f32_16x16x32_bf16 v[172:175], v[68:71], v[140:143], v[172:175]\n"
        "ds_read_b64_tr_b16 v[82:83], v101 offset:49664\n"
        "v_mfma_f32_16x16x32_bf16 v[176:179], v[68:71], v[116:119], v[176:179]\n"
        "s_waitcnt lgkmcnt(10)\n"
        "v_mfma_f32_16x16x32_bf16 v[160:163], v[72:75], v[144:147], v[160:163]\n"
        "ds_read_b128 v[128:131], v96 offset:768\n"
        "v_mfma_f32_16x16x32_bf16 v[164:167], v[72:75], v[148:151], v[164:167]\n"
        "ds_read_b128 v[132:135], v96 offset:17152\n"
        "ds_read_b128 v[136:139], v96 offset:33536\n"
        "ds_read_b128 v[140:143], v96 offset:49920\n"
        "s_waitcnt lgkmcnt(12)\n"
        "v_mfma_f32_16x16x32_bf16 v[168:171], v[72:75], v[152:155], v[168:171]\n"
        "ds_read_b64_tr_b16 v[84:85], v101 offset:53248\n"
        "v_mfma_f32_16x16x32_bf16 v[172:175], v[72:75], v[156:159], v[172:175]\n"
        "ds_read_b64_tr_b16 v[86:87], v101 offset:53760\n"
        "v_mfma_f32_16x16x32_bf16 v[176:179], v[72:75], v[116:119], v[176:179]\n"
        "s_waitcnt lgkmcnt(10)\n"
        "v_mfma_f32_16x16x32_bf16 v[160:163], v[76:79], v[180:183], v[160:163]\n"
        "ds_read_b128 v[144:147], v97 offset:768\n"
        "v_mfma_f32_16x16x32_bf16 v[164:167], v[76:79], v[184:187], v[164:167]\n"
        "ds_read_b128 v[148:151], v97 offset:17152\n"
        "ds_read_b128 v[152:155], v97 offset:33536\n"
        "ds_read_b128 v[156:159], v97 offset:49920\n"
        "s_waitcnt lgkmcnt(12)\n"
        "v_mfma_f32_16x16x32_bf16 v[168:171], v[76:79], v[188:191], v[168:171]\n"
        "ds_read_b64_tr_b16 v[88:89], v101 offset:57344\n"
        "v_mfma_f32_16x16x32_bf16 v[172:175], v[76:79], v[192:195], v[172:175]\n"
        "ds_read_b64_tr_b16 v[90:91], v101 offset:57856\n"
        "v_mfma_f32_16x16x32_bf16 v[176:179], v[76:79], v[116:119], v[176:179]\n"
        "s_waitcnt lgkmcnt(10)\n"
        "v_mfma_f32_16x16x32_bf16 v[160:163], v[80:83], v[128:131], v[160:163]\n"
        "ds_read_b128 v[180:183], v98 offset:768\n"
        "v_mfma_f32_16x16x32_bf16 v[164:167], v[80:83], v[132:135], v[164:167]\n"
        "ds_read_b128 v[184:187], v98 offset:17152\n"
        "ds_read_b128 v[188:191], v98 offset:33536\n"
        "ds_read_b128 v[192:195], v98 offset:49920\n"
        "s_waitcnt lgkmcnt(12)\n"
        "v_mfma_f32_16x16x32_bf16 v[168:171], v[80:83], v[136:139], v[168:171]\n"
        "ds_read_b64_tr_b16 v[92:93], v101 offset:61440\n"
        "v_mfma_f32_16x16x32_bf16 v[172:175], v[80:83], v[140:143], v[172:175]\n"
        "ds_read_b64_tr_b16 v[94:95], v101 offset:61952\n"
        "v_mfma_f32_16x16x32_bf16 v[176:179], v[80:83], v[116:119], v[176:179]\n"
        "s_waitcnt lgkmcnt(10)\n"
        "v_mfma_f32_16x16x32_bf16 v[160:163], v[84:87], v[144:147], v[160:163]\n"
        "ds_read_b128 v[128:131], v99 offset:768\n"
        "v_mfma_f32_16x16x32_bf16 v[164:167], v[84:87], v[148:151], v[164:167]\n"
        "ds_read_b128 v[132:135], v99 offset:17152\n"
        "ds_read_b128 v[136:139], v99 offset:33536\n"
        "ds_read_b128 v[140:143], v99 offset:49920\n"
        "s_waitcnt lgkmcnt(12)\n"
        "v_mfma_f32_16x16x32_bf16 v[168:171], v[84:87], v[152:155], v[168:171]\n"
        "v_mfma_f32_16x16x32_bf16 v[172:175], v[84:87], v[156:159], v[172:175]\n"
        "v_mfma_f32_16x16x32_bf16 v[176:179], v[84:87], v[116:119], v[176:179]\n"
        "s_waitcnt lgkmcnt(8)\n"
        "v_mfma_f32_16x16x32_bf16 v[160:163], v[88:91], v[180:183], v[160:163]\n"
        "v_mfma_f32_16x16x32_bf16 v[164:167], v[88:91], v[184:187], v[164:167]\n"
        "s_waitcnt lgkmcnt(6)\n"
        "v_mfma_f32_16x16x32_bf16 v[168:171], v[88:91], v[188:191], v[168:171]\n"
        "v_mfma_f32_16x16x32_bf16 v[172:175], v[88:91], v[192:195], v[172:175]\n"
        "v_mfma_f32_16x16x32_bf16 v[176:179], v[88:91], v[116:119], v[176:179]\n"
        "s_waitcnt lgkmcnt(2)\n"
        "v_mfma_f32_16x16x32_bf16 v[160:163], v[92:95], v[128:131], v[160:163]\n"
        "v_mfma_f32_16x16x32_bf16 v[164:167], v[92:95], v[132:135], v[164:167]\n"
        "s_waitcnt lgkmcnt(0)\n"
        "v_mfma_f32_16x16x32_bf16 v[168:171], v[92:95], v[136:139], v[168:171]\n"
        "v_mfma_f32_16x16x32_bf16 v[172:175], v[92:95], v[140:143], v[172:175]\n"
        "v_mfma_f32_16x16x32_bf16 v[176:179], v[92:95], v[116:119], v[176:179]\n"
        "ds_read2_b32 v[240:241], v8 offset0:0 offset1:64\n"
        "ds_read2_b32 v[242:243], v8 offset0:16 offset1:80\n"
        "ds_read2_b32 v[244:245], v8 offset0:32 offset1:96\n"
        "ds_read2_b32 v[246:247], v8 offset0:48 offset1:112\n"
        "ds_read_b64 v[204:205], v104 offset:0\n"
        "ds_read_b64 v[206:207], v104 offset:16384\n"
        "ds_read_b64 v[208:209], v104 offset:32768\n"
        "ds_read_b64 v[210:211], v104 offset:49152\n"
        "ds_read_b64_tr_b16 v[124:125], v122\n"
        "ds_read_b64_tr_b16 v[126:127], v122 offset:512\n"
        "s_waitcnt lgkmcnt(0)\n"
        "v_mov_b32 v112, v176\n"
        "v_mov_b32 v113, v177\n"
        "v_mov_b32 v114, v178\n"
        "v_mov_b32 v115, v179\n"
        "v_mul_f32 v241, 0x3eb8aa3b, v241\n"
        "v_mul_f32 v243, 0x3eb8aa3b, v243\n"
        "v_mul_f32 v245, 0x3eb8aa3b, v245\n"
        "v_mul_f32 v247, 0x3eb8aa3b, v247\n"
        "v_pk_fma_f32 v[160:161], v[240:241], v[176:177], v[160:161] op_sel_hi:[0,1,1] neg_lo:[1,0,0] neg_hi:[1,0,0]\n"
        "v_pk_fma_f32 v[162:163], v[240:241], v[178:179], v[162:163] op_sel_hi:[0,1,1] neg_lo:[1,0,0] neg_hi:[1,0,0]\n"
        "v_pk_fma_f32 v[164:165], v[242:243], v[176:177], v[164:165] op_sel_hi:[0,1,1] neg_lo:[1,0,0] neg_hi:[1,0,0]\n"
        "v_pk_fma_f32 v[166:167], v[242:243], v[178:179], v[166:167] op_sel_hi:[0,1,1] neg_lo:[1,0,0] neg_hi:[1,0,0]\n"
        "v_pk_fma_f32 v[168:169], v[244:245], v[176:177], v[168:169] op_sel_hi:[0,1,1] neg_lo:[1,0,0] neg_hi:[1,0,0]\n"
        "v_pk_fma_f32 v[170:171], v[244:245], v[178:179], v[170:171] op_sel_hi:[0,1,1] neg_lo:[1,0,0] neg_hi:[1,0,0]\n"
        "v_pk_fma_f32 v[172:173], v[246:247], v[176:177], v[172:173] op_sel_hi:[0,1,1] neg_lo:[1,0,0] neg_hi:[1,0,0]\n"
        "v_pk_fma_f32 v[174:175], v[246:247], v[178:179], v[174:175] op_sel_hi:[0,1,1] neg_lo:[1,0,0] neg_hi:[1,0,0]\n"
        "v_pk_mul_f32 v[160:161], v[240:241], v[160:161] op_sel:[1,0] op_sel_hi:[1,1]\n"
        "v_pk_mul_f32 v[162:163], v[240:241], v[162:163] op_sel:[1,0] op_sel_hi:[1,1]\n"
        "v_pk_mul_f32 v[164:165], v[242:243], v[164:165] op_sel:[1,0] op_sel_hi:[1,1]\n"
        "v_pk_mul_f32 v[166:167], v[242:243], v[166:167] op_sel:[1,0] op_sel_hi:[1,1]\n"
        "v_pk_mul_f32 v[168:169], v[244:245], v[168:169] op_sel:[1,0] op_sel_hi:[1,1]\n"
        "v_pk_mul_f32 v[170:171], v[244:245], v[170:171] op_sel:[1,0] op_sel_hi:[1,1]\n"
        "v_pk_mul_f32 v[172:173], v[246:247], v[172:173] op_sel:[1,0] op_sel_hi:[1,1]\n"
        "v_pk_mul_f32 v[174:175], v[246:247], v[174:175] op_sel:[1,0] op_sel_hi:[1,1]\n"
        "v_cvt_pk_bf16_f32 v192, v160, v161\n"
        "v_cvt_pk_bf16_f32 v193, v162, v163\n"
        "v_cvt_pk_bf16_f32 v194, v164, v165\n"
        "v_cvt_pk_bf16_f32 v195, v166, v167\n"
        "v_cvt_pk_bf16_f32 v196, v168, v169\n"
        "v_cvt_pk_bf16_f32 v197, v170, v171\n"
        "v_cvt_pk_bf16_f32 v198, v172, v173\n"
        "v_cvt_pk_bf16_f32 v199, v174, v175\n"
        "v_permlane16_swap_b32 v192, v194\n"
        "v_permlane16_swap_b32 v193, v195\n"
        "v_permlane16_swap_b32 v196, v198\n"
        "v_permlane16_swap_b32 v197, v199\n"
        "v_pk_mul_f32 v[128:129], v[160:161], v[160:161]\n"
        "v_pk_mul_f32 v[130:131], v[164:165], v[164:165]\n"
        "v_pk_mul_f32 v[132:133], v[168:169], v[168:169]\n"
        "v_pk_mul_f32 v[134:135], v[172:173], v[172:173]\n"
        "v_pk_fma_f32 v[128:129], v[162:163], v[162:163], v[128:129]\n"
        "v_pk_fma_f32 v[130:131], v[166:167], v[166:167], v[130:131]\n"
        "v_pk_fma_f32 v[132:133], v[170:171], v[170:171], v[132:133]\n"
        "v_pk_fma_f32 v[134:135], v[174:175], v[174:175], v[134:135]\n"
        "v_add_f32 v128, v128, v129\n"
        "v_add_f32 v130, v130, v131\n"
        "v_add_f32 v132, v132, v133\n"
        "v_add_f32 v134, v134, v135\n"
        "s_nop 0\n"
        "v_permlane32_swap_b32 v128, v132\n"
        "v_permlane32_swap_b32 v130, v134\n"
        "v_add_f32 v128, v128, v132\n"
        "v_add_f32 v130, v130, v134\n"
        "s_nop 1\n"
        "v_permlane16_swap_b32 v128, v130\n"
        "v_add_f32 v128, v128, v130\n"
        "s_nop 1\n"
        "v_max_u32_dpp v128, v128, v128 row_ror:8 row_mask:0xf bank_mask:0xf\n"
        "s_nop 1\n"
        "v_max_u32_dpp v128, v128, v128 row_ror:4 row_mask:0xf bank_mask:0xf\n"
        "s_nop 1\n"
        "v_max_u32_dpp v128, v128, v128 row_ror:2 row_mask:0xf bank_mask:0xf\n"
        "s_nop 1\n"
        "v_max_u32_dpp v128, v128, v128 row_ror:1 row_mask:0xf bank_mask:0xf\n"
        "v_mov_b32 v129, v128\n"
        "s_nop 1\n"
        "v_permlane16_swap_b32 v128, v129\n"
        "v_max_u32 v128, v128, v129\n"
        "v_mov_b32 v129, v128\n"
        "s_nop 1\n"
        "v_permlane32_swap_b32 v128, v129\n"
        "v_max_u32 v128, v128, v129\n"
        "ds_write_b128 v227, v[192:195]\n"
        "ds_write_b128 v227, v[196:199] offset:1024\n"
        "s_lshr_b32 s54, s81, 12\n"
        "s_add_u32 s54, s54, s52\n"
        "v_mov_b32 v129, s54\n"
        "ds_write_b32 v129, v128 offset:512\n"
        "s_waitcnt lgkmcnt(0)\n"
        "s_barrier\n"
        "s_lshr_b32 s55, s81, 1\n"
        "s_add_u32 s55, s55, s74\n"
        "s_add_u32 s55, s55, 4294934528\n"
        "buffer_load_dwordx4 v[212:215], v15, %[srd], s55 offen sc1\n"
        "v_lshlrev_b32 v80, 16, v204\n"
        "v_and_b32 v81, 0xffff0000, v204\n"
        "v_lshlrev_b32 v82, 16, v205\n"
        "v_and_b32 v83, 0xffff0000, v205\n"
        "v_lshlrev_b32 v84, 16, v206\n"
        "v_and_b32 v85, 0xffff0000, v206\n"
        "v_lshlrev_b32 v86, 16, v207\n"
        "v_and_b32 v87, 0xffff0000, v207\n"
        "v_lshlrev_b32 v88, 16, v208\n"
        "v_and_b32 v89, 0xffff0000, v208\n"
        "v_lshlrev_b32 v90, 16, v209\n"
        "v_and_b32 v91, 0xffff0000, v209\n"
        "v_lshlrev_b32 v92, 16, v210\n"
        "v_and_b32 v93, 0xffff0000, v210\n"
        "v_lshlrev_b32 v94, 16, v211\n"
        "v_and_b32 v95, 0xffff0000, v211\n"
        "s_waitcnt vmcnt(0)\n"
        "v_xor_b32 v214, s37, v214\n"
        "v_xor_b32 v215, s36, v215\n"
        "v_or_b32 v214, v214, v215\n"
        "v_cmp_ne_u32 vcc, 0, v214\n"
        "s_cbranch_vccnz Lf1spin_%=\n"
        "Lf1ok_%=:\n"
        "s_mov_b32 s58, 0xffff\n"
        "s_mov_b32 s59, 0xffff\n"
        "s_add_u32 s60, s74, s81\n"
        "s_add_u32 s60, s60, 4294803456\n"
        "s_lshr_b32 s54, s81, 1\n"
        "s_add_u32 s54, s54, s74\n"
        "s_add_u32 s54, s54, 4294934528\n"
        "buffer_load_dword a0, v15, %[srd], s54 offen sc1\n"
        "v_accvgpr_write_b32 a144, v14\n"
        "v_accvgpr_write_b32 a145, v14\n"
        "v_accvgpr_write_b32 a146, v14\n"
        "v_accvgpr_write_b32 a147, v14\n"
        "v_accvgpr_write_b32 a148, v14\n"
        "v_accvgpr_write_b32 a149, v14\n"
        "v_accvgpr_write_b32 a150, v14\n"
        "v_accvgpr_write_b32 a151, v14\n"
        "v_accvgpr_write_b32 a152, v14\n"
        "v_accvgpr_write_b32 a153, v14\n"
        "v_accvgpr_write_b32 a154, v14\n"
        "v_accvgpr_write_b32 a155, v14\n"
        "v_accvgpr_write_b32 a156, v14\n"
        "v_accvgpr_write_b32 a157, v14\n"
        "v_accvgpr_write_b32 a158, v14\n"
        "v_accvgpr_write_b32 a159, v14\n"
        "v_accvgpr_write_b32 a160, v14\n"
        "v_accvgpr_write_b32 a161, v14\n"
        "v_accvgpr_write_b32 a162, v14\n"
        "v_accvgpr_write_b32 a163, v14\n"
        "v_accvgpr_write_b32 a164, v14\n"
        "v_accvgpr_write_b32 a165, v14\n"
        "v_accvgpr_write_b32 a166, v14\n"
        "v_accvgpr_write_b32 a167, v14\n"
        "s_add_u32 s54, s80, 0\n"
        "s_and_b32 s54, s54, 0x3fff\n"
        "s_add_u32 s54, s54, s60\n"
        "buffer_load_dwordx4 a[64:67], v0, %[srd], s54 offen sc1\n"
        "v_accvgpr_write_b32 a168, v14\n"
        "v_accvgpr_write_b32 a169, v14\n"
        "v_accvgpr_write_b32 a170, v14\n"
        "v_accvgpr_write_b32 a171, v14\n"
        "v_accvgpr_write_b32 a172, v14\n"
        "v_accvgpr_write_b32 a173, v14\n"
        "v_accvgpr_write_b32 a174, v14\n"
        "v_accvgpr_write_b32 a175, v14\n"
        "v_accvgpr_write_b32 a176, v14\n"
        "v_accvgpr_write_b32 a177, v14\n"
        "v_accvgpr_write_b32 a178, v14\n"
        "v_accvgpr_write_b32 a179, v14\n"
        "v_accvgpr_write_b32 a180, v14\n"
        "v_accvgpr_write_b32 a181, v14\n"
        "v_accvgpr_write_b32 a182, v14\n"
        "v_accvgpr_write_b32 a183, v14\n"
        "v_accvgpr_write_b32 a184, v14\n"
        "v_accvgpr_write_b32 a185, v14\n"
        "v_accvgpr_write_b32 a186, v14\n"
        "v_accvgpr_write_b32 a187, v14\n"
        "v_accvgpr_write_b32 a188, v14\n"
        "v_accvgpr_write_b32 a189, v14\n"
        "v_accvgpr_write_b32 a190, v14\n"
        "v_accvgpr_write_b32 a191, v14\n"
        "s_mov_b64 exec, s[58:59]\n"
        "buffer_load_dwordx4 a[128:131], v230, %[srd], s54 offen sc1\n"
        "buffer_load_dwordx4 a[132:135], v230, %[srd], s54 offen offset:512 sc1\n"
        "s_mov_b64 exec, -1\n"
        "v_accvgpr_write_b32 a192, v14\n"
        "v_accvgpr_write_b32 a193, v14\n"
        "v_accvgpr_write_b32 a194, v14\n"
        "v_accvgpr_write_b32 a195, v14\n"
        "v_accvgpr_write_b32 a196, v14\n"
        "v_accvgpr_write_b32 a197, v14\n"
        "v_accvgpr_write_b32 a198, v14\n"
        "v_accvgpr_write_b32 a199, v14\n"
        "v_accvgpr_write_b32 a200, v14\n"
        "v_accvgpr_write_b32 a201, v14\n"
        "v_accvgpr_write_b32 a202, v14\n"
        "v_accvgpr_write_b32 a203, v14\n"
        "v_accvgpr_write_b32 a204, v14\n"
        "v_accvgpr_write_b32 a205, v14\n"
        "v_accvgpr_write_b32 a206, v14\n"
        "v_accvgpr_write_b32 a207, v14\n"
        "v_accvgpr_write_b32 a208, v14\n"
        "v_accvgpr_write_b32 a209, v14\n"
        "v_accvgpr_write_b32 a210, v14\n"
        "v_accvgpr_write_b32 a211, v14\n"
        "v_accvgpr_write_b32 a212, v14\n"
        "v_accvgpr_write_b32 a213, v14\n"
        "v_accvgpr_write_b32 a214, v14\n"
        "v_accvgpr_write_b32 a215, v14\n"
        "s_add_u32 s54, s80, 1024\n"
        "s_and_b32 s54, s54, 0x3fff\n"
        "s_add_u32 s54, s54, s60\n"
        "buffer_load_dwordx4 a[68:71], v0, %[srd], s54 offen sc1\n"
        "v_accvgpr_write_b32 a216, v14\n"
        "v_accvgpr_write_b32 a217, v14\n"
        "v_accvgpr_write_b32 a218, v14\n"
        "v_accvgpr_write_b32 a219, v14\n"
        "v_accvgpr_write_b32 a220, v14\n"
        "v_accvgpr_write_b32 a221, v14\n"
        "v_accvgpr_write_b32 a222, v14\n"
        "v_accvgpr_write_b32 a223, v14\n"
        "v_accvgpr_write_b32 a224, v14\n"
        "v_accvgpr_write_b32 a225, v14\n"
        "v_accvgpr_write_b32 a226, v14\n"
        "v_accvgpr_write_b32 a227, v14\n"
        "v_accvgpr_write_b32 a228, v14\n"
        "v_accvgpr_write_b32 a229, v14\n"
        "v_accvgpr_write_b32 a230, v14\n"
        "v_accvgpr_write_b32 a231, v14\n"
        "v_accvgpr_write_b32 a232, v14\n"
        "v_accvgpr_write_b32 a233, v14\n"
        "v_accvgpr_write_b32 a234, v14\n"
        "v_accvgpr_write_b32 a235, v14\n"
        "v_accvgpr_write_b32 a236, v14\n"
        "v_accvgpr_write_b32 a237, v14\n"
        "v_accvgpr_write_b32 a238, v14\n"
        "v_accvgpr_write_b32 a239, v14\n"
        "s_mov_b64 exec, s[58:59]\n"
        "buffer_load_dwordx4 a[136:139], v230, %[srd], s54 offen sc1\n"
        "buffer_load_dwordx4 a[140:143], v230, %[srd], s54 offen offset:512 sc1\n"
        "s_mov_b64 exec, -1\n"
        "v_accvgpr_write_b32 a240, v14\n"
        "v_accvgpr_write_b32 a241, v14\n"
        "v_accvgpr_write_b32 a242, v14\n"
        "v_accvgpr_write_b32 a243, v14\n"
        "v_accvgpr_write_b32 a244, v14\n"
        "v_accvgpr_write_b32 a245, v14\n"
        "v_accvgpr_write_b32 a246, v14\n"
        "v_accvgpr_write_b32 a247, v14\n"
        "v_accvgpr_write_b32 a248, v14\n"
        "v_accvgpr_write_b32 a249, v14\n"
        "v_accvgpr_write_b32 a250, v14\n"
        "v_accvgpr_write_b32 a251, v14\n"
        "v_accvgpr_write_b32 a252, v14\n"
        "v_accvgpr_write_b32 a253, v14\n"
        "v_accvgpr_write_b32 a254, v14\n"
        "v_accvgpr_write_b32 a255, v14\n"
        "; ---- round start: constants of the attention phase\n"
        "Ltop_%=:\n"
        "v_add_u32 v250, 15360, v228\n"
        "ds_read_b128 v[128:131], v250\n"
        "v_lshrrev_b32 v16, 1, v0\n"
        "v_mov_b32 v19, s50\n"
        "v_mov_b32 v20, s51\n"
        "v_mov_b32 v24, -1\n"
        "v_mov_b32 v25, -1\n"
        "v_mov_b32 v26, -1\n"
        "v_mov_b32 v27, -1\n"
        "v_mov_b32 v220, 0\n"
        "v_mov_b32 v221, 0\n"
        "v_mov_b32 v222, 0\n"
        "v_mov_b32 v223, 0\n"
        "v_mov_b32 v245, 0xdddddddd\n"
        "v_mov_b32 v250, s52\n"
        "v_and_b32 v240, 0x330, v0\n"
        "v_and_b32 v243, 0x40, v0\n"
        "v_lshl_or_b32 v240, v243, 1, v240\n"
        "v_bfe_u32 v243, v0, 7, 1\n"
        "v_lshl_or_b32 v240, v243, 6, v240\n"
        "v_lshrrev_b32 v243, 2, v240\n"
        "v_bfe_u32 v242, v0, 4, 2\n"
        "v_lshrrev_b32 v246, 8, v0\n"
        "v_lshlrev_b32 v246, 4, v246\n"
        "v_lshl_or_b32 v242, v242, 6, v246\n"
        "s_lshr_b32 s54, s81, 14\n"
        "s_mul_i32 s55, s54, 13824\n"
        "s_add_u32 s55, s55, s49\n"
        "s_add_u32 s55, s55, 65536\n"
        "v_add_u32 v246, s55, v0\n"
        "v_lshrrev_b32 v253, 2, v0\n"
        "s_add_u32 s55, s55, 2048\n"
        "v_add_u32 v247, s55, v253\n"
        "s_sub_u32 s55, s54, 1\n"
        "s_and_b32 s55, s55, 3\n"
        "s_mul_i32 s55, s55, 13824\n"
        "s_add_u32 s55, s55, s49\n"
        "s_add_u32 s55, s55, 65536\n"
        "v_add_u32 v248, s55, v0\n"
        "s_add_u32 s55, s55, 2048\n"
        "v_add_u32 v249, s55, v253\n"
        "s_sub_u32 s55, s54, 2\n"
        "s_and_b32 s55, s55, 3\n"
        "s_mul_i32 s55, s55, 13824\n"
        "s_add_u32 s55, s55, s49\n"
        "s_add_u32 s55, s55, 70144\n"
        "v_add_u32 v217, s55, v0\n"
        "s_add_u32 s55, s55, 2048\n"
        "v_add_u32 v219, s55, v253\n"
        "s_sub_u32 s55, s54, 3\n"
        "s_and_b32 s55, s55, 3\n"
        "s_mul_i32 s55, s55, 13824\n"
        "s_add_u32 s55, s55, s49\n"
        "s_add_u32 s55, s55, 74752\n"
        "v_add_u32 v218, s55, v0\n"
        "s_add_u32 s55, s55, 2048\n"
        "v_add_u32 v231, s55, v253\n"
        "s_waitcnt lgkmcnt(0)\n"
        "v_mov_b32 v17, v128\n"
        "v_mov_b32 v18, v129\n"
        "v_mov_b32 v241, v130\n"
        "v_mov_b32 v244, v131\n"
        "s_mov_b64 s[94:95], -1\n"
        "s_mov_b32 s57, 0\n"
        "; ---- attention A: norms -> softmax guard (falls back to slowA), first two q tiles while K/V arrive, then six more\n"
        "Lsa_%=:\n"
        "s_mov_b32 s58, 0xffff\n"
        "s_mov_b32 s59, 0xffff\n"
        "s_add_u32 s60, s74, s81\n"
        "s_add_u32 s60, s60, 4294803456\n"
        "s_and_b32 s61, s76, 0x7f80\n"
        "s_add_u32 s61, s61, s74\n"
        "s_lshr_b32 s54, s81, 3\n"
        "s_add_u32 s54, s54, 2048\n"
        "s_and_b32 s54, s54, 0x1fff\n"
        "v_add_u32 v251, s54, v228\n"
        "s_add_u32 s29, s49, 147456\n"
        "ds_read_b128 v[128:131], v250 offset:512\n"
        "ds_read_b128 v[192:195], v251\n"
        "ds_read_b128 v[196:199], v251 offset:1024\n"
        "v_mfma_f32_32x32x16_bf16 v[32:47], v[220:223], v[220:223], 0\n"
        "v_mfma_f32_32x32x16_bf16 v[48:63], v[220:223], v[220:223], 0\n"
        "v_mfma_f32_32x32x16_bf16 v[160:175], v[220:223], v[220:223], 0\n"
        "v_mfma_f32_32x32x16_bf16 v[176:191], v[220:223], v[220:223], 0\n"
        "s_waitcnt vmcnt(6)\n"
        "v_accvgpr_read_b32 v132, a0\n"
        "v_cmp_eq_u32 vcc, -1, v132\n"
        "s_cbranch_vccnz Lnavmiss_%=\n"
        "v_max_u32_dpp v132, v132, v132 quad_perm:[1,0,3,2] row_mask:0xf bank_mask:0xf\n"
        "s_waitcnt lgkmcnt(0)\n"
        "v_max_u32 v128, v128, v129\n"
        "v_max_u32_dpp v132, v132, v132 quad_perm:[2,3,0,1] row_mask:0xf bank_mask:0xf\n"
        "v_max3_u32 v128, v128, v130, v131\n"
        "s_nop 0\n"
        "v_max_u32_dpp v132, v132, v132 row_ror:8 row_mask:0xf bank_mask:0xf\n"
        "v_mov_b32 v133, v132\n"
        "s_nop 1\n"
        "v_permlane16_swap_b32 v132, v133\n"
        "v_max_u32 v132, v132, v133\n"
        "v_mov_b32 v133, v132\n"
        "s_nop 1\n"
        "v_permlane32_swap_b32 v132, v133\n"
        "v_max_u32 v132, v132, v133\n"
        "v_mul_f32 v128, v128, v132\n"
        "v_cmp_nge_f32 vcc, 0x46100000, v128\n"
        "s_nop 3\n"
        "s_and_b64 s[62:63], vcc, 0x0f0f0f0f\n"
        "v_cmp_nge_f32 vcc, 0x47800000, v132\n"
        "s_nop 3\n"
        "s_and_b64 vcc, vcc, 0xf0\n"
        "s_or_b64 vcc, vcc, s[62:63]\n"
        "s_cbranch_vccnz LslowA_%=\n"
        "s_lshr_b32 s54, s81, 12\n"
        "v_add_u32 v252, s54, v250\n"
        "v_mov_b32 v253, 0\n"
        "ds_write_b32 v252, v253 offset:528\n"
        "s_waitcnt vmcnt(3)\n"
        "v_mfma_f32_32x32x16_bf16 v[128:143], a[64:67], v[192:195], 0\n"
        "s_nop 11\n"
        "v_exp_f32 v128, v128\n"
        "v_exp_f32 v129, v129\n"
        "v_exp_f32 v130, v130\n"
        "v_exp_f32 v131, v131\n"
        "v_exp_f32 v132, v132\n"
        "v_exp_f32 v133, v133\n"
        "v_mfma_f32_32x32x16_bf16 v[144:159], a[64:67], v[196:199], 0\n"
        "v_exp_f32 v134, v134\n"
        "v_exp_f32 v135, v135\n"
        "s_add_u32 s54, s80, 2048\n"
        "v_exp_f32 v136, v136\n"
        "s_and_b32 s54, s54, 0x3fff\n"
        "v_exp_f32 v137, v137\n"
        "s_add_u32 s54, s54, s60\n"
        "v_exp_f32 v138, v138\n"
        "v_smfmac_f32_32x32x32_bf16 v[32:47], a[128:131], v[128:135], v245\n"
        "buffer_load_dwordx4 a[72:75], v0, %[srd], s54 offen sc1\n"
        "v_exp_f32 v139, v139\n"
        "v_exp_f32 v140, v140\n"
        "v_exp_f32 v141, v141\n"
        "v_exp_f32 v142, v142\n"
        "v_exp_f32 v143, v143\n"
        "v_exp_f32 v144, v144\n"
        "v_exp_f32 v145, v145\n"
        "v_smfmac_f32_32x32x32_bf16 v[32:47], a[132:135], v[136:143], v245\n"
        "v_exp_f32 v146, v146\n"
        "v_exp_f32 v147, v147\n"
        "v_exp_f32 v148, v148\n"
        "v_exp_f32 v149, v149\n"
        "s_waitcnt vmcnt(1)\n"
        "v_mfma_f32_32x32x16_bf16 v[128:143], a[68:71], v[192:195], 0\n"
        "v_exp_f32 v150, v150\n"
        "v_exp_f32 v151, v151\n"
        "v_exp_f32 v152, v152\n"
        "v_exp_f32 v153, v153\n"
        "v_exp_f32 v154, v154\n"
        "v_smfmac_f32_32x32x32_bf16 v[48:63], a[128:131], v[144:151], v245\n"
        "s_mov_b64 exec, s[58:59]\n"
        "buffer_load_dwordx4 a[144:147], v230, %[srd], s54 offen sc1\n"
        "buffer_load_dwordx4 a[148:151], v230, %[srd], s54 offen offset:512 sc1\n"
        "s_mov_b64 exec, -1\n"
        "v_exp_f32 v155, v155\n"
        "v_exp_f32 v156, v156\n"
        "v_exp_f32 v157, v157\n"
        "v_exp_f32 v158, v158\n"
        "v_exp_f32 v159, v159\n"
        "v_exp_f32 v128, v128\n"
        "v_exp_f32 v129, v129\n"
        "v_smfmac_f32_32x32x32_bf16 v[48:63], a[132:135], v[152:159], v245\n"
        "v_exp_f32 v130, v130\n"
        "v_exp_f32 v131, v131\n"
        "v_exp_f32 v132, v132\n"
        "v_exp_f32 v133, v133\n"
        "v_mfma_f32_32x32x16_bf16 v[144:159], a[68:71], v[196:199], 0\n"
        "v_exp_f32 v134, v134\n"
        "v_exp_f32 v135, v135\n"
        "s_add_u32 s54, s80, 3072\n"
        "v_exp_f32 v136, v136\n"
        "s_and_b32 s54, s54, 0x3fff\n"
        "v_exp_f32 v137, v137\n"
        "s_add_u32 s54, s54, s60\n"
        "v_exp_f32 v138, v138\n"
        "v_smfmac_f32_32x32x32_bf16 v[32:47], a[136:139], v[128:135], v245\n"
        "buffer_load_dwordx4 a[76:79], v0, %[srd], s54 offen sc1\n"
        "v_exp_f32 v139, v139\n"
        "v_exp_f32 v140, v140\n"
        "v_exp_f32 v141, v141\n"
        "v_exp_f32 v142, v142\n"
        "v_exp_f32 v143, v143\n"
        "v_exp_f32 v144, v144\n"
        "v_exp_f32 v145, v145\n"
        "v_smfmac_f32_32x32x32_bf16 v[32:47], a[140:143], v[136:143], v245\n"
        "v_exp_f32 v146, v146\n"
        "v_exp_f32 v147, v147\n"
        "v_exp_f32 v148, v148\n"
        "v_exp_f32 v149, v149\n"
        "s_waitcnt vmcnt(1)\n"
        "v_mfma_f32_32x32x16_bf16 v[128:143], a[72:75], v[192:195], 0\n"
        "v_exp_f32 v150, v150\n"
        "v_exp_f32 v151, v151\n"
        "v_exp_f32 v152, v152\n"
        "v_exp_f32 v153, v153\n"
        "v_exp_f32 v154, v154\n"
        "v_smfmac_f32_32x32x32_bf16 v[48:63], a[136:139], v[144:151], v245\n"
        "s_mov_b64 exec, s[58:59]\n"
        "buffer_load_dwordx4 a[152:155], v230, %[srd], s54 offen sc1\n"
        "buffer_load_dwordx4 a[156:159], v230, %[srd], s54 offen offset:512 sc1\n"
        "s_mov_b64 exec, -1\n"
        "v_exp_f32 v155, v155\n"
        "v_exp_f32 v156, v156\n"
        "v_exp_f32 v157, v157\n"
        "v_exp_f32 v158, v158\n"
        "v_exp_f32 v159, v159\n"
        "v_exp_f32 v128, v128\n"
        "v_exp_f32 v129, v129\n"
        "v_smfmac_f32_32x32x32_bf16 v[48:63], a[140:143], v[152:159], v245\n"
        "v_exp_f32 v130, v130\n"
        "v_exp_f32 v131, v131\n"
        "v_exp_f32 v132, v132\n"
        "v_exp_f32 v133, v133\n"
        "v_mfma_f32_32x32x16_bf16 v[144:159], a[72:75], v[196:199], 0\n"
        "v_exp_f32 v134, v134\n"
        "v_exp_f32 v135, v135\n"
        "s_add_u32 s54, s80, 4096\n"
        "v_exp_f32 v136, v136\n"
        "s_and_b32 s54, s54, 0x3fff\n"
        "v_exp_f32 v137, v137\n"
        "s_add_u32 s54, s54, s60\n"
        "v_exp_f32 v138, v138\n"
        "v_smfmac_f32_32x32x32_bf16 v[32:47], a[144:147], v[128:135], v245\n"
        "buffer_load_dwordx4 a[80:83], v0, %[srd], s54 offen sc1\n"
        "v_exp_f32 v139, v139\n"
        "v_exp_f32 v140, v140\n"
        "v_exp_f32 v141, v141\n"
        "v_exp_f32 v142, v142\n"
        "v_exp_f32 v143, v143\n"
        "v_exp_f32 v144, v144\n"
        "v_exp_f32 v145, v145\n"
        "v_smfmac_f32_32x32x32_bf16 v[32:47], a[148:151], v[136:143], v245\n"
        "v_exp_f32 v146, v146\n"
        "v_exp_f32 v147, v147\n"
        "v_exp_f32 v148, v148\n"
        "v_exp_f32 v149, v149\n"
        "s_waitcnt vmcnt(1)\n"
        "v_mfma_f32_32x32x16_bf16 v[128:143], a[76:79], v[192:195], 0\n"
        "v_exp_f32 v150, v150\n"
        "v_exp_f32 v151, v151\n"
        "v_exp_f32 v152, v152\n"
        "v_exp_f32 v153, v153\n"
        "v_exp_f32 v154, v154\n"
        "v_smfmac_f32_32x32x32_bf16 v[48:63], a[144:147], v[144:151], v245\n"
        "s_mov_b64 exec, s[58:59]\n"
        "buffer_load_dwordx4 a[160:163], v230, %[srd], s54 offen sc1\n"
        "buffer_load_dwordx4 a[164:167], v230, %[srd], s54 offen offset:512 sc1\n"
        "s_mov_b64 exec, -1\n"
        "v_exp_f32 v155, v155\n"
        "v_exp_f32 v156, v156\n"
        "v_exp_f32 v157, v157\n"
        "v_exp_f32 v158, v158\n"
        "v_exp_f32 v159, v159\n"
        "v_exp_f32 v128, v128\n"
        "v_exp_f32 v129, v129\n"
        "v_smfmac_f32_32x32x32_bf16 v[48:63], a[148:151], v[152:159], v245\n"
        "v_exp_f32 v130, v130\n"
        "v_exp_f32 v131, v131\n"
        "v_exp_f32 v132, v132\n"
        "v_exp_f32 v133, v133\n"
        "v_mfma_f32_32x32x16_bf16 v[144:159], a[76:79], v[196:199], 0\n"
        "v_exp_f32 v134, v134\n"
        "v_exp_f32 v135, v135\n"
        "s_add_u32 s54, s80, 5120\n"
        "v_exp_f32 v136, v136\n"
        "s_and_b32 s54, s54, 0x3fff\n"
        "v_exp_f32 v137, v137\n"
        "s_add_u32 s54, s54, s60\n"
        "v_exp_f32 v138, v138\n"
        "v_smfmac_f32_32x32x32_bf16 v[32:47], a[152:155], v[128:135], v245\n"
        "buffer_load_dwordx4 a[84:87], v0, %[srd], s54 offen sc1\n"
        "v_exp_f32 v139, v139\n"
        "v_exp_f32 v140, v140\n"
        "v_exp_f32 v141, v141\n"
        "v_exp_f32 v142, v142\n"
        "v_exp_f32 v143, v143\n"
        "v_exp_f32 v144, v144\n"
        "v_exp_f32 v145, v145\n"
        "v_smfmac_f32_32x32x32_bf16 v[32:47], a[156:159], v[136:143], v245\n"
        "v_exp_f32 v146, v146\n"
        "v_exp_f32 v147, v147\n"
        "v_exp_f32 v148, v148\n"
        "v_exp_f32 v149, v149\n"
        "s_waitcnt vmcnt(1)\n"
        "v_mfma_f32_32x32x16_bf16 v[128:143], a[80:83], v[192:195], 0\n"
        "v_exp_f32 v150, v150\n"
        "v_exp_f32 v151, v151\n"
        "v_exp_f32 v152, v152\n"
        "v_exp_f32 v153, v153\n"
        "v_exp_f32 v154, v154\n"
        "v_smfmac_f32_32x32x32_bf16 v[48:63], a[152:155], v[144:151], v245\n"
        "s_mov_b64 exec, s[58:59]\n"
        "buffer_load_dwordx4 a[168:171], v230, %[srd], s54 offen sc1\n"
        "buffer_load_dwordx4 a[172:175], v230, %[srd], s54 offen offset:512 sc1\n"
        "s_mov_b64 exec, -1\n"
        "v_exp_f32 v155, v155\n"
        "v_exp_f32 v156, v156\n"
        "v_exp_f32 v157, v157\n"
        "v_exp_f32 v158, v158\n"
        "v_exp_f32 v159, v159\n"
        "v_exp_f32 v128, v128\n"
        "v_exp_f32 v129, v129\n"
        "v_smfmac_f32_32x32x32_bf16 v[48:63], a[156:159], v[152:159], v245\n"
        "v_exp_f32 v130, v130\n"
        "v_exp_f32 v131, v131\n"
        "v_exp_f32 v132, v132\n"
        "v_exp_f32 v133, v133\n"
        "v_mfma_f32_32x32x16_bf16 v[144:159], a[80:83], v[196:199], 0\n"
        "v_exp_f32 v134, v134\n"
        "v_exp_f32 v135, v135\n"
        "s_add_u32 s54, s80, 6144\n"
        "v_exp_f32 v136, v136\n"
        "s_and_b32 s54, s54, 0x3fff\n"
        "v_exp_f32 v137, v137\n"
        "s_add_u32 s54, s54, s60\n"
        "v_exp_f32 v138, v138\n"
        "v_smfmac_f32_32x32x32_bf16 v[32:47], a[160:163], v[128:135], v245\n"
        "buffer_load_dwordx4 a[88:91], v0, %[srd], s54 offen sc1\n"
        "v_exp_f32 v139, v139\n"
        "v_exp_f32 v140, v140\n"
        "v_exp_f32 v141, v141\n"
        "v_exp_f32 v142, v142\n"
        "v_exp_f32 v143, v143\n"
        "v_exp_f32 v144, v144\n"
        "v_exp_f32 v145, v145\n"
        "v_smfmac_f32_32x32x32_bf16 v[32:47], a[164:167], v[136:143], v245\n"
        "v_exp_f32 v146, v146\n"
        "v_exp_f32 v147, v147\n"
        "v_exp_f32 v148, v148\n"
        "v_exp_f32 v149, v149\n"
        "s_waitcnt vmcnt(1)\n"
        "v_mfma_f32_32x32x16_bf16 v[128:143], a[84:87], v[192:195], 0\n"
        "v_exp_f32 v150, v150\n"
        "v_exp_f32 v151, v151\n"
        "v_exp_f32 v152, v152\n"
        "v_exp_f32 v153, v153\n"
        "v_exp_f32 v154, v154\n"
        "v_smfmac_f32_32x32x32_bf16 v[48:63], a[160:163], v[144:151], v245\n"
        "s_mov_b64 exec, s[58:59]\n"
        "buffer_load_dwordx4 a[176:179], v230, %[srd], s54 offen sc1\n"
        "buffer_load_dwordx4 a[180:183], v230, %[srd], s54 offen offset:512 sc1\n"
        "s_mov_b64 exec, -1\n"
        "v_exp_f32 v155, v155\n"
        "v_exp_f32 v156, v156\n"
        "v_exp_f32 v157, v157\n"
        "v_exp_f32 v158, v158\n"
        "v_exp_f32 v159, v159\n"
        "v_exp_f32 v128, v128\n"
        "v_exp_f32 v129, v129\n"
        "v_smfmac_f32_32x32x32_bf16 v[48:63], a[164:167], v[152:159], v245\n"
        "v_exp_f32 v130, v130\n"
        "v_exp_f32 v131, v131\n"
        "v_exp_f32 v132, v132\n"
        "v_exp_f32 v133, v133\n"
        "v_mfma_f32_32x32x16_bf16 v[144:159], a[84:87], v[196:199], 0\n"
        "v_exp_f32 v134, v134\n"
        "v_exp_f32 v135, v135\n"
        "s_add_u32 s54, s80, 7168\n"
        "v_exp_f32 v136, v136\n"
        "s_and_b32 s54, s54, 0x3fff\n"
        "v_exp_f32 v137, v137\n"
        "s_add_u32 s54, s54, s60\n"
        "v_exp_f32 v138, v138\n"
        "v_smfmac_f32_32x32x32_bf16 v[32:47], a[168:171], v[128:135], v245\n"
        "buffer_load_dwordx4 a[92:95], v0, %[srd], s54 offen sc1\n"
        "v_exp_f32 v139, v139\n"
        "v_exp_f32 v140, v140\n"
        "v_exp_f32 v141, v141\n"
        "v_exp_f32 v142, v142\n"
        "v_exp_f32 v143, v143\n"
        "v_exp_f32 v144, v144\n"
        "v_exp_f32 v145, v145\n"
        "v_smfmac_f32_32x32x32_bf16 v[32:47], a[172:175], v[136:143], v245\n"
        "v_exp_f32 v146, v146\n"
        "v_exp_f32 v147, v147\n"
        "v_exp_f32 v148, v148\n"
        "v_exp_f32 v149, v149\n"
        "s_waitcnt vmcnt(1)\n"
        "v_mfma_f32_32x32x16_bf16 v[128:143], a[88:91], v[192:195], 0\n"
        "v_exp_f32 v150, v150\n"
        "v_exp_f32 v151, v151\n"
        "v_exp_f32 v152, v152\n"
        "v_exp_f32 v153, v153\n"
        "v_exp_f32 v154, v154\n"
        "v_smfmac_f32_32x32x32_bf16 v[48:63], a[168:171], v[144:151], v245\n"
        "s_mov_b64 exec, s[58:59]\n"
        "buffer_load_dwordx4 a[184:187], v230, %[srd], s54 offen sc1\n"
        "buffer_load_dwordx4 a[188:191], v230, %[srd], s54 offen offset:512 sc1\n"
        "s_mov_b64 exec, -1\n"
        "v_exp_f32 v155, v155\n"
        "v_exp_f32 v156, v156\n"
        "v_exp_f32 v157, v157\n"
        "v_exp_f32 v158, v158\n"
        "v_exp_f32 v159, v159\n"
        "v_exp_f32 v128, v128\n"
        "v_exp_f32 v129, v129\n"
        "v_smfmac_f32_32x32x32_bf16 v[48:63], a[172:175], v[152:159], v245\n"
        "v_exp_f32 v130, v130\n"
        "v_exp_f32 v131, v131\n"
        "v_exp_f32 v132, v132\n"
        "v_exp_f32 v133, v133\n"
        "v_mfma_f32_32x32x16_bf16 v[144:159], a[88:91], v[196:199], 0\n"
        "v_exp_f32 v134, v134\n"
        "v_exp_f32 v135, v135\n"
        "s_add_u32 s54, s80, 8192\n"
        "v_exp_f32 v136, v136\n"
        "s_and_b32 s54, s54, 0x3fff\n"
        "v_exp_f32 v137, v137\n"
        "s_add_u32 s54, s54, s60\n"
        "v_exp_f32 v138, v138\n"
        "v_smfmac_f32_32x32x32_bf16 v[32:47], a[176:179], v[128:135], v245\n"
        "buffer_load_dwordx4 a[96:99], v0, %[srd], s54 offen sc1\n"
        "v_exp_f32 v139, v139\n"
        "v_exp_f32 v140, v140\n"
        "v_exp_f32 v141, v141\n"
        "v_exp_f32 v142, v142\n"
        "v_exp_f32 v143, v143\n"
        "v_exp_f32 v144, v144\n"
        "v_exp_f32 v145, v145\n"
        "v_smfmac_f32_32x32x32_bf16 v[32:47], a[180:183], v[136:143], v245\n"
        "v_exp_f32 v146, v146\n"
        "v_exp_f32 v147, v147\n"
        "v_exp_f32 v148, v148\n"
        "v_exp_f32 v149, v149\n"
        "s_waitcnt vmcnt(1)\n"
        "v_mfma_f32_32x32x16_bf16 v[128:143], a[92:95], v[192:195], 0\n"
        "v_exp_f32 v150, v150\n"
        "v_exp_f32 v151, v151\n"
        "v_exp_f32 v152, v152\n"
        "v_exp_f32 v153, v153\n"
        "v_exp_f32 v154, v154\n"
        "v_smfmac_f32_32x32x32_bf16 v[48:63], a[176:179], v[144:151], v245\n"
        "s_mov_b64 exec, s[58:59]\n"
        "buffer_load_dwordx4 a[192:195], v230, %[srd], s54 offen sc1\n"
        "buffer_load_dwordx4 a[196:199], v230, %[srd], s54 offen offset:512 sc1\n"
        "s_mov_b64 exec, -1\n"
        "v_exp_f32 v155, v155\n"
        "v_exp_f32 v156, v156\n"
        "v_exp_f32 v157, v157\n"
        "v_exp_f32 v158, v158\n"
        "v_exp_f32 v159, v159\n"
        "v_exp_f32 v128, v128\n"
        "v_exp_f32 v129, v129\n"
        "v_smfmac_f32_32x32x32_bf16 v[48:63], a[180:183], v[152:159], v245\n"
        "v_exp_f32 v130, v130\n"
        "v_exp_f32 v131, v131\n"
        "v_exp_f32 v132, v132\n"
        "v_exp_f32 v133, v133\n"
        "v_mfma_f32_32x32x16_bf16 v[144:159], a[92:95], v[196:199], 0\n"
        "v_exp_f32 v134, v134\n"
        "v_exp_f32 v135, v135\n"
        "s_add_u32 s54, s80, 9216\n"
        "v_exp_f32 v136, v136\n"
        "s_and_b32 s54, s54, 0x3fff\n"
        "v_exp_f32 v137, v137\n"
        "s_add_u32 s54, s54, s60\n"
        "v_exp_f32 v138, v138\n"
        "v_smfmac_f32_32x32x32_bf16 v[32:47], a[184:187], v[128:135], v245\n"
        "buffer_load_dwordx4 a[100:103], v0, %[srd], s54 offen sc1\n"
        "v_exp_f32 v139, v139\n"
        "v_exp_f32 v140, v140\n"
        "v_exp_f32 v141, v141\n"
        "v_exp_f32 v142, v142\n"
        "v_exp_f32 v143, v143\n"
        "v_exp_f32 v144, v144\n"
        "v_exp_f32 v145, v145\n"
        "v_smfmac_f32_32x32x32_bf16 v[32:47], a[188:191], v[136:143], v245\n"
        "v_exp_f32 v146, v146\n"
        "v_exp_f32 v147, v147\n"
        "v_exp_f32 v148, v148\n"
        "v_exp_f32 v149, v149\n"
        "s_waitcnt vmcnt(1)\n"
        "v_mfma_f32_32x32x16_bf16 v[128:143], a[96:99], v[192:195], 0\n"
        "v_exp_f32 v150, v150\n"
        "v_exp_f32 v151, v151\n"
        "v_exp_f32 v152, v152\n"
        "v_exp_f32 v153, v153\n"
        "v_exp_f32 v154, v154\n"
        "v_smfmac_f32_32x32x32_bf16 v[48:63], a[184:187], v[144:151], v245\n"
        "s_mov_b64 exec, s[58:59]\n"
        "buffer_load_dwordx4 a[200:203], v230, %[srd], s54 offen sc1\n"
        "buffer_load_dwordx4 a[204:207], v230, %[srd], s54 offen offset:512 sc1\n"
        "s_mov_b64 exec, -1\n"
        "v_exp_f32 v155, v155\n"
        "v_exp_f32 v156, v156\n"
        "v_exp_f32 v157, v157\n"
        "v_exp_f32 v158, v158\n"
        "v_exp_f32 v159, v159\n"
        "v_exp_f32 v128, v128\n"
        "v_exp_f32 v129, v129\n"
        "v_smfmac_f32_32x32x32_bf16 v[48:63], a[188:191], v[152:159], v245\n"
        "v_exp_f32 v130, v130\n"
        "v_exp_f32 v131, v131\n"
        "v_exp_f32 v132, v132\n"
        "v_exp_f32 v133, v133\n"
        "v_mfma_f32_32x32x16_bf16 v[144:159], a[96:99], v[196:199], 0\n"
        "v_exp_f32 v134, v134\n"
        "v_exp_f32 v135, v135\n"
        "s_add_u32 s54, s80, 10240\n"
        "v_exp_f32 v136, v136\n"
        "s_and_b32 s54, s54, 0x3fff\n"
        "v_exp_f32 v137, v137\n"
        "s_add_u32 s54, s54, s60\n"
        "v_exp_f32 v138, v138\n"
        "v_smfmac_f32_32x32x32_bf16 v[32:47], a[192:195], v[128:135], v245\n"
        "buffer_load_dwordx4 a[104:107], v0, %[srd], s54 offen sc1\n"
        "v_exp_f32 v139, v139\n"
        "v_exp_f32 v140, v140\n"
        "v_exp_f32 v141, v141\n"
        "v_exp_f32 v142, v142\n"
        "v_exp_f32 v143, v143\n"
        "v_exp_f32 v144, v144\n"
        "v_exp_f32 v145, v145\n"
        "v_smfmac_f32_32x32x32_bf16 v[32:47], a[196:199], v[136:143], v245\n"
        "v_exp_f32 v146, v146\n"
        "v_exp_f32 v147, v147\n"
        "v_exp_f32 v148, v148\n"
        "v_exp_f32 v149, v149\n"
        "s_waitcnt vmcnt(1)\n"
        "v_mfma_f32_32x32x16_bf16 v[128:143], a[100:103], v[192:195], 0\n"
        "v_exp_f32 v150, v150\n"
        "v_exp_f32 v151, v151\n"
        "v_exp_f32 v152, v152\n"
        "v_exp_f32 v153, v153\n"
        "v_exp_f32 v154, v154\n"
        "v_smfmac_f32_32x32x32_bf16 v[48:63], a[192:195], v[144:151], v245\n"
        "s_mov_b64 exec, s[58:59]\n"
        "buffer_load_dwordx4 a[208:211], v230, %[srd], s54 offen sc1\n"
        "buffer_load_dwordx4 a[212:215], v230, %[srd], s54 offen offset:512 sc1\n"
        "s_mov_b64 exec, -1\n"
        "v_exp_f32 v155, v155\n"
        "v_exp_f32 v156, v156\n"
        "v_exp_f32 v157, v157\n"
        "v_exp_f32 v158, v158\n"
        "v_exp_f32 v159, v159\n"
        "v_exp_f32 v128, v128\n"
        "v_exp_f32 v129, v129\n"
        "v_smfmac_f32_32x32x32_bf16 v[48:63], a[196:199], v[152:159], v245\n"
        "v_exp_f32 v130, v130\n"
        "v_exp_f32 v131, v131\n"
        "v_exp_f32 v132, v132\n"
        "v_exp_f32 v133, v133\n"
        "v_mfma_f32_32x32x16_bf16 v[144:159], a[100:103], v[196:199], 0\n"
        "v_exp_f32 v134, v134\n"
        "v_exp_f32 v135, v135\n"
        "s_add_u32 s54, s80, 11264\n"
        "v_exp_f32 v136, v136\n"
        "s_and_b32 s54, s54, 0x3fff\n"
        "v_exp_f32 v137, v137\n"
        "s_add_u32 s54, s54, s60\n"
        "v_exp_f32 v138, v138\n"
        "v_smfmac_f32_32x32x32_bf16 v[32:47], a[200:203], v[128:135], v245\n"
        "buffer_load_dwordx4 a[108:111], v0, %[srd], s54 offen sc1\n"
        "v_exp_f32 v139, v139\n"
        "v_exp_f32 v140, v140\n"
        "v_exp_f32 v141, v141\n"
        "v_exp_f32 v142, v142\n"
        "v_exp_f32 v143, v143\n"
        "v_exp_f32 v144, v144\n"
        "v_exp_f32 v145, v145\n"
        "v_smfmac_f32_32x32x32_bf16 v[32:47], a[204:207], v[136:143], v245\n"
        "v_exp_f32 v146, v146\n"
        "v_exp_f32 v147, v147\n"
        "v_exp_f32 v148, v148\n"
        "v_exp_f32 v149, v149\n"
        "s_waitcnt vmcnt(1)\n"
        "v_mfma_f32_32x32x16_bf16 v[128:143], a[104:107], v[192:195], 0\n"
        "v_exp_f32 v150, v150\n"
        "v_exp_f32 v151, v151\n"
        "v_exp_f32 v152, v152\n"
        "v_exp_f32 v153, v153\n"
        "v_exp_f32 v154, v154\n"
        "v_smfmac_f32_32x32x32_bf16 v[48:63], a[200:203], v[144:151], v245\n"
        "s_mov_b64 exec, s[58:59]\n"
        "buffer_load_dwordx4 a[216:219], v230, %[srd], s54 offen sc1\n"
        "buffer_load_dwordx4 a[220:223], v230, %[srd], s54 offen offset:512 sc1\n"
        "s_mov_b64 exec, -1\n"
        "v_exp_f32 v155, v155\n"
        "v_exp_f32 v156, v156\n"
        "v_exp_f32 v157, v157\n"
        "v_exp_f32 v158, v158\n"
        "v_exp_f32 v159, v159\n"
        "v_exp_f32 v128, v128\n"
        "v_exp_f32 v129, v129\n"
        "v_smfmac_f32_32x32x32_bf16 v[48:63], a[204:207], v[152:159], v245\n"
        "v_exp_f32 v130, v130\n"
        "v_exp_f32 v131, v131\n"
        "v_exp_f32 v132, v132\n"
        "v_exp_f32 v133, v133\n"
        "v_mfma_f32_32x32x16_bf16 v[144:159], a[104:107], v[196:199], 0\n"
        "v_exp_f32 v134, v134\n"
        "v_exp_f32 v135, v135\n"
        "s_add_u32 s54, s80, 12288\n"
        "v_exp_f32 v136, v136\n"
        "s_and_b32 s54, s54, 0x3fff\n"
        "v_exp_f32 v137, v137\n"
        "s_add_u32 s54, s54, s60\n"
        "v_exp_f32 v138, v138\n"
        "v_smfmac_f32_32x32x32_bf16 v[32:47], a[208:211], v[128:135], v245\n"
        "buffer_load_dwordx4 a[112:115], v0, %[srd], s54 offen sc1\n"
        "v_exp_f32 v139, v139\n"
        "v_exp_f32 v140, v140\n"
        "v_exp_f32 v141, v141\n"
        "v_exp_f32 v142, v142\n"
        "v_exp_f32 v143, v143\n"
        "v_exp_f32 v144, v144\n"
        "v_exp_f32 v145, v145\n"
        "v_smfmac_f32_32x32x32_bf16 v[32:47], a[212:215], v[136:143], v245\n"
        "v_exp_f32 v146, v146\n"
        "v_exp_f32 v147, v147\n"
        "v_exp_f32 v148, v148\n"
        "v_exp_f32 v149, v149\n"
        "s_waitcnt vmcnt(1)\n"
        "v_mfma_f32_32x32x16_bf16 v[128:143], a[108:111], v[192:195], 0\n"
        "v_exp_f32 v150, v150\n"
        "v_exp_f32 v151, v151\n"
        "v_exp_f32 v152, v152\n"
        "v_exp_f32 v153, v153\n"
        "v_exp_f32 v154, v154\n"
        "v_smfmac_f32_32x32x32_bf16 v[48:63], a[208:211], v[144:151], v245\n"
        "s_mov_b64 exec, s[58:59]\n"
        "buffer_load_dwordx4 a[224:227], v230, %[srd], s54 offen sc1\n"
        "buffer_load_dwordx4 a[228:231], v230, %[srd], s54 offen offset:512 sc1\n"
        "s_mov_b64 exec, -1\n"
        "v_exp_f32 v155, v155\n"
        "v_exp_f32 v156, v156\n"
        "v_exp_f32 v157, v157\n"
        "v_exp_f32 v158, v158\n"
        "v_exp_f32 v159, v159\n"
        "v_exp_f32 v128, v128\n"
        "v_exp_f32 v129, v129\n"
        "v_smfmac_f32_32x32x32_bf16 v[48:63], a[212:215], v[152:159], v245\n"
        "v_exp_f32 v130, v130\n"
        "v_exp_f32 v131, v131\n"
        "v_exp_f32 v132, v132\n"
        "v_exp_f32 v133, v133\n"
        "v_mfma_f32_32x32x16_bf16 v[144:159], a[108:111], v[196:199], 0\n"
        "v_exp_f32 v134, v134\n"
        "v_exp_f32 v135, v135\n"
        "s_add_u32 s54, s80, 13312\n"
        "v_exp_f32 v136, v136\n"
        "s_and_b32 s54, s54, 0x3fff\n"
        "v_exp_f32 v137, v137\n"
        "s_add_u32 s54, s54, s60\n"
        "v_exp_f32 v138, v138\n"
        "v_smfmac_f32_32x32x32_bf16 v[32:47], a[216:219], v[128:135], v245\n"
        "buffer_load_dwordx4 a[116:119], v0, %[srd], s54 offen sc1\n"
        "v_exp_f32 v139, v139\n"
        "v_exp_f32 v140, v140\n"
        "v_exp_f32 v141, v141\n"
        "v_exp_f32 v142, v142\n"
        "v_exp_f32 v143, v143\n"
        "v_exp_f32 v144, v144\n"
        "v_exp_f32 v145, v145\n"
        "v_smfmac_f32_32x32x32_bf16 v[32:47], a[220:223], v[136:143], v245\n"
        "v_exp_f32 v146, v146\n"
        "v_exp_f32 v147, v147\n"
        "v_exp_f32 v148, v148\n"
        "v_exp_f32 v149, v149\n"
        "s_waitcnt vmcnt(1)\n"
        "v_mfma_f32_32x32x16_bf16 v[128:143], a[112:115], v[192:195], 0\n"
        "v_exp_f32 v150, v150\n"
        "v_exp_f32 v151, v151\n"
        "v_exp_f32 v152, v152\n"
        "v_exp_f32 v153, v153\n"
        "v_exp_f32 v154, v154\n"
        "v_smfmac_f32_32x32x32_bf16 v[48:63], a[216:219], v[144:151], v245\n"
        "s_mov_b64 exec, s[58:59]\n"
        "buffer_load_dwordx4 a[232:235], v230, %[srd], s54 offen sc1\n"
        "buffer_load_dwordx4 a[236:239], v230, %[srd], s54 offen offset:512 sc1\n"
        "s_mov_b64 exec, -1\n"
        "v_exp_f32 v155, v155\n"
        "v_exp_f32 v156, v156\n"
        "v_exp_f32 v157, v157\n"
        "v_exp_f32 v158, v158\n"
        "v_exp_f32 v159, v159\n"
        "v_exp_f32 v128, v128\n"
        "v_exp_f32 v129, v129\n"
        "v_smfmac_f32_32x32x32_bf16 v[48:63], a[220:223], v[152:159], v245\n"
        "v_exp_f32 v130, v130\n"
        "v_exp_f32 v131, v131\n"
        "v_exp_f32 v132, v132\n"
        "v_exp_f32 v133, v133\n"
        "v_mfma_f32_32x32x16_bf16 v[144:159], a[112:115], v[196:199], 0\n"
        "v_exp_f32 v134, v134\n"
        "v_exp_f32 v135, v135\n"
        "s_add_u32 s54, s80, 14336\n"
        "v_exp_f32 v136, v136\n"
        "s_and_b32 s54, s54, 0x3fff\n"
        "v_exp_f32 v137, v137\n"
        "s_add_u32 s54, s54, s60\n"
        "v_exp_f32 v138, v138\n"
        "v_smfmac_f32_32x32x32_bf16 v[32:47], a[224:227], v[128:135], v245\n"
        "buffer_load_dwordx4 a[120:123], v0, %[srd], s54 offen sc1\n"
        "v_exp_f32 v139, v139\n"
        "v_exp_f32 v140, v140\n"
        "v_exp_f32 v141, v141\n"
        "v_exp_f32 v142, v142\n"
        "v_exp_f32 v143, v143\n"
        "v_exp_f32 v144, v144\n"
        "v_exp_f32 v145, v145\n"
        "v_smfmac_f32_32x32x32_bf16 v[32:47], a[228:231], v[136:143], v245\n"
        "v_exp_f32 v146, v146\n"
        "v_exp_f32 v147, v147\n"
        "v_exp_f32 v148, v148\n"
        "v_exp_f32 v149, v149\n"
        "s_waitcnt vmcnt(1)\n"
        "v_mfma_f32_32x32x16_bf16 v[128:143], a[116:119], v[192:195], 0\n"
        "v_exp_f32 v150, v150\n"
        "v_exp_f32 v151, v151\n"
        "v_exp_f32 v152, v152\n"
        "v_exp_f32 v153, v153\n"
        "v_exp_f32 v154, v154\n"
        "v_smfmac_f32_32x32x32_bf16 v[48:63], a[224:227], v[144:151], v245\n"
        "s_mov_b64 exec, s[58:59]\n"
        "buffer_load_dwordx4 a[240:243], v230, %[srd], s54 offen sc1\n"
        "buffer_load_dwordx4 a[244:247], v230, %[srd], s54 offen offset:512 sc1\n"
        "s_mov_b64 exec, -1\n"
        "v_exp_f32 v155, v155\n"
        "v_exp_f32 v156, v156\n"
        "v_exp_f32 v157, v157\n"
        "v_exp_f32 v158, v158\n"
        "v_exp_f32 v159, v159\n"
        "v_exp_f32 v128, v128\n"
        "v_exp_f32 v129, v129\n"
        "v_smfmac_f32_32x32x32_bf16 v[48:63], a[228:231], v[152:159], v245\n"
        "v_exp_f32 v130, v130\n"
        "v_exp_f32 v131, v131\n"
        "v_add_u32 v251, 2048, v251\n"
        "v_exp_f32 v132, v132\n"
        "v_exp_f32 v133, v133\n"
        "v_mfma_f32_32x32x16_bf16 v[144:159], a[116:119], v[196:199], 0\n"
        "v_exp_f32 v134, v134\n"
        "v_exp_f32 v135, v135\n"
        "s_add_u32 s54, s80, 15360\n"
        "v_and_b32 v251, 0x1fff, v251\n"
        "v_exp_f32 v136, v136\n"
        "s_and_b32 s54, s54, 0x3fff\n"
        "v_exp_f32 v137, v137\n"
        "s_add_u32 s54, s54, s60\n"
        "v_exp_f32 v138, v138\n"
        "v_smfmac_f32_32x32x32_bf16 v[32:47], a[232:235], v[128:135], v245\n"
        "buffer_load_dwordx4 a[124:127], v0, %[srd], s54 offen sc1\n"
        "v_exp_f32 v139, v139\n"
        "v_exp_f32 v140, v140\n"
        "v_or_b32 v251, s29, v251\n"
        "v_exp_f32 v141, v141\n"
        "v_exp_f32 v142, v142\n"
        "v_exp_f32 v143, v143\n"
        "v_exp_f32 v144, v144\n"
        "v_exp_f32 v145, v145\n"
        "v_smfmac_f32_32x32x32_bf16 v[32:47], a[236:239], v[136:143], v245\n"
        "v_exp_f32 v146, v146\n"
        "v_exp_f32 v147, v147\n"
        "v_exp_f32 v148, v148\n"
        "v_exp_f32 v149, v149\n"
        "s_waitcnt vmcnt(1)\n"
        "v_mfma_f32_32x32x16_bf16 v[128:143], a[120:123], v[192:195], 0\n"
        "v_exp_f32 v150, v150\n"
        "v_exp_f32 v151, v151\n"
        "v_exp_f32 v152, v152\n"
        "v_exp_f32 v153, v153\n"
        "v_exp_f32 v154, v154\n"
        "v_smfmac_f32_32x32x32_bf16 v[48:63], a[232:235], v[144:151], v245\n"
        "s_mov_b64 exec, s[58:59]\n"
        "buffer_load_dwordx4 a[248:251], v230, %[srd], s54 offen sc1\n"
        "buffer_load_dwordx4 a[252:255], v230, %[srd], s54 offen offset:512 sc1\n"
        "s_mov_b64 exec, -1\n"
        "v_exp_f32 v155, v155\n"
        "v_exp_f32 v156, v156\n"
        "v_exp_f32 v157, v157\n"
        "v_exp_f32 v158, v158\n"
        "v_exp_f32 v159, v159\n"
        "v_exp_f32 v128, v128\n"
        "v_exp_f32 v129, v129\n"
        "v_smfmac_f32_32x32x32_bf16 v[48:63], a[236:239], v[152:159], v245\n"
        "v_exp_f32 v130, v130\n"
        "v_exp_f32 v131, v131\n"
        "v_exp_f32 v132, v132\n"
        "v_exp_f32 v133, v133\n"
        "v_mfma_f32_32x32x16_bf16 v[144:159], a[120:123], v[196:199], 0\n"
        "v_exp_f32 v134, v134\n"
        "v_exp_f32 v135, v135\n"
        "v_exp_f32 v136, v136\n"
        "v_exp_f32 v137, v137\n"
        "v_exp_f32 v138, v138\n"
        "v_smfmac_f32_32x32x32_bf16 v[32:47], a[240:243], v[128:135], v245\n"
        "v_exp_f32 v139, v139\n"
        "v_exp_f32 v140, v140\n"
        "v_exp_f32 v141, v141\n"
        "v_exp_f32 v142, v142\n"
        "v_exp_f32 v143, v143\n"
        "s_nop 0\n"
        "v_exp_f32 v144, v144\n"
        "v_exp_f32 v145, v145\n"
        "v_smfmac_f32_32x32x32_bf16 v[32:47], a[244:247], v[136:143], v245\n"
        "v_exp_f32 v146, v146\n"
        "v_exp_f32 v147, v147\n"
        "v_exp_f32 v148, v148\n"
        "v_exp_f32 v149, v149\n"
        "s_waitcnt vmcnt(0)\n"
        "v_mfma_f32_32x32x16_bf16 v[128:143], a[124:127], v[192:195], 0\n"
        "v_exp_f32 v150, v150\n"
        "v_exp_f32 v151, v151\n"
        "v_exp_f32 v152, v152\n"
        "v_exp_f32 v153, v153\n"
        "v_exp_f32 v154, v154\n"
        "v_smfmac_f32_32x32x32_bf16 v[48:63], a[240:243], v[144:151], v245\n"
        "ds_read_b128 v[192:195], v251\n"
        "v_exp_f32 v155, v155\n"
        "v_exp_f32 v156, v156\n"
        "v_exp_f32 v157, v157\n"
        "v_exp_f32 v158, v158\n"
        "v_exp_f32 v159, v159\n"
        "v_exp_f32 v128, v128\n"
        "v_exp_f32 v129, v129\n"
        "v_smfmac_f32_32x32x32_bf16 v[48:63], a[244:247], v[152:159], v245\n"
        "v_exp_f32 v130, v130\n"
        "v_exp_f32 v131, v131\n"
        "v_exp_f32 v132, v132\n"
        "v_exp_f32 v133, v133\n"
        "v_mfma_f32_32x32x16_bf16 v[144:159], a[124:127], v[196:199], 0\n"
        "v_exp_f32 v134, v134\n"
        "v_exp_f32 v135, v135\n"
        "v_exp_f32 v136, v136\n"
        "v_exp_f32 v137, v137\n"
        "v_exp_f32 v138, v138\n"
        "v_smfmac_f32_32x32x32_bf16 v[32:47], a[248:251], v[128:135], v245\n"
        "v_exp_f32 v139, v139\n"
        "v_exp_f32 v140, v140\n"
        "v_exp_f32 v141, v141\n"
        "v_exp_f32 v142, v142\n"
        "v_exp_f32 v143, v143\n"
        "s_nop 0\n"
        "v_exp_f32 v144, v144\n"
        "v_exp_f32 v145, v145\n"
        "v_smfmac_f32_32x32x32_bf16 v[32:47], a[252:255], v[136:143], v245\n"
        "v_exp_f32 v146, v146\n"
        "v_exp_f32 v147, v147\n"
        "v_exp_f32 v148, v148\n"
        "v_exp_f32 v149, v149\n"
        "s_waitcnt lgkmcnt(0)\n"
        "v_mfma_f32_32x32x16_bf16 v[128:143], a[64:67], v[192:195], 0\n"
        "v_exp_f32 v150, v150\n"
        "v_exp_f32 v151, v151\n"
        "v_exp_f32 v152, v152\n"
        "v_exp_f32 v153, v153\n"
        "v_exp_f32 v154, v154\n"
        "v_smfmac_f32_32x32x32_bf16 v[48:63], a[248:251], v[144:151], v245\n"
        "v_exp_f32 v155, v155\n"
        "v_exp_f32 v156, v156\n"
        "v_exp_f32 v157, v157\n"
        "v_exp_f32 v158, v158\n"
        "v_exp_f32 v159, v159\n"
        "v_pk_add_f32 v[28:29], v[32:33], v[34:35]\n"
        "v_pk_add_f32 v[30:31], v[36:37], v[38:39]\n"
        "v_pk_add_f32 v[28:29], v[28:29], v[30:31]\n"
        "v_add_f32 v28, v28, v40\n"
        "v_cmp_u_f32 vcc, v28, v29\n"
        "s_nop 3\n"
        "s_and_b64 vcc, vcc, s[94:95]\n"
        "s_cbranch_vccnz Lp0bad_%=\n"
        "s_cmp_eq_u32 s87, 0\n"
        "s_cbranch_scc0 Lk70_%=\n"
        "s_barrier\n"
        "s_add_u32 s62, s72, 2621440\n"
        "s_add_u32 s63, s73, 40960\n"
        "s_cmp_lt_u32 s69, 16\n"
        "s_cbranch_scc0 Lp68k69_%=\n"
        "s_lshl_b32 s54, s69, 10\n"
        "s_sub_u32 s54, s54, s83\n"
        "s_and_b32 s54, s54, 0x3fff\n"
        "s_add_u32 s54, s54, s62\n"
        "buffer_store_dwordx4 v[124:127], v0, %[srd], s54 offen sc1\n"
        "s_branch Lpqdk69_%=\n"
        "Lp68k69_%=:\n"
        "s_cmp_eq_u32 s69, 16\n"
        "s_cbranch_scc0 Lpqdk69_%=\n"
        "v_lshrrev_b32 v120, 2, v0\n"
        "v_mov_b32 v121, 0\n"
        "s_mov_b32 exec_lo, 0xffff0000\n"
        "buffer_store_dword v121, v120, %[srd], s63 offen sc1\n"
        "s_mov_b64 exec, -1\n"
        "v_lshrrev_b32 v120, 4, v0\n"
        "s_mov_b32 exec_lo, 0x00010001\n"
        "s_mov_b32 exec_hi, 0x00010001\n"
        "buffer_store_dwordx4 v[112:115], v120, %[srd], s63 offen sc1\n"
        "s_mov_b64 exec, -1\n"
        "Lpqdk69_%=:\n"
        "v_add_u32 v122, 65536, v101\n"
        "ds_read_b64_tr_b16 v[64:65], v122 offset:0\n"
        "ds_read_b64_tr_b16 v[66:67], v122 offset:512\n"
        "ds_read_b64_tr_b16 v[68:69], v122 offset:4096\n"
        "ds_read_b64_tr_b16 v[70:71], v122 offset:4608\n"
        "ds_read_b64_tr_b16 v[72:73], v122 offset:8192\n"
        "ds_read_b64_tr_b16 v[74:75], v122 offset:8704\n"
        "ds_read_b64_tr_b16 v[76:77], v122 offset:12288\n"
        "ds_read_b64_tr_b16 v[78:79], v122 offset:12800\n"
        "s_nop 1\n"
        "s_waitcnt lgkmcnt(6)\n"
        "v_mfma_f32_16x16x32_bf16 v[104:107], v[116:119], v[64:67], 0\n"
        "s_waitcnt lgkmcnt(4)\n"
        "v_mfma_f32_16x16x32_bf16 v[104:107], v[116:119], v[68:71], v[104:107]\n"
        "s_waitcnt lgkmcnt(2)\n"
        "v_mfma_f32_16x16x32_bf16 v[104:107], v[116:119], v[72:75], v[104:107]\n"
        "s_waitcnt lgkmcnt(0)\n"
        "v_mfma_f32_16x16x32_bf16 v[104:107], v[116:119], v[76:79], v[104:107]\n"
        "s_nop 7\n"
        "s_cmp_eq_u32 s44, 0\n"
        "s_cbranch_scc1 Lk70_%=\n"
        "s_add_u32 s54, s35, 0\n"
        "s_and_b32 s54, s54, 0x3fff\n"
        "s_add_u32 s54, s54, s44\n"
        "buffer_store_dwordx4 v[64:67], v0, %[srd], s54 offen sc1\n"
        "s_add_u32 s54, s35, 1024\n"
        "s_and_b32 s54, s54, 0x3fff\n"
        "s_add_u32 s54, s54, s44\n"
        "buffer_store_dwordx4 v[68:71], v0, %[srd], s54 offen sc1\n"
        "s_add_u32 s54, s35, 2048\n"
        "s_and_b32 s54, s54, 0x3fff\n"
        "s_add_u32 s54, s54, s44\n"
        "buffer_store_dwordx4 v[72:75], v0, %[srd], s54 offen sc1\n"
        "s_add_u32 s54, s35, 3072\n"
        "s_and_b32 s54, s54, 0x3fff\n"
        "s_add_u32 s54, s54, s44\n"
        "buffer_store_dwordx4 v[76:79], v0, %[srd], s54 offen sc1\n"
        "v_lshrrev_b32 v120, 2, v0\n"
        "s_mov_b64 exec, 0xffff\n"
        "buffer_store_dword v104, v120, %[srd], s39 offen sc1\n"
        "s_mov_b64 exec, -1\n"
        "Lk70_%=:\n"
        "v_exp_f32 v128, v128\n"
        "v_exp_f32 v129, v129\n"
        "v_smfmac_f32_32x32x32_bf16 v[48:63], a[252:255], v[152:159], v245\n"
        "v_exp_f32 v130, v130\n"
        "v_exp_f32 v131, v131\n"
        "v_exp_f32 v132, v132\n"
        "v_exp_f32 v133, v133\n"
        "v_mfma_f32_32x32x16_bf16 v[144:159], a[68:71], v[192:195], 0\n"
        "v_exp_f32 v134, v134\n"
        "v_exp_f32 v135, v135\n"
        "v_exp_f32 v136, v136\n"
        "v_exp_f32 v137, v137\n"
        "v_exp_f32 v138, v138\n"
        "v_smfmac_f32_32x32x32_bf16 v[160:175], a[128:131], v[128:135], v245\n"
        "v_exp_f32 v139, v139\n"
        "v_exp_f32 v140, v140\n"
        "v_exp_f32 v141, v141\n"
        "v_exp_f32 v142, v142\n"
        "v_exp_f32 v143, v143\n"
        "s_nop 0\n"
        "v_exp_f32 v144, v144\n"
        "v_exp_f32 v145, v145\n"
        "v_smfmac_f32_32x32x32_bf16 v[160:175], a[132:135], v[136:143], v245\n"
        "v_exp_f32 v146, v146\n"
        "v_exp_f32 v147, v147\n"
        "v_exp_f32 v148, v148\n"
        "v_exp_f32 v149, v149\n"
        "v_mfma_f32_32x32x16_bf16 v[128:143], a[72:75], v[192:195], 0\n"
        "v_exp_f32 v150, v150\n"
        "v_exp_f32 v151, v151\n"
        "v_exp_f32 v152, v152\n"
        "v_exp_f32 v153, v153\n"
        "v_exp_f32 v154, v154\n"
        "v_smfmac_f32_32x32x32_bf16 v[160:175], a[136:139], v[144:151], v245\n"
        "ds_write_b128 v246, v[48:51] offset:2304\n"
        "v_exp_f32 v155, v155\n"
        "v_exp_f32 v156, v156\n"
        "v_exp_f32 v157, v157\n"
        "v_exp_f32 v158, v158\n"
        "v_exp_f32 v159, v159\n"
        "v_exp_f32 v128, v128\n"
        "v_exp_f32 v129, v129\n"
        "v_smfmac_f32_32x32x32_bf16 v[160:175], a[140:143], v[152:159], v245\n"
        "ds_write_b128 v246, v[52:55] offset:3328\n"
        "v_exp_f32 v130, v130\n"
        "v_exp_f32 v131, v131\n"
        "v_exp_f32 v132, v132\n"
        "v_exp_f32 v133, v133\n"
        "v_mfma_f32_32x32x16_bf16 v[144:159], a[76:79], v[192:195], 0\n"
        "v_exp_f32 v134, v134\n"
        "v_exp_f32 v135, v135\n"
        "v_exp_f32 v136, v136\n"
        "v_exp_f32 v137, v137\n"
        "v_exp_f32 v138, v138\n"
        "v_smfmac_f32_32x32x32_bf16 v[160:175], a[144:147], v[128:135], v245\n"
        "ds_write_b32 v247, v56 offset:2304\n"
        "v_exp_f32 v139, v139\n"
        "v_exp_f32 v140, v140\n"
        "v_exp_f32 v141, v141\n"
        "v_exp_f32 v142, v142\n"
        "v_exp_f32 v143, v143\n"
        "v_exp_f32 v144, v144\n"
        "v_exp_f32 v145, v145\n"
        "v_smfmac_f32_32x32x32_bf16 v[160:175], a[148:151], v[136:143], v245\n"
        "ds_read_b128 v[196:199], v251 offset:1024\n"
        "v_exp_f32 v146, v146\n"
        "v_exp_f32 v147, v147\n"
        "v_exp_f32 v148, v148\n"
        "v_exp_f32 v149, v149\n"
        "v_mfma_f32_32x32x16_bf16 v[128:143], a[80:83], v[192:195], 0\n"
        "v_exp_f32 v150, v150\n"
        "v_exp_f32 v151, v151\n"
        "v_exp_f32 v152, v152\n"
        "v_exp_f32 v153, v153\n"
        "v_exp_f32 v154, v154\n"
        "v_smfmac_f32_32x32x32_bf16 v[160:175], a[152:155], v[144:151], v245\n"
        "ds_write_b128 v246, v[32:35] offset:0\n"
        "v_exp_f32 v155, v155\n"
        "v_exp_f32 v156, v156\n"
        "v_exp_f32 v157, v157\n"
        "v_exp_f32 v158, v158\n"
        "v_exp_f32 v159, v159\n"
        "v_exp_f32 v128, v128\n"
        "v_exp_f32 v129, v129\n"
        "v_smfmac_f32_32x32x32_bf16 v[160:175], a[156:159], v[152:159], v245\n"
        "ds_write_b128 v246, v[36:39] offset:1024\n"
        "v_exp_f32 v130, v130\n"
        "v_exp_f32 v131, v131\n"
        "v_exp_f32 v132, v132\n"
        "v_exp_f32 v133, v133\n"
        "v_mfma_f32_32x32x16_bf16 v[144:159], a[84:87], v[192:195], 0\n"
        "v_exp_f32 v134, v134\n"
        "v_exp_f32 v135, v135\n"
        "v_exp_f32 v136, v136\n"
        "v_exp_f32 v137, v137\n"
        "v_exp_f32 v138, v138\n"
        "v_smfmac_f32_32x32x32_bf16 v[160:175], a[160:163], v[128:135], v245\n"
        "ds_write_b32 v247, v40 offset:0\n"
        "v_exp_f32 v139, v139\n"
        "v_exp_f32 v140, v140\n"
        "v_exp_f32 v141, v141\n"
        "v_exp_f32 v142, v142\n"
        "v_exp_f32 v143, v143\n"
        "v_exp_f32 v144, v144\n"
        "v_exp_f32 v145, v145\n"
        "v_smfmac_f32_32x32x32_bf16 v[160:175], a[164:167], v[136:143], v245\n"
        "s_nop 0\n"
        "v_exp_f32 v146, v146\n"
        "v_exp_f32 v147, v147\n"
        "s_waitcnt vmcnt(0)\n"
        "v_exp_f32 v148, v148\n"
        "v_exp_f32 v149, v149\n"
        "v_mfma_f32_32x32x16_bf16 v[128:143], a[88:91], v[192:195], 0\n"
        "v_exp_f32 v150, v150\n"
        "v_exp_f32 v151, v151\n"
        "v_exp_f32 v152, v152\n"
        "v_exp_f32 v153, v153\n"
        "v_exp_f32 v154, v154\n"
        "v_smfmac_f32_32x32x32_bf16 v[160:175], a[168:171], v[144:151], v245\n"
        "buffer_store_dwordx4 v[24:27], v0, %[srd], s64 offen offset:0 sc1\n"
        "v_exp_f32 v155, v155\n"
        "v_exp_f32 v156, v156\n"
        "v_exp_f32 v157, v157\n"
        "v_exp_f32 v158, v158\n"
        "v_exp_f32 v159, v159\n"
        "v_exp_f32 v128, v128\n"
        "s_add_u32 s54, s76, 0x8000\n"
        "v_exp_f32 v129, v129\n"
        "v_smfmac_f32_32x32x32_bf16 v[160:175], a[172:175], v[152:159], v245\n"
        "s_mov_b64 exec, 1\n"
        "buffer_store_dword v19, off, %[srd], s54 sc1\n"
        "s_mov_b64 exec, -1\n"
        "v_exp_f32 v130, v130\n"
        "v_exp_f32 v131, v131\n"
        "v_exp_f32 v132, v132\n"
        "v_exp_f32 v133, v133\n"
        "v_mfma_f32_32x32x16_bf16 v[144:159], a[92:95], v[192:195], 0\n"
        "v_exp_f32 v134, v134\n"
        "v_exp_f32 v135, v135\n"
        "v_exp_f32 v136, v136\n"
        "v_exp_f32 v137, v137\n"
        "v_exp_f32 v138, v138\n"
        "v_smfmac_f32_32x32x32_bf16 v[160:175], a[176:179], v[128:135], v245\n"
        "v_exp_f32 v139, v139\n"
        "v_exp_f32 v140, v140\n"
        "v_exp_f32 v141, v141\n"
        "v_exp_f32 v142, v142\n"
        "v_exp_f32 v143, v143\n"
        "s_nop 0\n"
        "v_exp_f32 v144, v144\n"
        "v_exp_f32 v145, v145\n"
        "v_smfmac_f32_32x32x32_bf16 v[160:175], a[180:183], v[136:143], v245\n"
        "v_exp_f32 v146, v146\n"
        "v_exp_f32 v147, v147\n"
        "v_exp_f32 v148, v148\n"
        "v_exp_f32 v149, v149\n"
        "v_mfma_f32_32x32x16_bf16 v[128:143], a[96:99], v[192:195], 0\n"
        "v_exp_f32 v150, v150\n"
        "v_exp_f32 v151, v151\n"
        "v_exp_f32 v152, v152\n"
        "v_exp_f32 v153, v153\n"
        "v_exp_f32 v154, v154\n"
        "v_smfmac_f32_32x32x32_bf16 v[160:175], a[184:187], v[144:151], v245\n"
        "buffer_store_dwordx4 v[24:27], v0, %[srd], s64 offen offset:1024 sc1\n"
        "v_exp_f32 v155, v155\n"
        "v_exp_f32 v156, v156\n"
        "v_exp_f32 v157, v157\n"
        "v_exp_f32 v158, v158\n"
        "v_exp_f32 v159, v159\n"
        "v_exp_f32 v128, v128\n"
        "v_exp_f32 v129, v129\n"
        "v_smfmac_f32_32x32x32_bf16 v[160:175], a[188:191], v[152:159], v245\n"
        "v_exp_f32 v130, v130\n"
        "v_exp_f32 v131, v131\n"
        "v_exp_f32 v132, v132\n"
        "v_exp_f32 v133, v133\n"
        "v_mfma_f32_32x32x16_bf16 v[144:159], a[100:103], v[192:195], 0\n"
        "v_exp_f32 v134, v134\n"
        "v_exp_f32 v135, v135\n"
        "v_exp_f32 v136, v136\n"
        "v_exp_f32 v137, v137\n"
        "s_add_u32 s54, s64, 0x800000\n"
        "v_exp_f32 v138, v138\n"
        "v_smfmac_f32_32x32x32_bf16 v[160:175], a[192:195], v[128:135], v245\n"
        "buffer_store_dwordx4 v[24:27], v0, %[srd], s54 offen offset:0 sc1\n"
        "v_exp_f32 v139, v139\n"
        "v_exp_f32 v140, v140\n"
        "v_exp_f32 v141, v141\n"
        "v_exp_f32 v142, v142\n"
        "v_exp_f32 v143, v143\n"
        "v_exp_f32 v144, v144\n"
        "v_exp_f32 v145, v145\n"
        "v_smfmac_f32_32x32x32_bf16 v[160:175], a[196:199], v[136:143], v245\n"
        "v_exp_f32 v146, v146\n"
        "v_exp_f32 v147, v147\n"
        "v_exp_f32 v148, v148\n"
        "v_exp_f32 v149, v149\n"
        "v_mfma_f32_32x32x16_bf16 v[128:143], a[104:107], v[192:195], 0\n"
        "v_exp_f32 v150, v150\n"
        "v_exp_f32 v151, v151\n"
        "v_exp_f32 v152, v152\n"
        "v_exp_f32 v153, v153\n"
        "v_exp_f32 v154, v154\n"
        "v_smfmac_f32_32x32x32_bf16 v[160:175], a[200:203], v[144:151], v245\n"
        "v_exp_f32 v155, v155\n"
        "v_exp_f32 v156, v156\n"
        "v_exp_f32 v157, v157\n"
        "v_exp_f32 v158, v158\n"
        "v_exp_f32 v159, v159\n"
        "s_nop 0\n"
        "v_exp_f32 v128, v128\n"
        "v_exp_f32 v129, v129\n"
        "v_smfmac_f32_32x32x32_bf16 v[160:175], a[204:207], v[152:159], v245\n"
        "v_exp_f32 v130, v130\n"
        "v_exp_f32 v131, v131\n"
        "v_exp_f32 v132, v132\n"
        "v_exp_f32 v133, v133\n"
        "v_mfma_f32_32x32x16_bf16 v[144:159], a[108:111], v[192:195], 0\n"
        "v_exp_f32 v134, v134\n"
        "v_exp_f32 v135, v135\n"
        "v_exp_f32 v136, v136\n"
        "v_exp_f32 v137, v137\n"
        "v_exp_f32 v138, v138\n"
        "v_smfmac_f32_32x32x32_bf16 v[160:175], a[208:211], v[128:135], v245\n"
        "buffer_store_dwordx4 v[24:27], v0, %[srd], s54 offen offset:1024 sc1\n"
        "v_exp_f32 v139, v139\n"
        "v_exp_f32 v140, v140\n"
        "v_exp_f32 v141, v141\n"
        "v_exp_f32 v142, v142\n"
        "v_exp_f32 v143, v143\n"
        "v_exp_f32 v144, v144\n"
        "v_exp_f32 v145, v145\n"
        "v_smfmac_f32_32x32x32_bf16 v[160:175], a[212:215], v[136:143], v245\n"
        "v_exp_f32 v146, v146\n"
        "v_exp_f32 v147, v147\n"
        "v_exp_f32 v148, v148\n"
        "v_exp_f32 v149, v149\n"
        "v_mfma_f32_32x32x16_bf16 v[128:143], a[112:115], v[192:195], 0\n"
        "v_exp_f32 v150, v150\n"
        "v_exp_f32 v151, v151\n"
        "v_exp_f32 v152, v152\n"
        "v_exp_f32 v153, v153\n"
        "v_exp_f32 v154, v154\n"
        "v_smfmac_f32_32x32x32_bf16 v[160:175], a[216:219], v[144:151], v245\n"
        "buffer_store_dwordx4 v[24:27], v0, %[srd], s68 offen offset:0 sc1\n"
        "v_exp_f32 v155, v155\n"
        "v_exp_f32 v156, v156\n"
        "v_exp_f32 v157, v157\n"
        "v_exp_f32 v158, v158\n"
        "v_exp_f32 v159, v159\n"
        "v_exp_f32 v128, v128\n"
        "v_exp_f32 v129, v129\n"
        "v_smfmac_f32_32x32x32_bf16 v[160:175], a[220:223], v[152:159], v245\n"
        "v_exp_f32 v130, v130\n"
        "v_exp_f32 v131, v131\n"
        "v_exp_f32 v132, v132\n"
        "v_exp_f32 v133, v133\n"
        "v_mfma_f32_32x32x16_bf16 v[144:159], a[116:119], v[192:195], 0\n"
        "v_exp_f32 v134, v134\n"
        "v_exp_f32 v135, v135\n"
        "v_exp_f32 v136, v136\n"
        "v_exp_f32 v137, v137\n"
        "v_exp_f32 v138, v138\n"
        "v_smfmac_f32_32x32x32_bf16 v[160:175], a[224:227], v[128:135], v245\n"
        "v_exp_f32 v139, v139\n"
        "v_exp_f32 v140, v140\n"
        "v_exp_f32 v141, v141\n"
        "v_exp_f32 v142, v142\n"
        "v_exp_f32 v143, v143\n"
        "s_nop 0\n"
        "v_exp_f32 v144, v144\n"
        "v_exp_f32 v145, v145\n"
        "v_smfmac_f32_32x32x32_bf16 v[160:175], a[228:231], v[136:143], v245\n"
        "v_exp_f32 v146, v146\n"
        "v_exp_f32 v147, v147\n"
        "v_exp_f32 v148, v148\n"
        "v_exp_f32 v149, v149\n"
        "v_mfma_f32_32x32x16_bf16 v[128:143], a[120:123], v[192:195], 0\n"
        "v_exp_f32 v150, v150\n"
        "v_exp_f32 v151, v151\n"
        "v_exp_f32 v152, v152\n"
        "v_exp_f32 v153, v153\n"
        "v_exp_f32 v154, v154\n"
        "v_smfmac_f32_32x32x32_bf16 v[160:175], a[232:235], v[144:151], v245\n"
        "buffer_store_dwordx4 v[24:27], v0, %[srd], s68 offen offset:1024 sc1\n"
        "v_exp_f32 v155, v155\n"
        "v_exp_f32 v156, v156\n"
        "v_exp_f32 v157, v157\n"
        "v_exp_f32 v158, v158\n"
        "v_exp_f32 v159, v159\n"
        "v_exp_f32 v128, v128\n"
        "v_exp_f32 v129, v129\n"
        "v_smfmac_f32_32x32x32_bf16 v[160:175], a[236:239], v[152:159], v245\n"
        "v_exp_f32 v130, v130\n"
        "v_exp_f32 v131, v131\n"
        "v_exp_f32 v132, v132\n"
        "v_exp_f32 v133, v133\n"
        "v_mfma_f32_32x32x16_bf16 v[144:159], a[124:127], v[192:195], 0\n"
        "v_exp_f32 v134, v134\n"
        "v_exp_f32 v135, v135\n"
        "v_exp_f32 v136, v136\n"
        "v_exp_f32 v137, v137\n"
        "s_add_u32 s54, s68, 0x10000\n"
        "v_exp_f32 v138, v138\n"
        "v_smfmac_f32_32x32x32_bf16 v[160:175], a[240:243], v[128:135], v245\n"
        "buffer_store_dwordx4 v[24:27], v0, %[srd], s54 offen offset:0 sc1\n"
        "v_exp_f32 v139, v139\n"
        "v_exp_f32 v140, v140\n"
        "v_exp_f32 v141, v141\n"
        "v_exp_f32 v142, v142\n"
        "v_exp_f32 v143, v143\n"
        "v_exp_f32 v144, v144\n"
        "v_exp_f32 v145, v145\n"
        "v_smfmac_f32_32x32x32_bf16 v[160:175], a[244:247], v[136:143], v245\n"
        "v_exp_f32 v146, v146\n"
        "v_exp_f32 v147, v147\n"
        "v_exp_f32 v148, v148\n"
        "v_exp_f32 v149, v149\n"
        "s_waitcnt lgkmcnt(0)\n"
        "v_mfma_f32_32x32x16_bf16 v[128:143], a[64:67], v[196:199], 0\n"
        "v_exp_f32 v150, v150\n"
        "v_exp_f32 v151, v151\n"
        "v_exp_f32 v152, v152\n"
        "v_exp_f32 v153, v153\n"
        "v_exp_f32 v154, v154\n"
        "v_smfmac_f32_32x32x32_bf16 v[160:175], a[248:251], v[144:151], v245\n"
        "v_exp_f32 v155, v155\n"
        "v_exp_f32 v156, v156\n"
        "v_exp_f32 v157, v157\n"
        "v_exp_f32 v158, v158\n"
        "v_exp_f32 v159, v159\n"
        "s_nop 0\n"
        "v_exp_f32 v128, v128\n"
        "v_exp_f32 v129, v129\n"
        "v_smfmac_f32_32x32x32_bf16 v[160:175], a[252:255], v[152:159], v245\n"
        "v_exp_f32 v130, v130\n"
        "v_exp_f32 v131, v131\n"
        "v_exp_f32 v132, v132\n"
        "v_exp_f32 v133, v133\n"
        "v_mfma_f32_32x32x16_bf16 v[144:159], a[68:71], v[196:199], 0\n"
        "v_exp_f32 v134, v134\n"
        "v_exp_f32 v135, v135\n"
        "v_exp_f32 v136, v136\n"
        "v_exp_f32 v137, v137\n"
        "v_exp_f32 v138, v138\n"
        "v_smfmac_f32_32x32x32_bf16 v[176:191], a[128:131], v[128:135], v245\n"
        "buffer_store_dwordx4 v[24:27], v0, %[srd], s54 offen offset:1024 sc1\n"
        "v_exp_f32 v139, v139\n"
        "v_exp_f32 v140, v140\n"
        "v_exp_f32 v141, v141\n"
        "v_exp_f32 v142, v142\n"
        "v_exp_f32 v143, v143\n"
        "v_exp_f32 v144, v144\n"
        "v_exp_f32 v145, v145\n"
        "v_smfmac_f32_32x32x32_bf16 v[176:191], a[132:135], v[136:143], v245\n"
        "v_exp_f32 v146, v146\n"
        "v_exp_f32 v147, v147\n"
        "v_exp_f32 v148, v148\n"
        "v_exp_f32 v149, v149\n"
        "v_mfma_f32_32x32x16_bf16 v[128:143], a[72:75], v[196:199], 0\n"
        "v_exp_f32 v150, v150\n"
        "v_exp_f32 v151, v151\n"
        "v_exp_f32 v152, v152\n"
        "v_exp_f32 v153, v153\n"
        "v_exp_f32 v154, v154\n"
        "v_smfmac_f32_32x32x32_bf16 v[176:191], a[136:139], v[144:151], v245\n"
        "ds_write_b128 v246, v[160:163] offset:4608\n"
        "v_exp_f32 v155, v155\n"
        "v_exp_f32 v156, v156\n"
        "v_exp_f32 v157, v157\n"
        "v_exp_f32 v158, v158\n"
        "v_exp_f32 v159, v159\n"
        "v_exp_f32 v128, v128\n"
        "v_exp_f32 v129, v129\n"
        "v_smfmac_f32_32x32x32_bf16 v[176:191], a[140:143], v[152:159], v245\n"
        "ds_write_b128 v246, v[164:167] offset:5632\n"
        "v_exp_f32 v130, v130\n"
        "v_exp_f32 v131, v131\n"
        "v_add_u32 v251, 2048, v251\n"
        "v_exp_f32 v132, v132\n"
        "v_exp_f32 v133, v133\n"
        "v_mfma_f32_32x32x16_bf16 v[144:159], a[76:79], v[196:199], 0\n"
        "v_exp_f32 v134, v134\n"
        "v_exp_f32 v135, v135\n"
        "v_and_b32 v251, 0x1fff, v251\n"
        "v_exp_f32 v136, v136\n"
        "v_exp_f32 v137, v137\n"
        "v_exp_f32 v138, v138\n"
        "v_smfmac_f32_32x32x32_bf16 v[176:191], a[144:147], v[128:135], v245\n"
        "ds_write_b32 v247, v168 offset:4608\n"
        "v_exp_f32 v139, v139\n"
        "v_exp_f32 v140, v140\n"
        "v_or_b32 v251, s29, v251\n"
        "v_exp_f32 v141, v141\n"
        "v_exp_f32 v142, v142\n"
        "v_exp_f32 v143, v143\n"
        "v_exp_f32 v144, v144\n"
        "v_exp_f32 v145, v145\n"
        "v_smfmac_f32_32x32x32_bf16 v[176:191], a[148:151], v[136:143], v245\n"
        "ds_read_b128 v[192:195], v251 offset:0\n"
        "v_exp_f32 v146, v146\n"
        "v_exp_f32 v147, v147\n"
        "v_exp_f32 v148, v148\n"
        "v_exp_f32 v149, v149\n"
        "v_mfma_f32_32x32x16_bf16 v[128:143], a[80:83], v[196:199], 0\n"
        "v_exp_f32 v150, v150\n"
        "v_exp_f32 v151, v151\n"
        "v_exp_f32 v152, v152\n"
        "v_exp_f32 v153, v153\n"
        "s_add_u32 s54, s67, 0x80000\n"
        "v_exp_f32 v154, v154\n"
        "v_smfmac_f32_32x32x32_bf16 v[176:191], a[152:155], v[144:151], v245\n"
        "s_mov_b64 exec, 0xffff\n"
        "buffer_store_dwordx2 v[24:25], v16, %[srd], s67 offen sc1\n"
        "buffer_store_dwordx2 v[24:25], v16, %[srd], s54 offen sc1\n"
        "s_mov_b64 exec, -1\n"
        "v_exp_f32 v155, v155\n"
        "v_exp_f32 v156, v156\n"
        "v_exp_f32 v157, v157\n"
        "v_exp_f32 v158, v158\n"
        "v_exp_f32 v159, v159\n"
        "v_exp_f32 v128, v128\n"
        "v_exp_f32 v129, v129\n"
        "v_smfmac_f32_32x32x32_bf16 v[176:191], a[156:159], v[152:159], v245\n"
        "v_exp_f32 v130, v130\n"
        "v_exp_f32 v131, v131\n"
        "v_exp_f32 v132, v132\n"
        "v_exp_f32 v133, v133\n"
        "v_mfma_f32_32x32x16_bf16 v[144:159], a[84:87], v[196:199], 0\n"
        "v_exp_f32 v134, v134\n"
        "v_exp_f32 v135, v135\n"
        "v_exp_f32 v136, v136\n"
        "v_exp_f32 v137, v137\n"
        "v_exp_f32 v138, v138\n"
        "v_smfmac_f32_32x32x32_bf16 v[176:191], a[160:163], v[128:135], v245\n"
        "v_exp_f32 v139, v139\n"
        "v_exp_f32 v140, v140\n"
        "v_exp_f32 v141, v141\n"
        "v_mfma_f32_32x32x16_bf16 v[160:175], v[220:223], v[220:223], 0\n"
        "v_exp_f32 v142, v142\n"
        "v_exp_f32 v143, v143\n"
        "v_exp_f32 v144, v144\n"
        "v_exp_f32 v145, v145\n"
        "v_smfmac_f32_32x32x32_bf16 v[176:191], a[164:167], v[136:143], v245\n"
        "v_exp_f32 v146, v146\n"
        "v_exp_f32 v147, v147\n"
        "v_exp_f32 v148, v148\n"
        "v_exp_f32 v149, v149\n"
        "v_mfma_f32_32x32x16_bf16 v[128:143], a[88:91], v[196:199], 0\n"
        "v_exp_f32 v150, v150\n"
        "v_exp_f32 v151, v151\n"
        "v_exp_f32 v152, v152\n"
        "v_exp_f32 v153, v153\n"
        "s_add_u32 s54, s64, 0x200000\n"
        "v_exp_f32 v154, v154\n"
        "v_smfmac_f32_32x32x32_bf16 v[176:191], a[168:171], v[144:151], v245\n"
        "buffer_store_dwordx4 v[24:27], v0, %[srd], s54 offen offset:0 sc1\n"
        "v_exp_f32 v155, v155\n"
        "v_exp_f32 v156, v156\n"
        "v_exp_f32 v157, v157\n"
        "v_exp_f32 v158, v158\n"
        "v_exp_f32 v159, v159\n"
        "v_exp_f32 v128, v128\n"
        "v_exp_f32 v129, v129\n"
        "v_smfmac_f32_32x32x32_bf16 v[176:191], a[172:175], v[152:159], v245\n"
        "v_exp_f32 v130, v130\n"
        "v_exp_f32 v131, v131\n"
        "v_exp_f32 v132, v132\n"
        "v_exp_f32 v133, v133\n"
        "v_mfma_f32_32x32x16_bf16 v[144:159], a[92:95], v[196:199], 0\n"
        "v_exp_f32 v134, v134\n"
        "v_exp_f32 v135, v135\n"
        "v_exp_f32 v136, v136\n"
        "v_exp_f32 v137, v137\n"
        "v_exp_f32 v138, v138\n"
        "v_smfmac_f32_32x32x32_bf16 v[176:191], a[176:179], v[128:135], v245\n"
        "buffer_store_dwordx4 v[24:27], v0, %[srd], s54 offen offset:1024 sc1\n"
        "v_exp_f32 v139, v139\n"
        "v_exp_f32 v140, v140\n"
        "v_exp_f32 v141, v141\n"
        "v_exp_f32 v142, v142\n"
        "v_exp_f32 v143, v143\n"
        "v_exp_f32 v144, v144\n"
        "v_exp_f32 v145, v145\n"
        "v_smfmac_f32_32x32x32_bf16 v[176:191], a[180:183], v[136:143], v245\n"
        "v_exp_f32 v146, v146\n"
        "v_exp_f32 v147, v147\n"
        "v_exp_f32 v148, v148\n"
        "v_exp_f32 v149, v149\n"
        "v_mfma_f32_32x32x16_bf16 v[128:143], a[96:99], v[196:199], 0\n"
        "v_exp_f32 v150, v150\n"
        "v_exp_f32 v151, v151\n"
        "v_exp_f32 v152, v152\n"
        "v_exp_f32 v153, v153\n"
        "v_exp_f32 v154, v154\n"
        "v_smfmac_f32_32x32x32_bf16 v[176:191], a[184:187], v[144:151], v245\n"
        "v_exp_f32 v155, v155\n"
        "v_exp_f32 v156, v156\n"
        "v_exp_f32 v157, v157\n"
        "v_exp_f32 v158, v158\n"
        "v_exp_f32 v159, v159\n"
        "s_nop 0\n"
        "v_exp_f32 v128, v128\n"
        "v_exp_f32 v129, v129\n"
        "v_smfmac_f32_32x32x32_bf16 v[176:191], a[188:191], v[152:159], v245\n"
        "v_exp_f32 v130, v130\n"
        "v_exp_f32 v131, v131\n"
        "v_exp_f32 v132, v132\n"
        "v_exp_f32 v133, v133\n"
        "v_mfma_f32_32x32x16_bf16 v[144:159], a[100:103], v[196:199], 0\n"
        "v_exp_f32 v134, v134\n"
        "v_exp_f32 v135, v135\n"
        "v_exp_f32 v136, v136\n"
        "v_exp_f32 v137, v137\n"
        "s_add_u32 s54, s64, 0xa00000\n"
        "v_exp_f32 v138, v138\n"
        "v_smfmac_f32_32x32x32_bf16 v[176:191], a[192:195], v[128:135], v245\n"
        "buffer_store_dwordx4 v[24:27], v0, %[srd], s54 offen offset:0 sc1\n"
        "v_exp_f32 v139, v139\n"
        "v_exp_f32 v140, v140\n"
        "v_exp_f32 v141, v141\n"
        "v_exp_f32 v142, v142\n"
        "v_exp_f32 v143, v143\n"
        "v_exp_f32 v144, v144\n"
        "v_exp_f32 v145, v145\n"
        "v_smfmac_f32_32x32x32_bf16 v[176:191], a[196:199], v[136:143], v245\n"
        "v_exp_f32 v146, v146\n"
        "v_exp_f32 v147, v147\n"
        "v_exp_f32 v148, v148\n"
        "v_exp_f32 v149, v149\n"
        "v_mfma_f32_32x32x16_bf16 v[128:143], a[104:107], v[196:199], 0\n"
        "v_exp_f32 v150, v150\n"
        "v_exp_f32 v151, v151\n"
        "v_exp_f32 v152, v152\n"
        "v_exp_f32 v153, v153\n"
        "v_exp_f32 v154, v154\n"
        "v_smfmac_f32_32x32x32_bf16 v[176:191], a[200:203], v[144:151], v245\n"
        "buffer_store_dwordx4 v[24:27], v0, %[srd], s54 offen offset:1024 sc1\n"
        "v_exp_f32 v155, v155\n"
        "v_exp_f32 v156, v156\n"
        "v_exp_f32 v157, v157\n"
        "v_exp_f32 v158, v158\n"
        "v_exp_f32 v159, v159\n"
        "v_exp_f32 v128, v128\n"
        "v_exp_f32 v129, v129\n"
        "v_smfmac_f32_32x32x32_bf16 v[176:191], a[204:207], v[152:159], v245\n"
        "v_exp_f32 v130, v130\n"
        "v_exp_f32 v131, v131\n"
        "v_exp_f32 v132, v132\n"
        "v_exp_f32 v133, v133\n"
        "v_mfma_f32_32x32x16_bf16 v[144:159], a[108:111], v[196:199], 0\n"
        "v_exp_f32 v134, v134\n"
        "v_exp_f32 v135, v135\n"
        "v_exp_f32 v136, v136\n"
        "v_exp_f32 v137, v137\n"
        "v_exp_f32 v138, v138\n"
        "v_smfmac_f32_32x32x32_bf16 v[176:191], a[208:211], v[128:135], v245\n"
        "v_exp_f32 v139, v139\n"
        "v_exp_f32 v140, v140\n"
        "v_exp_f32 v141, v141\n"
        "v_exp_f32 v142, v142\n"
        "v_exp_f32 v143, v143\n"
        "s_nop 0\n"
        "v_exp_f32 v144, v144\n"
        "v_exp_f32 v145, v145\n"
        "v_smfmac_f32_32x32x32_bf16 v[176:191], a[212:215], v[136:143], v245\n"
        "v_exp_f32 v146, v146\n"
        "v_exp_f32 v147, v147\n"
        "v_exp_f32 v148, v148\n"
        "v_exp_f32 v149, v149\n"
        "v_mfma_f32_32x32x16_bf16 v[128:143], a[112:115], v[196:199], 0\n"
        "v_exp_f32 v150, v150\n"
        "v_exp_f32 v151, v151\n"
        "v_exp_f32 v152, v152\n"
        "v_exp_f32 v153, v153\n"
        "s_add_u32 s54, s61, 0x20000\n"
        "v_exp_f32 v154, v154\n"
        "v_smfmac_f32_32x32x32_bf16 v[176:191], a[216:219], v[144:151], v245\n"
        "s_mov_b64 exec, 0xff\n"
        "buffer_store_dwordx4 v[24:27], v0, %[srd], s71 offen offset:0 sc1\n"
        "buffer_store_dwordx4 v[24:27], v0, %[srd], s54 offen offset:0 sc1\n"
        "s_mov_b64 exec, -1\n"
        "v_exp_f32 v155, v155\n"
        "v_exp_f32 v156, v156\n"
        "v_exp_f32 v157, v157\n"
        "v_exp_f32 v158, v158\n"
        "v_exp_f32 v159, v159\n"
        "v_exp_f32 v128, v128\n"
        "v_exp_f32 v129, v129\n"
        "v_smfmac_f32_32x32x32_bf16 v[176:191], a[220:223], v[152:159], v245\n"
        "v_exp_f32 v130, v130\n"
        "v_exp_f32 v131, v131\n"
        "v_exp_f32 v132, v132\n"
        "v_exp_f32 v133, v133\n"
        "v_mfma_f32_32x32x16_bf16 v[144:159], a[116:119], v[196:199], 0\n"
        "v_exp_f32 v134, v134\n"
        "v_exp_f32 v135, v135\n"
        "v_exp_f32 v136, v136\n"
        "v_exp_f32 v137, v137\n"
        "v_exp_f32 v138, v138\n"
        "v_smfmac_f32_32x32x32_bf16 v[176:191], a[224:227], v[128:135], v245\n"
        "buffer_store_dwordx2 v[24:25], v17, %[srd], s74 offen sc1\n"
        "v_exp_f32 v139, v139\n"
        "v_exp_f32 v140, v140\n"
        "v_exp_f32 v141, v141\n"
        "v_exp_f32 v142, v142\n"
        "v_exp_f32 v143, v143\n"
        "v_exp_f32 v144, v144\n"
        "v_exp_f32 v145, v145\n"
        "v_smfmac_f32_32x32x32_bf16 v[176:191], a[228:231], v[136:143], v245\n"
        "v_exp_f32 v146, v146\n"
        "v_exp_f32 v147, v147\n"
        "v_exp_f32 v148, v148\n"
        "v_exp_f32 v149, v149\n"
        "v_mfma_f32_32x32x16_bf16 v[128:143], a[120:123], v[196:199], 0\n"
        "v_exp_f32 v150, v150\n"
        "v_exp_f32 v151, v151\n"
        "v_exp_f32 v152, v152\n"
        "v_exp_f32 v153, v153\n"
        "v_exp_f32 v154, v154\n"
        "v_smfmac_f32_32x32x32_bf16 v[176:191], a[232:235], v[144:151], v245\n"
        "v_exp_f32 v155, v155\n"
        "v_exp_f32 v156, v156\n"
        "v_exp_f32 v157, v157\n"
        "v_exp_f32 v158, v158\n"
        "v_exp_f32 v159, v159\n"
        "s_nop 0\n"
        "v_exp_f32 v128, v128\n"
        "v_exp_f32 v129, v129\n"
        "v_smfmac_f32_32x32x32_bf16 v[176:191], a[236:239], v[152:159], v245\n"
        "v_exp_f32 v130, v130\n"
        "v_exp_f32 v131, v131\n"
        "v_exp_f32 v132, v132\n"
        "v_exp_f32 v133, v133\n"
        "v_mfma_f32_32x32x16_bf16 v[144:159], a[124:127], v[196:199], 0\n"
        "v_exp_f32 v134, v134\n"
        "v_exp_f32 v135, v135\n"
        "v_exp_f32 v136, v136\n"
        "v_exp_f32 v137, v137\n"
        "v_exp_f32 v138, v138\n"
        "v_smfmac_f32_32x32x32_bf16 v[176:191], a[240:243], v[128:135], v245\n"
        "v_exp_f32 v139, v139\n"
        "v_exp_f32 v140, v140\n"
        "v_exp_f32 v141, v141\n"
        "v_exp_f32 v142, v142\n"
        "v_exp_f32 v143, v143\n"
        "s_nop 0\n"
        "v_exp_f32 v144, v144\n"
        "v_exp_f32 v145, v145\n"
        "v_smfmac_f32_32x32x32_bf16 v[176:191], a[244:247], v[136:143], v245\n"
        "v_exp_f32 v146, v146\n"
        "v_exp_f32 v147, v147\n"
        "v_exp_f32 v148, v148\n"
        "v_exp_f32 v149, v149\n"
        "s_waitcnt lgkmcnt(0)\n"
        "v_mfma_f32_32x32x16_bf16 v[128:143], a[64:67], v[192:195], 0\n"
        "v_exp_f32 v150, v150\n"
        "v_exp_f32 v151, v151\n"
        "v_exp_f32 v152, v152\n"
        "v_exp_f32 v153, v153\n"
        "s_mov_b32 s54, 0x8000\n"
        "v_exp_f32 v154, v154\n"
        "v_smfmac_f32_32x32x32_bf16 v[176:191], a[248:251], v[144:151], v245\n"
        "buffer_load_dwordx4 v[28:31], v18, %[srd], s54 offen sc1\n"
        "v_exp_f32 v155, v155\n"
        "v_exp_f32 v156, v156\n"
        "v_exp_f32 v157, v157\n"
        "v_exp_f32 v158, v158\n"
        "v_exp_f32 v159, v159\n"
        "v_exp_f32 v128, v128\n"
        "v_exp_f32 v129, v129\n"
        "v_smfmac_f32_32x32x32_bf16 v[176:191], a[252:255], v[152:159], v245\n"
        "v_exp_f32 v130, v130\n"
        "v_exp_f32 v131, v131\n"
        "v_exp_f32 v132, v132\n"
        "v_exp_f32 v133, v133\n"
        "v_mfma_f32_32x32x16_bf16 v[144:159], a[68:71], v[192:195], 0\n"
        "v_exp_f32 v134, v134\n"
        "v_exp_f32 v135, v135\n"
        "v_exp_f32 v136, v136\n"
        "v_exp_f32 v137, v137\n"
        "v_exp_f32 v138, v138\n"
        "v_smfmac_f32_32x32x32_bf16 v[160:175], a[128:131], v[128:135], v245\n"
        "v_exp_f32 v139, v139\n"
        "v_exp_f32 v140, v140\n"
        "v_exp_f32 v141, v141\n"
        "v_exp_f32 v142, v142\n"
        "v_exp_f32 v143, v143\n"
        "s_nop 0\n"
        "v_exp_f32 v144, v144\n"
        "v_exp_f32 v145, v145\n"
        "v_smfmac_f32_32x32x32_bf16 v[160:175], a[132:135], v[136:143], v245\n"
        "v_exp_f32 v146, v146\n"
        "v_exp_f32 v147, v147\n"
        "v_exp_f32 v148, v148\n"
        "v_exp_f32 v149, v149\n"
        "v_mfma_f32_32x32x16_bf16 v[128:143], a[72:75], v[192:195], 0\n"
        "v_exp_f32 v150, v150\n"
        "v_exp_f32 v151, v151\n"
        "v_exp_f32 v152, v152\n"
        "v_exp_f32 v153, v153\n"
        "v_exp_f32 v154, v154\n"
        "v_smfmac_f32_32x32x32_bf16 v[160:175], a[136:139], v[144:151], v245\n"
        "ds_write_b128 v246, v[176:179] offset:6912\n"
        "v_exp_f32 v155, v155\n"
        "v_exp_f32 v156, v156\n"
        "v_exp_f32 v157, v157\n"
        "v_exp_f32 v158, v158\n"
        "v_exp_f32 v159, v159\n"
        "v_exp_f32 v128, v128\n"
        "v_exp_f32 v129, v129\n"
        "v_smfmac_f32_32x32x32_bf16 v[160:175], a[140:143], v[152:159], v245\n"
        "ds_write_b128 v246, v[180:183] offset:7936\n"
        "v_exp_f32 v130, v130\n"
        "v_exp_f32 v131, v131\n"
        "v_exp_f32 v132, v132\n"
        "v_exp_f32 v133, v133\n"
        "v_mfma_f32_32x32x16_bf16 v[144:159], a[76:79], v[192:195], 0\n"
        "v_exp_f32 v134, v134\n"
        "v_exp_f32 v135, v135\n"
        "v_exp_f32 v136, v136\n"
        "v_exp_f32 v137, v137\n"
        "v_exp_f32 v138, v138\n"
        "v_smfmac_f32_32x32x32_bf16 v[160:175], a[144:147], v[128:135], v245\n"
        "ds_write_b32 v247, v184 offset:6912\n"
        "v_exp_f32 v139, v139\n"
        "v_exp_f32 v140, v140\n"
        "v_exp_f32 v141, v141\n"
        "v_exp_f32 v142, v142\n"
        "v_exp_f32 v143, v143\n"
        "v_exp_f32 v144, v144\n"
        "v_exp_f32 v145, v145\n"
        "v_smfmac_f32_32x32x32_bf16 v[160:175], a[148:151], v[136:143], v245\n"
        "ds_read_b128 v[196:199], v251 offset:1024\n"
        "v_exp_f32 v146, v146\n"
        "v_exp_f32 v147, v147\n"
        "v_exp_f32 v148, v148\n"
        "v_exp_f32 v149, v149\n"
        "v_mfma_f32_32x32x16_bf16 v[128:143], a[80:83], v[192:195], 0\n"
        "v_exp_f32 v150, v150\n"
        "v_exp_f32 v151, v151\n"
        "v_exp_f32 v152, v152\n"
        "v_exp_f32 v153, v153\n"
        "v_exp_f32 v154, v154\n"
        "v_smfmac_f32_32x32x32_bf16 v[160:175], a[152:155], v[144:151], v245\n"
        "v_exp_f32 v155, v155\n"
        "v_exp_f32 v156, v156\n"
        "v_exp_f32 v157, v157\n"
        "v_exp_f32 v158, v158\n"
        "v_exp_f32 v159, v159\n"
        "s_nop 0\n"
        "v_exp_f32 v128, v128\n"
        "v_exp_f32 v129, v129\n"
        "v_smfmac_f32_32x32x32_bf16 v[160:175], a[156:159], v[152:159], v245\n"
        "v_exp_f32 v130, v130\n"
        "v_exp_f32 v131, v131\n"
        "s_waitcnt vmcnt(0)\n"
        "v_xor_b32 v28, v28, v19\n"
        "v_xor_b32 v29, v29, v19\n"
        "v_xor_b32 v30, v30, v19\n"
        "v_xor_b32 v31, v31, v19\n"
        "v_or3_b32 v28, v28, v29, v30\n"
        "v_or_b32 v28, v28, v31\n"
        "v_cmp_ne_u32 vcc, 0, v28\n"
        "s_cbranch_vccnz Lf2spin_%=\n"
        "Lf2back_%=:\n"
        "v_exp_f32 v132, v132\n"
        "v_exp_f32 v133, v133\n"
        "v_mfma_f32_32x32x16_bf16 v[144:159], a[84:87], v[192:195], 0\n"
        "v_exp_f32 v134, v134\n"
        "v_exp_f32 v135, v135\n"
        "v_exp_f32 v136, v136\n"
        "v_exp_f32 v137, v137\n"
        "s_add_u32 s54, s76, s75\n"
        "v_exp_f32 v138, v138\n"
        "v_smfmac_f32_32x32x32_bf16 v[160:175], a[160:163], v[128:135], v245\n"
        "s_mov_b64 exec, 1\n"
        "buffer_store_dword v20, off, %[srd], s54 sc1\n"
        "s_mov_b64 exec, -1\n"
        "v_exp_f32 v139, v139\n"
        "v_exp_f32 v140, v140\n"
        "v_exp_f32 v141, v141\n"
        "v_mfma_f32_32x32x16_bf16 v[176:191], v[220:223], v[220:223], 0\n"
        "v_exp_f32 v142, v142\n"
        "v_exp_f32 v143, v143\n"
        "v_exp_f32 v144, v144\n"
        "s_add_u32 s54, s73, 8192\n"
        "v_exp_f32 v145, v145\n"
        "v_smfmac_f32_32x32x32_bf16 v[160:175], a[164:167], v[136:143], v245\n"
        "buffer_load_dwordx4 v[200:203], v242, %[srd], s54 offen sc1\n"
        "v_exp_f32 v146, v146\n"
        "v_exp_f32 v147, v147\n"
        "v_exp_f32 v148, v148\n"
        "v_exp_f32 v149, v149\n"
        "v_mfma_f32_32x32x16_bf16 v[128:143], a[88:91], v[192:195], 0\n"
        "v_exp_f32 v150, v150\n"
        "v_exp_f32 v151, v151\n"
        "v_exp_f32 v152, v152\n"
        "v_exp_f32 v153, v153\n"
        "s_add_u32 s54, s73, 16384\n"
        "v_exp_f32 v154, v154\n"
        "v_smfmac_f32_32x32x32_bf16 v[160:175], a[168:171], v[144:151], v245\n"
        "buffer_load_dwordx4 v[204:207], v242, %[srd], s54 offen sc1\n"
        "v_exp_f32 v155, v155\n"
        "v_exp_f32 v156, v156\n"
        "v_exp_f32 v157, v157\n"
        "v_exp_f32 v158, v158\n"
        "v_exp_f32 v159, v159\n"
        "v_exp_f32 v128, v128\n"
        "v_exp_f32 v129, v129\n"
        "v_smfmac_f32_32x32x32_bf16 v[160:175], a[172:175], v[152:159], v245\n"
        "v_exp_f32 v130, v130\n"
        "v_exp_f32 v131, v131\n"
        "v_exp_f32 v132, v132\n"
        "v_exp_f32 v133, v133\n"
        "v_mfma_f32_32x32x16_bf16 v[144:159], a[92:95], v[192:195], 0\n"
        "v_exp_f32 v134, v134\n"
        "v_exp_f32 v135, v135\n"
        "v_exp_f32 v136, v136\n"
        "v_exp_f32 v137, v137\n"
        "s_add_u32 s54, s73, 40960\n"
        "v_exp_f32 v138, v138\n"
        "v_smfmac_f32_32x32x32_bf16 v[160:175], a[176:179], v[128:135], v245\n"
        "buffer_load_dwordx4 v[208:211], v242, %[srd], s54 offen sc1\n"
        "v_exp_f32 v139, v139\n"
        "v_exp_f32 v140, v140\n"
        "v_exp_f32 v141, v141\n"
        "v_exp_f32 v142, v142\n"
        "v_exp_f32 v143, v143\n"
        "v_exp_f32 v144, v144\n"
        "v_exp_f32 v145, v145\n"
        "v_smfmac_f32_32x32x32_bf16 v[160:175], a[180:183], v[136:143], v245\n"
        "buffer_load_dwordx4 v[212:215], v244, %[srd], s85 offen sc1\n"
        "v_exp_f32 v146, v146\n"
        "v_exp_f32 v147, v147\n"
        "v_exp_f32 v148, v148\n"
        "v_exp_f32 v149, v149\n"
        "v_mfma_f32_32x32x16_bf16 v[128:143], a[96:99], v[192:195], 0\n"
        "v_exp_f32 v150, v150\n"
        "v_exp_f32 v151, v151\n"
        "v_exp_f32 v152, v152\n"
        "v_exp_f32 v153, v153\n"
        "s_add_u32 s54, s73, 24576\n"
        "v_exp_f32 v154, v154\n"
        "v_smfmac_f32_32x32x32_bf16 v[160:175], a[184:187], v[144:151], v245\n"
        "buffer_load_dword v216, v243, %[srd], s54 offen sc1\n"
        "v_exp_f32 v155, v155\n"
        "v_exp_f32 v156, v156\n"
        "v_exp_f32 v157, v157\n"
        "v_exp_f32 v158, v158\n"
        "v_exp_f32 v159, v159\n"
        "v_exp_f32 v128, v128\n"
        "v_exp_f32 v129, v129\n"
        "v_smfmac_f32_32x32x32_bf16 v[160:175], a[188:191], v[152:159], v245\n"
        "v_exp_f32 v130, v130\n"
        "v_exp_f32 v131, v131\n"
        "v_exp_f32 v132, v132\n"
        "v_exp_f32 v133, v133\n"
        "v_mfma_f32_32x32x16_bf16 v[144:159], a[100:103], v[192:195], 0\n"
        "v_exp_f32 v134, v134\n"
        "v_exp_f32 v135, v135\n"
        "v_exp_f32 v136, v136\n"
        "v_exp_f32 v137, v137\n"
        "s_add_u32 s54, s72, 0\n"
        "v_exp_f32 v138, v138\n"
        "v_smfmac_f32_32x32x32_bf16 v[160:175], a[192:195], v[128:135], v245\n"
        "buffer_load_dwordx4 a[0:3], v0, %[srd], s54 offen offset:0 sc1\n"
        "v_exp_f32 v139, v139\n"
        "v_exp_f32 v140, v140\n"
        "v_exp_f32 v141, v141\n"
        "v_exp_f32 v142, v142\n"
        "v_exp_f32 v143, v143\n"
        "v_exp_f32 v144, v144\n"
        "v_exp_f32 v145, v145\n"
        "v_smfmac_f32_32x32x32_bf16 v[160:175], a[196:199], v[136:143], v245\n"
        "buffer_load_dwordx4 a[4:7], v0, %[srd], s54 offen offset:1024 sc1\n"
        "v_exp_f32 v146, v146\n"
        "v_exp_f32 v147, v147\n"
        "v_exp_f32 v148, v148\n"
        "v_exp_f32 v149, v149\n"
        "v_mfma_f32_32x32x16_bf16 v[128:143], a[104:107], v[192:195], 0\n"
        "v_exp_f32 v150, v150\n"
        "v_exp_f32 v151, v151\n"
        "v_exp_f32 v152, v152\n"
        "v_exp_f32 v153, v153\n"
        "v_exp_f32 v154, v154\n"
        "v_smfmac_f32_32x32x32_bf16 v[160:175], a[200:203], v[144:151], v245\n"
        "buffer_load_dwordx4 a[8:11], v0, %[srd], s54 offen offset:2048 sc1\n"
        "v_exp_f32 v155, v155\n"
        "v_exp_f32 v156, v156\n"
        "v_exp_f32 v157, v157\n"
        "v_exp_f32 v158, v158\n"
        "v_exp_f32 v159, v159\n"
        "v_exp_f32 v128, v128\n"
        "v_exp_f32 v129, v129\n"
        "v_smfmac_f32_32x32x32_bf16 v[160:175], a[204:207], v[152:159], v245\n"
        "v_exp_f32 v130, v130\n"
        "v_exp_f32 v131, v131\n"
        "v_exp_f32 v132, v132\n"
        "v_exp_f32 v133, v133\n"
        "v_mfma_f32_32x32x16_bf16 v[144:159], a[108:111], v[192:195], 0\n"
        "v_exp_f32 v134, v134\n"
        "v_exp_f32 v135, v135\n"
        "v_exp_f32 v136, v136\n"
        "v_exp_f32 v137, v137\n"
        "v_exp_f32 v138, v138\n"
        "v_smfmac_f32_32x32x32_bf16 v[160:175], a[208:211], v[128:135], v245\n"
        "buffer_load_dwordx4 a[12:15], v0, %[srd], s54 offen offset:3072 sc1\n"
        "v_exp_f32 v139, v139\n"
        "v_exp_f32 v140, v140\n"
        "v_exp_f32 v141, v141\n"
        "v_exp_f32 v142, v142\n"
        "v_exp_f32 v143, v143\n"
        "v_exp_f32 v144, v144\n"
        "s_add_u32 s54, s72, 4096\n"
        "v_exp_f32 v145, v145\n"
        "v_smfmac_f32_32x32x32_bf16 v[160:175], a[212:215], v[136:143], v245\n"
        "buffer_load_dwordx4 a[16:19], v0, %[srd], s54 offen offset:0 sc1\n"
        "v_exp_f32 v146, v146\n"
        "v_exp_f32 v147, v147\n"
        "v_exp_f32 v148, v148\n"
        "v_exp_f32 v149, v149\n"
        "v_mfma_f32_32x32x16_bf16 v[128:143], a[112:115], v[192:195], 0\n"
        "v_exp_f32 v150, v150\n"
        "v_exp_f32 v151, v151\n"
        "v_exp_f32 v152, v152\n"
        "v_exp_f32 v153, v153\n"
        "v_exp_f32 v154, v154\n"
        "v_smfmac_f32_32x32x32_bf16 v[160:175], a[216:219], v[144:151], v245\n"
        "buffer_load_dwordx4 a[20:23], v0, %[srd], s54 offen offset:1024 sc1\n"
        "v_exp_f32 v155, v155\n"
        "v_exp_f32 v156, v156\n"
        "v_exp_f32 v157, v157\n"
        "v_exp_f32 v158, v158\n"
        "v_exp_f32 v159, v159\n"
        "v_exp_f32 v128, v128\n"
        "v_exp_f32 v129, v129\n"
        "v_smfmac_f32_32x32x32_bf16 v[160:175], a[220:223], v[152:159], v245\n"
        "v_exp_f32 v130, v130\n"
        "v_exp_f32 v131, v131\n"
        "v_exp_f32 v132, v132\n"
        "v_exp_f32 v133, v133\n"
        "v_mfma_f32_32x32x16_bf16 v[144:159], a[116:119], v[192:195], 0\n"
        "v_exp_f32 v134, v134\n"
        "v_exp_f32 v135, v135\n"
        "v_exp_f32 v136, v136\n"
        "v_exp_f32 v137, v137\n"
        "v_exp_f32 v138, v138\n"
        "v_smfmac_f32_32x32x32_bf16 v[160:175], a[224:227], v[128:135], v245\n"
        "buffer_load_dwordx4 a[24:27], v0, %[srd], s54 offen offset:2048 sc1\n"
        "v_exp_f32 v139, v139\n"
        "v_exp_f32 v140, v140\n"
        "v_exp_f32 v141, v141\n"
        "v_exp_f32 v142, v142\n"
        "v_exp_f32 v143, v143\n"
        "v_exp_f32 v144, v144\n"
        "v_exp_f32 v145, v145\n"
        "v_smfmac_f32_32x32x32_bf16 v[160:175], a[228:231], v[136:143], v245\n"
        "buffer_load_dwordx4 a[28:31], v0, %[srd], s54 offen offset:3072 sc1\n"
        "v_exp_f32 v146, v146\n"
        "v_exp_f32 v147, v147\n"
        "v_exp_f32 v148, v148\n"
        "v_exp_f32 v149, v149\n"
        "v_mfma_f32_32x32x16_bf16 v[128:143], a[120:123], v[192:195], 0\n"
        "v_exp_f32 v150, v150\n"
        "v_exp_f32 v151, v151\n"
        "v_exp_f32 v152, v152\n"
        "v_exp_f32 v153, v153\n"
        "s_add_u32 s54, s72, 8192\n"
        "v_exp_f32 v154, v154\n"
        "v_smfmac_f32_32x32x32_bf16 v[160:175], a[232:235], v[144:151], v245\n"
        "buffer_load_dwordx4 a[32:35], v0, %[srd], s54 offen offset:0 sc1\n"
        "v_exp_f32 v155, v155\n"
        "v_exp_f32 v156, v156\n"
        "v_exp_f32 v157, v157\n"
        "v_exp_f32 v158, v158\n"
        "v_exp_f32 v159, v159\n"
        "v_exp_f32 v128, v128\n"
        "v_exp_f32 v129, v129\n"
        "v_smfmac_f32_32x32x32_bf16 v[160:175], a[236:239], v[152:159], v245\n"
        "v_exp_f32 v130, v130\n"
        "v_exp_f32 v131, v131\n"
        "v_exp_f32 v132, v132\n"
        "v_exp_f32 v133, v133\n"
        "v_mfma_f32_32x32x16_bf16 v[144:159], a[124:127], v[192:195], 0\n"
        "v_exp_f32 v134, v134\n"
        "v_exp_f32 v135, v135\n"
        "v_exp_f32 v136, v136\n"
        "v_exp_f32 v137, v137\n"
        "v_exp_f32 v138, v138\n"
        "v_smfmac_f32_32x32x32_bf16 v[160:175], a[240:243], v[128:135], v245\n"
        "buffer_load_dwordx4 a[36:39], v0, %[srd], s54 offen offset:1024 sc1\n"
        "v_exp_f32 v139, v139\n"
        "v_exp_f32 v140, v140\n"
        "v_exp_f32 v141, v141\n"
        "v_exp_f32 v142, v142\n"
        "v_exp_f32 v143, v143\n"
        "v_exp_f32 v144, v144\n"
        "v_exp_f32 v145, v145\n"
        "v_smfmac_f32_32x32x32_bf16 v[160:175], a[244:247], v[136:143], v245\n"
        "buffer_load_dwordx4 a[40:43], v0, %[srd], s54 offen offset:2048 sc1\n"
        "v_exp_f32 v146, v146\n"
        "v_exp_f32 v147, v147\n"
        "v_exp_f32 v148, v148\n"
        "v_exp_f32 v149, v149\n"
        "s_waitcnt lgkmcnt(0)\n"
        "v_mfma_f32_32x32x16_bf16 v[128:143], a[64:67], v[196:199], 0\n"
        "v_exp_f32 v150, v150\n"
        "v_exp_f32 v151, v151\n"
        "v_exp_f32 v152, v152\n"
        "v_exp_f32 v153, v153\n"
        "v_exp_f32 v154, v154\n"
        "v_smfmac_f32_32x32x32_bf16 v[160:175], a[248:251], v[144:151], v245\n"
        "buffer_load_dwordx4 a[44:47], v0, %[srd], s54 offen offset:3072 sc1\n"
        "v_exp_f32 v155, v155\n"
        "v_exp_f32 v156, v156\n"
        "v_exp_f32 v157, v157\n"
        "v_exp_f32 v158, v158\n"
        "v_exp_f32 v159, v159\n"
        "v_exp_f32 v128, v128\n"
        "s_add_u32 s54, s72, 12288\n"
        "v_exp_f32 v129, v129\n"
        "v_smfmac_f32_32x32x32_bf16 v[160:175], a[252:255], v[152:159], v245\n"
        "buffer_load_dwordx4 a[48:51], v0, %[srd], s54 offen offset:0 sc1\n"
        "v_exp_f32 v130, v130\n"
        "v_exp_f32 v131, v131\n"
        "v_exp_f32 v132, v132\n"
        "v_exp_f32 v133, v133\n"
        "v_mfma_f32_32x32x16_bf16 v[144:159], a[68:71], v[196:199], 0\n"
        "v_exp_f32 v134, v134\n"
        "v_exp_f32 v135, v135\n"
        "v_exp_f32 v136, v136\n"
        "v_exp_f32 v137, v137\n"
        "v_exp_f32 v138, v138\n"
        "v_smfmac_f32_32x32x32_bf16 v[176:191], a[128:131], v[128:135], v245\n"
        "buffer_load_dwordx4 a[52:55], v0, %[srd], s54 offen offset:1024 sc1\n"
        "v_exp_f32 v139, v139\n"
        "v_exp_f32 v140, v140\n"
        "v_exp_f32 v141, v141\n"
        "v_exp_f32 v142, v142\n"
        "v_exp_f32 v143, v143\n"
        "v_exp_f32 v144, v144\n"
        "v_exp_f32 v145, v145\n"
        "v_smfmac_f32_32x32x32_bf16 v[176:191], a[132:135], v[136:143], v245\n"
        "buffer_load_dwordx4 a[56:59], v0, %[srd], s54 offen offset:2048 sc1\n"
        "v_exp_f32 v146, v146\n"
        "v_exp_f32 v147, v147\n"
        "v_exp_f32 v148, v148\n"
        "v_exp_f32 v149, v149\n"
        "v_mfma_f32_32x32x16_bf16 v[128:143], a[72:75], v[196:199], 0\n"
        "v_exp_f32 v150, v150\n"
        "v_exp_f32 v151, v151\n"
        "v_exp_f32 v152, v152\n"
        "v_exp_f32 v153, v153\n"
        "v_exp_f32 v154, v154\n"
        "v_smfmac_f32_32x32x32_bf16 v[176:191], a[136:139], v[144:151], v245\n"
        "ds_write_b128 v246, v[160:163] offset:9216\n"
        "v_exp_f32 v155, v155\n"
        "v_exp_f32 v156, v156\n"
        "v_exp_f32 v157, v157\n"
        "v_exp_f32 v158, v158\n"
        "v_exp_f32 v159, v159\n"
        "v_exp_f32 v128, v128\n"
        "v_exp_f32 v129, v129\n"
        "v_smfmac_f32_32x32x32_bf16 v[176:191], a[140:143], v[152:159], v245\n"
        "ds_write_b128 v246, v[164:167] offset:10240\n"
        "v_exp_f32 v130, v130\n"
        "v_exp_f32 v131, v131\n"
        "v_add_u32 v251, 2048, v251\n"
        "v_exp_f32 v132, v132\n"
        "v_exp_f32 v133, v133\n"
        "v_mfma_f32_32x32x16_bf16 v[144:159], a[76:79], v[196:199], 0\n"
        "v_exp_f32 v134, v134\n"
        "v_exp_f32 v135, v135\n"
        "v_and_b32 v251, 0x1fff, v251\n"
        "v_exp_f32 v136, v136\n"
        "v_exp_f32 v137, v137\n"
        "v_exp_f32 v138, v138\n"
        "v_smfmac_f32_32x32x32_bf16 v[176:191], a[144:147], v[128:135], v245\n"
        "ds_write_b32 v247, v168 offset:9216\n"
        "v_exp_f32 v139, v139\n"
        "v_exp_f32 v140, v140\n"
        "v_or_b32 v251, s29, v251\n"
        "v_exp_f32 v141, v141\n"
        "v_exp_f32 v142, v142\n"
        "v_exp_f32 v143, v143\n"
        "v_exp_f32 v144, v144\n"
        "v_exp_f32 v145, v145\n"
        "v_smfmac_f32_32x32x32_bf16 v[176:191], a[148:151], v[136:143], v245\n"
        "ds_read_b128 v[192:195], v251 offset:0\n"
        "v_exp_f32 v146, v146\n"
        "v_exp_f32 v147, v147\n"
        "v_exp_f32 v148, v148\n"
        "v_exp_f32 v149, v149\n"
        "v_mfma_f32_32x32x16_bf16 v[128:143], a[80:83], v[196:199], 0\n"
        "v_exp_f32 v150, v150\n"
        "v_exp_f32 v151, v151\n"
        "v_exp_f32 v152, v152\n"
        "v_exp_f32 v153, v153\n"
        "v_exp_f32 v154, v154\n"
        "v_smfmac_f32_32x32x32_bf16 v[176:191], a[152:155], v[144:151], v245\n"
        "v_exp_f32 v155, v155\n"
        "v_exp_f32 v156, v156\n"
        "v_exp_f32 v157, v157\n"
        "s_waitcnt lgkmcnt(0)\n"
        "s_barrier\n"
        "v_exp_f32 v158, v158\n"
        "v_exp_f32 v159, v159\n"
        "v_exp_f32 v128, v128\n"
        "v_exp_f32 v129, v129\n"
        "v_smfmac_f32_32x32x32_bf16 v[176:191], a[156:159], v[152:159], v245\n"
        "buffer_load_dwordx4 a[60:63], v0, %[srd], s54 offen offset:3072 sc1\n"
        "v_exp_f32 v130, v130\n"
        "v_exp_f32 v131, v131\n"
        "v_exp_f32 v132, v132\n"
        "v_exp_f32 v133, v133\n"
        "v_mfma_f32_32x32x16_bf16 v[144:159], a[84:87], v[196:199], 0\n"
        "v_exp_f32 v134, v134\n"
        "v_exp_f32 v135, v135\n"
        "v_exp_f32 v136, v136\n"
        "v_exp_f32 v137, v137\n"
        "v_exp_f32 v138, v138\n"
        "v_smfmac_f32_32x32x32_bf16 v[176:191], a[160:163], v[128:135], v245\n"
        "ds_read_b128 v[172:175], v250 offset:528\n"
        "v_exp_f32 v139, v139\n"
        "v_exp_f32 v140, v140\n"
        "v_exp_f32 v141, v141\n"
        "v_exp_f32 v142, v142\n"
        "v_exp_f32 v143, v143\n"
        "v_exp_f32 v144, v144\n"
        "s_add_u32 s54, s72, 1048576\n"
        "v_exp_f32 v145, v145\n"
        "v_smfmac_f32_32x32x32_bf16 v[176:191], a[164:167], v[136:143], v245\n"
        "buffer_load_dwordx4 v[96:99], v0, %[srd], s54 offen offset:0 sc1\n"
        "v_exp_f32 v146, v146\n"
        "v_exp_f32 v147, v147\n"
        "s_waitcnt lgkmcnt(0)\n"
        "v_or3_b32 v172, v172, v173, v174\n"
        "v_or_b32 v172, v172, v175\n"
        "v_cmp_ne_u32 vcc, 0, v172\n"
        "s_cbranch_vccnz LslowA2_%=\n"
        "v_exp_f32 v148, v148\n"
        "v_exp_f32 v149, v149\n"
        "v_mfma_f32_32x32x16_bf16 v[128:143], a[88:91], v[196:199], 0\n"
        "v_exp_f32 v150, v150\n"
        "v_exp_f32 v151, v151\n"
        "v_exp_f32 v152, v152\n"
        "v_exp_f32 v153, v153\n"
        "v_exp_f32 v154, v154\n"
        "v_smfmac_f32_32x32x32_bf16 v[176:191], a[168:171], v[144:151], v245\n"
        "ds_read_b128 v[160:163], v248 offset:0\n"
        "ds_read_b128 v[164:167], v248 offset:1024\n"
        "ds_read_b32 v168, v249 offset:0\n"
        "v_exp_f32 v155, v155\n"
        "v_exp_f32 v156, v156\n"
        "v_exp_f32 v157, v157\n"
        "v_exp_f32 v158, v158\n"
        "v_exp_f32 v159, v159\n"
        "v_exp_f32 v128, v128\n"
        "v_exp_f32 v129, v129\n"
        "v_smfmac_f32_32x32x32_bf16 v[176:191], a[172:175], v[152:159], v245\n"
        "buffer_load_dwordx4 v[100:103], v0, %[srd], s54 offen offset:1024 sc1\n"
        "v_exp_f32 v130, v130\n"
        "v_exp_f32 v131, v131\n"
        "v_exp_f32 v132, v132\n"
        "v_exp_f32 v133, v133\n"
        "v_mfma_f32_32x32x16_bf16 v[144:159], a[92:95], v[196:199], 0\n"
        "v_exp_f32 v134, v134\n"
        "v_exp_f32 v135, v135\n"
        "v_exp_f32 v136, v136\n"
        "v_exp_f32 v137, v137\n"
        "v_exp_f32 v138, v138\n"
        "v_smfmac_f32_32x32x32_bf16 v[176:191], a[176:179], v[128:135], v245\n"
        "ds_read_b128 v[172:175], v217 offset:0\n"
        "ds_read_b32 v169, v219 offset:0\n"
        "v_exp_f32 v139, v139\n"
        "v_exp_f32 v140, v140\n"
        "v_exp_f32 v141, v141\n"
        "v_exp_f32 v142, v142\n"
        "v_exp_f32 v143, v143\n"
        "v_exp_f32 v144, v144\n"
        "v_exp_f32 v145, v145\n"
        "v_smfmac_f32_32x32x32_bf16 v[176:191], a[180:183], v[136:143], v245\n"
        "buffer_load_dwordx4 v[104:107], v0, %[srd], s54 offen offset:2048 sc1\n"
        "v_exp_f32 v146, v146\n"
        "v_exp_f32 v147, v147\n"
        "s_waitcnt lgkmcnt(0)\n"
        "v_pk_add_f32 v[160:161], v[160:161], v[172:173]\n"
        "v_pk_add_f32 v[162:163], v[162:163], v[174:175]\n"
        "v_add_f32 v168, v168, v169\n"
        "v_exp_f32 v148, v148\n"
        "v_exp_f32 v149, v149\n"
        "v_mfma_f32_32x32x16_bf16 v[128:143], a[96:99], v[196:199], 0\n"
        "v_exp_f32 v150, v150\n"
        "v_exp_f32 v151, v151\n"
        "v_exp_f32 v152, v152\n"
        "v_exp_f32 v153, v153\n"
        "v_exp_f32 v154, v154\n"
        "v_smfmac_f32_32x32x32_bf16 v[176:191], a[184:187], v[144:151], v245\n"
        "ds_read_b128 v[172:175], v217 offset:1024\n"
        "v_exp_f32 v155, v155\n"
        "v_exp_f32 v156, v156\n"
        "v_exp_f32 v157, v157\n"
        "v_exp_f32 v158, v158\n"
        "v_exp_f32 v159, v159\n"
        "v_exp_f32 v128, v128\n"
        "v_exp_f32 v129, v129\n"
        "v_smfmac_f32_32x32x32_bf16 v[176:191], a[188:191], v[152:159], v245\n"
        "buffer_load_dwordx4 v[108:111], v0, %[srd], s54 offen offset:3072 sc1\n"
        "v_exp_f32 v130, v130\n"
        "v_exp_f32 v131, v131\n"
        "s_waitcnt lgkmcnt(0)\n"
        "v_pk_add_f32 v[164:165], v[164:165], v[172:173]\n"
        "v_pk_add_f32 v[166:167], v[166:167], v[174:175]\n"
        "v_exp_f32 v132, v132\n"
        "v_exp_f32 v133, v133\n"
        "v_mfma_f32_32x32x16_bf16 v[144:159], a[100:103], v[196:199], 0\n"
        "v_exp_f32 v134, v134\n"
        "v_exp_f32 v135, v135\n"
        "v_exp_f32 v136, v136\n"
        "v_exp_f32 v137, v137\n"
        "v_exp_f32 v138, v138\n"
        "v_smfmac_f32_32x32x32_bf16 v[176:191], a[192:195], v[128:135], v245\n"
        "ds_read_b128 v[172:175], v218 offset:0\n"
        "ds_read_b32 v169, v231 offset:0\n"
        "v_exp_f32 v139, v139\n"
        "v_exp_f32 v140, v140\n"
        "v_exp_f32 v141, v141\n"
        "v_exp_f32 v142, v142\n"
        "v_exp_f32 v143, v143\n"
        "v_exp_f32 v144, v144\n"
        "s_add_u32 s54, s72, 1052672\n"
        "v_exp_f32 v145, v145\n"
        "v_smfmac_f32_32x32x32_bf16 v[176:191], a[196:199], v[136:143], v245\n"
        "buffer_load_dwordx4 v[112:115], v0, %[srd], s54 offen offset:0 sc1\n"
        "v_exp_f32 v146, v146\n"
        "v_exp_f32 v147, v147\n"
        "s_waitcnt lgkmcnt(0)\n"
        "v_pk_add_f32 v[160:161], v[160:161], v[172:173]\n"
        "v_pk_add_f32 v[162:163], v[162:163], v[174:175]\n"
        "v_add_f32 v168, v168, v169\n"
        "v_exp_f32 v148, v148\n"
        "v_exp_f32 v149, v149\n"
        "v_mfma_f32_32x32x16_bf16 v[128:143], a[104:107], v[196:199], 0\n"
        "v_exp_f32 v150, v150\n"
        "v_exp_f32 v151, v151\n"
        "v_exp_f32 v152, v152\n"
        "v_exp_f32 v153, v153\n"
        "v_exp_f32 v154, v154\n"
        "v_smfmac_f32_32x32x32_bf16 v[176:191], a[200:203], v[144:151], v245\n"
        "ds_read_b128 v[172:175], v218 offset:1024\n"
        "v_exp_f32 v155, v155\n"
        "v_exp_f32 v156, v156\n"
        "v_exp_f32 v157, v157\n"
        "v_exp_f32 v158, v158\n"
        "v_exp_f32 v159, v159\n"
        "v_exp_f32 v128, v128\n"
        "v_exp_f32 v129, v129\n"
        "v_smfmac_f32_32x32x32_bf16 v[176:191], a[204:207], v[152:159], v245\n"
        "buffer_load_dwordx4 v[116:119], v0, %[srd], s54 offen offset:1024 sc1\n"
        "v_exp_f32 v130, v130\n"
        "v_exp_f32 v131, v131\n"
        "s_waitcnt lgkmcnt(0)\n"
        "v_pk_add_f32 v[164:165], v[164:165], v[172:173]\n"
        "v_pk_add_f32 v[166:167], v[166:167], v[174:175]\n"
        "v_exp_f32 v132, v132\n"
        "v_exp_f32 v133, v133\n"
        "v_mfma_f32_32x32x16_bf16 v[144:159], a[108:111], v[196:199], 0\n"
        "v_exp_f32 v134, v134\n"
        "v_exp_f32 v135, v135\n"
        "v_exp_f32 v136, v136\n"
        "v_exp_f32 v137, v137\n"
        "v_exp_f32 v138, v138\n"
        "v_smfmac_f32_32x32x32_bf16 v[176:191], a[208:211], v[128:135], v245\n"
        "v_exp_f32 v139, v139\n"
        "v_exp_f32 v140, v140\n"
        "v_exp_f32 v141, v141\n"
        "v_exp_f32 v142, v142\n"
        "v_exp_f32 v143, v143\n"
        "s_nop 0\n"
        "v_exp_f32 v144, v144\n"
        "v_exp_f32 v145, v145\n"
        "v_smfmac_f32_32x32x32_bf16 v[176:191], a[212:215], v[136:143], v245\n"
        "buffer_load_dwordx4 v[120:123], v0, %[srd], s54 offen offset:2048 sc1\n"
        "v_exp_f32 v146, v146\n"
        "v_exp_f32 v147, v147\n"
        "v_exp_f32 v148, v148\n"
        "v_exp_f32 v149, v149\n"
        "v_mfma_f32_32x32x16_bf16 v[128:143], a[112:115], v[196:199], 0\n"
        "v_exp_f32 v150, v150\n"
        "v_exp_f32 v151, v151\n"
        "v_exp_f32 v152, v152\n"
        "v_exp_f32 v153, v153\n"
        "v_exp_f32 v154, v154\n"
        "v_smfmac_f32_32x32x32_bf16 v[176:191], a[216:219], v[144:151], v245\n"
        "buffer_load_dwordx4 v[124:127], v0, %[srd], s54 offen offset:3072 sc1\n"
        "v_exp_f32 v155, v155\n"
        "v_exp_f32 v156, v156\n"
        "v_exp_f32 v157, v157\n"
        "v_exp_f32 v158, v158\n"
        "v_exp_f32 v159, v159\n"
        "v_exp_f32 v128, v128\n"
        "s_add_u32 s54, s72, 524288\n"
        "v_exp_f32 v129, v129\n"
        "v_smfmac_f32_32x32x32_bf16 v[176:191], a[220:223], v[152:159], v245\n"
        "buffer_load_dwordx4 v[16:19], v0, %[srd], s54 offen offset:0 sc1\n"
        "v_exp_f32 v130, v130\n"
        "v_exp_f32 v131, v131\n"
        "v_exp_f32 v132, v132\n"
        "v_exp_f32 v133, v133\n"
        "v_mfma_f32_32x32x16_bf16 v[144:159], a[116:119], v[196:199], 0\n"
        "v_exp_f32 v134, v134\n"
        "v_exp_f32 v135, v135\n"
        "v_exp_f32 v136, v136\n"
        "v_exp_f32 v137, v137\n"
        "v_exp_f32 v138, v138\n"
        "v_smfmac_f32_32x32x32_bf16 v[176:191], a[224:227], v[128:135], v245\n"
        "v_exp_f32 v139, v139\n"
        "v_exp_f32 v140, v140\n"
        "v_exp_f32 v141, v141\n"
        "v_exp_f32 v142, v142\n"
        "v_exp_f32 v143, v143\n"
        "s_nop 0\n"
        "v_exp_f32 v144, v144\n"
        "v_exp_f32 v145, v145\n"
        "v_smfmac_f32_32x32x32_bf16 v[176:191], a[228:231], v[136:143], v245\n"
        "buffer_load_dwordx4 v[20:23], v0, %[srd], s54 offen offset:1024 sc1\n"
        "v_exp_f32 v146, v146\n"
        "v_exp_f32 v147, v147\n"
        "v_exp_f32 v148, v148\n"
        "v_exp_f32 v149, v149\n"
        "v_mfma_f32_32x32x16_bf16 v[128:143], a[120:123], v[196:199], 0\n"
        "v_exp_f32 v150, v150\n"
        "v_exp_f32 v151, v151\n"
        "v_exp_f32 v152, v152\n"
        "v_exp_f32 v153, v153\n"
        "v_exp_f32 v154, v154\n"
        "v_smfmac_f32_32x32x32_bf16 v[176:191], a[232:235], v[144:151], v245\n"
        "buffer_load_dwordx4 v[24:27], v0, %[srd], s54 offen offset:2048 sc1\n"
        "v_exp_f32 v155, v155\n"
        "v_exp_f32 v156, v156\n"
        "v_exp_f32 v157, v157\n"
        "v_exp_f32 v158, v158\n"
        "v_exp_f32 v159, v159\n"
        "v_exp_f32 v128, v128\n"
        "v_exp_f32 v129, v129\n"
        "v_smfmac_f32_32x32x32_bf16 v[176:191], a[236:239], v[152:159], v245\n"
        "buffer_load_dwordx4 v[28:31], v0, %[srd], s54 offen offset:3072 sc1\n"
        "v_exp_f32 v130, v130\n"
        "v_exp_f32 v131, v131\n"
        "v_exp_f32 v132, v132\n"
        "v_exp_f32 v133, v133\n"
        "v_mfma_f32_32x32x16_bf16 v[144:159], a[124:127], v[196:199], 0\n"
        "v_exp_f32 v134, v134\n"
        "v_exp_f32 v135, v135\n"
        "v_exp_f32 v136, v136\n"
        "v_exp_f32 v137, v137\n"
        "v_exp_f32 v138, v138\n"
        "v_smfmac_f32_32x32x32_bf16 v[176:191], a[240:243], v[128:135], v245\n"
        "v_exp_f32 v139, v139\n"
        "v_exp_f32 v140, v140\n"
        "v_exp_f32 v141, v141\n"
        "v_exp_f32 v142, v142\n"
        "v_exp_f32 v143, v143\n"
        "s_nop 0\n"
        "v_exp_f32 v144, v144\n"
        "s_add_u32 s54, s72, 528384\n"
        "v_exp_f32 v145, v145\n"
        "v_smfmac_f32_32x32x32_bf16 v[176:191], a[244:247], v[136:143], v245\n"
        "buffer_load_dwordx4 v[32:35], v0, %[srd], s54 offen offset:0 sc1\n"
        "v_exp_f32 v146, v146\n"
        "v_exp_f32 v147, v147\n"
        "v_exp_f32 v148, v148\n"
        "v_exp_f32 v149, v149\n"
        "s_waitcnt lgkmcnt(0)\n"
        "v_mfma_f32_32x32x16_bf16 v[128:143], a[64:67], v[192:195], 0\n"
        "v_exp_f32 v150, v150\n"
        "v_exp_f32 v151, v151\n"
        "v_exp_f32 v152, v152\n"
        "v_exp_f32 v153, v153\n"
        "v_exp_f32 v154, v154\n"
        "v_smfmac_f32_32x32x32_bf16 v[176:191], a[248:251], v[144:151], v245\n"
        "buffer_load_dwordx4 v[36:39], v0, %[srd], s54 offen offset:1024 sc1\n"
        "v_exp_f32 v155, v155\n"
        "v_exp_f32 v156, v156\n"
        "v_exp_f32 v157, v157\n"
        "v_exp_f32 v158, v158\n"
        "v_exp_f32 v159, v159\n"
        "v_exp_f32 v128, v128\n"
        "v_exp_f32 v129, v129\n"
        "v_smfmac_f32_32x32x32_bf16 v[176:191], a[252:255], v[152:159], v245\n"
        "buffer_load_dwordx4 v[40:43], v0, %[srd], s54 offen offset:2048 sc1\n"
        "v_exp_f32 v130, v130\n"
        "v_exp_f32 v131, v131\n"
        "v_exp_f32 v132, v132\n"
        "v_exp_f32 v133, v133\n"
        "v_mfma_f32_32x32x16_bf16 v[144:159], a[68:71], v[192:195], 0\n"
        "v_exp_f32 v134, v134\n"
        "v_exp_f32 v135, v135\n"
        "v_exp_f32 v136, v136\n"
        "v_exp_f32 v137, v137\n"
        "v_exp_f32 v138, v138\n"
        "v_smfmac_f32_32x32x32_bf16 v[160:175], a[128:131], v[128:135], v245\n"
        "v_exp_f32 v139, v139\n"
        "v_exp_f32 v140, v140\n"
        "v_exp_f32 v141, v141\n"
        "v_exp_f32 v142, v142\n"
        "v_exp_f32 v143, v143\n"
        "s_nop 0\n"
        "v_exp_f32 v144, v144\n"
        "v_exp_f32 v145, v145\n"
        "v_smfmac_f32_32x32x32_bf16 v[160:175], a[132:135], v[136:143], v245\n"
        "buffer_load_dwordx4 v[44:47], v0, %[srd], s54 offen offset:3072 sc1\n"
        "v_exp_f32 v146, v146\n"
        "v_exp_f32 v147, v147\n"
        "v_exp_f32 v148, v148\n"
        "v_exp_f32 v149, v149\n"
        "v_mfma_f32_32x32x16_bf16 v[128:143], a[72:75], v[192:195], 0\n"
        "v_exp_f32 v150, v150\n"
        "v_exp_f32 v151, v151\n"
        "v_exp_f32 v152, v152\n"
        "v_exp_f32 v153, v153\n"
        "v_exp_f32 v154, v154\n"
        "v_smfmac_f32_32x32x32_bf16 v[160:175], a[136:139], v[144:151], v245\n"
        "ds_write_b128 v246, v[176:179] offset:11520\n"
        "v_exp_f32 v155, v155\n"
        "v_exp_f32 v156, v156\n"
        "v_exp_f32 v157, v157\n"
        "v_exp_f32 v158, v158\n"
        "v_exp_f32 v159, v159\n"
        "v_exp_f32 v128, v128\n"
        "v_exp_f32 v129, v129\n"
        "v_smfmac_f32_32x32x32_bf16 v[160:175], a[140:143], v[152:159], v245\n"
        "ds_write_b128 v246, v[180:183] offset:12544\n"
        "v_exp_f32 v130, v130\n"
        "v_exp_f32 v131, v131\n"
        "v_exp_f32 v132, v132\n"
        "v_exp_f32 v133, v133\n"
        "v_mfma_f32_32x32x16_bf16 v[144:159], a[76:79], v[192:195], 0\n"
        "v_exp_f32 v134, v134\n"
        "v_exp_f32 v135, v135\n"
        "v_exp_f32 v136, v136\n"
        "v_exp_f32 v137, v137\n"
        "v_exp_f32 v138, v138\n"
        "v_smfmac_f32_32x32x32_bf16 v[160:175], a[144:147], v[128:135], v245\n"
        "ds_write_b32 v247, v184 offset:11520\n"
        "v_exp_f32 v139, v139\n"
        "v_exp_f32 v140, v140\n"
        "v_exp_f32 v141, v141\n"
        "v_exp_f32 v142, v142\n"
        "v_exp_f32 v143, v143\n"
        "v_exp_f32 v144, v144\n"
        "v_exp_f32 v145, v145\n"
        "v_smfmac_f32_32x32x32_bf16 v[160:175], a[148:151], v[136:143], v245\n"
        "ds_read_b128 v[196:199], v251 offset:1024\n"
        "v_exp_f32 v146, v146\n"
        "v_exp_f32 v147, v147\n"
        "v_exp_f32 v148, v148\n"
        "v_exp_f32 v149, v149\n"
        "v_mfma_f32_32x32x16_bf16 v[128:143], a[80:83], v[192:195], 0\n"
        "v_exp_f32 v150, v150\n"
        "v_exp_f32 v151, v151\n"
        "v_exp_f32 v152, v152\n"
        "v_exp_f32 v153, v153\n"
        "v_exp_f32 v154, v154\n"
        "v_smfmac_f32_32x32x32_bf16 v[160:175], a[152:155], v[144:151], v245\n"
        "v_exp_f32 v155, v155\n"
        "v_exp_f32 v156, v156\n"
        "v_exp_f32 v157, v157\n"
        "s_waitcnt lgkmcnt(0)\n"
        "s_barrier\n"
        "v_exp_f32 v158, v158\n"
        "v_exp_f32 v159, v159\n"
        "v_exp_f32 v128, v128\n"
        "s_add_u32 s54, s72, 532480\n"
        "v_exp_f32 v129, v129\n"
        "v_smfmac_f32_32x32x32_bf16 v[160:175], a[156:159], v[152:159], v245\n"
        "buffer_load_dwordx4 v[48:51], v0, %[srd], s54 offen offset:0 sc1\n"
        "v_exp_f32 v130, v130\n"
        "v_exp_f32 v131, v131\n"
        "v_exp_f32 v132, v132\n"
        "v_exp_f32 v133, v133\n"
        "v_mfma_f32_32x32x16_bf16 v[144:159], a[84:87], v[192:195], 0\n"
        "v_exp_f32 v134, v134\n"
        "v_exp_f32 v135, v135\n"
        "v_exp_f32 v136, v136\n"
        "v_exp_f32 v137, v137\n"
        "v_exp_f32 v138, v138\n"
        "v_smfmac_f32_32x32x32_bf16 v[160:175], a[160:163], v[128:135], v245\n"
        "ds_read_b128 v[176:179], v248 offset:2304\n"
        "ds_read_b128 v[180:183], v248 offset:3328\n"
        "ds_read_b32 v184, v249 offset:2304\n"
        "v_exp_f32 v139, v139\n"
        "v_exp_f32 v140, v140\n"
        "v_exp_f32 v141, v141\n"
        "v_exp_f32 v142, v142\n"
        "v_exp_f32 v143, v143\n"
        "v_exp_f32 v144, v144\n"
        "v_exp_f32 v145, v145\n"
        "v_smfmac_f32_32x32x32_bf16 v[160:175], a[164:167], v[136:143], v245\n"
        "buffer_load_dwordx4 v[52:55], v0, %[srd], s54 offen offset:1024 sc1\n"
        "v_exp_f32 v146, v146\n"
        "v_exp_f32 v147, v147\n"
        "v_exp_f32 v148, v148\n"
        "v_exp_f32 v149, v149\n"
        "v_mfma_f32_32x32x16_bf16 v[128:143], a[88:91], v[192:195], 0\n"
        "v_exp_f32 v150, v150\n"
        "v_exp_f32 v151, v151\n"
        "v_exp_f32 v152, v152\n"
        "v_exp_f32 v153, v153\n"
        "v_exp_f32 v154, v154\n"
        "v_smfmac_f32_32x32x32_bf16 v[160:175], a[168:171], v[144:151], v245\n"
        "ds_read_b128 v[188:191], v217 offset:2304\n"
        "ds_read_b32 v185, v219 offset:2304\n"
        "v_exp_f32 v155, v155\n"
        "v_exp_f32 v156, v156\n"
        "v_exp_f32 v157, v157\n"
        "v_exp_f32 v158, v158\n"
        "v_exp_f32 v159, v159\n"
        "v_exp_f32 v128, v128\n"
        "v_exp_f32 v129, v129\n"
        "v_smfmac_f32_32x32x32_bf16 v[160:175], a[172:175], v[152:159], v245\n"
        "buffer_load_dwordx4 v[56:59], v0, %[srd], s54 offen offset:2048 sc1\n"
        "v_exp_f32 v130, v130\n"
        "v_exp_f32 v131, v131\n"
        "s_waitcnt lgkmcnt(0)\n"
        "v_pk_add_f32 v[176:177], v[176:177], v[188:189]\n"
        "v_pk_add_f32 v[178:179], v[178:179], v[190:191]\n"
        "v_add_f32 v184, v184, v185\n"
        "v_exp_f32 v132, v132\n"
        "v_exp_f32 v133, v133\n"
        "v_mfma_f32_32x32x16_bf16 v[144:159], a[92:95], v[192:195], 0\n"
        "v_exp_f32 v134, v134\n"
        "v_exp_f32 v135, v135\n"
        "v_exp_f32 v136, v136\n"
        "v_exp_f32 v137, v137\n"
        "v_exp_f32 v138, v138\n"
        "v_smfmac_f32_32x32x32_bf16 v[160:175], a[176:179], v[128:135], v245\n"
        "ds_read_b128 v[188:191], v217 offset:3328\n"
        "v_exp_f32 v139, v139\n"
        "v_exp_f32 v140, v140\n"
        "v_exp_f32 v141, v141\n"
        "v_exp_f32 v142, v142\n"
        "v_exp_f32 v143, v143\n"
        "v_exp_f32 v144, v144\n"
        "v_exp_f32 v145, v145\n"
        "v_smfmac_f32_32x32x32_bf16 v[160:175], a[180:183], v[136:143], v245\n"
        "buffer_load_dwordx4 v[60:63], v0, %[srd], s54 offen offset:3072 sc1\n"
        "v_exp_f32 v146, v146\n"
        "v_exp_f32 v147, v147\n"
        "s_waitcnt lgkmcnt(0)\n"
        "v_pk_add_f32 v[180:181], v[180:181], v[188:189]\n"
        "v_pk_add_f32 v[182:183], v[182:183], v[190:191]\n"
        "v_exp_f32 v148, v148\n"
        "v_exp_f32 v149, v149\n"
        "v_mfma_f32_32x32x16_bf16 v[128:143], a[96:99], v[192:195], 0\n"
        "v_exp_f32 v150, v150\n"
        "v_exp_f32 v151, v151\n"
        "v_exp_f32 v152, v152\n"
        "v_exp_f32 v153, v153\n"
        "v_exp_f32 v154, v154\n"
        "v_smfmac_f32_32x32x32_bf16 v[160:175], a[184:187], v[144:151], v245\n"
        "ds_read_b128 v[188:191], v218 offset:2304\n"
        "ds_read_b32 v185, v231 offset:2304\n"
        "v_exp_f32 v155, v155\n"
        "v_exp_f32 v156, v156\n"
        "v_exp_f32 v157, v157\n"
        "v_exp_f32 v158, v158\n"
        "v_exp_f32 v159, v159\n"
        "v_exp_f32 v128, v128\n"
        "s_add_u32 s54, s72, 536576\n"
        "v_exp_f32 v129, v129\n"
        "v_smfmac_f32_32x32x32_bf16 v[160:175], a[188:191], v[152:159], v245\n"
        "buffer_load_dwordx4 v[64:67], v0, %[srd], s54 offen offset:0 sc1\n"
        "v_exp_f32 v130, v130\n"
        "v_exp_f32 v131, v131\n"
        "s_waitcnt lgkmcnt(0)\n"
        "v_pk_add_f32 v[176:177], v[176:177], v[188:189]\n"
        "v_pk_add_f32 v[178:179], v[178:179], v[190:191]\n"
        "v_add_f32 v184, v184, v185\n"
        "v_exp_f32 v132, v132\n"
        "v_exp_f32 v133, v133\n"
        "v_mfma_f32_32x32x16_bf16 v[144:159], a[100:103], v[192:195], 0\n"
        "v_exp_f32 v134, v134\n"
        "v_exp_f32 v135, v135\n"
        "v_exp_f32 v136, v136\n"
        "v_exp_f32 v137, v137\n"
        "v_exp_f32 v138, v138\n"
        "v_smfmac_f32_32x32x32_bf16 v[160:175], a[192:195], v[128:135], v245\n"
        "ds_read_b128 v[188:191], v218 offset:3328\n"
        "v_exp_f32 v139, v139\n"
        "v_exp_f32 v140, v140\n"
        "v_exp_f32 v141, v141\n"
        "v_exp_f32 v142, v142\n"
        "v_exp_f32 v143, v143\n"
        "v_exp_f32 v144, v144\n"
        "v_exp_f32 v145, v145\n"
        "v_smfmac_f32_32x32x32_bf16 v[160:175], a[196:199], v[136:143], v245\n"
        "buffer_load_dwordx4 v[68:71], v0, %[srd], s54 offen offset:1024 sc1\n"
        "v_exp_f32 v146, v146\n"
        "v_exp_f32 v147, v147\n"
        "s_waitcnt lgkmcnt(0)\n"
        "v_pk_add_f32 v[180:181], v[180:181], v[188:189]\n"
        "v_pk_add_f32 v[182:183], v[182:183], v[190:191]\n"
        "v_exp_f32 v148, v148\n"
        "v_exp_f32 v149, v149\n"
        "v_mfma_f32_32x32x16_bf16 v[128:143], a[104:107], v[192:195], 0\n"
        "v_exp_f32 v150, v150\n"
        "v_exp_f32 v151, v151\n"
        "v_exp_f32 v152, v152\n"
        "v_exp_f32 v153, v153\n"
        "v_exp_f32 v154, v154\n"
        "v_smfmac_f32_32x32x32_bf16 v[160:175], a[200:203], v[144:151], v245\n"
        "v_exp_f32 v155, v155\n"
        "v_exp_f32 v156, v156\n"
        "v_exp_f32 v157, v157\n"
        "v_exp_f32 v158, v158\n"
        "v_exp_f32 v159, v159\n"
        "s_nop 0\n"
        "v_exp_f32 v128, v128\n"
        "v_exp_f32 v129, v129\n"
        "v_smfmac_f32_32x32x32_bf16 v[160:175], a[204:207], v[152:159], v245\n"
        "buffer_load_dwordx4 v[72:75], v0, %[srd], s54 offen offset:2048 sc1\n"
        "v_exp_f32 v130, v130\n"
        "v_exp_f32 v131, v131\n"
        "v_exp_f32 v132, v132\n"
        "v_exp_f32 v133, v133\n"
        "v_mfma_f32_32x32x16_bf16 v[144:159], a[108:111], v[192:195], 0\n"
        "v_exp_f32 v134, v134\n"
        "v_exp_f32 v135, v135\n"
        "v_exp_f32 v136, v136\n"
        "v_exp_f32 v137, v137\n"
        "v_exp_f32 v138, v138\n"
        "v_smfmac_f32_32x32x32_bf16 v[160:175], a[208:211], v[128:135], v245\n"
        "v_exp_f32 v139, v139\n"
        "v_exp_f32 v140, v140\n"
        "v_exp_f32 v141, v141\n"
        "v_exp_f32 v142, v142\n"
        "v_exp_f32 v143, v143\n"
        "s_nop 0\n"
        "v_exp_f32 v144, v144\n"
        "v_exp_f32 v145, v145\n"
        "v_smfmac_f32_32x32x32_bf16 v[160:175], a[212:215], v[136:143], v245\n"
        "buffer_load_dwordx4 v[76:79], v0, %[srd], s54 offen offset:3072 sc1\n"
        "v_exp_f32 v146, v146\n"
        "v_exp_f32 v147, v147\n"
        "v_exp_f32 v148, v148\n"
        "v_exp_f32 v149, v149\n"
        "v_mfma_f32_32x32x16_bf16 v[128:143], a[112:115], v[192:195], 0\n"
        "v_exp_f32 v150, v150\n"
        "v_exp_f32 v151, v151\n"
        "v_exp_f32 v152, v152\n"
        "v_exp_f32 v153, v153\n"
        "v_exp_f32 v154, v154\n"
        "v_smfmac_f32_32x32x32_bf16 v[160:175], a[216:219], v[144:151], v245\n"
        "v_exp_f32 v155, v155\n"
        "v_exp_f32 v156, v156\n"
        "v_exp_f32 v157, v157\n"
        "v_exp_f32 v158, v158\n"
        "v_exp_f32 v159, v159\n"
        "s_nop 0\n"
        "v_exp_f32 v128, v128\n"
        "v_exp_f32 v129, v129\n"
        "v_smfmac_f32_32x32x32_bf16 v[160:175], a[220:223], v[152:159], v245\n"
        "v_exp_f32 v130, v130\n"
        "v_exp_f32 v131, v131\n"
        "v_exp_f32 v132, v132\n"
        "v_exp_f32 v133, v133\n"
        "v_mfma_f32_32x32x16_bf16 v[144:159], a[116:119], v[192:195], 0\n"
        "v_exp_f32 v134, v134\n"
        "v_exp_f32 v135, v135\n"
        "v_exp_f32 v136, v136\n"
        "v_exp_f32 v137, v137\n"
        "v_exp_f32 v138, v138\n"
        "v_smfmac_f32_32x32x32_bf16 v[160:175], a[224:227], v[128:135], v245\n"
        "v_exp_f32 v139, v139\n"
        "v_exp_f32 v140, v140\n"
        "v_exp_f32 v141, v141\n"
        "v_exp_f32 v142, v142\n"
        "v_exp_f32 v143, v143\n"
        "s_nop 0\n"
        "v_exp_f32 v144, v144\n"
        "v_exp_f32 v145, v145\n"
        "v_smfmac_f32_32x32x32_bf16 v[160:175], a[228:231], v[136:143], v245\n"
        "v_exp_f32 v146, v146\n"
        "v_exp_f32 v147, v147\n"
        "v_exp_f32 v148, v148\n"
        "v_exp_f32 v149, v149\n"
        "v_mfma_f32_32x32x16_bf16 v[128:143], a[120:123], v[192:195], 0\n"
        "v_exp_f32 v150, v150\n"
        "v_exp_f32 v151, v151\n"
        "v_exp_f32 v152, v152\n"
        "v_exp_f32 v153, v153\n"
        "v_exp_f32 v154, v154\n"
        "v_smfmac_f32_32x32x32_bf16 v[160:175], a[232:235], v[144:151], v245\n"
        "v_exp_f32 v155, v155\n"
        "v_exp_f32 v156, v156\n"
        "v_exp_f32 v157, v157\n"
        "v_exp_f32 v158, v158\n"
        "v_exp_f32 v159, v159\n"
        "s_nop 0\n"
        "v_exp_f32 v128, v128\n"
        "v_exp_f32 v129, v129\n"
        "v_smfmac_f32_32x32x32_bf16 v[160:175], a[236:239], v[152:159], v245\n"
        "v_exp_f32 v130, v130\n"
        "v_exp_f32 v131, v131\n"
        "v_exp_f32 v132, v132\n"
        "v_exp_f32 v133, v133\n"
        "v_mfma_f32_32x32x16_bf16 v[144:159], a[124:127], v[192:195], 0\n"
        "v_exp_f32 v134, v134\n"
        "v_exp_f32 v135, v135\n"
        "v_exp_f32 v136, v136\n"
        "v_exp_f32 v137, v137\n"
        "v_exp_f32 v138, v138\n"
        "v_smfmac_f32_32x32x32_bf16 v[160:175], a[240:243], v[128:135], v245\n"
        "v_exp_f32 v139, v139\n"
        "v_exp_f32 v140, v140\n"
        "v_exp_f32 v141, v141\n"
        "v_exp_f32 v142, v142\n"
        "v_exp_f32 v143, v143\n"
        "s_nop 0\n"
        "v_exp_f32 v144, v144\n"
        "v_exp_f32 v145, v145\n"
        "v_smfmac_f32_32x32x32_bf16 v[160:175], a[244:247], v[136:143], v245\n"
        "v_exp_f32 v146, v146\n"
        "v_exp_f32 v147, v147\n"
        "v_exp_f32 v148, v148\n"
        "v_exp_f32 v149, v149\n"
        "s_waitcnt lgkmcnt(0)\n"
        "v_mfma_f32_32x32x16_bf16 v[128:143], a[64:67], v[196:199], 0\n"
        "v_exp_f32 v150, v150\n"
        "v_exp_f32 v151, v151\n"
        "v_exp_f32 v152, v152\n"
        "v_exp_f32 v153, v153\n"
        "v_exp_f32 v154, v154\n"
        "v_smfmac_f32_32x32x32_bf16 v[160:175], a[248:251], v[144:151], v245\n"
        "v_exp_f32 v155, v155\n"
        "v_exp_f32 v156, v156\n"
        "v_exp_f32 v157, v157\n"
        "v_exp_f32 v158, v158\n"
        "v_exp_f32 v159, v159\n"
        "s_nop 0\n"
        "v_exp_f32 v128, v128\n"
        "s_waitcnt vmcnt(0)\n"
        "v_exp_f32 v129, v129\n"
        "v_smfmac_f32_32x32x32_bf16 v[160:175], a[252:255], v[152:159], v245\n"
        "v_exp_f32 v130, v130\n"
        "v_exp_f32 v131, v131\n"
        "v_exp_f32 v132, v132\n"
        "v_exp_f32 v133, v133\n"
        "v_mfma_f32_32x32x16_bf16 v[144:159], a[68:71], v[196:199], 0\n"
        "v_exp_f32 v134, v134\n"
        "v_exp_f32 v135, v135\n"
        "v_exp_f32 v136, v136\n"
        "v_exp_f32 v137, v137\n"
        "v_exp_f32 v138, v138\n"
        "v_smfmac_f32_32x32x32_bf16 v[176:191], a[128:131], v[128:135], v245\n"
        "buffer_load_dwordx4 a[64:67], v241, %[srd], s75 offen sc1\n"
        "v_exp_f32 v139, v139\n"
        "v_exp_f32 v140, v140\n"
        "v_exp_f32 v141, v141\n"
        "v_exp_f32 v142, v142\n"
        "v_exp_f32 v143, v143\n"
        "v_exp_f32 v144, v144\n"
        "s_add_u32 s54, s72, 1056768\n"
        "v_exp_f32 v145, v145\n"
        "v_smfmac_f32_32x32x32_bf16 v[176:191], a[132:135], v[136:143], v245\n"
        "buffer_load_dwordx4 a[128:131], v0, %[srd], s54 offen offset:0 sc1\n"
        "v_exp_f32 v146, v146\n"
        "v_exp_f32 v147, v147\n"
        "v_exp_f32 v148, v148\n"
        "v_exp_f32 v149, v149\n"
        "v_mfma_f32_32x32x16_bf16 v[128:143], a[72:75], v[196:199], 0\n"
        "v_exp_f32 v150, v150\n"
        "v_exp_f32 v151, v151\n"
        "v_exp_f32 v152, v152\n"
        "v_exp_f32 v153, v153\n"
        "v_exp_f32 v154, v154\n"
        "v_smfmac_f32_32x32x32_bf16 v[176:191], a[136:139], v[144:151], v245\n"
        "buffer_load_dwordx4 a[132:135], v0, %[srd], s54 offen offset:1024 sc1\n"
        "v_exp_f32 v155, v155\n"
        "v_exp_f32 v156, v156\n"
        "v_exp_f32 v157, v157\n"
        "v_exp_f32 v158, v158\n"
        "v_exp_f32 v159, v159\n"
        "v_exp_f32 v128, v128\n"
        "v_exp_f32 v129, v129\n"
        "v_smfmac_f32_32x32x32_bf16 v[176:191], a[140:143], v[152:159], v245\n"
        "buffer_load_dwordx4 a[136:139], v0, %[srd], s54 offen offset:2048 sc1\n"
        "v_exp_f32 v130, v130\n"
        "v_exp_f32 v131, v131\n"
        "v_exp_f32 v132, v132\n"
        "v_exp_f32 v133, v133\n"
        "v_mfma_f32_32x32x16_bf16 v[144:159], a[76:79], v[196:199], 0\n"
        "v_exp_f32 v134, v134\n"
        "v_exp_f32 v135, v135\n"
        "v_exp_f32 v136, v136\n"
        "v_exp_f32 v137, v137\n"
        "v_exp_f32 v138, v138\n"
        "v_smfmac_f32_32x32x32_bf16 v[176:191], a[144:147], v[128:135], v245\n"
        "buffer_load_dwordx4 a[140:143], v0, %[srd], s54 offen offset:3072 sc1\n"
        "v_exp_f32 v139, v139\n"
        "v_exp_f32 v140, v140\n"
        "v_exp_f32 v141, v141\n"
        "v_exp_f32 v142, v142\n"
        "v_exp_f32 v143, v143\n"
        "v_exp_f32 v144, v144\n"
        "s_add_u32 s54, s72, 1060864\n"
        "v_exp_f32 v145, v145\n"
        "v_smfmac_f32_32x32x32_bf16 v[176:191], a[148:151], v[136:143], v245\n"
        "buffer_load_dwordx4 a[144:147], v0, %[srd], s54 offen offset:0 sc1\n"
        "v_exp_f32 v146, v146\n"
        "v_exp_f32 v147, v147\n"
        "v_exp_f32 v148, v148\n"
        "v_exp_f32 v149, v149\n"
        "v_mfma_f32_32x32x16_bf16 v[128:143], a[80:83], v[196:199], 0\n"
        "v_exp_f32 v150, v150\n"
        "v_exp_f32 v151, v151\n"
        "v_exp_f32 v152, v152\n"
        "v_exp_f32 v153, v153\n"
        "v_exp_f32 v154, v154\n"
        "v_smfmac_f32_32x32x32_bf16 v[176:191], a[152:155], v[144:151], v245\n"
        "v_exp_f32 v155, v155\n"
        "v_exp_f32 v156, v156\n"
        "v_exp_f32 v157, v157\n"
        "v_exp_f32 v158, v158\n"
        "v_exp_f32 v159, v159\n"
        "s_nop 0\n"
        "v_exp_f32 v128, v128\n"
        "v_exp_f32 v129, v129\n"
        "v_smfmac_f32_32x32x32_bf16 v[176:191], a[156:159], v[152:159], v245\n"
        "buffer_load_dwordx4 a[148:151], v0, %[srd], s54 offen offset:1024 sc1\n"
        "v_exp_f32 v130, v130\n"
        "v_exp_f32 v131, v131\n"
        "v_exp_f32 v132, v132\n"
        "v_exp_f32 v133, v133\n"
        "v_mfma_f32_32x32x16_bf16 v[144:159], a[84:87], v[196:199], 0\n"
        "v_exp_f32 v134, v134\n"
        "v_exp_f32 v135, v135\n"
        "v_exp_f32 v136, v136\n"
        "v_exp_f32 v137, v137\n"
        "v_exp_f32 v138, v138\n"
        "v_smfmac_f32_32x32x32_bf16 v[176:191], a[160:163], v[128:135], v245\n"
        "buffer_load_dwordx4 a[152:155], v0, %[srd], s54 offen offset:2048 sc1\n"
        "v_exp_f32 v139, v139\n"
        "v_exp_f32 v140, v140\n"
        "v_exp_f32 v141, v141\n"
        "v_exp_f32 v142, v142\n"
        "v_exp_f32 v143, v143\n"
        "v_exp_f32 v144, v144\n"
        "v_exp_f32 v145, v145\n"
        "v_smfmac_f32_32x32x32_bf16 v[176:191], a[164:167], v[136:143], v245\n"
        "buffer_load_dwordx4 a[156:159], v0, %[srd], s54 offen offset:3072 sc1\n"
        "v_exp_f32 v146, v146\n"
        "v_exp_f32 v147, v147\n"
        "v_exp_f32 v148, v148\n"
        "v_exp_f32 v149, v149\n"
        "v_mfma_f32_32x32x16_bf16 v[128:143], a[88:91], v[196:199], 0\n"
        "v_exp_f32 v150, v150\n"
        "v_exp_f32 v151, v151\n"
        "v_exp_f32 v152, v152\n"
        "v_exp_f32 v153, v153\n"
        "v_exp_f32 v154, v154\n"
        "v_smfmac_f32_32x32x32_bf16 v[176:191], a[168:171], v[144:151], v245\n"
        "v_exp_f32 v155, v155\n"
        "v_exp_f32 v156, v156\n"
        "v_exp_f32 v157, v157\n"
        "v_exp_f32 v158, v158\n"
        "v_exp_f32 v159, v159\n"
        "s_nop 0\n"
        "v_exp_f32 v128, v128\n"
        "s_add_u32 s54, s72, 1572864\n"
        "v_exp_f32 v129, v129\n"
        "v_smfmac_f32_32x32x32_bf16 v[176:191], a[172:175], v[152:159], v245\n"
        "buffer_load_dwordx4 a[160:163], v240, %[srd], s54 offen offset:0 sc1\n"
        "v_exp_f32 v130, v130\n"
        "v_exp_f32 v131, v131\n"
        "v_exp_f32 v132, v132\n"
        "v_exp_f32 v133, v133\n"
        "v_mfma_f32_32x32x16_bf16 v[144:159], a[92:95], v[196:199], 0\n"
        "v_exp_f32 v134, v134\n"
        "v_exp_f32 v135, v135\n"
        "v_exp_f32 v136, v136\n"
        "v_exp_f32 v137, v137\n"
        "v_exp_f32 v138, v138\n"
        "v_smfmac_f32_32x32x32_bf16 v[176:191], a[176:179], v[128:135], v245\n"
        "buffer_load_dwordx4 a[164:167], v240, %[srd], s54 offen offset:1024 sc1\n"
        "v_exp_f32 v139, v139\n"
        "v_exp_f32 v140, v140\n"
        "v_exp_f32 v141, v141\n"
        "v_exp_f32 v142, v142\n"
        "v_exp_f32 v143, v143\n"
        "v_exp_f32 v144, v144\n"
        "v_exp_f32 v145, v145\n"
        "v_smfmac_f32_32x32x32_bf16 v[176:191], a[180:183], v[136:143], v245\n"
        "buffer_load_dwordx4 a[168:171], v240, %[srd], s54 offen offset:2048 sc1\n"
        "v_exp_f32 v146, v146\n"
        "v_exp_f32 v147, v147\n"
        "v_exp_f32 v148, v148\n"
        "v_exp_f32 v149, v149\n"
        "v_mfma_f32_32x32x16_bf16 v[128:143], a[96:99], v[196:199], 0\n"
        "v_exp_f32 v150, v150\n"
        "v_exp_f32 v151, v151\n"
        "v_exp_f32 v152, v152\n"
        "v_exp_f32 v153, v153\n"
        "v_exp_f32 v154, v154\n"
        "v_smfmac_f32_32x32x32_bf16 v[176:191], a[184:187], v[144:151], v245\n"
        "buffer_load_dwordx4 a[172:175], v240, %[srd], s54 offen offset:3072 sc1\n"
        "v_exp_f32 v155, v155\n"
        "v_exp_f32 v156, v156\n"
        "v_exp_f32 v157, v157\n"
        "v_exp_f32 v158, v158\n"
        "v_exp_f32 v159, v159\n"
        "v_exp_f32 v128, v128\n"
        "s_add_u32 s54, s72, 1576960\n"
        "v_exp_f32 v129, v129\n"
        "v_smfmac_f32_32x32x32_bf16 v[176:191], a[188:191], v[152:159], v245\n"
        "buffer_load_dwordx4 a[176:179], v240, %[srd], s54 offen offset:0 sc1\n"
        "v_exp_f32 v130, v130\n"
        "v_exp_f32 v131, v131\n"
        "v_exp_f32 v132, v132\n"
        "v_exp_f32 v133, v133\n"
        "v_mfma_f32_32x32x16_bf16 v[144:159], a[100:103], v[196:199], 0\n"
        "v_exp_f32 v134, v134\n"
        "v_exp_f32 v135, v135\n"
        "v_exp_f32 v136, v136\n"
        "v_exp_f32 v137, v137\n"
        "v_exp_f32 v138, v138\n"
        "v_smfmac_f32_32x32x32_bf16 v[176:191], a[192:195], v[128:135], v245\n"
        "v_exp_f32 v139, v139\n"
        "v_exp_f32 v140, v140\n"
        "v_exp_f32 v141, v141\n"
        "v_exp_f32 v142, v142\n"
        "v_exp_f32 v143, v143\n"
        "s_nop 0\n"
        "v_exp_f32 v144, v144\n"
        "v_exp_f32 v145, v145\n"
        "v_smfmac_f32_32x32x32_bf16 v[176:191], a[196:199], v[136:143], v245\n"
        "buffer_load_dwordx4 a[180:183], v240, %[srd], s54 offen offset:1024 sc1\n"
        "v_exp_f32 v146, v146\n"
        "v_exp_f32 v147, v147\n"
        "v_exp_f32 v148, v148\n"
        "v_exp_f32 v149, v149\n"
        "v_mfma_f32_32x32x16_bf16 v[128:143], a[104:107], v[196:199], 0\n"
        "v_exp_f32 v150, v150\n"
        "v_exp_f32 v151, v151\n"
        "v_exp_f32 v152, v152\n"
        "v_exp_f32 v153, v153\n"
        "v_exp_f32 v154, v154\n"
        "v_smfmac_f32_32x32x32_bf16 v[176:191], a[200:203], v[144:151], v245\n"
        "buffer_load_dwordx4 a[184:187], v240, %[srd], s54 offen offset:2048 sc1\n"
        "v_exp_f32 v155, v155\n"
        "v_exp_f32 v156, v156\n"
        "v_exp_f32 v157, v157\n"
        "v_exp_f32 v158, v158\n"
        "v_exp_f32 v159, v159\n"
        "v_exp_f32 v128, v128\n"
        "v_exp_f32 v129, v129\n"
        "v_smfmac_f32_32x32x32_bf16 v[176:191], a[204:207], v[152:159], v245\n"
        "buffer_load_dwordx4 a[188:191], v240, %[srd], s54 offen offset:3072 sc1\n"
        "v_exp_f32 v130, v130\n"
        "v_exp_f32 v131, v131\n"
        "v_exp_f32 v132, v132\n"
        "v_exp_f32 v133, v133\n"
        "v_mfma_f32_32x32x16_bf16 v[144:159], a[108:111], v[196:199], 0\n"
        "v_exp_f32 v134, v134\n"
        "v_exp_f32 v135, v135\n"
        "v_exp_f32 v136, v136\n"
        "v_exp_f32 v137, v137\n"
        "v_exp_f32 v138, v138\n"
        "v_smfmac_f32_32x32x32_bf16 v[176:191], a[208:211], v[128:135], v245\n"
        "v_exp_f32 v139, v139\n"
        "v_exp_f32 v140, v140\n"
        "v_exp_f32 v141, v141\n"
        "v_exp_f32 v142, v142\n"
        "v_exp_f32 v143, v143\n"
        "s_nop 0\n"
        "v_exp_f32 v144, v144\n"
        "s_add_u32 s54, s72, 1581056\n"
        "v_exp_f32 v145, v145\n"
        "v_smfmac_f32_32x32x32_bf16 v[176:191], a[212:215], v[136:143], v245\n"
        "buffer_load_dwordx4 a[192:195], v240, %[srd], s54 offen offset:0 sc1\n"
        "v_exp_f32 v146, v146\n"
        "v_exp_f32 v147, v147\n"
        "v_exp_f32 v148, v148\n"
        "v_exp_f32 v149, v149\n"
        "v_mfma_f32_32x32x16_bf16 v[128:143], a[112:115], v[196:199], 0\n"
        "v_exp_f32 v150, v150\n"
        "v_exp_f32 v151, v151\n"
        "v_exp_f32 v152, v152\n"
        "v_exp_f32 v153, v153\n"
        "v_exp_f32 v154, v154\n"
        "v_smfmac_f32_32x32x32_bf16 v[176:191], a[216:219], v[144:151], v245\n"
        "buffer_load_dwordx4 a[196:199], v240, %[srd], s54 offen offset:1024 sc1\n"
        "v_exp_f32 v155, v155\n"
        "v_exp_f32 v156, v156\n"
        "v_exp_f32 v157, v157\n"
        "v_exp_f32 v158, v158\n"
        "v_exp_f32 v159, v159\n"
        "v_exp_f32 v128, v128\n"
        "v_exp_f32 v129, v129\n"
        "v_smfmac_f32_32x32x32_bf16 v[176:191], a[220:223], v[152:159], v245\n"
        "buffer_load_dwordx4 a[200:203], v240, %[srd], s54 offen offset:2048 sc1\n"
        "v_exp_f32 v130, v130\n"
        "v_exp_f32 v131, v131\n"
        "v_exp_f32 v132, v132\n"
        "v_exp_f32 v133, v133\n"
        "v_mfma_f32_32x32x16_bf16 v[144:159], a[116:119], v[196:199], 0\n"
        "v_exp_f32 v134, v134\n"
        "v_exp_f32 v135, v135\n"
        "v_exp_f32 v136, v136\n"
        "v_exp_f32 v137, v137\n"
        "v_exp_f32 v138, v138\n"
        "v_smfmac_f32_32x32x32_bf16 v[176:191], a[224:227], v[128:135], v245\n"
        "buffer_load_dwordx4 a[204:207], v240, %[srd], s54 offen offset:3072 sc1\n"
        "v_exp_f32 v139, v139\n"
        "v_exp_f32 v140, v140\n"
        "v_exp_f32 v141, v141\n"
        "v_exp_f32 v142, v142\n"
        "v_exp_f32 v143, v143\n"
        "v_exp_f32 v144, v144\n"
        "s_add_u32 s54, s72, 1585152\n"
        "v_exp_f32 v145, v145\n"
        "v_smfmac_f32_32x32x32_bf16 v[176:191], a[228:231], v[136:143], v245\n"
        "buffer_load_dwordx4 a[208:211], v240, %[srd], s54 offen offset:0 sc1\n"
        "v_exp_f32 v146, v146\n"
        "v_exp_f32 v147, v147\n"
        "v_exp_f32 v148, v148\n"
        "v_exp_f32 v149, v149\n"
        "v_mfma_f32_32x32x16_bf16 v[128:143], a[120:123], v[196:199], 0\n"
        "v_exp_f32 v150, v150\n"
        "v_exp_f32 v151, v151\n"
        "v_exp_f32 v152, v152\n"
        "v_exp_f32 v153, v153\n"
        "v_exp_f32 v154, v154\n"
        "v_smfmac_f32_32x32x32_bf16 v[176:191], a[232:235], v[144:151], v245\n"
        "v_exp_f32 v155, v155\n"
        "v_exp_f32 v156, v156\n"
        "v_exp_f32 v157, v157\n"
        "v_exp_f32 v158, v158\n"
        "v_exp_f32 v159, v159\n"
        "s_nop 0\n"
        "v_exp_f32 v128, v128\n"
        "v_exp_f32 v129, v129\n"
        "v_smfmac_f32_32x32x32_bf16 v[176:191], a[236:239], v[152:159], v245\n"
        "buffer_load_dwordx4 a[212:215], v240, %[srd], s54 offen offset:1024 sc1\n"
        "v_exp_f32 v130, v130\n"
        "v_exp_f32 v131, v131\n"
        "v_exp_f32 v132, v132\n"
        "v_exp_f32 v133, v133\n"
        "v_mfma_f32_32x32x16_bf16 v[144:159], a[124:127], v[196:199], 0\n"
        "v_exp_f32 v134, v134\n"
        "v_exp_f32 v135, v135\n"
        "v_exp_f32 v136, v136\n"
        "v_exp_f32 v137, v137\n"
        "v_exp_f32 v138, v138\n"
        "v_smfmac_f32_32x32x32_bf16 v[176:191], a[240:243], v[128:135], v245\n"
        "buffer_load_dwordx4 a[216:219], v240, %[srd], s54 offen offset:2048 sc1\n"
        "v_exp_f32 v139, v139\n"
        "v_exp_f32 v140, v140\n"
        "v_exp_f32 v141, v141\n"
        "v_exp_f32 v142, v142\n"
        "v_exp_f32 v143, v143\n"
        "v_exp_f32 v144, v144\n"
        "v_exp_f32 v145, v145\n"
        "v_smfmac_f32_32x32x32_bf16 v[176:191], a[244:247], v[136:143], v245\n"
        "buffer_load_dwordx4 a[220:223], v240, %[srd], s54 offen offset:3072 sc1\n"
        "v_exp_f32 v146, v146\n"
        "v_exp_f32 v147, v147\n"
        "v_exp_f32 v148, v148\n"
        "v_exp_f32 v149, v149\n"
        "v_exp_f32 v150, v150\n"
        "v_exp_f32 v151, v151\n"
        "v_exp_f32 v152, v152\n"
        "v_exp_f32 v153, v153\n"
        "v_exp_f32 v154, v154\n"
        "v_smfmac_f32_32x32x32_bf16 v[176:191], a[248:251], v[144:151], v245\n"
        "v_exp_f32 v155, v155\n"
        "v_exp_f32 v156, v156\n"
        "v_exp_f32 v157, v157\n"
        "v_exp_f32 v158, v158\n"
        "v_exp_f32 v159, v159\n"
        "s_nop 1\n"
        "v_smfmac_f32_32x32x32_bf16 v[176:191], a[252:255], v[152:159], v245\n"
        "v_rcp_f32 v168, v168\n"
        "s_nop 0\n"
        "v_pk_mul_f32 v[160:161], v[160:161], v[168:169] op_sel_hi:[1,0]\n"
        "v_pk_mul_f32 v[162:163], v[162:163], v[168:169] op_sel_hi:[1,0]\n"
        "v_pk_mul_f32 v[164:165], v[164:165], v[168:169] op_sel_hi:[1,0]\n"
        "v_pk_mul_f32 v[166:167], v[166:167], v[168:169] op_sel_hi:[1,0]\n"
        "v_cvt_pk_bf16_f32 v136, v160, v161\n"
        "v_cvt_pk_bf16_f32 v137, v162, v163\n"
        "v_cvt_pk_bf16_f32 v138, v164, v165\n"
        "v_cvt_pk_bf16_f32 v139, v166, v167\n"
        "v_min_u32 v136, -2, v136\n"
        "v_min_u32 v137, -2, v137\n"
        "v_min_u32 v138, -2, v138\n"
        "v_min_u32 v139, -2, v139\n"
        "v_rcp_f32 v184, v184\n"
        "s_nop 0\n"
        "v_pk_mul_f32 v[176:177], v[176:177], v[184:185] op_sel_hi:[1,0]\n"
        "v_pk_mul_f32 v[178:179], v[178:179], v[184:185] op_sel_hi:[1,0]\n"
        "v_pk_mul_f32 v[180:181], v[180:181], v[184:185] op_sel_hi:[1,0]\n"
        "v_pk_mul_f32 v[182:183], v[182:183], v[184:185] op_sel_hi:[1,0]\n"
        "v_cvt_pk_bf16_f32 v140, v176, v177\n"
        "v_cvt_pk_bf16_f32 v141, v178, v179\n"
        "v_cvt_pk_bf16_f32 v142, v180, v181\n"
        "v_cvt_pk_bf16_f32 v143, v182, v183\n"
        "v_min_u32 v140, -2, v140\n"
        "v_min_u32 v141, -2, v141\n"
        "v_min_u32 v142, -2, v142\n"
        "v_min_u32 v143, -2, v143\n"
        "s_mov_b32 s54, s64\n"
        "buffer_store_dwordx4 v[136:139], v1, %[srd], s54 offen sc1\n"
        "buffer_store_dwordx4 v[140:143], v1, %[srd], s54 offen offset:512 sc1\n"
        "s_lshr_b32 s54, s64, 4\n"
        "s_and_b32 s54, s54, 0xf80\n"
        "s_add_u32 s54, s54, s34\n"
        "v_mov_b32 v254, s50\n"
        "s_add_u32 s54, s54, 0\n"
        "v_lshrrev_b32 v252, 2, v0\n"
        "s_mov_b32 exec_hi, 0\n"
        "buffer_store_dword v254, v252, %[srd], s54 offen sc1\n"
        "s_mov_b64 exec, -1\n"
        "ds_write_b128 v2, v[136:139]\n"
        "ds_write_b128 v2, v[140:143] offset:512\n"
        "; ---- column sums, markers of the neighbours are down\n"
        "Lredone_%=:\n"
        "v_add_f32_dpp v200, v200, v200 quad_perm:[1,0,3,2] row_mask:0xf bank_mask:0xf\n"
        "v_add_f32_dpp v201, v201, v201 quad_perm:[1,0,3,2] row_mask:0xf bank_mask:0xf\n"
        "v_add_f32_dpp v202, v202, v202 quad_perm:[1,0,3,2] row_mask:0xf bank_mask:0xf\n"
        "v_add_f32_dpp v203, v203, v203 quad_perm:[1,0,3,2] row_mask:0xf bank_mask:0xf\n"
        "v_add_f32_dpp v204, v204, v204 quad_perm:[1,0,3,2] row_mask:0xf bank_mask:0xf\n"
        "v_add_f32_dpp v205, v205, v205 quad_perm:[1,0,3,2] row_mask:0xf bank_mask:0xf\n"
        "v_add_f32_dpp v206, v206, v206 quad_perm:[1,0,3,2] row_mask:0xf bank_mask:0xf\n"
        "v_add_f32_dpp v207, v207, v207 quad_perm:[1,0,3,2] row_mask:0xf bank_mask:0xf\n"
        "v_add_f32_dpp v208, v208, v208 quad_perm:[1,0,3,2] row_mask:0xf bank_mask:0xf\n"
        "v_add_f32_dpp v209, v209, v209 quad_perm:[1,0,3,2] row_mask:0xf bank_mask:0xf\n"
        "v_add_f32_dpp v210, v210, v210 quad_perm:[1,0,3,2] row_mask:0xf bank_mask:0xf\n"
        "v_add_f32_dpp v211, v211, v211 quad_perm:[1,0,3,2] row_mask:0xf bank_mask:0xf\n"
        "v_add_f32_dpp v213, v213, v213 quad_perm:[1,0,3,2] row_mask:0xf bank_mask:0xf\n"
        "v_add_f32_dpp v214, v214, v214 quad_perm:[1,0,3,2] row_mask:0xf bank_mask:0xf\n"
        "v_add_f32_dpp v215, v215, v215 quad_perm:[1,0,3,2] row_mask:0xf bank_mask:0xf\n"
        "v_add_f32_dpp v253, v212, v212 quad_perm:[1,0,3,2] row_mask:0xf bank_mask:0xf\n"
        "v_add_f32_dpp v192, v200, v200 quad_perm:[2,3,0,1] row_mask:0xf bank_mask:0xf\n"
        "v_add_f32_dpp v193, v201, v201 quad_perm:[2,3,0,1] row_mask:0xf bank_mask:0xf\n"
        "v_add_f32_dpp v194, v202, v202 quad_perm:[2,3,0,1] row_mask:0xf bank_mask:0xf\n"
        "v_add_f32_dpp v195, v203, v203 quad_perm:[2,3,0,1] row_mask:0xf bank_mask:0xf\n"
        "v_add_f32_dpp v204, v204, v204 quad_perm:[2,3,0,1] row_mask:0xf bank_mask:0xf\n"
        "v_add_f32_dpp v205, v205, v205 quad_perm:[2,3,0,1] row_mask:0xf bank_mask:0xf\n"
        "v_add_f32_dpp v206, v206, v206 quad_perm:[2,3,0,1] row_mask:0xf bank_mask:0xf\n"
        "v_add_f32_dpp v207, v207, v207 quad_perm:[2,3,0,1] row_mask:0xf bank_mask:0xf\n"
        "v_add_f32_dpp v232, v208, v208 quad_perm:[2,3,0,1] row_mask:0xf bank_mask:0xf\n"
        "v_add_f32_dpp v233, v209, v209 quad_perm:[2,3,0,1] row_mask:0xf bank_mask:0xf\n"
        "v_add_f32_dpp v234, v210, v210 quad_perm:[2,3,0,1] row_mask:0xf bank_mask:0xf\n"
        "v_add_f32_dpp v235, v211, v211 quad_perm:[2,3,0,1] row_mask:0xf bank_mask:0xf\n"
        "v_add_f32_dpp v236, v253, v253 quad_perm:[2,3,0,1] row_mask:0xf bank_mask:0xf\n"
        "v_add_f32_dpp v237, v213, v213 quad_perm:[2,3,0,1] row_mask:0xf bank_mask:0xf\n"
        "v_add_f32_dpp v238, v214, v214 quad_perm:[2,3,0,1] row_mask:0xf bank_mask:0xf\n"
        "v_add_f32_dpp v239, v215, v215 quad_perm:[2,3,0,1] row_mask:0xf bank_mask:0xf\n"
        "v_mov_b32 v253, v216\n"
        "v_mov_b32 v254, v212\n"
        "s_nop 0\n"
        "v_permlane16_swap_b32 v216, v253\n"
        "v_permlane16_swap_b32 v212, v254\n"
        "v_add_f32 v216, v216, v253\n"
        "v_add_f32 v212, v212, v254\n"
        "v_mov_b32 v253, v216\n"
        "v_mov_b32 v254, v212\n"
        "s_nop 0\n"
        "v_permlane32_swap_b32 v216, v253\n"
        "v_permlane32_swap_b32 v212, v254\n"
        "v_add_f32 v216, v216, v253\n"
        "v_add_f32 v212, v212, v254\n"
        "s_cmp_lt_u32 s86, 4\n"
        "s_cselect_b64 vcc, 0, -1\n"
        "v_cndmask_b32 v236, v236, v212, vcc\n"
        "s_waitcnt vmcnt(3)\n"
        "v_accvgpr_read_b32 v128, a64\n"
        "v_accvgpr_read_b32 v129, a65\n"
        "v_accvgpr_read_b32 v130, a66\n"
        "v_accvgpr_read_b32 v131, a67\n"
        "v_xor_b32 v128, s51, v128\n"
        "v_xor_b32 v129, s51, v129\n"
        "v_xor_b32 v130, s51, v130\n"
        "v_xor_b32 v131, s51, v131\n"
        "v_or3_b32 v128, v128, v129, v130\n"
        "v_or_b32 v128, v128, v131\n"
        "v_cmp_ne_u32 vcc, 0, v128\n"
        "s_cbranch_vccnz Lf3spin_%=\n"
        "; ---- exchange of the attention output + out-projection A (residual as C input)\n"
        "Lsb_%=:\n"
        "s_waitcnt lgkmcnt(0)\n"
        "s_barrier\n"
        "ds_read_b128 v[128:131], v3 offset:0\n"
        "ds_read_b128 v[132:135], v3 offset:256\n"
        "ds_read_b128 v[136:139], v3 offset:512\n"
        "ds_read_b128 v[140:143], v3 offset:768\n"
        "ds_read_b128 v[144:147], v3 offset:4096\n"
        "ds_read_b128 v[148:151], v3 offset:4352\n"
        "ds_read_b128 v[152:155], v3 offset:4608\n"
        "ds_read_b128 v[156:159], v3 offset:4864\n"
        "s_add_u32 s60, s65, 0\n"
        "s_add_u32 s61, s78, 0\n"
        "s_add_u32 s55, s34, 0\n"
        "s_waitcnt lgkmcnt(6)\n"
        "v_mfma_f32_16x16x32_bf16 v[160:163], a[0:3], v[128:131], v[80:83]\n"
        "v_mfma_f32_16x16x32_bf16 v[164:167], a[0:3], v[132:135], v[84:87]\n"
        "s_waitcnt lgkmcnt(4)\n"
        "v_mfma_f32_16x16x32_bf16 v[168:171], a[0:3], v[136:139], v[88:91]\n"
        "v_mfma_f32_16x16x32_bf16 v[172:175], a[0:3], v[140:143], v[92:95]\n"
        "s_waitcnt lgkmcnt(2)\n"
        "v_mfma_f32_16x16x32_bf16 v[160:163], a[4:7], v[144:147], v[160:163]\n"
        "v_mfma_f32_16x16x32_bf16 v[164:167], a[4:7], v[148:151], v[164:167]\n"
        "s_waitcnt lgkmcnt(0)\n"
        "v_mfma_f32_16x16x32_bf16 v[168:171], a[4:7], v[152:155], v[168:171]\n"
        "v_mfma_f32_16x16x32_bf16 v[172:175], a[4:7], v[156:159], v[172:175]\n"
        "s_sleep 4\n"
        "s_mov_b32 s56, 0\n"
        "Lsp9_%=:\n"
        "s_add_u32 s56, s56, 1\n"
        "s_cmp_gt_u32 s56, 16384\n"
        "s_cbranch_scc1 Lsq10_%=\n"
        "buffer_load_dword v252, v5, %[srd], s55 offen sc1\n"
        "s_waitcnt vmcnt(0)\n"
        "v_cmp_ne_u32 vcc, s50, v252\n"
        "s_nop 3\n"
        "s_and_b64 vcc, vcc, s[30:31]\n"
        "s_cbranch_vccnz Lsp9_%=\n"
        "Lsq10_%=:\n"
        "s_mov_b32 m0, s61\n"
        "s_add_u32 s54, s77, 0\n"
        "s_and_b32 s54, s54, 0xffff\n"
        "s_add_u32 s54, s54, s60\n"
        "buffer_load_dwordx4 v0, %[srd], s54 offen sc1 lds\n"
        "s_add_u32 m0, m0, 4096\n"
        "s_add_u32 s54, s77, 4096\n"
        "s_and_b32 s54, s54, 0xffff\n"
        "s_add_u32 s54, s54, s60\n"
        "buffer_load_dwordx4 v0, %[srd], s54 offen sc1 lds\n"
        "s_add_u32 m0, m0, 4096\n"
        "s_add_u32 s54, s77, 8192\n"
        "s_and_b32 s54, s54, 0xffff\n"
        "s_add_u32 s54, s54, s60\n"
        "buffer_load_dwordx4 v0, %[srd], s54 offen sc1 lds\n"
        "s_add_u32 m0, m0, 4096\n"
        "s_add_u32 s54, s77, 12288\n"
        "s_and_b32 s54, s54, 0xffff\n"
        "s_add_u32 s54, s54, s60\n"
        "buffer_load_dwordx4 v0, %[srd], s54 offen sc1 lds\n"
        "s_add_u32 m0, m0, 4096\n"
        "s_add_u32 s54, s77, 16384\n"
        "s_and_b32 s54, s54, 0xffff\n"
        "s_add_u32 s54, s54, s60\n"
        "buffer_load_dwordx4 v0, %[srd], s54 offen sc1 lds\n"
        "s_add_u32 m0, m0, 4096\n"
        "s_add_u32 s54, s77, 20480\n"
        "s_and_b32 s54, s54, 0xffff\n"
        "s_add_u32 s54, s54, s60\n"
        "buffer_load_dwordx4 v0, %[srd], s54 offen sc1 lds\n"
        "s_add_u32 m0, m0, 4096\n"
        "s_add_u32 s54, s77, 24576\n"
        "s_and_b32 s54, s54, 0xffff\n"
        "s_add_u32 s54, s54, s60\n"
        "buffer_load_dwordx4 v0, %[srd], s54 offen sc1 lds\n"
        "s_add_u32 m0, m0, 4096\n"
        "s_add_u32 s54, s77, 28672\n"
        "s_and_b32 s54, s54, 0xffff\n"
        "s_add_u32 s54, s54, s60\n"
        "buffer_load_dwordx4 v0, %[srd], s54 offen sc1 lds\n"
        "s_add_u32 m0, m0, 4096\n"
        "s_add_u32 s54, s77, 32768\n"
        "s_and_b32 s54, s54, 0xffff\n"
        "s_add_u32 s54, s54, s60\n"
        "buffer_load_dwordx4 v0, %[srd], s54 offen sc1 lds\n"
        "s_add_u32 m0, m0, 4096\n"
        "s_add_u32 s54, s77, 36864\n"
        "s_and_b32 s54, s54, 0xffff\n"
        "s_add_u32 s54, s54, s60\n"
        "buffer_load_dwordx4 v0, %[srd], s54 offen sc1 lds\n"
        "s_add_u32 m0, m0, 4096\n"
        "s_add_u32 s54, s77, 40960\n"
        "s_and_b32 s54, s54, 0xffff\n"
        "s_add_u32 s54, s54, s60\n"
        "buffer_load_dwordx4 v0, %[srd], s54 offen sc1 lds\n"
        "s_add_u32 m0, m0, 4096\n"
        "s_add_u32 s54, s77, 45056\n"
        "s_and_b32 s54, s54, 0xffff\n"
        "s_add_u32 s54, s54, s60\n"
        "buffer_load_dwordx4 v0, %[srd], s54 offen sc1 lds\n"
        "s_add_u32 m0, m0, 4096\n"
        "s_add_u32 s54, s77, 49152\n"
        "s_and_b32 s54, s54, 0xffff\n"
        "s_add_u32 s54, s54, s60\n"
        "buffer_load_dwordx4 v0, %[srd], s54 offen sc1 lds\n"
        "s_add_u32 m0, m0, 4096\n"
        "s_add_u32 s54, s77, 53248\n"
        "s_and_b32 s54, s54, 0xffff\n"
        "s_add_u32 s54, s54, s60\n"
        "buffer_load_dwordx4 v0, %[srd], s54 offen sc1 lds\n"
        "s_waitcnt vmcnt(9)\n"
        "s_barrier\n"
        "ds_read_b128 v[128:131], v3 offset:8192\n"
        "ds_read_b128 v[132:135], v3 offset:8448\n"
        "ds_read_b128 v[136:139], v3 offset:8704\n"
        "ds_read_b128 v[140:143], v3 offset:8960\n"
        "ds_read_b128 v[144:147], v3 offset:12288\n"
        "ds_read_b128 v[148:151], v3 offset:12544\n"
        "ds_read_b128 v[152:155], v3 offset:12800\n"
        "ds_read_b128 v[156:159], v3 offset:13056\n"
        "s_waitcnt lgkmcnt(6)\n"
        "v_mfma_f32_16x16x32_bf16 v[160:163], a[8:11], v[128:131], v[160:163]\n"
        "ds_read_b128 v[128:131], v3 offset:16384\n"
        "v_mfma_f32_16x16x32_bf16 v[164:167], a[8:11], v[132:135], v[164:167]\n"
        "ds_read_b128 v[132:135], v3 offset:16640\n"
        "s_waitcnt lgkmcnt(6)\n"
        "v_mfma_f32_16x16x32_bf16 v[168:171], a[8:11], v[136:139], v[168:171]\n"
        "ds_read_b128 v[136:139], v3 offset:16896\n"
        "v_mfma_f32_16x16x32_bf16 v[172:175], a[8:11], v[140:143], v[172:175]\n"
        "ds_read_b128 v[140:143], v3 offset:17152\n"
        "s_waitcnt lgkmcnt(6)\n"
        "v_mfma_f32_16x16x32_bf16 v[160:163], a[12:15], v[144:147], v[160:163]\n"
        "ds_read_b128 v[144:147], v3 offset:20480\n"
        "v_mfma_f32_16x16x32_bf16 v[164:167], a[12:15], v[148:151], v[164:167]\n"
        "ds_read_b128 v[148:151], v3 offset:20736\n"
        "s_waitcnt lgkmcnt(6)\n"
        "v_mfma_f32_16x16x32_bf16 v[168:171], a[12:15], v[152:155], v[168:171]\n"
        "ds_read_b128 v[152:155], v3 offset:20992\n"
        "v_mfma_f32_16x16x32_bf16 v[172:175], a[12:15], v[156:159], v[172:175]\n"
        "ds_read_b128 v[156:159], v3 offset:21248\n"
        "s_waitcnt lgkmcnt(6)\n"
        "v_mfma_f32_16x16x32_bf16 v[160:163], a[16:19], v[128:131], v[160:163]\n"
        "ds_read_b128 v[128:131], v3 offset:24576\n"
        "v_mfma_f32_16x16x32_bf16 v[164:167], a[16:19], v[132:135], v[164:167]\n"
        "ds_read_b128 v[132:135], v3 offset:24832\n"
        "s_waitcnt lgkmcnt(6)\n"
        "v_mfma_f32_16x16x32_bf16 v[168:171], a[16:19], v[136:139], v[168:171]\n"
        "ds_read_b128 v[136:139], v3 offset:25088\n"
        "v_mfma_f32_16x16x32_bf16 v[172:175], a[16:19], v[140:143], v[172:175]\n"
        "ds_read_b128 v[140:143], v3 offset:25344\n"
        "s_waitcnt vmcnt(4)\n"
        "s_barrier\n"
        "s_waitcnt lgkmcnt(6)\n"
        "v_mfma_f32_16x16x32_bf16 v[160:163], a[20:23], v[144:147], v[160:163]\n"
        "ds_read_b128 v[144:147], v3 offset:28672\n"
        "v_mfma_f32_16x16x32_bf16 v[164:167], a[20:23], v[148:151], v[164:167]\n"
        "ds_read_b128 v[148:151], v3 offset:28928\n"
        "s_waitcnt lgkmcnt(6)\n"
        "v_mfma_f32_16x16x32_bf16 v[168:171], a[20:23], v[152:155], v[168:171]\n"
        "ds_read_b128 v[152:155], v3 offset:29184\n"
        "v_mfma_f32_16x16x32_bf16 v[172:175], a[20:23], v[156:159], v[172:175]\n"
        "ds_read_b128 v[156:159], v3 offset:29440\n"
        "s_waitcnt lgkmcnt(6)\n"
        "v_mfma_f32_16x16x32_bf16 v[160:163], a[24:27], v[128:131], v[160:163]\n"
        "ds_read_b128 v[128:131], v3 offset:32768\n"
        "v_mfma_f32_16x16x32_bf16 v[164:167], a[24:27], v[132:135], v[164:167]\n"
        "ds_read_b128 v[132:135], v3 offset:33024\n"
        "s_waitcnt lgkmcnt(6)\n"
        "v_mfma_f32_16x16x32_bf16 v[168:171], a[24:27], v[136:139], v[168:171]\n"
        "ds_read_b128 v[136:139], v3 offset:33280\n"
        "v_mfma_f32_16x16x32_bf16 v[172:175], a[24:27], v[140:143], v[172:175]\n"
        "ds_read_b128 v[140:143], v3 offset:33536\n"
        "s_waitcnt lgkmcnt(6)\n"
        "v_mfma_f32_16x16x32_bf16 v[160:163], a[28:31], v[144:147], v[160:163]\n"
        "ds_read_b128 v[144:147], v3 offset:36864\n"
        "v_mfma_f32_16x16x32_bf16 v[164:167], a[28:31], v[148:151], v[164:167]\n"
        "ds_read_b128 v[148:151], v3 offset:37120\n"
        "s_waitcnt lgkmcnt(6)\n"
        "v_mfma_f32_16x16x32_bf16 v[168:171], a[28:31], v[152:155], v[168:171]\n"
        "ds_read_b128 v[152:155], v3 offset:37376\n"
        "v_mfma_f32_16x16x32_bf16 v[172:175], a[28:31], v[156:159], v[172:175]\n"
        "ds_read_b128 v[156:159], v3 offset:37632\n"
        "s_waitcnt lgkmcnt(6)\n"
        "v_mfma_f32_16x16x32_bf16 v[160:163], a[32:35], v[128:131], v[160:163]\n"
        "ds_read_b128 v[128:131], v3 offset:40960\n"
        "v_mfma_f32_16x16x32_bf16 v[164:167], a[32:35], v[132:135], v[164:167]\n"
        "ds_read_b128 v[132:135], v3 offset:41216\n"
        "s_waitcnt lgkmcnt(6)\n"
        "v_mfma_f32_16x16x32_bf16 v[168:171], a[32:35], v[136:139], v[168:171]\n"
        "ds_read_b128 v[136:139], v3 offset:41472\n"
        "v_mfma_f32_16x16x32_bf16 v[172:175], a[32:35], v[140:143], v[172:175]\n"
        "ds_read_b128 v[140:143], v3 offset:41728\n"
        "s_waitcnt lgkmcnt(6)\n"
        "v_mfma_f32_16x16x32_bf16 v[160:163], a[36:39], v[144:147], v[160:163]\n"
        "ds_read_b128 v[144:147], v3 offset:45056\n"
        "v_mfma_f32_16x16x32_bf16 v[164:167], a[36:39], v[148:151], v[164:167]\n"
        "ds_read_b128 v[148:151], v3 offset:45312\n"
        "s_waitcnt lgkmcnt(6)\n"
        "v_mfma_f32_16x16x32_bf16 v[168:171], a[36:39], v[152:155], v[168:171]\n"
        "ds_read_b128 v[152:155], v3 offset:45568\n"
        "v_mfma_f32_16x16x32_bf16 v[172:175], a[36:39], v[156:159], v[172:175]\n"
        "ds_read_b128 v[156:159], v3 offset:45824\n"
        "s_waitcnt vmcnt(0)\n"
        "s_barrier\n"
        "s_waitcnt lgkmcnt(6)\n"
        "v_mfma_f32_16x16x32_bf16 v[160:163], a[40:43], v[128:131], v[160:163]\n"
        "ds_read_b128 v[128:131], v3 offset:49152\n"
        "v_mfma_f32_16x16x32_bf16 v[164:167], a[40:43], v[132:135], v[164:167]\n"
        "ds_read_b128 v[132:135], v3 offset:49408\n"
        "s_waitcnt lgkmcnt(6)\n"
        "v_mfma_f32_16x16x32_bf16 v[168:171], a[40:43], v[136:139], v[168:171]\n"
        "ds_read_b128 v[136:139], v3 offset:49664\n"
        "v_mfma_f32_16x16x32_bf16 v[172:175], a[40:43], v[140:143], v[172:175]\n"
        "ds_read_b128 v[140:143], v3 offset:49920\n"
        "s_waitcnt lgkmcnt(6)\n"
        "v_mfma_f32_16x16x32_bf16 v[160:163], a[44:47], v[144:147], v[160:163]\n"
        "ds_read_b128 v[144:147], v3 offset:53248\n"
        "v_mfma_f32_16x16x32_bf16 v[164:167], a[44:47], v[148:151], v[164:167]\n"
        "ds_read_b128 v[148:151], v3 offset:53504\n"
        "s_waitcnt lgkmcnt(6)\n"
        "v_mfma_f32_16x16x32_bf16 v[168:171], a[44:47], v[152:155], v[168:171]\n"
        "ds_read_b128 v[152:155], v3 offset:53760\n"
        "v_mfma_f32_16x16x32_bf16 v[172:175], a[44:47], v[156:159], v[172:175]\n"
        "ds_read_b128 v[156:159], v3 offset:54016\n"
        "s_waitcnt lgkmcnt(6)\n"
        "v_mfma_f32_16x16x32_bf16 v[160:163], a[48:51], v[128:131], v[160:163]\n"
        "ds_read_b128 v[128:131], v3 offset:57344\n"
        "v_mfma_f32_16x16x32_bf16 v[164:167], a[48:51], v[132:135], v[164:167]\n"
        "ds_read_b128 v[132:135], v3 offset:57600\n"
        "s_waitcnt lgkmcnt(6)\n"
        "v_mfma_f32_16x16x32_bf16 v[168:171], a[48:51], v[136:139], v[168:171]\n"
        "ds_read_b128 v[136:139], v3 offset:57856\n"
        "v_mfma_f32_16x16x32_bf16 v[172:175], a[48:51], v[140:143], v[172:175]\n"
        "ds_read_b128 v[140:143], v3 offset:58112\n"
        "s_waitcnt lgkmcnt(6)\n"
        "v_mfma_f32_16x16x32_bf16 v[160:163], a[52:55], v[144:147], v[160:163]\n"
        "ds_read_b128 v[144:147], v3 offset:61440\n"
        "v_mfma_f32_16x16x32_bf16 v[164:167], a[52:55], v[148:151], v[164:167]\n"
        "ds_read_b128 v[148:151], v3 offset:61696\n"
        "s_waitcnt lgkmcnt(6)\n"
        "v_mfma_f32_16x16x32_bf16 v[168:171], a[52:55], v[152:155], v[168:171]\n"
        "ds_read_b128 v[152:155], v3 offset:61952\n"
        "v_mfma_f32_16x16x32_bf16 v[172:175], a[52:55], v[156:159], v[172:175]\n"
        "ds_read_b128 v[156:159], v3 offset:62208\n"
        "s_waitcnt lgkmcnt(6)\n"
        "v_mfma_f32_16x16x32_bf16 v[160:163], a[56:59], v[128:131], v[160:163]\n"
        "v_mfma_f32_16x16x32_bf16 v[164:167], a[56:59], v[132:135], v[164:167]\n"
        "s_waitcnt lgkmcnt(4)\n"
        "v_mfma_f32_16x16x32_bf16 v[168:171], a[56:59], v[136:139], v[168:171]\n"
        "v_mfma_f32_16x16x32_bf16 v[172:175], a[56:59], v[140:143], v[172:175]\n"
        "s_waitcnt lgkmcnt(2)\n"
        "v_mfma_f32_16x16x32_bf16 v[160:163], a[60:63], v[144:147], v[160:163]\n"
        "v_mfma_f32_16x16x32_bf16 v[164:167], a[60:63], v[148:151], v[164:167]\n"
        "s_waitcnt lgkmcnt(0)\n"
        "v_mfma_f32_16x16x32_bf16 v[168:171], a[60:63], v[152:155], v[168:171]\n"
        "v_mfma_f32_16x16x32_bf16 v[172:175], a[60:63], v[156:159], v[172:175]\n"
        "s_nop 4\n"
        "v_cmp_u_f32 vcc, v160, v164\n"
        "s_nop 1\n"
        "v_cmp_u_f32 s[62:63], v168, v172\n"
        "s_nop 3\n"
        "s_or_b64 vcc, vcc, s[62:63]\n"
        "s_cbranch_vccnz Lsbbad_%=\n"
        "; ---- publish x, partial LN sums; exchange x; k, v GEMM of layer B\n"
        "Lsbok_%=:\n"
        "v_cvt_pk_bf16_f32 v128, v160, v161\n"
        "v_cvt_pk_bf16_f32 v129, v162, v163\n"
        "v_cvt_pk_bf16_f32 v130, v164, v165\n"
        "v_cvt_pk_bf16_f32 v131, v166, v167\n"
        "v_cvt_pk_bf16_f32 v132, v168, v169\n"
        "v_cvt_pk_bf16_f32 v133, v170, v171\n"
        "v_cvt_pk_bf16_f32 v134, v172, v173\n"
        "v_cvt_pk_bf16_f32 v135, v174, v175\n"
        "v_permlane16_swap_b32 v128, v130\n"
        "v_permlane16_swap_b32 v129, v131\n"
        "v_permlane16_swap_b32 v132, v134\n"
        "v_permlane16_swap_b32 v133, v135\n"
        "v_min_u32 v128, -2, v128\n"
        "v_min_u32 v129, -2, v129\n"
        "v_min_u32 v130, -2, v130\n"
        "v_min_u32 v131, -2, v131\n"
        "v_min_u32 v132, -2, v132\n"
        "v_min_u32 v133, -2, v133\n"
        "v_min_u32 v134, -2, v134\n"
        "v_min_u32 v135, -2, v135\n"
        "s_add_u32 s54, s64, 8388608\n"
        "buffer_store_dwordx4 v[128:131], v1, %[srd], s54 offen sc1\n"
        "buffer_store_dwordx4 v[132:135], v1, %[srd], s54 offen offset:512 sc1\n"
        "s_lshr_b32 s54, s64, 4\n"
        "s_and_b32 s54, s54, 0xf80\n"
        "s_add_u32 s54, s54, s34\n"
        "v_mov_b32 v254, s50\n"
        "s_add_u32 s54, s54, 131072\n"
        "v_lshrrev_b32 v252, 2, v0\n"
        "s_mov_b32 exec_hi, 0\n"
        "buffer_store_dword v254, v252, %[srd], s54 offen sc1\n"
        "s_mov_b64 exec, -1\n"
        "v_add_u32 v254, 0x10000, v2\n"
        "ds_write_b128 v254, v[128:131]\n"
        "ds_write_b128 v254, v[132:135] offset:512\n"
        "v_pk_add_f32 v[136:137], v[160:161], v[162:163]\n"
        "v_pk_mul_f32 v[144:145], v[160:161], v[160:161]\n"
        "v_pk_add_f32 v[138:139], v[164:165], v[166:167]\n"
        "v_pk_mul_f32 v[146:147], v[164:165], v[164:165]\n"
        "v_pk_add_f32 v[140:141], v[168:169], v[170:171]\n"
        "v_pk_mul_f32 v[148:149], v[168:169], v[168:169]\n"
        "v_pk_add_f32 v[142:143], v[172:173], v[174:175]\n"
        "v_pk_mul_f32 v[150:151], v[172:173], v[172:173]\n"
        "v_add_f32 v136, v136, v137\n"
        "v_pk_fma_f32 v[144:145], v[162:163], v[162:163], v[144:145]\n"
        "v_add_f32 v138, v138, v139\n"
        "v_pk_fma_f32 v[146:147], v[166:167], v[166:167], v[146:147]\n"
        "v_add_f32 v140, v140, v141\n"
        "v_pk_fma_f32 v[148:149], v[170:171], v[170:171], v[148:149]\n"
        "v_add_f32 v142, v142, v143\n"
        "v_pk_fma_f32 v[150:151], v[174:175], v[174:175], v[150:151]\n"
        "v_add_f32 v144, v144, v145\n"
        "v_add_f32 v146, v146, v147\n"
        "v_add_f32 v148, v148, v149\n"
        "v_add_f32 v150, v150, v151\n"
        "v_permlane32_swap_b32 v136, v140\n"
        "v_permlane32_swap_b32 v144, v148\n"
        "v_permlane32_swap_b32 v138, v142\n"
        "v_permlane32_swap_b32 v146, v150\n"
        "v_add_f32 v136, v136, v140\n"
        "v_add_f32 v144, v144, v148\n"
        "v_add_f32 v138, v138, v142\n"
        "v_add_f32 v146, v146, v150\n"
        "s_nop 0\n"
        "v_permlane16_swap_b32 v136, v138\n"
        "v_permlane16_swap_b32 v144, v146\n"
        "v_add_f32 v136, v136, v138\n"
        "v_add_f32 v144, v144, v146\n"
        "v_mov_b32 v137, v144\n"
        "ds_write_b64 v10, v[136:137]\n"
        "s_waitcnt lgkmcnt(0)\n"
        "s_barrier\n"
        "v_cvt_pk_bf16_f32 v80, v160, v161\n"
        "v_cvt_pk_bf16_f32 v82, v162, v163\n"
        "v_cvt_pk_bf16_f32 v84, v164, v165\n"
        "v_cvt_pk_bf16_f32 v86, v166, v167\n"
        "v_cvt_pk_bf16_f32 v88, v168, v169\n"
        "v_cvt_pk_bf16_f32 v90, v170, v171\n"
        "v_cvt_pk_bf16_f32 v92, v172, v173\n"
        "v_cvt_pk_bf16_f32 v94, v174, v175\n"
        "ds_read_b128 v[128:131], v4 offset:0\n"
        "ds_read_b128 v[132:135], v4 offset:256\n"
        "ds_read_b128 v[136:139], v4 offset:512\n"
        "ds_read_b128 v[140:143], v4 offset:768\n"
        "ds_read_b128 v[144:147], v4 offset:4096\n"
        "ds_read_b128 v[148:151], v4 offset:4352\n"
        "ds_read_b128 v[152:155], v4 offset:4608\n"
        "ds_read_b128 v[156:159], v4 offset:4864\n"
        "s_add_u32 s60, s65, 8388608\n"
        "s_add_u32 s61, s78, 65536\n"
        "s_add_u32 s55, s34, 131072\n"
        "s_waitcnt lgkmcnt(6)\n"
        "v_mfma_f32_16x16x32_bf16 v[160:163], v[96:99], v[128:131], 0\n"
        "v_mfma_f32_16x16x32_bf16 v[176:179], v[128:131], a[160:163], 0\n"
        "v_mfma_f32_16x16x32_bf16 v[164:167], v[96:99], v[132:135], 0\n"
        "v_mfma_f32_16x16x32_bf16 v[180:183], v[132:135], a[160:163], 0\n"
        "s_waitcnt lgkmcnt(4)\n"
        "v_mfma_f32_16x16x32_bf16 v[168:171], v[96:99], v[136:139], 0\n"
        "v_mfma_f32_16x16x32_bf16 v[184:187], v[136:139], a[160:163], 0\n"
        "v_mfma_f32_16x16x32_bf16 v[172:175], v[96:99], v[140:143], 0\n"
        "v_mfma_f32_16x16x32_bf16 v[188:191], v[140:143], a[160:163], 0\n"
        "s_waitcnt lgkmcnt(2)\n"
        "v_mfma_f32_16x16x32_bf16 v[160:163], v[100:103], v[144:147], v[160:163]\n"
        "v_mfma_f32_16x16x32_bf16 v[176:179], v[144:147], a[164:167], v[176:179]\n"
        "v_mfma_f32_16x16x32_bf16 v[164:167], v[100:103], v[148:151], v[164:167]\n"
        "v_mfma_f32_16x16x32_bf16 v[180:183], v[148:151], a[164:167], v[180:183]\n"
        "s_waitcnt lgkmcnt(0)\n"
        "v_mfma_f32_16x16x32_bf16 v[168:171], v[100:103], v[152:155], v[168:171]\n"
        "v_mfma_f32_16x16x32_bf16 v[184:187], v[152:155], a[164:167], v[184:187]\n"
        "v_mfma_f32_16x16x32_bf16 v[172:175], v[100:103], v[156:159], v[172:175]\n"
        "v_mfma_f32_16x16x32_bf16 v[188:191], v[156:159], a[164:167], v[188:191]\n"
        "s_mov_b32 s56, 0\n"
        "Lsp15_%=:\n"
        "s_add_u32 s56, s56, 1\n"
        "s_cmp_gt_u32 s56, 16384\n"
        "s_cbranch_scc1 Lsq16_%=\n"
        "buffer_load_dword v252, v5, %[srd], s55 offen sc1\n"
        "s_waitcnt vmcnt(0)\n"
        "v_cmp_ne_u32 vcc, s50, v252\n"
        "s_nop 3\n"
        "s_and_b64 vcc, vcc, s[30:31]\n"
        "s_cbranch_vccnz Lsp15_%=\n"
        "Lsq16_%=:\n"
        "s_mov_b32 m0, s61\n"
        "s_add_u32 s54, s77, 0\n"
        "s_and_b32 s54, s54, 0xffff\n"
        "s_add_u32 s54, s54, s60\n"
        "buffer_load_dwordx4 v0, %[srd], s54 offen sc1 lds\n"
        "ds_read_b64 v[136:137], v11 offset:0\n"
        "ds_read_b64 v[138:139], v11 offset:512\n"
        "ds_read_b64 v[140:141], v11 offset:1024\n"
        "ds_read_b64 v[142:143], v11 offset:1536\n"
        "v_lshrrev_b32 v253, 1, v0\n"
        "s_add_u32 s55, s67, 0\n"
        "v_and_b32 v81, 0xffff0000, v80\n"
        "v_lshlrev_b32 v80, 16, v80\n"
        "v_and_b32 v83, 0xffff0000, v82\n"
        "v_lshlrev_b32 v82, 16, v82\n"
        "v_and_b32 v85, 0xffff0000, v84\n"
        "v_lshlrev_b32 v84, 16, v84\n"
        "s_waitcnt lgkmcnt(0)\n"
        "v_pk_add_f32 v[136:137], v[136:137], v[138:139]\n"
        "s_add_u32 m0, m0, 4096\n"
        "s_add_u32 s54, s77, 4096\n"
        "s_and_b32 s54, s54, 0xffff\n"
        "s_add_u32 s54, s54, s60\n"
        "buffer_load_dwordx4 v0, %[srd], s54 offen sc1 lds\n"
        "v_pk_add_f32 v[140:141], v[140:141], v[142:143]\n"
        "v_pk_add_f32 v[136:137], v[136:137], v[140:141]\n"
        "v_min_u32 v136, -2, v136\n"
        "v_min_u32 v137, -2, v137\n"
        "s_mov_b64 exec, 0xffff\n"
        "buffer_store_dwordx2 v[136:137], v253, %[srd], s55 offen sc1\n"
        "s_mov_b64 exec, -1\n"
        "v_and_b32 v87, 0xffff0000, v86\n"
        "v_lshlrev_b32 v86, 16, v86\n"
        "v_and_b32 v89, 0xffff0000, v88\n"
        "v_lshlrev_b32 v88, 16, v88\n"
        "v_and_b32 v91, 0xffff0000, v90\n"
        "v_lshlrev_b32 v90, 16, v90\n"
        "v_and_b32 v93, 0xffff0000, v92\n"
        "s_add_u32 m0, m0, 4096\n"
        "s_add_u32 s54, s77, 8192\n"
        "s_and_b32 s54, s54, 0xffff\n"
        "s_add_u32 s54, s54, s60\n"
        "buffer_load_dwordx4 v0, %[srd], s54 offen sc1 lds\n"
        "v_lshlrev_b32 v92, 16, v92\n"
        "v_and_b32 v95, 0xffff0000, v94\n"
        "v_lshlrev_b32 v94, 16, v94\n"
        "s_cmp_ge_u32 s69, 4\n"
        "s_cselect_b64 vcc, -1, 0\n"
        "v_cndmask_b32 v254, 0, v14, vcc\n"
        "v_accvgpr_write_b32 a0, v254\n"
        "v_accvgpr_write_b32 a1, v254\n"
        "v_accvgpr_write_b32 a2, v254\n"
        "v_accvgpr_write_b32 a3, v254\n"
        "v_accvgpr_write_b32 a4, v254\n"
        "v_accvgpr_write_b32 a5, v254\n"
        "v_accvgpr_write_b32 a6, v254\n"
        "v_accvgpr_write_b32 a7, v254\n"
        "s_add_u32 m0, m0, 4096\n"
        "s_add_u32 s54, s77, 12288\n"
        "s_and_b32 s54, s54, 0xffff\n"
        "s_add_u32 s54, s54, s60\n"
        "buffer_load_dwordx4 v0, %[srd], s54 offen sc1 lds\n"
        "v_accvgpr_write_b32 a8, v254\n"
        "v_accvgpr_write_b32 a9, v254\n"
        "v_accvgpr_write_b32 a10, v254\n"
        "v_accvgpr_write_b32 a11, v254\n"
        "v_accvgpr_write_b32 a12, v254\n"
        "v_accvgpr_write_b32 a13, v254\n"
        "v_accvgpr_write_b32 a14, v254\n"
        "v_accvgpr_write_b32 a15, v254\n"
        "s_cmp_ge_u32 s69, 3\n"
        "s_cselect_b64 vcc, -1, 0\n"
        "v_cndmask_b32 v254, 0, v14, vcc\n"
        "v_accvgpr_write_b32 a16, v254\n"
        "v_accvgpr_write_b32 a17, v254\n"
        "v_accvgpr_write_b32 a18, v254\n"
        "s_add_u32 m0, m0, 4096\n"
        "s_add_u32 s54, s77, 16384\n"
        "s_and_b32 s54, s54, 0xffff\n"
        "s_add_u32 s54, s54, s60\n"
        "buffer_load_dwordx4 v0, %[srd], s54 offen sc1 lds\n"
        "v_accvgpr_write_b32 a19, v254\n"
        "v_accvgpr_write_b32 a20, v254\n"
        "v_accvgpr_write_b32 a21, v254\n"
        "v_accvgpr_write_b32 a22, v254\n"
        "v_accvgpr_write_b32 a23, v254\n"
        "v_accvgpr_write_b32 a24, v254\n"
        "v_accvgpr_write_b32 a25, v254\n"
        "v_accvgpr_write_b32 a26, v254\n"
        "v_accvgpr_write_b32 a27, v254\n"
        "v_accvgpr_write_b32 a28, v254\n"
        "v_accvgpr_write_b32 a29, v254\n"
        "v_accvgpr_write_b32 a30, v254\n"
        "v_accvgpr_write_b32 a31, v254\n"
        "s_cmp_ge_u32 s69, 2\n"
        "s_cselect_b64 vcc, -1, 0\n"
        "v_cndmask_b32 v254, 0, v14, vcc\n"
        "s_add_u32 m0, m0, 4096\n"
        "s_add_u32 s54, s77, 20480\n"
        "s_and_b32 s54, s54, 0xffff\n"
        "s_add_u32 s54, s54, s60\n"
        "buffer_load_dwordx4 v0, %[srd], s54 offen sc1 lds\n"
        "v_accvgpr_write_b32 a32, v254\n"
        "v_accvgpr_write_b32 a33, v254\n"
        "v_accvgpr_write_b32 a34, v254\n"
        "v_accvgpr_write_b32 a35, v254\n"
        "v_accvgpr_write_b32 a36, v254\n"
        "v_accvgpr_write_b32 a37, v254\n"
        "v_accvgpr_write_b32 a38, v254\n"
        "v_accvgpr_write_b32 a39, v254\n"
        "v_accvgpr_write_b32 a40, v254\n"
        "v_accvgpr_write_b32 a41, v254\n"
        "v_accvgpr_write_b32 a42, v254\n"
        "v_accvgpr_write_b32 a43, v254\n"
        "v_accvgpr_write_b32 a44, v254\n"
        "v_accvgpr_write_b32 a45, v254\n"
        "s_add_u32 m0, m0, 4096\n"
        "s_add_u32 s54, s77, 24576\n"
        "s_and_b32 s54, s54, 0xffff\n"
        "s_add_u32 s54, s54, s60\n"
        "buffer_load_dwordx4 v0, %[srd], s54 offen sc1 lds\n"
        "v_accvgpr_write_b32 a46, v254\n"
        "v_accvgpr_write_b32 a47, v254\n"
        "s_cmp_ge_u32 s69, 1\n"
        "s_cselect_b64 vcc, -1, 0\n"
        "v_cndmask_b32 v254, 0, v14, vcc\n"
        "v_accvgpr_write_b32 a48, v254\n"
        "v_accvgpr_write_b32 a49, v254\n"
        "v_accvgpr_write_b32 a50, v254\n"
        "v_accvgpr_write_b32 a51, v254\n"
        "v_accvgpr_write_b32 a52, v254\n"
        "v_accvgpr_write_b32 a53, v254\n"
        "v_accvgpr_write_b32 a54, v254\n"
        "v_accvgpr_write_b32 a55, v254\n"
        "v_accvgpr_write_b32 a56, v254\n"
        "s_add_u32 m0, m0, 4096\n"
        "s_add_u32 s54, s77, 28672\n"
        "s_and_b32 s54, s54, 0xffff\n"
        "s_add_u32 s54, s54, s60\n"
        "buffer_load_dwordx4 v0, %[srd], s54 offen sc1 lds\n"
        "v_accvgpr_write_b32 a57, v254\n"
        "v_accvgpr_write_b32 a58, v254\n"
        "v_accvgpr_write_b32 a59, v254\n"
        "v_accvgpr_write_b32 a60, v254\n"
        "v_accvgpr_write_b32 a61, v254\n"
        "v_accvgpr_write_b32 a62, v254\n"
        "v_accvgpr_write_b32 a63, v254\n"
        "s_add_u32 m0, m0, 4096\n"
        "s_add_u32 s54, s77, 32768\n"
        "s_and_b32 s54, s54, 0xffff\n"
        "s_add_u32 s54, s54, s60\n"
        "buffer_load_dwordx4 v0, %[srd], s54 offen sc1 lds\n"
        "s_waitcnt vmcnt(8)\n"
        "s_barrier\n"
        "ds_read_b128 v[128:131], v4 offset:8192\n"
        "ds_read_b128 v[132:135], v4 offset:8448\n"
        "ds_read_b128 v[136:139], v4 offset:8704\n"
        "ds_read_b128 v[140:143], v4 offset:8960\n"
        "ds_read_b128 v[144:147], v4 offset:12288\n"
        "ds_read_b128 v[148:151], v4 offset:12544\n"
        "ds_read_b128 v[152:155], v4 offset:12800\n"
        "ds_read_b128 v[156:159], v4 offset:13056\n"
        "s_add_u32 m0, m0, 4096\n"
        "s_add_u32 s54, s77, 36864\n"
        "s_and_b32 s54, s54, 0xffff\n"
        "s_add_u32 s54, s54, s60\n"
        "buffer_load_dwordx4 v0, %[srd], s54 offen sc1 lds\n"
        "s_waitcnt lgkmcnt(6)\n"
        "v_mfma_f32_16x16x32_bf16 v[160:163], v[104:107], v[128:131], v[160:163]\n"
        "v_mfma_f32_16x16x32_bf16 v[176:179], v[128:131], a[168:171], v[176:179]\n"
        "v_mfma_f32_16x16x32_bf16 v[164:167], v[104:107], v[132:135], v[164:167]\n"
        "v_mfma_f32_16x16x32_bf16 v[180:183], v[132:135], a[168:171], v[180:183]\n"
        "s_add_u32 m0, m0, 4096\n"
        "s_add_u32 s54, s77, 40960\n"
        "s_and_b32 s54, s54, 0xffff\n"
        "s_add_u32 s54, s54, s60\n"
        "buffer_load_dwordx4 v0, %[srd], s54 offen sc1 lds\n"
        "s_waitcnt lgkmcnt(4)\n"
        "v_mfma_f32_16x16x32_bf16 v[168:171], v[104:107], v[136:139], v[168:171]\n"
        "v_mfma_f32_16x16x32_bf16 v[184:187], v[136:139], a[168:171], v[184:187]\n"
        "v_mfma_f32_16x16x32_bf16 v[172:175], v[104:107], v[140:143], v[172:175]\n"
        "v_mfma_f32_16x16x32_bf16 v[188:191], v[140:143], a[168:171], v[188:191]\n"
        "s_add_u32 m0, m0, 4096\n"
        "s_add_u32 s54, s77, 45056\n"
        "s_and_b32 s54, s54, 0xffff\n"
        "s_add_u32 s54, s54, s60\n"
        "buffer_load_dwordx4 v0, %[srd], s54 offen sc1 lds\n"
        "s_waitcnt vmcnt(7)\n"
        "s_barrier\n"
        "ds_read_b128 v[128:131], v4 offset:16384\n"
        "ds_read_b128 v[132:135], v4 offset:16640\n"
        "ds_read_b128 v[136:139], v4 offset:16896\n"
        "ds_read_b128 v[140:143], v4 offset:17152\n"
        "s_add_u32 m0, m0, 4096\n"
        "s_add_u32 s54, s77, 49152\n"
        "s_and_b32 s54, s54, 0xffff\n"
        "s_add_u32 s54, s54, s60\n"
        "buffer_load_dwordx4 v0, %[srd], s54 offen sc1 lds\n"
        "s_waitcnt lgkmcnt(6)\n"
        "v_mfma_f32_16x16x32_bf16 v[160:163], v[108:111], v[144:147], v[160:163]\n"
        "v_mfma_f32_16x16x32_bf16 v[176:179], v[144:147], a[172:175], v[176:179]\n"
        "ds_read_b128 v[144:147], v4 offset:20480\n"
        "v_mfma_f32_16x16x32_bf16 v[164:167], v[108:111], v[148:151], v[164:167]\n"
        "v_mfma_f32_16x16x32_bf16 v[180:183], v[148:151], a[172:175], v[180:183]\n"
        "ds_read_b128 v[148:151], v4 offset:20736\n"
        "s_add_u32 m0, m0, 4096\n"
        "s_add_u32 s54, s77, 53248\n"
        "s_and_b32 s54, s54, 0xffff\n"
        "s_add_u32 s54, s54, s60\n"
        "buffer_load_dwordx4 v0, %[srd], s54 offen sc1 lds\n"
        "s_waitcnt lgkmcnt(6)\n"
        "v_mfma_f32_16x16x32_bf16 v[168:171], v[108:111], v[152:155], v[168:171]\n"
        "v_mfma_f32_16x16x32_bf16 v[184:187], v[152:155], a[172:175], v[184:187]\n"
        "ds_read_b128 v[152:155], v4 offset:20992\n"
        "v_mfma_f32_16x16x32_bf16 v[172:175], v[108:111], v[156:159], v[172:175]\n"
        "v_mfma_f32_16x16x32_bf16 v[188:191], v[156:159], a[172:175], v[188:191]\n"
        "ds_read_b128 v[156:159], v4 offset:21248\n"
        "s_add_u32 s54, s66, 0\n"
        "buffer_load_dwordx2 v[248:249], v6, %[srd], s54 offen sc1\n"
        "s_waitcnt lgkmcnt(6)\n"
        "v_mfma_f32_16x16x32_bf16 v[160:163], v[112:115], v[128:131], v[160:163]\n"
        "v_mfma_f32_16x16x32_bf16 v[176:179], v[128:131], a[176:179], v[176:179]\n"
        "ds_read_b128 v[128:131], v4 offset:24576\n"
        "v_mfma_f32_16x16x32_bf16 v[164:167], v[112:115], v[132:135], v[164:167]\n"
        "v_mfma_f32_16x16x32_bf16 v[180:183], v[132:135], a[176:179], v[180:183]\n"
        "ds_read_b128 v[132:135], v4 offset:24832\n"
        "buffer_load_dwordx2 v[250:251], v6, %[srd], s54 offen offset:512 sc1\n"
        "s_waitcnt lgkmcnt(6)\n"
        "v_mfma_f32_16x16x32_bf16 v[168:171], v[112:115], v[136:139], v[168:171]\n"
        "v_mfma_f32_16x16x32_bf16 v[184:187], v[136:139], a[176:179], v[184:187]\n"
        "ds_read_b128 v[136:139], v4 offset:25088\n"
        "v_mfma_f32_16x16x32_bf16 v[172:175], v[112:115], v[140:143], v[172:175]\n"
        "v_mfma_f32_16x16x32_bf16 v[188:191], v[140:143], a[176:179], v[188:191]\n"
        "ds_read_b128 v[140:143], v4 offset:25344\n"
        "s_waitcnt lgkmcnt(6)\n"
        "v_mfma_f32_16x16x32_bf16 v[160:163], v[116:119], v[144:147], v[160:163]\n"
        "v_mfma_f32_16x16x32_bf16 v[176:179], v[144:147], a[180:183], v[176:179]\n"
        "v_mfma_f32_16x16x32_bf16 v[164:167], v[116:119], v[148:151], v[164:167]\n"
        "v_mfma_f32_16x16x32_bf16 v[180:183], v[148:151], a[180:183], v[180:183]\n"
        "s_waitcnt lgkmcnt(4)\n"
        "v_mfma_f32_16x16x32_bf16 v[168:171], v[116:119], v[152:155], v[168:171]\n"
        "v_mfma_f32_16x16x32_bf16 v[184:187], v[152:155], a[180:183], v[184:187]\n"
        "v_mfma_f32_16x16x32_bf16 v[172:175], v[116:119], v[156:159], v[172:175]\n"
        "v_mfma_f32_16x16x32_bf16 v[188:191], v[156:159], a[180:183], v[188:191]\n"
        "s_waitcnt vmcnt(7)\n"
        "s_barrier\n"
        "ds_read_b128 v[144:147], v4 offset:28672\n"
        "ds_read_b128 v[148:151], v4 offset:28928\n"
        "ds_read_b128 v[152:155], v4 offset:29184\n"
        "ds_read_b128 v[156:159], v4 offset:29440\n"
        "s_waitcnt lgkmcnt(6)\n"
        "v_mfma_f32_16x16x32_bf16 v[160:163], v[120:123], v[128:131], v[160:163]\n"
        "v_mfma_f32_16x16x32_bf16 v[176:179], v[128:131], a[184:187], v[176:179]\n"
        "ds_read_b128 v[128:131], v4 offset:32768\n"
        "v_mfma_f32_16x16x32_bf16 v[164:167], v[120:123], v[132:135], v[164:167]\n"
        "v_mfma_f32_16x16x32_bf16 v[180:183], v[132:135], a[184:187], v[180:183]\n"
        "ds_read_b128 v[132:135], v4 offset:33024\n"
        "s_waitcnt lgkmcnt(6)\n"
        "v_mfma_f32_16x16x32_bf16 v[168:171], v[120:123], v[136:139], v[168:171]\n"
        "v_mfma_f32_16x16x32_bf16 v[184:187], v[136:139], a[184:187], v[184:187]\n"
        "ds_read_b128 v[136:139], v4 offset:33280\n"
        "v_mfma_f32_16x16x32_bf16 v[172:175], v[120:123], v[140:143], v[172:175]\n"
        "v_mfma_f32_16x16x32_bf16 v[188:191], v[140:143], a[184:187], v[188:191]\n"
        "ds_read_b128 v[140:143], v4 offset:33536\n"
        "s_waitcnt lgkmcnt(6)\n"
        "v_mfma_f32_16x16x32_bf16 v[160:163], v[124:127], v[144:147], v[160:163]\n"
        "v_mfma_f32_16x16x32_bf16 v[176:179], v[144:147], a[188:191], v[176:179]\n"
        "ds_read_b128 v[144:147], v4 offset:36864\n"
        "v_mfma_f32_16x16x32_bf16 v[164:167], v[124:127], v[148:151], v[164:167]\n"
        "v_mfma_f32_16x16x32_bf16 v[180:183], v[148:151], a[188:191], v[180:183]\n"
        "ds_read_b128 v[148:151], v4 offset:37120\n"
        "s_waitcnt lgkmcnt(6)\n"
        "v_mfma_f32_16x16x32_bf16 v[168:171], v[124:127], v[152:155], v[168:171]\n"
        "v_mfma_f32_16x16x32_bf16 v[184:187], v[152:155], a[188:191], v[184:187]\n"
        "ds_read_b128 v[152:155], v4 offset:37376\n"
        "v_mfma_f32_16x16x32_bf16 v[172:175], v[124:127], v[156:159], v[172:175]\n"
        "v_mfma_f32_16x16x32_bf16 v[188:191], v[156:159], a[188:191], v[188:191]\n"
        "ds_read_b128 v[156:159], v4 offset:37632\n"
        "s_waitcnt lgkmcnt(6)\n"
        "v_mfma_f32_16x16x32_bf16 v[160:163], a[128:131], v[128:131], v[160:163]\n"
        "v_mfma_f32_16x16x32_bf16 v[176:179], v[128:131], a[192:195], v[176:179]\n"
        "ds_read_b128 v[128:131], v4 offset:40960\n"
        "v_mfma_f32_16x16x32_bf16 v[164:167], a[128:131], v[132:135], v[164:167]\n"
        "v_mfma_f32_16x16x32_bf16 v[180:183], v[132:135], a[192:195], v[180:183]\n"
        "ds_read_b128 v[132:135], v4 offset:41216\n"
        "s_waitcnt lgkmcnt(6)\n"
        "v_mfma_f32_16x16x32_bf16 v[168:171], a[128:131], v[136:139], v[168:171]\n"
        "v_mfma_f32_16x16x32_bf16 v[184:187], v[136:139], a[192:195], v[184:187]\n"
        "ds_read_b128 v[136:139], v4 offset:41472\n"
        "v_mfma_f32_16x16x32_bf16 v[172:175], a[128:131], v[140:143], v[172:175]\n"
        "v_mfma_f32_16x16x32_bf16 v[188:191], v[140:143], a[192:195], v[188:191]\n"
        "ds_read_b128 v[140:143], v4 offset:41728\n"
        "s_waitcnt lgkmcnt(6)\n"
        "v_mfma_f32_16x16x32_bf16 v[160:163], a[132:135], v[144:147], v[160:163]\n"
        "v_mfma_f32_16x16x32_bf16 v[176:179], v[144:147], a[196:199], v[176:179]\n"
        "v_mfma_f32_16x16x32_bf16 v[164:167], a[132:135], v[148:151], v[164:167]\n"
        "v_mfma_f32_16x16x32_bf16 v[180:183], v[148:151], a[196:199], v[180:183]\n"
        "s_waitcnt lgkmcnt(4)\n"
        "v_mfma_f32_16x16x32_bf16 v[168:171], a[132:135], v[152:155], v[168:171]\n"
        "v_mfma_f32_16x16x32_bf16 v[184:187], v[152:155], a[196:199], v[184:187]\n"
        "v_mfma_f32_16x16x32_bf16 v[172:175], a[132:135], v[156:159], v[172:175]\n"
        "v_mfma_f32_16x16x32_bf16 v[188:191], v[156:159], a[196:199], v[188:191]\n"
        "s_waitcnt vmcnt(0)\n"
        "s_waitcnt lgkmcnt(0)\n"
        "v_mfma_f32_16x16x32_bf16 v[160:163], a[136:139], v[128:131], v[160:163]\n"
        "v_pk_add_f32 v[248:249], v[248:249], v[250:251]\n"
        "v_mov_b32 v253, v248\n"
        "v_mfma_f32_16x16x32_bf16 v[176:179], v[128:131], a[200:203], v[176:179]\n"
        "v_mov_b32 v254, v249\n"
        "v_permlane16_swap_b32 v248, v253\n"
        "s_nop 0\n"
        "v_permlane16_swap_b32 v249, v254\n"
        "v_mfma_f32_16x16x32_bf16 v[164:167], a[136:139], v[132:135], v[164:167]\n"
        "v_add_f32 v248, v248, v253\n"
        "v_add_f32 v249, v249, v254\n"
        "v_mfma_f32_16x16x32_bf16 v[180:183], v[132:135], a[200:203], v[180:183]\n"
        "v_mov_b32 v253, v248\n"
        "v_mov_b32 v254, v249\n"
        "s_nop 0\n"
        "v_permlane32_swap_b32 v248, v253\n"
        "v_mfma_f32_16x16x32_bf16 v[168:171], a[136:139], v[136:139], v[168:171]\n"
        "v_permlane32_swap_b32 v249, v254\n"
        "v_add_f32 v248, v248, v253\n"
        "v_mfma_f32_16x16x32_bf16 v[184:187], v[136:139], a[200:203], v[184:187]\n"
        "v_add_f32 v249, v249, v254\n"
        "v_mul_f32 v248, 0x3b000000, v248\n"
        "v_mul_f32 v249, 0x3b000000, v249\n"
        "v_mfma_f32_16x16x32_bf16 v[172:175], a[136:139], v[140:143], v[172:175]\n"
        "v_fma_f32 v249, -v248, v248, v249\n"
        "v_and_b32 v249, 0x7fffffff, v249\n"
        "v_mfma_f32_16x16x32_bf16 v[188:191], v[140:143], a[200:203], v[188:191]\n"
        "v_add_f32 v249, 0x3727c5ac, v249\n"
        "v_rsq_f32 v249, v249\n"
        "s_mov_b64 exec, 0xffff\n"
        "ds_write2_b32 v7, v248, v249 offset1:64\n"
        "s_mov_b64 exec, -1\n"
        "s_waitcnt lgkmcnt(0)\n"
        "s_barrier\n"
        "ds_read_b128 v[144:147], v4 offset:45056\n"
        "ds_read_b128 v[148:151], v4 offset:45312\n"
        "ds_read_b128 v[152:155], v4 offset:45568\n"
        "ds_read_b128 v[156:159], v4 offset:45824\n"
        "ds_read_b128 v[128:131], v4 offset:49152\n"
        "ds_read_b128 v[132:135], v4 offset:49408\n"
        "ds_read_b128 v[136:139], v4 offset:49664\n"
        "ds_read_b128 v[140:143], v4 offset:49920\n"
        "s_waitcnt lgkmcnt(6)\n"
        "v_mfma_f32_16x16x32_bf16 v[160:163], a[140:143], v[144:147], v[160:163]\n"
        "v_mfma_f32_16x16x32_bf16 v[176:179], v[144:147], a[204:207], v[176:179]\n"
        "ds_read_b128 v[144:147], v4 offset:53248\n"
        "v_mfma_f32_16x16x32_bf16 v[164:167], a[140:143], v[148:151], v[164:167]\n"
        "v_mfma_f32_16x16x32_bf16 v[180:183], v[148:151], a[204:207], v[180:183]\n"
        "ds_read_b128 v[148:151], v4 offset:53504\n"
        "s_add_u32 s54, s72, 2629632\n"
        "buffer_load_dwordx4 a[224:227], v0, %[srd], s54 offen offset:0 sc1\n"
        "s_waitcnt lgkmcnt(6)\n"
        "v_mfma_f32_16x16x32_bf16 v[168:171], a[140:143], v[152:155], v[168:171]\n"
        "v_mfma_f32_16x16x32_bf16 v[184:187], v[152:155], a[204:207], v[184:187]\n"
        "ds_read_b128 v[152:155], v4 offset:53760\n"
        "v_mfma_f32_16x16x32_bf16 v[172:175], a[140:143], v[156:159], v[172:175]\n"
        "v_mfma_f32_16x16x32_bf16 v[188:191], v[156:159], a[204:207], v[188:191]\n"
        "ds_read_b128 v[156:159], v4 offset:54016\n"
        "buffer_load_dwordx4 a[228:231], v0, %[srd], s54 offen offset:1024 sc1\n"
        "s_waitcnt lgkmcnt(6)\n"
        "v_mfma_f32_16x16x32_bf16 v[160:163], a[144:147], v[128:131], v[160:163]\n"
        "v_mfma_f32_16x16x32_bf16 v[176:179], v[128:131], a[208:211], v[176:179]\n"
        "ds_read_b128 v[128:131], v4 offset:57344\n"
        "v_mfma_f32_16x16x32_bf16 v[164:167], a[144:147], v[132:135], v[164:167]\n"
        "v_mfma_f32_16x16x32_bf16 v[180:183], v[132:135], a[208:211], v[180:183]\n"
        "ds_read_b128 v[132:135], v4 offset:57600\n"
        "buffer_load_dwordx4 a[232:235], v0, %[srd], s54 offen offset:2048 sc1\n"
        "s_waitcnt lgkmcnt(6)\n"
        "v_mfma_f32_16x16x32_bf16 v[168:171], a[144:147], v[136:139], v[168:171]\n"
        "v_mfma_f32_16x16x32_bf16 v[184:187], v[136:139], a[208:211], v[184:187]\n"
        "ds_read_b128 v[136:139], v4 offset:57856\n"
        "v_mfma_f32_16x16x32_bf16 v[172:175], a[144:147], v[140:143], v[172:175]\n"
        "v_mfma_f32_16x16x32_bf16 v[188:191], v[140:143], a[208:211], v[188:191]\n"
        "ds_read_b128 v[140:143], v4 offset:58112\n"
        "buffer_load_dwordx4 a[236:239], v0, %[srd], s54 offen offset:3072 sc1\n"
        "s_waitcnt lgkmcnt(6)\n"
        "v_mfma_f32_16x16x32_bf16 v[160:163], a[148:151], v[144:147], v[160:163]\n"
        "v_mfma_f32_16x16x32_bf16 v[176:179], v[144:147], a[212:215], v[176:179]\n"
        "ds_read_b128 v[144:147], v4 offset:61440\n"
        "v_mfma_f32_16x16x32_bf16 v[164:167], a[148:151], v[148:151], v[164:167]\n"
        "v_mfma_f32_16x16x32_bf16 v[180:183], v[148:151], a[212:215], v[180:183]\n"
        "ds_read_b128 v[148:151], v4 offset:61696\n"
        "s_add_u32 s54, s72, 2633728\n"
        "buffer_load_dwordx4 a[240:243], v0, %[srd], s54 offen offset:0 sc1\n"
        "s_waitcnt lgkmcnt(6)\n"
        "v_mfma_f32_16x16x32_bf16 v[168:171], a[148:151], v[152:155], v[168:171]\n"
        "v_mfma_f32_16x16x32_bf16 v[184:187], v[152:155], a[212:215], v[184:187]\n"
        "ds_read_b128 v[152:155], v4 offset:61952\n"
        "v_mfma_f32_16x16x32_bf16 v[172:175], a[148:151], v[156:159], v[172:175]\n"
        "v_mfma_f32_16x16x32_bf16 v[188:191], v[156:159], a[212:215], v[188:191]\n"
        "ds_read_b128 v[156:159], v4 offset:62208\n"
        "buffer_load_dwordx4 a[244:247], v0, %[srd], s54 offen offset:1024 sc1\n"
        "s_waitcnt lgkmcnt(6)\n"
        "v_mfma_f32_16x16x32_bf16 v[160:163], a[152:155], v[128:131], v[160:163]\n"
        "v_mfma_f32_16x16x32_bf16 v[176:179], v[128:131], a[216:219], v[176:179]\n"
        "ds_read2_b32 v[240:241], v8 offset0:0 offset1:64\n"
        "ds_read2_b32 v[242:243], v8 offset0:16 offset1:80\n"
        "v_mfma_f32_16x16x32_bf16 v[164:167], a[152:155], v[132:135], v[164:167]\n"
        "v_mfma_f32_16x16x32_bf16 v[180:183], v[132:135], a[216:219], v[180:183]\n"
        "ds_read2_b32 v[244:245], v8 offset0:32 offset1:96\n"
        "ds_read2_b32 v[246:247], v8 offset0:48 offset1:112\n"
        "buffer_load_dwordx4 a[248:251], v0, %[srd], s54 offen offset:2048 sc1\n"
        "s_waitcnt lgkmcnt(8)\n"
        "v_mfma_f32_16x16x32_bf16 v[168:171], a[152:155], v[136:139], v[168:171]\n"
        "v_mfma_f32_16x16x32_bf16 v[184:187], v[136:139], a[216:219], v[184:187]\n"
        "ds_read_b128 v[96:99], v9 offset:0\n"
        "ds_read_b128 v[100:103], v9 offset:256\n"
        "v_mfma_f32_16x16x32_bf16 v[172:175], a[152:155], v[140:143], v[172:175]\n"
        "v_mfma_f32_16x16x32_bf16 v[188:191], v[140:143], a[216:219], v[188:191]\n"
        "ds_read_b128 v[104:107], v9 offset:64\n"
        "ds_read_b128 v[108:111], v9 offset:320\n"
        "buffer_load_dwordx4 a[252:255], v0, %[srd], s54 offen offset:3072 sc1\n"
        "s_waitcnt lgkmcnt(10)\n"
        "v_mfma_f32_16x16x32_bf16 v[160:163], a[156:159], v[144:147], v[160:163]\n"
        "v_mfma_f32_16x16x32_bf16 v[176:179], v[144:147], a[220:223], v[176:179]\n"
        "ds_read_b128 v[112:115], v9 offset:128\n"
        "ds_read_b128 v[116:119], v9 offset:384\n"
        "v_mfma_f32_16x16x32_bf16 v[164:167], a[156:159], v[148:151], v[164:167]\n"
        "v_mfma_f32_16x16x32_bf16 v[180:183], v[148:151], a[220:223], v[180:183]\n"
        "ds_read_b128 v[120:123], v9 offset:192\n"
        "ds_read_b128 v[124:127], v9 offset:448\n"
        "s_waitcnt lgkmcnt(12)\n"
        "v_mfma_f32_16x16x32_bf16 v[168:171], a[156:159], v[152:155], v[168:171]\n"
        "v_mfma_f32_16x16x32_bf16 v[184:187], v[152:155], a[220:223], v[184:187]\n"
        "v_mfma_f32_16x16x32_bf16 v[172:175], a[156:159], v[156:159], v[172:175]\n"
        "v_mfma_f32_16x16x32_bf16 v[188:191], v[156:159], a[220:223], v[188:191]\n"
        "s_waitcnt lgkmcnt(0)\n"
        "v_pk_fma_f32 v[160:161], v[240:241], v[204:205], v[160:161] op_sel_hi:[0,1,1] neg_lo:[1,0,0] neg_hi:[1,0,0]\n"
        "v_pk_fma_f32 v[162:163], v[240:241], v[206:207], v[162:163] op_sel_hi:[0,1,1] neg_lo:[1,0,0] neg_hi:[1,0,0]\n"
        "v_pk_fma_f32 v[164:165], v[242:243], v[204:205], v[164:165] op_sel_hi:[0,1,1] neg_lo:[1,0,0] neg_hi:[1,0,0]\n"
        "v_pk_fma_f32 v[166:167], v[242:243], v[206:207], v[166:167] op_sel_hi:[0,1,1] neg_lo:[1,0,0] neg_hi:[1,0,0]\n"
        "v_pk_fma_f32 v[168:169], v[244:245], v[204:205], v[168:169] op_sel_hi:[0,1,1] neg_lo:[1,0,0] neg_hi:[1,0,0]\n"
        "v_pk_fma_f32 v[170:171], v[244:245], v[206:207], v[170:171] op_sel_hi:[0,1,1] neg_lo:[1,0,0] neg_hi:[1,0,0]\n"
        "v_pk_fma_f32 v[172:173], v[246:247], v[204:205], v[172:173] op_sel_hi:[0,1,1] neg_lo:[1,0,0] neg_hi:[1,0,0]\n"
        "v_pk_fma_f32 v[174:175], v[246:247], v[206:207], v[174:175] op_sel_hi:[0,1,1] neg_lo:[1,0,0] neg_hi:[1,0,0]\n"
        "v_pk_mul_f32 v[160:161], v[240:241], v[160:161] op_sel:[1,0] op_sel_hi:[1,1]\n"
        "v_pk_mul_f32 v[162:163], v[240:241], v[162:163] op_sel:[1,0] op_sel_hi:[1,1]\n"
        "v_pk_mul_f32 v[164:165], v[242:243], v[164:165] op_sel:[1,0] op_sel_hi:[1,1]\n"
        "v_pk_mul_f32 v[166:167], v[242:243], v[166:167] op_sel:[1,0] op_sel_hi:[1,1]\n"
        "v_pk_mul_f32 v[168:169], v[244:245], v[168:169] op_sel:[1,0] op_sel_hi:[1,1]\n"
        "v_pk_mul_f32 v[170:171], v[244:245], v[170:171] op_sel:[1,0] op_sel_hi:[1,1]\n"
        "v_pk_mul_f32 v[172:173], v[246:247], v[172:173] op_sel:[1,0] op_sel_hi:[1,1]\n"
        "v_pk_mul_f32 v[174:175], v[246:247], v[174:175] op_sel:[1,0] op_sel_hi:[1,1]\n"
        "v_pk_fma_f32 v[176:177], v[96:97], v[216:217], v[176:177] op_sel_hi:[1,0,1] neg_lo:[1,0,0] neg_hi:[1,0,0]\n"
        "v_pk_fma_f32 v[178:179], v[98:99], v[216:217], v[178:179] op_sel_hi:[1,0,1] neg_lo:[1,0,0] neg_hi:[1,0,0]\n"
        "v_pk_fma_f32 v[180:181], v[104:105], v[216:217], v[180:181] op_sel_hi:[1,0,1] neg_lo:[1,0,0] neg_hi:[1,0,0]\n"
        "v_pk_fma_f32 v[182:183], v[106:107], v[216:217], v[182:183] op_sel_hi:[1,0,1] neg_lo:[1,0,0] neg_hi:[1,0,0]\n"
        "v_pk_fma_f32 v[184:185], v[112:113], v[216:217], v[184:185] op_sel_hi:[1,0,1] neg_lo:[1,0,0] neg_hi:[1,0,0]\n"
        "v_pk_fma_f32 v[186:187], v[114:115], v[216:217], v[186:187] op_sel_hi:[1,0,1] neg_lo:[1,0,0] neg_hi:[1,0,0]\n"
        "v_pk_fma_f32 v[188:189], v[120:121], v[216:217], v[188:189] op_sel_hi:[1,0,1] neg_lo:[1,0,0] neg_hi:[1,0,0]\n"
        "v_pk_fma_f32 v[190:191], v[122:123], v[216:217], v[190:191] op_sel_hi:[1,0,1] neg_lo:[1,0,0] neg_hi:[1,0,0]\n"
        "v_cmp_u_f32 vcc, v160, v164\n"
        "v_cmp_u_f32 s[62:63], v168, v172\n"
        "s_nop 3\n"
        "s_or_b64 vcc, vcc, s[62:63]\n"
        "s_cbranch_vccnz Lscbad_%=\n"
        "; ---- publish k, v; q GEMM; softmax guard of layer B (falls back to slowB); own and window steps\n"
        "Lscok_%=:\n"
        "v_pk_mul_f32 v[176:177], v[100:101], v[176:177]\n"
        "v_pk_mul_f32 v[178:179], v[102:103], v[178:179]\n"
        "v_pk_mul_f32 v[180:181], v[108:109], v[180:181]\n"
        "v_pk_mul_f32 v[182:183], v[110:111], v[182:183]\n"
        "v_pk_mul_f32 v[184:185], v[116:117], v[184:185]\n"
        "v_pk_mul_f32 v[186:187], v[118:119], v[186:187]\n"
        "v_pk_mul_f32 v[188:189], v[124:125], v[188:189]\n"
        "v_pk_mul_f32 v[190:191], v[126:127], v[190:191]\n"
        "v_cvt_pk_bf16_f32 v200, v160, v161\n"
        "v_cvt_pk_bf16_f32 v201, v162, v163\n"
        "v_cvt_pk_bf16_f32 v202, v164, v165\n"
        "v_cvt_pk_bf16_f32 v203, v166, v167\n"
        "v_cvt_pk_bf16_f32 v204, v168, v169\n"
        "v_cvt_pk_bf16_f32 v205, v170, v171\n"
        "v_cvt_pk_bf16_f32 v206, v172, v173\n"
        "v_cvt_pk_bf16_f32 v207, v174, v175\n"
        "v_cvt_pk_bf16_f32 v208, v176, v177\n"
        "v_cvt_pk_bf16_f32 v209, v178, v179\n"
        "v_cvt_pk_bf16_f32 v210, v180, v181\n"
        "v_cvt_pk_bf16_f32 v211, v182, v183\n"
        "v_permlane16_swap_b32 v200, v202\n"
        "v_permlane16_swap_b32 v201, v203\n"
        "v_permlane16_swap_b32 v204, v206\n"
        "v_permlane16_swap_b32 v205, v207\n"
        "v_cvt_pk_bf16_f32 v216, v184, v185\n"
        "v_cvt_pk_bf16_f32 v217, v186, v187\n"
        "v_cvt_pk_bf16_f32 v218, v188, v189\n"
        "v_cvt_pk_bf16_f32 v219, v190, v191\n"
        "v_permlane16_swap_b32 v208, v210\n"
        "v_permlane16_swap_b32 v209, v211\n"
        "v_min_u32 v200, -2, v200\n"
        "v_min_u32 v201, -2, v201\n"
        "v_min_u32 v202, -2, v202\n"
        "v_min_u32 v203, -2, v203\n"
        "v_min_u32 v204, -2, v204\n"
        "v_min_u32 v205, -2, v205\n"
        "v_min_u32 v206, -2, v206\n"
        "v_min_u32 v207, -2, v207\n"
        "v_permlane16_swap_b32 v216, v218\n"
        "v_permlane16_swap_b32 v217, v219\n"
        "s_add_u32 s54, s68, 0x10000\n"
        "buffer_store_dwordx4 v[200:203], v0, %[srd], s68 offen sc1\n"
        "buffer_store_dwordx4 v[204:207], v0, %[srd], s54 offen sc1\n"
        "v_min_u32 v208, -2, v208\n"
        "v_min_u32 v209, -2, v209\n"
        "v_min_u32 v210, -2, v210\n"
        "v_min_u32 v211, -2, v211\n"
        "buffer_store_dwordx4 v[208:211], v13, %[srd], s68 offen sc1\n"
        "v_min_u32 v216, -2, v216\n"
        "v_min_u32 v217, -2, v217\n"
        "v_min_u32 v218, -2, v218\n"
        "v_min_u32 v219, -2, v219\n"
        "buffer_store_dwordx4 v[216:219], v13, %[srd], s54 offen sc1\n"
        "s_mov_b32 s58, 0xffff\n"
        "s_mov_b32 s59, 0xffff\n"
        "v_max3_f32 v250, |v176|, |v177|, |v178|\n"
        "v_max3_f32 v250, v250, |v179|, |v180|\n"
        "v_max3_f32 v250, v250, |v181|, |v182|\n"
        "v_max3_f32 v250, v250, |v183|, |v184|\n"
        "v_max3_f32 v250, v250, |v185|, |v186|\n"
        "v_max3_f32 v250, v250, |v187|, |v188|\n"
        "v_max3_f32 v250, v250, |v189|, |v190|\n"
        "v_max_f32 v251, v250, |v191|\n"
        "v_pk_mul_f32 v[176:177], v[160:161], v[160:161]\n"
        "v_pk_mul_f32 v[178:179], v[164:165], v[164:165]\n"
        "v_pk_mul_f32 v[180:181], v[168:169], v[168:169]\n"
        "v_pk_mul_f32 v[182:183], v[172:173], v[172:173]\n"
        "v_pk_fma_f32 v[176:177], v[162:163], v[162:163], v[176:177]\n"
        "v_pk_fma_f32 v[178:179], v[166:167], v[166:167], v[178:179]\n"
        "v_pk_fma_f32 v[180:181], v[170:171], v[170:171], v[180:181]\n"
        "v_pk_fma_f32 v[182:183], v[174:175], v[174:175], v[182:183]\n"
        "v_add_f32 v176, v176, v177\n"
        "v_add_f32 v178, v178, v179\n"
        "v_add_f32 v180, v180, v181\n"
        "v_add_f32 v182, v182, v183\n"
        "s_nop 0\n"
        "v_permlane32_swap_b32 v176, v180\n"
        "v_max_u32_dpp v251, v251, v251 row_ror:8 row_mask:0xf bank_mask:0xf\n"
        "v_permlane32_swap_b32 v178, v182\n"
        "s_nop 0\n"
        "v_max_u32_dpp v251, v251, v251 row_ror:4 row_mask:0xf bank_mask:0xf\n"
        "v_add_f32 v176, v176, v180\n"
        "s_nop 0\n"
        "v_max_u32_dpp v251, v251, v251 row_ror:2 row_mask:0xf bank_mask:0xf\n"
        "v_add_f32 v178, v178, v182\n"
        "s_nop 0\n"
        "v_max_u32_dpp v251, v251, v251 row_ror:1 row_mask:0xf bank_mask:0xf\n"
        "v_permlane16_swap_b32 v176, v178\n"
        "v_mov_b32 v184, v251\n"
        "v_add_f32 v176, v176, v178\n"
        "s_nop 0\n"
        "v_permlane16_swap_b32 v251, v184\n"
        "v_max_u32_dpp v176, v176, v176 row_ror:8 row_mask:0xf bank_mask:0xf\n"
        "v_max_u32 v251, v251, v184\n"
        "s_nop 0\n"
        "v_max_u32_dpp v176, v176, v176 row_ror:4 row_mask:0xf bank_mask:0xf\n"
        "v_mov_b32 v184, v251\n"
        "s_nop 0\n"
        "v_max_u32_dpp v176, v176, v176 row_ror:2 row_mask:0xf bank_mask:0xf\n"
        "v_permlane32_swap_b32 v251, v184\n"
        "s_nop 0\n"
        "v_max_u32_dpp v176, v176, v176 row_ror:1 row_mask:0xf bank_mask:0xf\n"
        "v_max_u32 v251, v251, v184\n"
        "v_mov_b32 v177, v176\n"
        "s_nop 1\n"
        "v_permlane16_swap_b32 v176, v177\n"
        "v_max_u32 v176, v176, v177\n"
        "v_mov_b32 v177, v176\n"
        "s_nop 1\n"
        "v_permlane32_swap_b32 v176, v177\n"
        "v_max_u32 v176, v176, v177\n"
        "v_min_u32 v250, -2, v176\n"
        "v_min_u32 v251, -2, v251\n"
        "v_lshrrev_b32 v184, 1, v0\n"
        "s_mov_b64 exec, 0xffff\n"
        "buffer_store_dwordx2 v[250:251], v184, %[srd], s71 offen sc1\n"
        "s_mov_b64 exec, -1\n"
        "v_mov_b32 v212, v225\n"
        "v_mov_b32 v213, v225\n"
        "v_mov_b32 v214, v225\n"
        "v_mov_b32 v215, v225\n"
        "v_mov_b32 v220, v225\n"
        "v_mov_b32 v221, v225\n"
        "v_mov_b32 v222, v225\n"
        "v_mov_b32 v223, v225\n"
        "v_permlane16_swap_b32 v208, v212\n"
        "v_permlane16_swap_b32 v209, v213\n"
        "v_permlane16_swap_b32 v210, v214\n"
        "v_permlane16_swap_b32 v211, v215\n"
        "v_permlane16_swap_b32 v216, v220\n"
        "v_permlane16_swap_b32 v217, v221\n"
        "v_permlane16_swap_b32 v218, v222\n"
        "v_permlane16_swap_b32 v219, v223\n"
        "ds_read_b128 v[128:131], v4 offset:0\n"
        "ds_read_b128 v[132:135], v4 offset:256\n"
        "ds_read_b128 v[136:139], v4 offset:512\n"
        "ds_read_b128 v[140:143], v4 offset:768\n"
        "ds_read_b128 v[144:147], v4 offset:4096\n"
        "ds_read_b128 v[148:151], v4 offset:4352\n"
        "ds_read_b128 v[152:155], v4 offset:4608\n"
        "ds_read_b128 v[156:159], v4 offset:4864\n"
        "s_waitcnt lgkmcnt(6)\n"
        "v_mfma_f32_16x16x32_bf16 v[160:163], v[16:19], v[128:131], 0\n"
        "ds_read_b128 v[128:131], v4 offset:8192\n"
        "v_mfma_f32_16x16x32_bf16 v[164:167], v[16:19], v[132:135], 0\n"
        "ds_read_b128 v[132:135], v4 offset:8448\n"
        "s_waitcnt lgkmcnt(6)\n"
        "v_mfma_f32_16x16x32_bf16 v[168:171], v[16:19], v[136:139], 0\n"
        "ds_read_b128 v[136:139], v4 offset:8704\n"
        "v_mfma_f32_16x16x32_bf16 v[172:175], v[16:19], v[140:143], 0\n"
        "ds_read_b128 v[140:143], v4 offset:8960\n"
        "s_waitcnt lgkmcnt(6)\n"
        "v_mfma_f32_16x16x32_bf16 v[160:163], v[20:23], v[144:147], v[160:163]\n"
        "ds_read_b128 v[144:147], v4 offset:12288\n"
        "v_mfma_f32_16x16x32_bf16 v[164:167], v[20:23], v[148:151], v[164:167]\n"
        "ds_read_b128 v[148:151], v4 offset:12544\n"
        "s_waitcnt lgkmcnt(6)\n"
        "v_mfma_f32_16x16x32_bf16 v[168:171], v[20:23], v[152:155], v[168:171]\n"
        "ds_read_b128 v[152:155], v4 offset:12800\n"
        "v_mfma_f32_16x16x32_bf16 v[172:175], v[20:23], v[156:159], v[172:175]\n"
        "ds_read_b128 v[156:159], v4 offset:13056\n"
        "s_waitcnt lgkmcnt(6)\n"
        "v_mfma_f32_16x16x32_bf16 v[160:163], v[24:27], v[128:131], v[160:163]\n"
        "ds_read_b128 v[128:131], v4 offset:16384\n"
        "v_mfma_f32_16x16x32_bf16 v[164:167], v[24:27], v[132:135], v[164:167]\n"
        "ds_read_b128 v[132:135], v4 offset:16640\n"
        "s_waitcnt lgkmcnt(6)\n"
        "v_mfma_f32_16x16x32_bf16 v[168:171], v[24:27], v[136:139], v[168:171]\n"
        "ds_read_b128 v[136:139], v4 offset:16896\n"
        "v_mfma_f32_16x16x32_bf16 v[172:175], v[24:27], v[140:143], v[172:175]\n"
        "ds_read_b128 v[140:143], v4 offset:17152\n"
        "s_waitcnt lgkmcnt(6)\n"
        "v_mfma_f32_16x16x32_bf16 v[160:163], v[28:31], v[144:147], v[160:163]\n"
        "ds_read_b128 v[144:147], v4 offset:20480\n"
        "v_mfma_f32_16x16x32_bf16 v[164:167], v[28:31], v[148:151], v[164:167]\n"
        "ds_read_b128 v[148:151], v4 offset:20736\n"
        "s_waitcnt lgkmcnt(6)\n"
        "v_mfma_f32_16x16x32_bf16 v[168:171], v[28:31], v[152:155], v[168:171]\n"
        "ds_read_b128 v[152:155], v4 offset:20992\n"
        "v_mfma_f32_16x16x32_bf16 v[172:175], v[28:31], v[156:159], v[172:175]\n"
        "ds_read_b128 v[156:159], v4 offset:21248\n"
        "s_waitcnt lgkmcnt(6)\n"
        "v_mfma_f32_16x16x32_bf16 v[160:163], v[32:35], v[128:131], v[160:163]\n"
        "ds_read_b128 v[128:131], v4 offset:24576\n"
        "v_mfma_f32_16x16x32_bf16 v[164:167], v[32:35], v[132:135], v[164:167]\n"
        "ds_read_b128 v[132:135], v4 offset:24832\n"
        "s_waitcnt lgkmcnt(6)\n"
        "v_mfma_f32_16x16x32_bf16 v[168:171], v[32:35], v[136:139], v[168:171]\n"
        "ds_read_b128 v[136:139], v4 offset:25088\n"
        "v_mfma_f32_16x16x32_bf16 v[172:175], v[32:35], v[140:143], v[172:175]\n"
        "ds_read_b128 v[140:143], v4 offset:25344\n"
        "s_waitcnt lgkmcnt(6)\n"
        "v_mfma_f32_16x16x32_bf16 v[160:163], v[36:39], v[144:147], v[160:163]\n"
        "ds_read_b128 v[144:147], v4 offset:28672\n"
        "v_mfma_f32_16x16x32_bf16 v[164:167], v[36:39], v[148:151], v[164:167]\n"
        "ds_read_b128 v[148:151], v4 offset:28928\n"
        "s_waitcnt lgkmcnt(6)\n"
        "v_mfma_f32_16x16x32_bf16 v[168:171], v[36:39], v[152:155], v[168:171]\n"
        "ds_read_b128 v[152:155], v4 offset:29184\n"
        "v_mfma_f32_16x16x32_bf16 v[172:175], v[36:39], v[156:159], v[172:175]\n"
        "ds_read_b128 v[156:159], v4 offset:29440\n"
        "s_waitcnt lgkmcnt(6)\n"
        "v_mfma_f32_16x16x32_bf16 v[160:163], v[40:43], v[128:131], v[160:163]\n"
        "ds_read_b128 v[128:131], v4 offset:32768\n"
        "v_mfma_f32_16x16x32_bf16 v[164:167], v[40:43], v[132:135], v[164:167]\n"
        "ds_read_b128 v[132:135], v4 offset:33024\n"
        "s_waitcnt lgkmcnt(6)\n"
        "v_mfma_f32_16x16x32_bf16 v[168:171], v[40:43], v[136:139], v[168:171]\n"
        "ds_read_b128 v[136:139], v4 offset:33280\n"
        "v_mfma_f32_16x16x32_bf16 v[172:175], v[40:43], v[140:143], v[172:175]\n"
        "ds_read_b128 v[140:143], v4 offset:33536\n"
        "s_waitcnt lgkmcnt(6)\n"
        "v_mfma_f32_16x16x32_bf16 v[160:163], v[44:47], v[144:147], v[160:163]\n"
        "ds_read_b128 v[144:147], v4 offset:36864\n"
        "v_mfma_f32_16x16x32_bf16 v[164:167], v[44:47], v[148:151], v[164:167]\n"
        "ds_read_b128 v[148:151], v4 offset:37120\n"
        "s_waitcnt lgkmcnt(6)\n"
        "v_mfma_f32_16x16x32_bf16 v[168:171], v[44:47], v[152:155], v[168:171]\n"
        "ds_read_b128 v[152:155], v4 offset:37376\n"
        "v_mfma_f32_16x16x32_bf16 v[172:175], v[44:47], v[156:159], v[172:175]\n"
        "ds_read_b128 v[156:159], v4 offset:37632\n"
        "s_waitcnt lgkmcnt(6)\n"
        "v_mfma_f32_16x16x32_bf16 v[160:163], v[48:51], v[128:131], v[160:163]\n"
        "ds_read_b128 v[128:131], v4 offset:40960\n"
        "v_mfma_f32_16x16x32_bf16 v[164:167], v[48:51], v[132:135], v[164:167]\n"
        "ds_read_b128 v[132:135], v4 offset:41216\n"
        "s_waitcnt lgkmcnt(6)\n"
        "v_mfma_f32_16x16x32_bf16 v[168:171], v[48:51], v[136:139], v[168:171]\n"
        "ds_read_b128 v[136:139], v4 offset:41472\n"
        "v_mfma_f32_16x16x32_bf16 v[172:175], v[48:51], v[140:143], v[172:175]\n"
        "ds_read_b128 v[140:143], v4 offset:41728\n"
        "s_waitcnt lgkmcnt(6)\n"
        "v_mfma_f32_16x16x32_bf16 v[160:163], v[52:55], v[144:147], v[160:163]\n"
        "ds_read_b128 v[144:147], v4 offset:45056\n"
        "v_mfma_f32_16x16x32_bf16 v[164:167], v[52:55], v[148:151], v[164:167]\n"
        "ds_read_b128 v[148:151], v4 offset:45312\n"
        "s_waitcnt lgkmcnt(6)\n"
        "v_mfma_f32_16x16x32_bf16 v[168:171], v[52:55], v[152:155], v[168:171]\n"
        "ds_read_b128 v[152:155], v4 offset:45568\n"
        "v_mfma_f32_16x16x32_bf16 v[172:175], v[52:55], v[156:159], v[172:175]\n"
        "ds_read_b128 v[156:159], v4 offset:45824\n"
        "s_waitcnt lgkmcnt(6)\n"
        "v_mfma_f32_16x16x32_bf16 v[160:163], v[56:59], v[128:131], v[160:163]\n"
        "ds_read_b128 v[128:131], v4 offset:49152\n"
        "v_mfma_f32_16x16x32_bf16 v[164:167], v[56:59], v[132:135], v[164:167]\n"
        "ds_read_b128 v[132:135], v4 offset:49408\n"
        "s_waitcnt lgkmcnt(6)\n"
        "v_mfma_f32_16x16x32_bf16 v[168:171], v[56:59], v[136:139], v[168:171]\n"
        "ds_read_b128 v[136:139], v4 offset:49664\n"
        "v_mfma_f32_16x16x32_bf16 v[172:175], v[56:59], v[140:143], v[172:175]\n"
        "ds_read_b128 v[140:143], v4 offset:49920\n"
        "s_waitcnt lgkmcnt(6)\n"
        "v_mfma_f32_16x16x32_bf16 v[160:163], v[60:63], v[144:147], v[160:163]\n"
        "ds_read_b128 v[144:147], v4 offset:53248\n"
        "v_mfma_f32_16x16x32_bf16 v[164:167], v[60:63], v[148:151], v[164:167]\n"
        "ds_read_b128 v[148:151], v4 offset:53504\n"
        "s_waitcnt lgkmcnt(6)\n"
        "v_mfma_f32_16x16x32_bf16 v[168:171], v[60:63], v[152:155], v[168:171]\n"
        "ds_read_b128 v[152:155], v4 offset:53760\n"
        "v_mfma_f32_16x16x32_bf16 v[172:175], v[60:63], v[156:159], v[172:175]\n"
        "ds_read_b128 v[156:159], v4 offset:54016\n"
        "buffer_load_dword v249, v224, %[srd], s70 offen sc1\n"
        "s_waitcnt lgkmcnt(6)\n"
        "v_mfma_f32_16x16x32_bf16 v[160:163], v[64:67], v[128:131], v[160:163]\n"
        "ds_read_b128 v[128:131], v4 offset:57344\n"
        "v_mfma_f32_16x16x32_bf16 v[164:167], v[64:67], v[132:135], v[164:167]\n"
        "ds_read_b128 v[132:135], v4 offset:57600\n"
        "s_waitcnt lgkmcnt(6)\n"
        "v_mfma_f32_16x16x32_bf16 v[168:171], v[64:67], v[136:139], v[168:171]\n"
        "ds_read_b128 v[136:139], v4 offset:57856\n"
        "v_mfma_f32_16x16x32_bf16 v[172:175], v[64:67], v[140:143], v[172:175]\n"
        "ds_read_b128 v[140:143], v4 offset:58112\n"
        "s_waitcnt lgkmcnt(6)\n"
        "v_mfma_f32_16x16x32_bf16 v[160:163], v[68:71], v[144:147], v[160:163]\n"
        "ds_read_b128 v[144:147], v4 offset:61440\n"
        "v_mfma_f32_16x16x32_bf16 v[164:167], v[68:71], v[148:151], v[164:167]\n"
        "ds_read_b128 v[148:151], v4 offset:61696\n"
        "s_waitcnt lgkmcnt(6)\n"
        "v_mfma_f32_16x16x32_bf16 v[168:171], v[68:71], v[152:155], v[168:171]\n"
        "ds_read_b128 v[152:155], v4 offset:61952\n"
        "v_mfma_f32_16x16x32_bf16 v[172:175], v[68:71], v[156:159], v[172:175]\n"
        "ds_read_b128 v[156:159], v4 offset:62208\n"
        "s_waitcnt lgkmcnt(6)\n"
        "v_mfma_f32_16x16x32_bf16 v[160:163], v[72:75], v[128:131], v[160:163]\n"
        "v_mfma_f32_16x16x32_bf16 v[164:167], v[72:75], v[132:135], v[164:167]\n"
        "s_waitcnt lgkmcnt(4)\n"
        "v_mfma_f32_16x16x32_bf16 v[168:171], v[72:75], v[136:139], v[168:171]\n"
        "v_mfma_f32_16x16x32_bf16 v[172:175], v[72:75], v[140:143], v[172:175]\n"
        "s_waitcnt lgkmcnt(2)\n"
        "v_mfma_f32_16x16x32_bf16 v[160:163], v[76:79], v[144:147], v[160:163]\n"
        "v_mfma_f32_16x16x32_bf16 v[164:167], v[76:79], v[148:151], v[164:167]\n"
        "s_waitcnt lgkmcnt(0)\n"
        "v_mfma_f32_16x16x32_bf16 v[168:171], v[76:79], v[152:155], v[168:171]\n"
        "v_mfma_f32_16x16x32_bf16 v[172:175], v[76:79], v[156:159], v[172:175]\n"
        "ds_read_b128 v[16:19], v228 offset:11264\n"
        "ds_read_b128 v[20:23], v228 offset:12288\n"
        "ds_read_b128 v[24:27], v228 offset:13312\n"
        "ds_read_b128 v[28:31], v228 offset:14336\n"
        "v_mov_b32 v32, 0\n"
        "v_mov_b32 v33, 0\n"
        "v_mov_b32 v34, 0\n"
        "v_mov_b32 v35, 0\n"
        "v_mov_b32 v253, 0xdddddddd\n"
        "s_mov_b32 s57, 0\n"
        "v_mul_f32 v241, 0x3eb8aa3b, v241\n"
        "v_mul_f32 v243, 0x3eb8aa3b, v243\n"
        "v_mul_f32 v245, 0x3eb8aa3b, v245\n"
        "v_mul_f32 v247, 0x3eb8aa3b, v247\n"
        "v_pk_fma_f32 v[160:161], v[240:241], v[192:193], v[160:161] op_sel_hi:[0,1,1] neg_lo:[1,0,0] neg_hi:[1,0,0]\n"
        "v_pk_fma_f32 v[162:163], v[240:241], v[194:195], v[162:163] op_sel_hi:[0,1,1] neg_lo:[1,0,0] neg_hi:[1,0,0]\n"
        "v_pk_fma_f32 v[164:165], v[242:243], v[192:193], v[164:165] op_sel_hi:[0,1,1] neg_lo:[1,0,0] neg_hi:[1,0,0]\n"
        "v_pk_fma_f32 v[166:167], v[242:243], v[194:195], v[166:167] op_sel_hi:[0,1,1] neg_lo:[1,0,0] neg_hi:[1,0,0]\n"
        "v_pk_fma_f32 v[168:169], v[244:245], v[192:193], v[168:169] op_sel_hi:[0,1,1] neg_lo:[1,0,0] neg_hi:[1,0,0]\n"
        "v_pk_fma_f32 v[170:171], v[244:245], v[194:195], v[170:171] op_sel_hi:[0,1,1] neg_lo:[1,0,0] neg_hi:[1,0,0]\n"
        "v_pk_fma_f32 v[172:173], v[246:247], v[192:193], v[172:173] op_sel_hi:[0,1,1] neg_lo:[1,0,0] neg_hi:[1,0,0]\n"
        "v_pk_fma_f32 v[174:175], v[246:247], v[194:195], v[174:175] op_sel_hi:[0,1,1] neg_lo:[1,0,0] neg_hi:[1,0,0]\n"
        "v_pk_mul_f32 v[160:161], v[240:241], v[160:161] op_sel:[1,0] op_sel_hi:[1,1]\n"
        "v_pk_mul_f32 v[162:163], v[240:241], v[162:163] op_sel:[1,0] op_sel_hi:[1,1]\n"
        "v_pk_mul_f32 v[164:165], v[242:243], v[164:165] op_sel:[1,0] op_sel_hi:[1,1]\n"
        "v_pk_mul_f32 v[166:167], v[242:243], v[166:167] op_sel:[1,0] op_sel_hi:[1,1]\n"
        "v_pk_mul_f32 v[168:169], v[244:245], v[168:169] op_sel:[1,0] op_sel_hi:[1,1]\n"
        "v_pk_mul_f32 v[170:171], v[244:245], v[170:171] op_sel:[1,0] op_sel_hi:[1,1]\n"
        "v_pk_mul_f32 v[172:173], v[246:247], v[172:173] op_sel:[1,0] op_sel_hi:[1,1]\n"
        "v_pk_mul_f32 v[174:175], v[246:247], v[174:175] op_sel:[1,0] op_sel_hi:[1,1]\n"
        "v_cvt_pk_bf16_f32 v192, v160, v161\n"
        "v_cvt_pk_bf16_f32 v193, v162, v163\n"
        "v_cvt_pk_bf16_f32 v194, v164, v165\n"
        "v_cvt_pk_bf16_f32 v195, v166, v167\n"
        "v_cvt_pk_bf16_f32 v196, v168, v169\n"
        "v_cvt_pk_bf16_f32 v197, v170, v171\n"
        "v_cvt_pk_bf16_f32 v198, v172, v173\n"
        "v_cvt_pk_bf16_f32 v199, v174, v175\n"
        "v_permlane16_swap_b32 v192, v194\n"
        "v_permlane16_swap_b32 v193, v195\n"
        "v_permlane16_swap_b32 v196, v198\n"
        "v_permlane16_swap_b32 v197, v199\n"
        "s_waitcnt vmcnt(0) lgkmcnt(0)\n"
        "v_cmp_eq_u32 vcc, -1, v249\n"
        "s_cbranch_vccnz LwayB_%=\n"
        "s_add_u32 s54, s68, 4294443008\n"
        "s_cmp_ge_u32 s69, 4\n"
        "s_cselect_b32 s54, s54, 0x7f000000\n"
        "buffer_load_dwordx4 v[96:99], v0, %[srd], s54 offen sc1\n"
        "v_pk_mul_f32 v[40:41], v[160:161], v[160:161]\n"
        "v_xor_b32 v56, 0xff800000, v16\n"
        "v_pk_mul_f32 v[42:43], v[164:165], v[164:165]\n"
        "v_xor_b32 v57, 0xff800000, v17\n"
        "v_pk_mul_f32 v[44:45], v[168:169], v[168:169]\n"
        "v_xor_b32 v58, 0xff800000, v18\n"
        "v_pk_mul_f32 v[46:47], v[172:173], v[172:173]\n"
        "v_xor_b32 v59, 0xff800000, v19\n"
        "v_pk_fma_f32 v[40:41], v[162:163], v[162:163], v[40:41]\n"
        "v_xor_b32 v60, 0xff800000, v20\n"
        "v_pk_fma_f32 v[42:43], v[166:167], v[166:167], v[42:43]\n"
        "s_mov_b64 exec, s[58:59]\n"
        "buffer_load_dwordx4 a[0:3], v12, %[srd], s54 offen sc1\n"
        "buffer_load_dwordx4 a[4:7], v12, %[srd], s54 offen offset:512 sc1\n"
        "s_mov_b64 exec, -1\n"
        "v_xor_b32 v61, 0xff800000, v21\n"
        "v_pk_fma_f32 v[44:45], v[170:171], v[170:171], v[44:45]\n"
        "v_xor_b32 v62, 0xff800000, v22\n"
        "v_pk_fma_f32 v[46:47], v[174:175], v[174:175], v[46:47]\n"
        "v_xor_b32 v63, 0xff800000, v23\n"
        "v_add_f32 v40, v40, v41\n"
        "v_xor_b32 v64, 0xff800000, v24\n"
        "v_add_f32 v42, v42, v43\n"
        "v_xor_b32 v65, 0xff800000, v25\n"
        "v_add_f32 v44, v44, v45\n"
        "v_xor_b32 v66, 0xff800000, v26\n"
        "s_add_u32 s54, s68, 4294508544\n"
        "s_cmp_ge_u32 s69, 4\n"
        "s_cselect_b32 s54, s54, 0x7f000000\n"
        "buffer_load_dwordx4 v[100:103], v0, %[srd], s54 offen sc1\n"
        "v_add_f32 v46, v46, v47\n"
        "v_xor_b32 v67, 0xff800000, v27\n"
        "v_permlane32_swap_b32 v40, v44\n"
        "v_xor_b32 v68, 0xff800000, v28\n"
        "v_permlane32_swap_b32 v42, v46\n"
        "v_xor_b32 v69, 0xff800000, v29\n"
        "v_add_f32 v40, v40, v44\n"
        "v_xor_b32 v70, 0xff800000, v30\n"
        "v_add_f32 v42, v42, v46\n"
        "v_xor_b32 v71, 0xff800000, v31\n"
        "s_nop 0\n"
        "s_mov_b64 exec, s[58:59]\n"
        "buffer_load_dwordx4 a[8:11], v12, %[srd], s54 offen sc1\n"
        "buffer_load_dwordx4 a[12:15], v12, %[srd], s54 offen offset:512 sc1\n"
        "s_mov_b64 exec, -1\n"
        "v_permlane16_swap_b32 v40, v42\n"
        "v_max_u32_dpp v48, v249, v249 row_ror:2 row_mask:0xf bank_mask:0xf\n"
        "v_add_f32 v40, v40, v42\n"
        "s_nop 0\n"
        "v_max_u32_dpp v48, v48, v48 row_ror:4 row_mask:0xf bank_mask:0xf\n"
        "v_max_u32_dpp v40, v40, v40 row_ror:8 row_mask:0xf bank_mask:0xf\n"
        "s_nop 0\n"
        "v_max_u32_dpp v48, v48, v48 row_ror:8 row_mask:0xf bank_mask:0xf\n"
        "v_max_u32_dpp v40, v40, v40 row_ror:4 row_mask:0xf bank_mask:0xf\n"
        "s_nop 1\n"
        "v_max_u32_dpp v40, v40, v40 row_ror:2 row_mask:0xf bank_mask:0xf\n"
        "s_add_u32 s54, s68, 4294574080\n"
        "s_cmp_ge_u32 s69, 3\n"
        "s_cselect_b32 s54, s54, 0x7f000000\n"
        "buffer_load_dwordx4 v[104:107], v0, %[srd], s54 offen sc1\n"
        "s_nop 1\n"
        "v_max_u32_dpp v40, v40, v40 row_ror:1 row_mask:0xf bank_mask:0xf\n"
        "v_mov_b32 v41, v40\n"
        "s_nop 1\n"
        "v_permlane16_swap_b32 v40, v41\n"
        "v_max_u32 v40, v40, v41\n"
        "v_mov_b32 v41, v40\n"
        "s_nop 1\n"
        "v_permlane32_swap_b32 v40, v41\n"
        "v_max_u32 v40, v40, v41\n"
        "v_max_u32 v49, v48, v250\n"
        "s_mov_b64 exec, s[58:59]\n"
        "buffer_load_dwordx4 a[16:19], v12, %[srd], s54 offen sc1\n"
        "buffer_load_dwordx4 a[20:23], v12, %[srd], s54 offen offset:512 sc1\n"
        "s_mov_b64 exec, -1\n"
        "v_max_u32 v48, v48, v251\n"
        "v_mul_f32 v49, v49, v40\n"
        "v_mfma_f32_32x32x16_bf16 v[160:175], v[32:35], v[32:35], 0\n"
        "v_mfma_f32_32x32x16_bf16 v[176:191], v[32:35], v[32:35], 0\n"
        "v_cmp_nge_f32 vcc, 0x46100000, v49\n"
        "s_nop 3\n"
        "s_and_b64 s[62:63], vcc, 1\n"
        "v_cmp_nge_f32 vcc, 0x47800000, v48\n"
        "s_nop 3\n"
        "s_and_b64 vcc, vcc, 2\n"
        "s_or_b64 vcc, vcc, s[62:63]\n"
        "s_cbranch_vccnz LslowB0a_%=\n"
        "v_mfma_f32_32x32x16_bf16 v[128:143], v[200:203], v[192:195], v[16:31]\n"
        "s_nop 15\n"
        "v_exp_f32 v128, v128\n"
        "s_add_u32 s54, s68, 4294639616\n"
        "s_cmp_ge_u32 s69, 3\n"
        "s_cselect_b32 s54, s54, 0x7f000000\n"
        "v_exp_f32 v129, v129\n"
        "buffer_load_dwordx4 v[108:111], v0, %[srd], s54 offen sc1\n"
        "v_exp_f32 v130, v130\n"
        "v_exp_f32 v131, v131\n"
        "v_exp_f32 v132, v132\n"
        "v_exp_f32 v133, v133\n"
        "v_mfma_f32_32x32x16_bf16 v[144:159], v[200:203], v[196:199], 0\n"
        "v_exp_f32 v134, v134\n"
        "v_exp_f32 v135, v135\n"
        "v_exp_f32 v136, v136\n"
        "v_exp_f32 v137, v137\n"
        "v_exp_f32 v138, v138\n"
        "v_smfmac_f32_32x32x32_bf16 v[160:175], v[208:211], v[128:135], v253\n"
        "s_mov_b64 exec, s[58:59]\n"
        "buffer_load_dwordx4 a[24:27], v12, %[srd], s54 offen sc1\n"
        "buffer_load_dwordx4 a[28:31], v12, %[srd], s54 offen offset:512 sc1\n"
        "s_mov_b64 exec, -1\n"
        "v_exp_f32 v139, v139\n"
        "v_exp_f32 v140, v140\n"
        "v_exp_f32 v141, v141\n"
        "v_exp_f32 v142, v142\n"
        "v_exp_f32 v143, v143\n"
        "v_exp_f32 v144, v144\n"
        "s_add_u32 s54, s68, 4294705152\n"
        "s_cmp_ge_u32 s69, 2\n"
        "s_cselect_b32 s54, s54, 0x7f000000\n"
        "v_exp_f32 v145, v145\n"
        "v_smfmac_f32_32x32x32_bf16 v[160:175], v[212:215], v[136:143], v253\n"
        "buffer_load_dwordx4 v[112:115], v0, %[srd], s54 offen sc1\n"
        "v_exp_f32 v146, v146\n"
        "v_exp_f32 v147, v147\n"
        "v_exp_f32 v148, v148\n"
        "v_exp_f32 v149, v149\n"
        "v_mfma_f32_32x32x16_bf16 v[128:143], v[204:207], v[196:199], v[16:31]\n"
        "v_exp_f32 v150, v150\n"
        "v_exp_f32 v151, v151\n"
        "v_exp_f32 v152, v152\n"
        "v_exp_f32 v153, v153\n"
        "v_exp_f32 v154, v154\n"
        "v_smfmac_f32_32x32x32_bf16 v[176:191], v[208:211], v[144:151], v253\n"
        "s_mov_b64 exec, s[58:59]\n"
        "buffer_load_dwordx4 a[32:35], v12, %[srd], s54 offen sc1\n"
        "buffer_load_dwordx4 a[36:39], v12, %[srd], s54 offen offset:512 sc1\n"
        "s_mov_b64 exec, -1\n"
        "v_exp_f32 v155, v155\n"
        "v_exp_f32 v156, v156\n"
        "v_exp_f32 v157, v157\n"
        "v_exp_f32 v158, v158\n"
        "v_exp_f32 v159, v159\n"
        "v_exp_f32 v128, v128\n"
        "s_add_u32 s54, s68, 4294770688\n"
        "s_cmp_ge_u32 s69, 2\n"
        "s_cselect_b32 s54, s54, 0x7f000000\n"
        "v_exp_f32 v129, v129\n"
        "v_smfmac_f32_32x32x32_bf16 v[176:191], v[212:215], v[152:159], v253\n"
        "buffer_load_dwordx4 v[116:119], v0, %[srd], s54 offen sc1\n"
        "v_exp_f32 v130, v130\n"
        "v_exp_f32 v131, v131\n"
        "v_exp_f32 v132, v132\n"
        "v_exp_f32 v133, v133\n"
        "s_waitcnt vmcnt(13)\n"
        "v_mfma_f32_32x32x16_bf16 v[144:159], v[96:99], v[192:195], v[56:71]\n"
        "v_exp_f32 v134, v134\n"
        "v_exp_f32 v135, v135\n"
        "v_exp_f32 v136, v136\n"
        "v_exp_f32 v137, v137\n"
        "v_exp_f32 v138, v138\n"
        "v_smfmac_f32_32x32x32_bf16 v[176:191], v[216:219], v[128:135], v253\n"
        "s_mov_b64 exec, s[58:59]\n"
        "buffer_load_dwordx4 a[40:43], v12, %[srd], s54 offen sc1\n"
        "buffer_load_dwordx4 a[44:47], v12, %[srd], s54 offen offset:512 sc1\n"
        "s_mov_b64 exec, -1\n"
        "v_exp_f32 v139, v139\n"
        "v_exp_f32 v140, v140\n"
        "v_exp_f32 v141, v141\n"
        "v_exp_f32 v142, v142\n"
        "v_exp_f32 v143, v143\n"
        "v_exp_f32 v144, v144\n"
        "s_add_u32 s54, s68, 4294836224\n"
        "s_cmp_ge_u32 s69, 1\n"
        "s_cselect_b32 s54, s54, 0x7f000000\n"
        "v_exp_f32 v145, v145\n"
        "v_smfmac_f32_32x32x32_bf16 v[176:191], v[220:223], v[136:143], v253\n"
        "buffer_load_dwordx4 v[120:123], v0, %[srd], s54 offen sc1\n"
        "v_exp_f32 v146, v146\n"
        "v_exp_f32 v147, v147\n"
        "v_exp_f32 v148, v148\n"
        "v_exp_f32 v149, v149\n"
        "s_waitcnt vmcnt(13)\n"
        "v_mfma_f32_32x32x16_bf16 v[128:143], v[100:103], v[192:195], 0\n"
        "v_exp_f32 v150, v150\n"
        "v_exp_f32 v151, v151\n"
        "v_exp_f32 v152, v152\n"
        "v_exp_f32 v153, v153\n"
        "v_exp_f32 v154, v154\n"
        "v_smfmac_f32_32x32x32_bf16 v[160:175], a[0:3], v[144:151], v253\n"
        "s_mov_b64 exec, s[58:59]\n"
        "buffer_load_dwordx4 a[48:51], v12, %[srd], s54 offen sc1\n"
        "buffer_load_dwordx4 a[52:55], v12, %[srd], s54 offen offset:512 sc1\n"
        "s_mov_b64 exec, -1\n"
        "v_exp_f32 v155, v155\n"
        "v_exp_f32 v156, v156\n"
        "v_exp_f32 v157, v157\n"
        "v_exp_f32 v158, v158\n"
        "v_exp_f32 v159, v159\n"
        "v_exp_f32 v128, v128\n"
        "s_add_u32 s54, s68, 4294901760\n"
        "s_cmp_ge_u32 s69, 1\n"
        "s_cselect_b32 s54, s54, 0x7f000000\n"
        "v_exp_f32 v129, v129\n"
        "v_smfmac_f32_32x32x32_bf16 v[160:175], a[4:7], v[152:159], v253\n"
        "buffer_load_dwordx4 v[124:127], v0, %[srd], s54 offen sc1\n"
        "v_exp_f32 v130, v130\n"
        "v_exp_f32 v131, v131\n"
        "v_exp_f32 v132, v132\n"
        "v_exp_f32 v133, v133\n"
        "v_mfma_f32_32x32x16_bf16 v[144:159], v[100:103], v[196:199], v[56:71]\n"
        "v_exp_f32 v134, v134\n"
        "v_exp_f32 v135, v135\n"
        "v_exp_f32 v136, v136\n"
        "v_exp_f32 v137, v137\n"
        "v_exp_f32 v138, v138\n"
        "v_smfmac_f32_32x32x32_bf16 v[160:175], a[8:11], v[128:135], v253\n"
        "s_mov_b64 exec, s[58:59]\n"
        "buffer_load_dwordx4 a[56:59], v12, %[srd], s54 offen sc1\n"
        "buffer_load_dwordx4 a[60:63], v12, %[srd], s54 offen offset:512 sc1\n"
        "s_mov_b64 exec, -1\n"
        "v_exp_f32 v139, v139\n"
        "v_exp_f32 v140, v140\n"
        "v_exp_f32 v141, v141\n"
        "v_exp_f32 v142, v142\n"
        "v_exp_f32 v143, v143\n"
        "v_exp_f32 v144, v144\n"
        "s_add_u32 s55, s72, 2097152\n"
        "v_exp_f32 v145, v145\n"
        "v_smfmac_f32_32x32x32_bf16 v[160:175], a[12:15], v[136:143], v253\n"
        "buffer_load_dwordx4 a[128:131], v0, %[srd], s55 offen offset:0 sc1\n"
        "v_exp_f32 v146, v146\n"
        "v_exp_f32 v147, v147\n"
        "v_exp_f32 v148, v148\n"
        "v_exp_f32 v149, v149\n"
        "s_waitcnt vmcnt(16)\n"
        "v_mfma_f32_32x32x16_bf16 v[128:143], v[104:107], v[192:195], 0\n"
        "v_exp_f32 v150, v150\n"
        "v_exp_f32 v151, v151\n"
        "v_exp_f32 v152, v152\n"
        "v_exp_f32 v153, v153\n"
        "v_exp_f32 v154, v154\n"
        "v_smfmac_f32_32x32x32_bf16 v[176:191], a[8:11], v[144:151], v253\n"
        "buffer_load_dwordx4 a[132:135], v0, %[srd], s55 offen offset:1024 sc1\n"
        "v_exp_f32 v155, v155\n"
        "v_exp_f32 v156, v156\n"
        "v_exp_f32 v157, v157\n"
        "v_exp_f32 v158, v158\n"
        "v_exp_f32 v159, v159\n"
        "v_exp_f32 v128, v128\n"
        "v_exp_f32 v129, v129\n"
        "v_smfmac_f32_32x32x32_bf16 v[176:191], a[12:15], v[152:159], v253\n"
        "buffer_load_dwordx4 a[136:139], v0, %[srd], s55 offen offset:2048 sc1\n"
        "v_exp_f32 v130, v130\n"
        "v_exp_f32 v131, v131\n"
        "v_exp_f32 v132, v132\n"
        "v_exp_f32 v133, v133\n"
        "v_mfma_f32_32x32x16_bf16 v[144:159], v[104:107], v[196:199], 0\n"
        "v_exp_f32 v134, v134\n"
        "v_exp_f32 v135, v135\n"
        "v_exp_f32 v136, v136\n"
        "v_exp_f32 v137, v137\n"
        "v_exp_f32 v138, v138\n"
        "v_smfmac_f32_32x32x32_bf16 v[160:175], a[16:19], v[128:135], v253\n"
        "buffer_load_dwordx4 a[140:143], v0, %[srd], s55 offen offset:3072 sc1\n"
        "v_exp_f32 v139, v139\n"
        "v_exp_f32 v140, v140\n"
        "v_exp_f32 v141, v141\n"
        "v_exp_f32 v142, v142\n"
        "v_exp_f32 v143, v143\n"
        "v_exp_f32 v144, v144\n"
        "s_add_u32 s55, s72, 2101248\n"
        "v_exp_f32 v145, v145\n"
        "v_smfmac_f32_32x32x32_bf16 v[160:175], a[20:23], v[136:143], v253\n"
        "buffer_load_dwordx4 a[144:147], v0, %[srd], s55 offen offset:0 sc1\n"
        "v_exp_f32 v146, v146\n"
        "v_exp_f32 v147, v147\n"
        "v_exp_f32 v148, v148\n"
        "v_exp_f32 v149, v149\n"
        "s_waitcnt vmcnt(17)\n"
        "v_mfma_f32_32x32x16_bf16 v[128:143], v[108:111], v[192:195], 0\n"
        "v_exp_f32 v150, v150\n"
        "v_exp_f32 v151, v151\n"
        "v_exp_f32 v152, v152\n"
        "v_exp_f32 v153, v153\n"
        "v_exp_f32 v154, v154\n"
        "v_smfmac_f32_32x32x32_bf16 v[176:191], a[16:19], v[144:151], v253\n"
        "buffer_load_dwordx4 a[148:151], v0, %[srd], s55 offen offset:1024 sc1\n"
        "v_exp_f32 v155, v155\n"
        "v_exp_f32 v156, v156\n"
        "v_exp_f32 v157, v157\n"
        "v_exp_f32 v158, v158\n"
        "v_exp_f32 v159, v159\n"
        "v_exp_f32 v128, v128\n"
        "v_exp_f32 v129, v129\n"
        "v_smfmac_f32_32x32x32_bf16 v[176:191], a[20:23], v[152:159], v253\n"
        "buffer_load_dwordx4 a[152:155], v0, %[srd], s55 offen offset:2048 sc1\n"
        "v_exp_f32 v130, v130\n"
        "v_exp_f32 v131, v131\n"
        "v_exp_f32 v132, v132\n"
        "v_exp_f32 v133, v133\n"
        "v_mfma_f32_32x32x16_bf16 v[144:159], v[108:111], v[196:199], 0\n"
        "v_exp_f32 v134, v134\n"
        "v_exp_f32 v135, v135\n"
        "v_exp_f32 v136, v136\n"
        "v_exp_f32 v137, v137\n"
        "v_exp_f32 v138, v138\n"
        "v_smfmac_f32_32x32x32_bf16 v[160:175], a[24:27], v[128:135], v253\n"
        "buffer_load_dwordx4 a[156:159], v0, %[srd], s55 offen offset:3072 sc1\n"
        "v_exp_f32 v139, v139\n"
        "v_exp_f32 v140, v140\n"
        "v_exp_f32 v141, v141\n"
        "v_exp_f32 v142, v142\n"
        "v_exp_f32 v143, v143\n"
        "v_exp_f32 v144, v144\n"
        "s_add_u32 s55, s72, 2105344\n"
        "v_exp_f32 v145, v145\n"
        "v_smfmac_f32_32x32x32_bf16 v[160:175], a[28:31], v[136:143], v253\n"
        "buffer_load_dwordx4 a[160:163], v0, %[srd], s55 offen offset:0 sc1\n"
        "v_exp_f32 v146, v146\n"
        "v_exp_f32 v147, v147\n"
        "v_exp_f32 v148, v148\n"
        "v_exp_f32 v149, v149\n"
        "s_waitcnt vmcnt(18)\n"
        "v_mfma_f32_32x32x16_bf16 v[128:143], v[112:115], v[192:195], 0\n"
        "v_exp_f32 v150, v150\n"
        "v_exp_f32 v151, v151\n"
        "v_exp_f32 v152, v152\n"
        "v_exp_f32 v153, v153\n"
        "v_exp_f32 v154, v154\n"
        "v_smfmac_f32_32x32x32_bf16 v[176:191], a[24:27], v[144:151], v253\n"
        "buffer_load_dwordx4 a[164:167], v0, %[srd], s55 offen offset:1024 sc1\n"
        "v_exp_f32 v155, v155\n"
        "v_exp_f32 v156, v156\n"
        "v_exp_f32 v157, v157\n"
        "v_exp_f32 v158, v158\n"
        "v_exp_f32 v159, v159\n"
        "v_exp_f32 v128, v128\n"
        "v_exp_f32 v129, v129\n"
        "v_smfmac_f32_32x32x32_bf16 v[176:191], a[28:31], v[152:159], v253\n"
        "buffer_load_dwordx4 a[168:171], v0, %[srd], s55 offen offset:2048 sc1\n"
        "v_exp_f32 v130, v130\n"
        "v_exp_f32 v131, v131\n"
        "v_exp_f32 v132, v132\n"
        "v_exp_f32 v133, v133\n"
        "v_mfma_f32_32x32x16_bf16 v[144:159], v[112:115], v[196:199], 0\n"
        "v_exp_f32 v134, v134\n"
        "v_exp_f32 v135, v135\n"
        "v_exp_f32 v136, v136\n"
        "v_exp_f32 v137, v137\n"
        "v_exp_f32 v138, v138\n"
        "v_smfmac_f32_32x32x32_bf16 v[160:175], a[32:35], v[128:135], v253\n"
        "buffer_load_dwordx4 a[172:175], v0, %[srd], s55 offen offset:3072 sc1\n"
        "v_exp_f32 v139, v139\n"
        "v_exp_f32 v140, v140\n"
        "v_exp_f32 v141, v141\n"
        "v_exp_f32 v142, v142\n"
        "v_exp_f32 v143, v143\n"
        "v_exp_f32 v144, v144\n"
        "s_add_u32 s55, s72, 2109440\n"
        "v_exp_f32 v145, v145\n"
        "v_smfmac_f32_32x32x32_bf16 v[160:175], a[36:39], v[136:143], v253\n"
        "buffer_load_dwordx4 a[176:179], v0, %[srd], s55 offen offset:0 sc1\n"
        "v_exp_f32 v146, v146\n"
        "v_exp_f32 v147, v147\n"
        "v_exp_f32 v148, v148\n"
        "v_exp_f32 v149, v149\n"
        "s_waitcnt vmcnt(19)\n"
        "v_mfma_f32_32x32x16_bf16 v[128:143], v[116:119], v[192:195], 0\n"
        "v_exp_f32 v150, v150\n"
        "v_exp_f32 v151, v151\n"
        "v_exp_f32 v152, v152\n"
        "v_exp_f32 v153, v153\n"
        "v_exp_f32 v154, v154\n"
        "v_smfmac_f32_32x32x32_bf16 v[176:191], a[32:35], v[144:151], v253\n"
        "buffer_load_dwordx4 a[180:183], v0, %[srd], s55 offen offset:1024 sc1\n"
        "v_exp_f32 v155, v155\n"
        "v_exp_f32 v156, v156\n"
        "v_exp_f32 v157, v157\n"
        "v_exp_f32 v158, v158\n"
        "v_exp_f32 v159, v159\n"
        "v_exp_f32 v128, v128\n"
        "v_exp_f32 v129, v129\n"
        "v_smfmac_f32_32x32x32_bf16 v[176:191], a[36:39], v[152:159], v253\n"
        "buffer_load_dwordx4 a[184:187], v0, %[srd], s55 offen offset:2048 sc1\n"
        "v_exp_f32 v130, v130\n"
        "v_exp_f32 v131, v131\n"
        "v_exp_f32 v132, v132\n"
        "v_exp_f32 v133, v133\n"
        "v_mfma_f32_32x32x16_bf16 v[144:159], v[116:119], v[196:199], 0\n"
        "v_exp_f32 v134, v134\n"
        "v_exp_f32 v135, v135\n"
        "v_exp_f32 v136, v136\n"
        "v_exp_f32 v137, v137\n"
        "v_exp_f32 v138, v138\n"
        "v_smfmac_f32_32x32x32_bf16 v[160:175], a[40:43], v[128:135], v253\n"
        "buffer_load_dwordx4 a[188:191], v0, %[srd], s55 offen offset:3072 sc1\n"
        "v_exp_f32 v139, v139\n"
        "v_exp_f32 v140, v140\n"
        "v_exp_f32 v141, v141\n"
        "v_exp_f32 v142, v142\n"
        "v_exp_f32 v143, v143\n"
        "v_exp_f32 v144, v144\n"
        "s_add_u32 s54, s72, 2621440\n"
        "v_exp_f32 v145, v145\n"
        "v_smfmac_f32_32x32x32_bf16 v[160:175], a[44:47], v[136:143], v253\n"
        "buffer_load_dwordx4 a[192:195], v0, %[srd], s54 offen offset:0 sc1\n"
        "v_exp_f32 v146, v146\n"
        "v_exp_f32 v147, v147\n"
        "v_exp_f32 v148, v148\n"
        "v_exp_f32 v149, v149\n"
        "s_waitcnt vmcnt(20)\n"
        "v_mfma_f32_32x32x16_bf16 v[128:143], v[120:123], v[192:195], 0\n"
        "v_exp_f32 v150, v150\n"
        "v_exp_f32 v151, v151\n"
        "v_exp_f32 v152, v152\n"
        "v_exp_f32 v153, v153\n"
        "v_exp_f32 v154, v154\n"
        "v_smfmac_f32_32x32x32_bf16 v[176:191], a[40:43], v[144:151], v253\n"
        "buffer_load_dwordx4 a[196:199], v0, %[srd], s54 offen offset:1024 sc1\n"
        "v_exp_f32 v155, v155\n"
        "v_exp_f32 v156, v156\n"
        "v_exp_f32 v157, v157\n"
        "v_exp_f32 v158, v158\n"
        "v_exp_f32 v159, v159\n"
        "v_exp_f32 v128, v128\n"
        "v_exp_f32 v129, v129\n"
        "v_smfmac_f32_32x32x32_bf16 v[176:191], a[44:47], v[152:159], v253\n"
        "buffer_load_dwordx4 a[200:203], v0, %[srd], s54 offen offset:2048 sc1\n"
        "v_exp_f32 v130, v130\n"
        "v_exp_f32 v131, v131\n"
        "v_exp_f32 v132, v132\n"
        "v_exp_f32 v133, v133\n"
        "v_mfma_f32_32x32x16_bf16 v[144:159], v[120:123], v[196:199], 0\n"
        "v_exp_f32 v134, v134\n"
        "v_exp_f32 v135, v135\n"
        "v_exp_f32 v136, v136\n"
        "v_exp_f32 v137, v137\n"
        "v_exp_f32 v138, v138\n"
        "v_smfmac_f32_32x32x32_bf16 v[160:175], a[48:51], v[128:135], v253\n"
        "buffer_load_dwordx4 a[204:207], v0, %[srd], s54 offen offset:3072 sc1\n"
        "v_exp_f32 v139, v139\n"
        "v_exp_f32 v140, v140\n"
        "v_exp_f32 v141, v141\n"
        "v_exp_f32 v142, v142\n"
        "v_exp_f32 v143, v143\n"
        "v_exp_f32 v144, v144\n"
        "s_add_u32 s54, s72, 2625536\n"
        "v_exp_f32 v145, v145\n"
        "v_smfmac_f32_32x32x32_bf16 v[160:175], a[52:55], v[136:143], v253\n"
        "buffer_load_dwordx4 a[208:211], v0, %[srd], s54 offen offset:0 sc1\n"
        "v_exp_f32 v146, v146\n"
        "v_exp_f32 v147, v147\n"
        "v_exp_f32 v148, v148\n"
        "v_exp_f32 v149, v149\n"
        "s_waitcnt vmcnt(21)\n"
        "v_mfma_f32_32x32x16_bf16 v[128:143], v[124:127], v[192:195], 0\n"
        "v_exp_f32 v150, v150\n"
        "v_exp_f32 v151, v151\n"
        "v_exp_f32 v152, v152\n"
        "v_exp_f32 v153, v153\n"
        "v_exp_f32 v154, v154\n"
        "v_smfmac_f32_32x32x32_bf16 v[176:191], a[48:51], v[144:151], v253\n"
        "buffer_load_dwordx4 a[212:215], v0, %[srd], s54 offen offset:1024 sc1\n"
        "v_exp_f32 v155, v155\n"
        "v_exp_f32 v156, v156\n"
        "v_exp_f32 v157, v157\n"
        "v_exp_f32 v158, v158\n"
        "v_exp_f32 v159, v159\n"
        "v_exp_f32 v128, v128\n"
        "v_exp_f32 v129, v129\n"
        "v_smfmac_f32_32x32x32_bf16 v[176:191], a[52:55], v[152:159], v253\n"
        "buffer_load_dwordx4 a[216:219], v0, %[srd], s54 offen offset:2048 sc1\n"
        "v_exp_f32 v130, v130\n"
        "v_exp_f32 v131, v131\n"
        "v_exp_f32 v132, v132\n"
        "v_exp_f32 v133, v133\n"
        "v_mfma_f32_32x32x16_bf16 v[144:159], v[124:127], v[196:199], 0\n"
        "v_exp_f32 v134, v134\n"
        "v_exp_f32 v135, v135\n"
        "v_exp_f32 v136, v136\n"
        "v_exp_f32 v137, v137\n"
        "v_exp_f32 v138, v138\n"
        "v_smfmac_f32_32x32x32_bf16 v[160:175], a[56:59], v[128:135], v253\n"
        "buffer_load_dwordx4 a[220:223], v0, %[srd], s54 offen offset:3072 sc1\n"
        "v_exp_f32 v139, v139\n"
        "s_lshr_b32 s54, s81, 2\n"
        "v_exp_f32 v140, v140\n"
        "s_add_u32 s54, s54, s83\n"
        "v_exp_f32 v141, v141\n"
        "s_add_u32 s54, s54, 0\n"
        "v_exp_f32 v142, v142\n"
        "s_and_b32 s54, s54, 0x3fff\n"
        "s_add_u32 s54, s54, s84\n"
        "v_exp_f32 v143, v143\n"
        "buffer_load_dwordx4 a[64:67], v0, %[srd], s54 offen sc1\n"
        "v_exp_f32 v144, v144\n"
        "v_exp_f32 v145, v145\n"
        "v_smfmac_f32_32x32x32_bf16 v[160:175], a[60:63], v[136:143], v253\n"
        "buffer_load_dwordx4 a[68:71], v0, %[srd], s54 offen offset:1024 sc1\n"
        "v_exp_f32 v146, v146\n"
        "v_exp_f32 v147, v147\n"
        "v_exp_f32 v148, v148\n"
        "v_exp_f32 v149, v149\n"
        "v_exp_f32 v150, v150\n"
        "s_lshr_b32 s54, s81, 2\n"
        "v_exp_f32 v151, v151\n"
        "s_add_u32 s54, s54, s83\n"
        "v_exp_f32 v152, v152\n"
        "s_add_u32 s54, s54, 2048\n"
        "v_exp_f32 v153, v153\n"
        "s_and_b32 s54, s54, 0x3fff\n"
        "s_add_u32 s54, s54, s84\n"
        "v_exp_f32 v154, v154\n"
        "v_smfmac_f32_32x32x32_bf16 v[176:191], a[56:59], v[144:151], v253\n"
        "buffer_load_dwordx4 a[72:75], v0, %[srd], s54 offen sc1\n"
        "v_exp_f32 v155, v155\n"
        "v_exp_f32 v156, v156\n"
        "v_exp_f32 v157, v157\n"
        "v_exp_f32 v158, v158\n"
        "v_exp_f32 v159, v159\n"
        "buffer_load_dwordx4 a[76:79], v0, %[srd], s54 offen offset:1024 sc1\n"
        "s_nop 1\n"
        "v_smfmac_f32_32x32x32_bf16 v[176:191], a[60:63], v[152:159], v253\n"
        "; ---- attention B done: check, normalise, publish; exchange + out-projection B\n"
        "Lbend_%=:\n"
        "v_cmp_u_f32 vcc, v160, v161\n"
        "v_cmp_u_f32 s[62:63], v162, v163\n"
        "v_cmp_u_f32 s[60:61], v164, v165\n"
        "v_cmp_u_f32 s[54:55], v166, v167\n"
        "v_rcp_f32 v168, v168\n"
        "s_nop 2\n"
        "s_or_b64 vcc, vcc, s[62:63]\n"
        "s_or_b64 s[60:61], s[60:61], s[54:55]\n"
        "s_or_b64 vcc, vcc, s[60:61]\n"
        "s_cbranch_vccnz LslowB1_%=\n"
        "Lbok_%=:\n"
        "v_pk_mul_f32 v[160:161], v[160:161], v[168:169] op_sel_hi:[1,0]\n"
        "v_pk_mul_f32 v[162:163], v[162:163], v[168:169] op_sel_hi:[1,0]\n"
        "v_pk_mul_f32 v[164:165], v[164:165], v[168:169] op_sel_hi:[1,0]\n"
        "v_pk_mul_f32 v[166:167], v[166:167], v[168:169] op_sel_hi:[1,0]\n"
        "v_rcp_f32 v184, v184\n"
        "v_cvt_pk_bf16_f32 v136, v160, v161\n"
        "v_cvt_pk_bf16_f32 v137, v162, v163\n"
        "v_cvt_pk_bf16_f32 v138, v164, v165\n"
        "v_cvt_pk_bf16_f32 v139, v166, v167\n"
        "v_pk_mul_f32 v[176:177], v[176:177], v[184:185] op_sel_hi:[1,0]\n"
        "v_pk_mul_f32 v[178:179], v[178:179], v[184:185] op_sel_hi:[1,0]\n"
        "v_pk_mul_f32 v[180:181], v[180:181], v[184:185] op_sel_hi:[1,0]\n"
        "v_pk_mul_f32 v[182:183], v[182:183], v[184:185] op_sel_hi:[1,0]\n"
        "v_min_u32 v136, -2, v136\n"
        "v_min_u32 v137, -2, v137\n"
        "v_min_u32 v138, -2, v138\n"
        "v_min_u32 v139, -2, v139\n"
        "v_cvt_pk_bf16_f32 v140, v176, v177\n"
        "v_cvt_pk_bf16_f32 v141, v178, v179\n"
        "v_cvt_pk_bf16_f32 v142, v180, v181\n"
        "v_cvt_pk_bf16_f32 v143, v182, v183\n"
        "v_min_u32 v140, -2, v140\n"
        "v_min_u32 v141, -2, v141\n"
        "v_min_u32 v142, -2, v142\n"
        "v_min_u32 v143, -2, v143\n"
        "s_add_u32 s54, s64, 2097152\n"
        "buffer_store_dwordx4 v[136:139], v1, %[srd], s54 offen sc1\n"
        "buffer_store_dwordx4 v[140:143], v1, %[srd], s54 offen offset:512 sc1\n"
        "s_lshr_b32 s54, s64, 4\n"
        "s_and_b32 s54, s54, 0xf80\n"
        "s_add_u32 s54, s54, s34\n"
        "v_mov_b32 v254, s50\n"
        "s_add_u32 s54, s54, 262144\n"
        "v_lshrrev_b32 v252, 2, v0\n"
        "s_mov_b32 exec_hi, 0\n"
        "buffer_store_dword v254, v252, %[srd], s54 offen sc1\n"
        "s_mov_b64 exec, -1\n"
        "ds_write_b128 v2, v[136:139]\n"
        "ds_write_b128 v2, v[140:143] offset:512\n"
        "s_waitcnt lgkmcnt(0)\n"
        "s_barrier\n"
        "ds_read_b128 v[128:131], v3 offset:0\n"
        "ds_read_b128 v[132:135], v3 offset:256\n"
        "ds_read_b128 v[136:139], v3 offset:512\n"
        "ds_read_b128 v[140:143], v3 offset:768\n"
        "ds_read_b128 v[144:147], v3 offset:4096\n"
        "ds_read_b128 v[148:151], v3 offset:4352\n"
        "ds_read_b128 v[152:155], v3 offset:4608\n"
        "ds_read_b128 v[156:159], v3 offset:4864\n"
        "s_add_u32 s60, s65, 2097152\n"
        "s_add_u32 s61, s78, 0\n"
        "s_add_u32 s55, s34, 262144\n"
        "s_waitcnt vmcnt(3)\n"
        "s_waitcnt lgkmcnt(6)\n"
        "v_mfma_f32_16x16x32_bf16 v[160:163], a[128:131], v[128:131], v[80:83]\n"
        "v_mfma_f32_16x16x32_bf16 v[164:167], a[128:131], v[132:135], v[84:87]\n"
        "s_waitcnt lgkmcnt(4)\n"
        "v_mfma_f32_16x16x32_bf16 v[168:171], a[128:131], v[136:139], v[88:91]\n"
        "v_mfma_f32_16x16x32_bf16 v[172:175], a[128:131], v[140:143], v[92:95]\n"
        "s_waitcnt lgkmcnt(2)\n"
        "v_mfma_f32_16x16x32_bf16 v[160:163], a[132:135], v[144:147], v[160:163]\n"
        "v_mfma_f32_16x16x32_bf16 v[164:167], a[132:135], v[148:151], v[164:167]\n"
        "s_waitcnt lgkmcnt(0)\n"
        "v_mfma_f32_16x16x32_bf16 v[168:171], a[132:135], v[152:155], v[168:171]\n"
        "v_mfma_f32_16x16x32_bf16 v[172:175], a[132:135], v[156:159], v[172:175]\n"
        "s_sleep 10\n"
        "s_mov_b32 s56, 0\n"
        "Lsp34_%=:\n"
        "s_add_u32 s56, s56, 1\n"
        "s_cmp_gt_u32 s56, 16384\n"
        "s_cbranch_scc1 Lsq35_%=\n"
        "buffer_load_dword v252, v5, %[srd], s55 offen sc1\n"
        "s_waitcnt vmcnt(0)\n"
        "v_cmp_ne_u32 vcc, s50, v252\n"
        "s_nop 3\n"
        "s_and_b64 vcc, vcc, s[30:31]\n"
        "s_cbranch_vccnz Lsp34_%=\n"
        "Lsq35_%=:\n"
        "s_mov_b32 m0, s61\n"
        "s_add_u32 s54, s77, 0\n"
        "s_and_b32 s54, s54, 0xffff\n"
        "s_add_u32 s54, s54, s60\n"
        "buffer_load_dwordx4 v0, %[srd], s54 offen sc1 lds\n"
        "s_add_u32 m0, m0, 4096\n"
        "s_add_u32 s54, s77, 4096\n"
        "s_and_b32 s54, s54, 0xffff\n"
        "s_add_u32 s54, s54, s60\n"
        "buffer_load_dwordx4 v0, %[srd], s54 offen sc1 lds\n"
        "s_add_u32 m0, m0, 4096\n"
        "s_add_u32 s54, s77, 8192\n"
        "s_and_b32 s54, s54, 0xffff\n"
        "s_add_u32 s54, s54, s60\n"
        "buffer_load_dwordx4 v0, %[srd], s54 offen sc1 lds\n"
        "s_add_u32 m0, m0, 4096\n"
        "s_add_u32 s54, s77, 12288\n"
        "s_and_b32 s54, s54, 0xffff\n"
        "s_add_u32 s54, s54, s60\n"
        "buffer_load_dwordx4 v0, %[srd], s54 offen sc1 lds\n"
        "s_add_u32 m0, m0, 4096\n"
        "s_add_u32 s54, s77, 16384\n"
        "s_and_b32 s54, s54, 0xffff\n"
        "s_add_u32 s54, s54, s60\n"
        "buffer_load_dwordx4 v0, %[srd], s54 offen sc1 lds\n"
        "s_add_u32 m0, m0, 4096\n"
        "s_add_u32 s54, s77, 20480\n"
        "s_and_b32 s54, s54, 0xffff\n"
        "s_add_u32 s54, s54, s60\n"
        "buffer_load_dwordx4 v0, %[srd], s54 offen sc1 lds\n"
        "s_add_u32 m0, m0, 4096\n"
        "s_add_u32 s54, s77, 24576\n"
        "s_and_b32 s54, s54, 0xffff\n"
        "s_add_u32 s54, s54, s60\n"
        "buffer_load_dwordx4 v0, %[srd], s54 offen sc1 lds\n"
        "s_add_u32 m0, m0, 4096\n"
        "s_add_u32 s54, s77, 28672\n"
        "s_and_b32 s54, s54, 0xffff\n"
        "s_add_u32 s54, s54, s60\n"
        "buffer_load_dwordx4 v0, %[srd], s54 offen sc1 lds\n"
        "s_add_u32 m0, m0, 4096\n"
        "s_add_u32 s54, s77, 32768\n"
        "s_and_b32 s54, s54, 0xffff\n"
        "s_add_u32 s54, s54, s60\n"
        "buffer_load_dwordx4 v0, %[srd], s54 offen sc1 lds\n"
        "s_add_u32 m0, m0, 4096\n"
        "s_add_u32 s54, s77, 36864\n"
        "s_and_b32 s54, s54, 0xffff\n"
        "s_add_u32 s54, s54, s60\n"
        "buffer_load_dwordx4 v0, %[srd], s54 offen sc1 lds\n"
        "s_add_u32 m0, m0, 4096\n"
        "s_add_u32 s54, s77, 40960\n"
        "s_and_b32 s54, s54, 0xffff\n"
        "s_add_u32 s54, s54, s60\n"
        "buffer_load_dwordx4 v0, %[srd], s54 offen sc1 lds\n"
        "s_add_u32 m0, m0, 4096\n"
        "s_add_u32 s54, s77, 45056\n"
        "s_and_b32 s54, s54, 0xffff\n"
        "s_add_u32 s54, s54, s60\n"
        "buffer_load_dwordx4 v0, %[srd], s54 offen sc1 lds\n"
        "s_add_u32 m0, m0, 4096\n"
        "s_add_u32 s54, s77, 49152\n"
        "s_and_b32 s54, s54, 0xffff\n"
        "s_add_u32 s54, s54, s60\n"
        "buffer_load_dwordx4 v0, %[srd], s54 offen sc1 lds\n"
        "s_add_u32 m0, m0, 4096\n"
        "s_add_u32 s54, s77, 53248\n"
        "s_and_b32 s54, s54, 0xffff\n"
        "s_add_u32 s54, s54, s60\n"
        "buffer_load_dwordx4 v0, %[srd], s54 offen sc1 lds\n"
        "s_waitcnt vmcnt(9)\n"
        "s_barrier\n"
        "ds_read_b128 v[128:131], v3 offset:8192\n"
        "ds_read_b128 v[132:135], v3 offset:8448\n"
        "ds_read_b128 v[136:139], v3 offset:8704\n"
        "ds_read_b128 v[140:143], v3 offset:8960\n"
        "ds_read_b128 v[144:147], v3 offset:12288\n"
        "ds_read_b128 v[148:151], v3 offset:12544\n"
        "ds_read_b128 v[152:155], v3 offset:12800\n"
        "ds_read_b128 v[156:159], v3 offset:13056\n"
        "s_waitcnt lgkmcnt(6)\n"
        "v_mfma_f32_16x16x32_bf16 v[160:163], a[136:139], v[128:131], v[160:163]\n"
        "ds_read_b128 v[128:131], v3 offset:16384\n"
        "v_mfma_f32_16x16x32_bf16 v[164:167], a[136:139], v[132:135], v[164:167]\n"
        "ds_read_b128 v[132:135], v3 offset:16640\n"
        "s_waitcnt lgkmcnt(6)\n"
        "v_mfma_f32_16x16x32_bf16 v[168:171], a[136:139], v[136:139], v[168:171]\n"
        "ds_read_b128 v[136:139], v3 offset:16896\n"
        "v_mfma_f32_16x16x32_bf16 v[172:175], a[136:139], v[140:143], v[172:175]\n"
        "ds_read_b128 v[140:143], v3 offset:17152\n"
        "s_waitcnt lgkmcnt(6)\n"
        "v_mfma_f32_16x16x32_bf16 v[160:163], a[140:143], v[144:147], v[160:163]\n"
        "ds_read_b128 v[144:147], v3 offset:20480\n"
        "v_mfma_f32_16x16x32_bf16 v[164:167], a[140:143], v[148:151], v[164:167]\n"
        "ds_read_b128 v[148:151], v3 offset:20736\n"
        "s_waitcnt lgkmcnt(6)\n"
        "v_mfma_f32_16x16x32_bf16 v[168:171], a[140:143], v[152:155], v[168:171]\n"
        "ds_read_b128 v[152:155], v3 offset:20992\n"
        "v_mfma_f32_16x16x32_bf16 v[172:175], a[140:143], v[156:159], v[172:175]\n"
        "ds_read_b128 v[156:159], v3 offset:21248\n"
        "s_waitcnt lgkmcnt(6)\n"
        "v_mfma_f32_16x16x32_bf16 v[160:163], a[144:147], v[128:131], v[160:163]\n"
        "ds_read_b128 v[128:131], v3 offset:24576\n"
        "v_mfma_f32_16x16x32_bf16 v[164:167], a[144:147], v[132:135], v[164:167]\n"
        "ds_read_b128 v[132:135], v3 offset:24832\n"
        "s_waitcnt lgkmcnt(6)\n"
        "v_mfma_f32_16x16x32_bf16 v[168:171], a[144:147], v[136:139], v[168:171]\n"
        "ds_read_b128 v[136:139], v3 offset:25088\n"
        "v_mfma_f32_16x16x32_bf16 v[172:175], a[144:147], v[140:143], v[172:175]\n"
        "ds_read_b128 v[140:143], v3 offset:25344\n"
        "s_waitcnt vmcnt(4)\n"
        "s_barrier\n"
        "s_waitcnt lgkmcnt(6)\n"
        "v_mfma_f32_16x16x32_bf16 v[160:163], a[148:151], v[144:147], v[160:163]\n"
        "ds_read_b128 v[144:147], v3 offset:28672\n"
        "v_mfma_f32_16x16x32_bf16 v[164:167], a[148:151], v[148:151], v[164:167]\n"
        "ds_read_b128 v[148:151], v3 offset:28928\n"
        "s_waitcnt lgkmcnt(6)\n"
        "v_mfma_f32_16x16x32_bf16 v[168:171], a[148:151], v[152:155], v[168:171]\n"
        "ds_read_b128 v[152:155], v3 offset:29184\n"
        "v_mfma_f32_16x16x32_bf16 v[172:175], a[148:151], v[156:159], v[172:175]\n"
        "ds_read_b128 v[156:159], v3 offset:29440\n"
        "s_waitcnt lgkmcnt(6)\n"
        "v_mfma_f32_16x16x32_bf16 v[160:163], a[152:155], v[128:131], v[160:163]\n"
        "ds_read_b128 v[128:131], v3 offset:32768\n"
        "v_mfma_f32_16x16x32_bf16 v[164:167], a[152:155], v[132:135], v[164:167]\n"
        "ds_read_b128 v[132:135], v3 offset:33024\n"
        "s_waitcnt lgkmcnt(6)\n"
        "v_mfma_f32_16x16x32_bf16 v[168:171], a[152:155], v[136:139], v[168:171]\n"
        "ds_read_b128 v[136:139], v3 offset:33280\n"
        "v_mfma_f32_16x16x32_bf16 v[172:175], a[152:155], v[140:143], v[172:175]\n"
        "ds_read_b128 v[140:143], v3 offset:33536\n"
        "s_waitcnt lgkmcnt(6)\n"
        "v_mfma_f32_16x16x32_bf16 v[160:163], a[156:159], v[144:147], v[160:163]\n"
        "ds_read_b128 v[144:147], v3 offset:36864\n"
        "v_mfma_f32_16x16x32_bf16 v[164:167], a[156:159], v[148:151], v[164:167]\n"
        "ds_read_b128 v[148:151], v3 offset:37120\n"
        "s_waitcnt lgkmcnt(6)\n"
        "v_mfma_f32_16x16x32_bf16 v[168:171], a[156:159], v[152:155], v[168:171]\n"
        "ds_read_b128 v[152:155], v3 offset:37376\n"
        "v_mfma_f32_16x16x32_bf16 v[172:175], a[156:159], v[156:159], v[172:175]\n"
        "ds_read_b128 v[156:159], v3 offset:37632\n"
        "s_waitcnt lgkmcnt(6)\n"
        "v_mfma_f32_16x16x32_bf16 v[160:163], a[160:163], v[128:131], v[160:163]\n"
        "ds_read_b128 v[128:131], v3 offset:40960\n"
        "v_mfma_f32_16x16x32_bf16 v[164:167], a[160:163], v[132:135], v[164:167]\n"
        "ds_read_b128 v[132:135], v3 offset:41216\n"
        "s_waitcnt lgkmcnt(6)\n"
        "v_mfma_f32_16x16x32_bf16 v[168:171], a[160:163], v[136:139], v[168:171]\n"
        "ds_read_b128 v[136:139], v3 offset:41472\n"
        "v_mfma_f32_16x16x32_bf16 v[172:175], a[160:163], v[140:143], v[172:175]\n"
        "ds_read_b128 v[140:143], v3 offset:41728\n"
        "s_waitcnt lgkmcnt(6)\n"
        "v_mfma_f32_16x16x32_bf16 v[160:163], a[164:167], v[144:147], v[160:163]\n"
        "ds_read_b128 v[144:147], v3 offset:45056\n"
        "v_mfma_f32_16x16x32_bf16 v[164:167], a[164:167], v[148:151], v[164:167]\n"
        "ds_read_b128 v[148:151], v3 offset:45312\n"
        "s_waitcnt lgkmcnt(6)\n"
        "v_mfma_f32_16x16x32_bf16 v[168:171], a[164:167], v[152:155], v[168:171]\n"
        "ds_read_b128 v[152:155], v3 offset:45568\n"
        "v_mfma_f32_16x16x32_bf16 v[172:175], a[164:167], v[156:159], v[172:175]\n"
        "ds_read_b128 v[156:159], v3 offset:45824\n"
        "s_waitcnt vmcnt(0)\n"
        "s_barrier\n"
        "s_waitcnt lgkmcnt(6)\n"
        "v_mfma_f32_16x16x32_bf16 v[160:163], a[168:171], v[128:131], v[160:163]\n"
        "ds_read_b128 v[128:131], v3 offset:49152\n"
        "v_mfma_f32_16x16x32_bf16 v[164:167], a[168:171], v[132:135], v[164:167]\n"
        "ds_read_b128 v[132:135], v3 offset:49408\n"
        "s_waitcnt lgkmcnt(6)\n"
        "v_mfma_f32_16x16x32_bf16 v[168:171], a[168:171], v[136:139], v[168:171]\n"
        "ds_read_b128 v[136:139], v3 offset:49664\n"
        "v_mfma_f32_16x16x32_bf16 v[172:175], a[168:171], v[140:143], v[172:175]\n"
        "ds_read_b128 v[140:143], v3 offset:49920\n"
        "s_waitcnt lgkmcnt(6)\n"
        "v_mfma_f32_16x16x32_bf16 v[160:163], a[172:175], v[144:147], v[160:163]\n"
        "ds_read_b128 v[144:147], v3 offset:53248\n"
        "v_mfma_f32_16x16x32_bf16 v[164:167], a[172:175], v[148:151], v[164:167]\n"
        "ds_read_b128 v[148:151], v3 offset:53504\n"
        "s_waitcnt lgkmcnt(6)\n"
        "v_mfma_f32_16x16x32_bf16 v[168:171], a[172:175], v[152:155], v[168:171]\n"
        "ds_read_b128 v[152:155], v3 offset:53760\n"
        "v_mfma_f32_16x16x32_bf16 v[172:175], a[172:175], v[156:159], v[172:175]\n"
        "ds_read_b128 v[156:159], v3 offset:54016\n"
        "s_waitcnt lgkmcnt(6)\n"
        "v_mfma_f32_16x16x32_bf16 v[160:163], a[176:179], v[128:131], v[160:163]\n"
        "ds_read_b128 v[128:131], v3 offset:57344\n"
        "v_mfma_f32_16x16x32_bf16 v[164:167], a[176:179], v[132:135], v[164:167]\n"
        "ds_read_b128 v[132:135], v3 offset:57600\n"
        "s_waitcnt lgkmcnt(6)\n"
        "v_mfma_f32_16x16x32_bf16 v[168:171], a[176:179], v[136:139], v[168:171]\n"
        "ds_read_b128 v[136:139], v3 offset:57856\n"
        "v_mfma_f32_16x16x32_bf16 v[172:175], a[176:179], v[140:143], v[172:175]\n"
        "ds_read_b128 v[140:143], v3 offset:58112\n"
        "s_waitcnt lgkmcnt(6)\n"
        "v_mfma_f32_16x16x32_bf16 v[160:163], a[180:183], v[144:147], v[160:163]\n"
        "ds_read_b128 v[144:147], v3 offset:61440\n"
        "v_mfma_f32_16x16x32_bf16 v[164:167], a[180:183], v[148:151], v[164:167]\n"
        "ds_read_b128 v[148:151], v3 offset:61696\n"
        "s_waitcnt lgkmcnt(6)\n"
        "v_mfma_f32_16x16x32_bf16 v[168:171], a[180:183], v[152:155], v[168:171]\n"
        "ds_read_b128 v[152:155], v3 offset:61952\n"
        "v_mfma_f32_16x16x32_bf16 v[172:175], a[180:183], v[156:159], v[172:175]\n"
        "ds_read_b128 v[156:159], v3 offset:62208\n"
        "s_waitcnt lgkmcnt(6)\n"
        "v_mfma_f32_16x16x32_bf16 v[160:163], a[184:187], v[128:131], v[160:163]\n"
        "v_mfma_f32_16x16x32_bf16 v[164:167], a[184:187], v[132:135], v[164:167]\n"
        "s_waitcnt lgkmcnt(4)\n"
        "v_mfma_f32_16x16x32_bf16 v[168:171], a[184:187], v[136:139], v[168:171]\n"
        "v_mfma_f32_16x16x32_bf16 v[172:175], a[184:187], v[140:143], v[172:175]\n"
        "s_waitcnt lgkmcnt(2)\n"
        "v_mfma_f32_16x16x32_bf16 v[160:163], a[188:191], v[144:147], v[160:163]\n"
        "v_mfma_f32_16x16x32_bf16 v[164:167], a[188:191], v[148:151], v[164:167]\n"
        "s_waitcnt lgkmcnt(0)\n"
        "v_mfma_f32_16x16x32_bf16 v[168:171], a[188:191], v[152:155], v[168:171]\n"
        "v_mfma_f32_16x16x32_bf16 v[172:175], a[188:191], v[156:159], v[172:175]\n"
        "s_nop 4\n"
        "v_cmp_u_f32 vcc, v160, v164\n"
        "s_nop 1\n"
        "v_cmp_u_f32 s[62:63], v168, v172\n"
        "s_nop 3\n"
        "s_or_b64 vcc, vcc, s[62:63]\n"
        "s_cbranch_vccnz Lsdbad_%=\n"
        "; ---- last round -> final; else publish x, exchange, q and K/V tile of the next layer A\n"
        "Lsdok_%=:\n"
        "s_cmp_eq_u32 s87, 0\n"
        "s_cbranch_scc0 Lfinal_%=\n"
        "v_cvt_pk_bf16_f32 v128, v160, v161\n"
        "v_cvt_pk_bf16_f32 v129, v162, v163\n"
        "v_cvt_pk_bf16_f32 v130, v164, v165\n"
        "v_cvt_pk_bf16_f32 v131, v166, v167\n"
        "v_cvt_pk_bf16_f32 v132, v168, v169\n"
        "v_cvt_pk_bf16_f32 v133, v170, v171\n"
        "v_cvt_pk_bf16_f32 v134, v172, v173\n"
        "v_cvt_pk_bf16_f32 v135, v174, v175\n"
        "v_permlane16_swap_b32 v128, v130\n"
        "v_permlane16_swap_b32 v129, v131\n"
        "v_permlane16_swap_b32 v132, v134\n"
        "v_permlane16_swap_b32 v133, v135\n"
        "v_min_u32 v128, -2, v128\n"
        "v_min_u32 v129, -2, v129\n"
        "v_min_u32 v130, -2, v130\n"
        "v_min_u32 v131, -2, v131\n"
        "v_min_u32 v132, -2, v132\n"
        "v_min_u32 v133, -2, v133\n"
        "v_min_u32 v134, -2, v134\n"
        "v_min_u32 v135, -2, v135\n"
        "s_add_u32 s54, s64, 10485760\n"
        "buffer_store_dwordx4 v[128:131], v1, %[srd], s54 offen sc1\n"
        "buffer_store_dwordx4 v[132:135], v1, %[srd], s54 offen offset:512 sc1\n"
        "s_lshr_b32 s54, s64, 4\n"
        "s_and_b32 s54, s54, 0xf80\n"
        "s_add_u32 s54, s54, s34\n"
        "v_mov_b32 v254, s50\n"
        "s_add_u32 s54, s54, 393216\n"
        "v_lshrrev_b32 v252, 2, v0\n"
        "s_mov_b32 exec_hi, 0\n"
        "buffer_store_dword v254, v252, %[srd], s54 offen sc1\n"
        "s_mov_b64 exec, -1\n"
        "v_add_u32 v254, 0x10000, v2\n"
        "ds_write_b128 v254, v[128:131]\n"
        "ds_write_b128 v254, v[132:135] offset:512\n"
        "v_pk_add_f32 v[136:137], v[160:161], v[162:163]\n"
        "v_pk_mul_f32 v[144:145], v[160:161], v[160:161]\n"
        "v_pk_add_f32 v[138:139], v[164:165], v[166:167]\n"
        "v_pk_mul_f32 v[146:147], v[164:165], v[164:165]\n"
        "v_pk_add_f32 v[140:141], v[168:169], v[170:171]\n"
        "v_pk_mul_f32 v[148:149], v[168:169], v[168:169]\n"
        "v_pk_add_f32 v[142:143], v[172:173], v[174:175]\n"
        "v_pk_mul_f32 v[150:151], v[172:173], v[172:173]\n"
        "v_add_f32 v136, v136, v137\n"
        "v_pk_fma_f32 v[144:145], v[162:163], v[162:163], v[144:145]\n"
        "v_add_f32 v138, v138, v139\n"
        "v_pk_fma_f32 v[146:147], v[166:167], v[166:167], v[146:147]\n"
        "v_add_f32 v140, v140, v141\n"
        "v_pk_fma_f32 v[148:149], v[170:171], v[170:171], v[148:149]\n"
        "v_add_f32 v142, v142, v143\n"
        "v_pk_fma_f32 v[150:151], v[174:175], v[174:175], v[150:151]\n"
        "v_add_f32 v144, v144, v145\n"
        "v_add_f32 v146, v146, v147\n"
        "v_add_f32 v148, v148, v149\n"
        "v_add_f32 v150, v150, v151\n"
        "v_permlane32_swap_b32 v136, v140\n"
        "v_permlane32_swap_b32 v144, v148\n"
        "v_permlane32_swap_b32 v138, v142\n"
        "v_permlane32_swap_b32 v146, v150\n"
        "v_add_f32 v136, v136, v140\n"
        "v_add_f32 v144, v144, v148\n"
        "v_add_f32 v138, v138, v142\n"
        "v_add_f32 v146, v146, v150\n"
        "s_nop 0\n"
        "v_permlane16_swap_b32 v136, v138\n"
        "v_permlane16_swap_b32 v144, v146\n"
        "v_add_f32 v136, v136, v138\n"
        "v_add_f32 v144, v144, v146\n"
        "v_mov_b32 v137, v144\n"
        "ds_write_b64 v10, v[136:137]\n"
        "s_waitcnt lgkmcnt(0)\n"
        "s_barrier\n"
        "v_cvt_pk_bf16_f32 v80, v160, v161\n"
        "v_cvt_pk_bf16_f32 v82, v162, v163\n"
        "v_cvt_pk_bf16_f32 v84, v164, v165\n"
        "v_cvt_pk_bf16_f32 v86, v166, v167\n"
        "v_cvt_pk_bf16_f32 v88, v168, v169\n"
        "v_cvt_pk_bf16_f32 v90, v170, v171\n"
        "v_cvt_pk_bf16_f32 v92, v172, v173\n"
        "v_cvt_pk_bf16_f32 v94, v174, v175\n"
        "ds_read_b128 v[128:131], v4 offset:0\n"
        "ds_read_b128 v[132:135], v4 offset:256\n"
        "ds_read_b128 v[136:139], v4 offset:512\n"
        "ds_read_b128 v[140:143], v4 offset:768\n"
        "ds_read_b128 v[144:147], v4 offset:4096\n"
        "ds_read_b128 v[148:151], v4 offset:4352\n"
        "ds_read_b128 v[152:155], v4 offset:4608\n"
        "ds_read_b128 v[156:159], v4 offset:4864\n"
        "s_add_u32 s60, s65, 10485760\n"
        "s_add_u32 s61, s78, 65536\n"
        "s_add_u32 s55, s34, 393216\n"
        "s_lshr_b32 s54, s81, 1\n"
        "s_add_u32 s54, s54, s75\n"
        "buffer_load_dwordx4 v[192:195], v15, %[srd], s54 offen sc1\n"
        "s_waitcnt lgkmcnt(6)\n"
        "v_mfma_f32_16x16x32_bf16 v[160:163], a[192:195], v[128:131], 0\n"
        "v_mfma_f32_16x16x32_bf16 v[164:167], a[192:195], v[132:135], 0\n"
        "s_waitcnt lgkmcnt(4)\n"
        "v_mfma_f32_16x16x32_bf16 v[168:171], a[192:195], v[136:139], 0\n"
        "v_mfma_f32_16x16x32_bf16 v[172:175], a[192:195], v[140:143], 0\n"
        "s_waitcnt lgkmcnt(2)\n"
        "v_mfma_f32_16x16x32_bf16 v[160:163], a[196:199], v[144:147], v[160:163]\n"
        "v_mfma_f32_16x16x32_bf16 v[164:167], a[196:199], v[148:151], v[164:167]\n"
        "s_waitcnt lgkmcnt(0)\n"
        "v_mfma_f32_16x16x32_bf16 v[168:171], a[196:199], v[152:155], v[168:171]\n"
        "v_mfma_f32_16x16x32_bf16 v[172:175], a[196:199], v[156:159], v[172:175]\n"
        "s_mov_b32 s56, 0\n"
        "Lsp49_%=:\n"
        "s_add_u32 s56, s56, 1\n"
        "s_cmp_gt_u32 s56, 16384\n"
        "s_cbranch_scc1 Lsq50_%=\n"
        "buffer_load_dword v252, v5, %[srd], s55 offen sc1\n"
        "s_waitcnt vmcnt(0)\n"
        "v_cmp_ne_u32 vcc, s50, v252\n"
        "s_nop 3\n"
        "s_and_b64 vcc, vcc, s[30:31]\n"
        "s_cbranch_vccnz Lsp49_%=\n"
        "Lsq50_%=:\n"
        "v_xor_b32 v192, s51, v192\n"
        "v_xor_b32 v193, s51, v193\n"
        "v_xor_b32 v194, s51, v194\n"
        "v_xor_b32 v195, s51, v195\n"
        "v_or3_b32 v192, v192, v193, v194\n"
        "v_or_b32 v192, v192, v195\n"
        "v_cmp_ne_u32 vcc, 0, v192\n"
        "s_cbranch_vccnz Lf3g_%=\n"
        "Lse_%=:\n"
        "s_mov_b32 m0, s61\n"
        "s_add_u32 s54, s77, 0\n"
        "s_and_b32 s54, s54, 0xffff\n"
        "s_add_u32 s54, s54, s60\n"
        "buffer_load_dwordx4 v0, %[srd], s54 offen sc1 lds\n"
        "ds_read_b64 v[136:137], v11 offset:0\n"
        "ds_read_b64 v[138:139], v11 offset:512\n"
        "ds_read_b64 v[140:141], v11 offset:1024\n"
        "ds_read_b64 v[142:143], v11 offset:1536\n"
        "v_lshrrev_b32 v253, 1, v0\n"
        "s_add_u32 s55, s67, 524288\n"
        "v_and_b32 v81, 0xffff0000, v80\n"
        "v_lshlrev_b32 v80, 16, v80\n"
        "s_add_u32 m0, m0, 4096\n"
        "s_add_u32 s54, s77, 4096\n"
        "s_and_b32 s54, s54, 0xffff\n"
        "s_add_u32 s54, s54, s60\n"
        "buffer_load_dwordx4 v0, %[srd], s54 offen sc1 lds\n"
        "v_and_b32 v83, 0xffff0000, v82\n"
        "v_lshlrev_b32 v82, 16, v82\n"
        "v_and_b32 v85, 0xffff0000, v84\n"
        "v_lshlrev_b32 v84, 16, v84\n"
        "s_waitcnt lgkmcnt(0)\n"
        "v_pk_add_f32 v[136:137], v[136:137], v[138:139]\n"
        "v_pk_add_f32 v[140:141], v[140:141], v[142:143]\n"
        "v_pk_add_f32 v[136:137], v[136:137], v[140:141]\n"
        "s_add_u32 m0, m0, 4096\n"
        "s_add_u32 s54, s77, 8192\n"
        "s_and_b32 s54, s54, 0xffff\n"
        "s_add_u32 s54, s54, s60\n"
        "buffer_load_dwordx4 v0, %[srd], s54 offen sc1 lds\n"
        "v_min_u32 v136, -2, v136\n"
        "v_min_u32 v137, -2, v137\n"
        "s_mov_b64 exec, 0xffff\n"
        "buffer_store_dwordx2 v[136:137], v253, %[srd], s55 offen sc1\n"
        "s_mov_b64 exec, -1\n"
        "v_and_b32 v87, 0xffff0000, v86\n"
        "v_lshlrev_b32 v86, 16, v86\n"
        "v_and_b32 v89, 0xffff0000, v88\n"
        "s_add_u32 m0, m0, 4096\n"
        "s_add_u32 s54, s77, 12288\n"
        "s_and_b32 s54, s54, 0xffff\n"
        "s_add_u32 s54, s54, s60\n"
        "buffer_load_dwordx4 v0, %[srd], s54 offen sc1 lds\n"
        "v_lshlrev_b32 v88, 16, v88\n"
        "v_and_b32 v91, 0xffff0000, v90\n"
        "v_lshlrev_b32 v90, 16, v90\n"
        "v_and_b32 v93, 0xffff0000, v92\n"
        "v_lshlrev_b32 v92, 16, v92\n"
        "v_and_b32 v95, 0xffff0000, v94\n"
        "v_lshlrev_b32 v94, 16, v94\n"
        "v_accvgpr_write_b32 a128, v14\n"
        "s_add_u32 m0, m0, 4096\n"
        "s_add_u32 s54, s77, 16384\n"
        "s_and_b32 s54, s54, 0xffff\n"
        "s_add_u32 s54, s54, s60\n"
        "buffer_load_dwordx4 v0, %[srd], s54 offen sc1 lds\n"
        "v_accvgpr_write_b32 a129, v14\n"
        "v_accvgpr_write_b32 a130, v14\n"
        "v_accvgpr_write_b32 a131, v14\n"
        "v_accvgpr_write_b32 a132, v14\n"
        "v_accvgpr_write_b32 a133, v14\n"
        "v_accvgpr_write_b32 a134, v14\n"
        "v_accvgpr_write_b32 a135, v14\n"
        "v_accvgpr_write_b32 a136, v14\n"
        "s_add_u32 m0, m0, 4096\n"
        "s_add_u32 s54, s77, 20480\n"
        "s_and_b32 s54, s54, 0xffff\n"
        "s_add_u32 s54, s54, s60\n"
        "buffer_load_dwordx4 v0, %[srd], s54 offen sc1 lds\n"
        "v_accvgpr_write_b32 a137, v14\n"
        "v_accvgpr_write_b32 a138, v14\n"
        "v_accvgpr_write_b32 a139, v14\n"
        "v_accvgpr_write_b32 a140, v14\n"
        "v_accvgpr_write_b32 a141, v14\n"
        "v_accvgpr_write_b32 a142, v14\n"
        "v_accvgpr_write_b32 a143, v14\n"
        "v_accvgpr_write_b32 a144, v14\n"
        "s_add_u32 m0, m0, 4096\n"
        "s_add_u32 s54, s77, 24576\n"
        "s_and_b32 s54, s54, 0xffff\n"
        "s_add_u32 s54, s54, s60\n"
        "buffer_load_dwordx4 v0, %[srd], s54 offen sc1 lds\n"
        "v_accvgpr_write_b32 a145, v14\n"
        "v_accvgpr_write_b32 a146, v14\n"
        "v_accvgpr_write_b32 a147, v14\n"
        "v_accvgpr_write_b32 a148, v14\n"
        "v_accvgpr_write_b32 a149, v14\n"
        "v_accvgpr_write_b32 a150, v14\n"
        "v_accvgpr_write_b32 a151, v14\n"
        "v_accvgpr_write_b32 a152, v14\n"
        "s_add_u32 m0, m0, 4096\n"
        "s_add_u32 s54, s77, 28672\n"
        "s_and_b32 s54, s54, 0xffff\n"
        "s_add_u32 s54, s54, s60\n"
        "buffer_load_dwordx4 v0, %[srd], s54 offen sc1 lds\n"
        "v_accvgpr_write_b32 a153, v14\n"
        "v_accvgpr_write_b32 a154, v14\n"
        "v_accvgpr_write_b32 a155, v14\n"
        "v_accvgpr_write_b32 a156, v14\n"
        "v_accvgpr_write_b32 a157, v14\n"
        "v_accvgpr_write_b32 a158, v14\n"
        "v_accvgpr_write_b32 a159, v14\n"
        "v_accvgpr_write_b32 a160, v14\n"
        "s_add_u32 m0, m0, 4096\n"
        "s_add_u32 s54, s77, 32768\n"
        "s_and_b32 s54, s54, 0xffff\n"
        "s_add_u32 s54, s54, s60\n"
        "buffer_load_dwordx4 v0, %[srd], s54 offen sc1 lds\n"
        "v_accvgpr_write_b32 a161, v14\n"
        "v_accvgpr_write_b32 a162, v14\n"
        "v_accvgpr_write_b32 a163, v14\n"
        "v_accvgpr_write_b32 a164, v14\n"
        "v_accvgpr_write_b32 a165, v14\n"
        "v_accvgpr_write_b32 a166, v14\n"
        "v_accvgpr_write_b32 a167, v14\n"
        "v_accvgpr_write_b32 a168, v14\n"
        "s_add_u32 m0, m0, 4096\n"
        "s_add_u32 s54, s77, 36864\n"
        "s_and_b32 s54, s54, 0xffff\n"
        "s_add_u32 s54, s54, s60\n"
        "buffer_load_dwordx4 v0, %[srd], s54 offen sc1 lds\n"
        "v_accvgpr_write_b32 a169, v14\n"
        "v_accvgpr_write_b32 a170, v14\n"
        "v_accvgpr_write_b32 a171, v14\n"
        "v_accvgpr_write_b32 a172, v14\n"
        "v_accvgpr_write_b32 a173, v14\n"
        "v_accvgpr_write_b32 a174, v14\n"
        "v_accvgpr_write_b32 a175, v14\n"
        "v_accvgpr_write_b32 a176, v14\n"
        "s_add_u32 m0, m0, 4096\n"
        "s_add_u32 s54, s77, 40960\n"
        "s_and_b32 s54, s54, 0xffff\n"
        "s_add_u32 s54, s54, s60\n"
        "buffer_load_dwordx4 v0, %[srd], s54 offen sc1 lds\n"
        "v_accvgpr_write_b32 a177, v14\n"
        "v_accvgpr_write_b32 a178, v14\n"
        "v_accvgpr_write_b32 a179, v14\n"
        "v_accvgpr_write_b32 a180, v14\n"
        "v_accvgpr_write_b32 a181, v14\n"
        "v_accvgpr_write_b32 a182, v14\n"
        "v_accvgpr_write_b32 a183, v14\n"
        "v_accvgpr_write_b32 a184, v14\n"
        "s_add_u32 m0, m0, 4096\n"
        "s_add_u32 s54, s77, 45056\n"
        "s_and_b32 s54, s54, 0xffff\n"
        "s_add_u32 s54, s54, s60\n"
        "buffer_load_dwordx4 v0, %[srd], s54 offen sc1 lds\n"
        "v_accvgpr_write_b32 a185, v14\n"
        "v_accvgpr_write_b32 a186, v14\n"
        "v_accvgpr_write_b32 a187, v14\n"
        "v_accvgpr_write_b32 a188, v14\n"
        "v_accvgpr_write_b32 a189, v14\n"
        "v_accvgpr_write_b32 a190, v14\n"
        "v_accvgpr_write_b32 a191, v14\n"
        "s_add_u32 m0, m0, 4096\n"
        "s_add_u32 s54, s77, 49152\n"
        "s_and_b32 s54, s54, 0xffff\n"
        "s_add_u32 s54, s54, s60\n"
        "buffer_load_dwordx4 v0, %[srd], s54 offen sc1 lds\n"
        "s_add_u32 m0, m0, 4096\n"
        "s_add_u32 s54, s77, 53248\n"
        "s_and_b32 s54, s54, 0xffff\n"
        "s_add_u32 s54, s54, s60\n"
        "buffer_load_dwordx4 v0, %[srd], s54 offen sc1 lds\n"
        "s_waitcnt vmcnt(9)\n"
        "s_barrier\n"
        "ds_read_b128 v[128:131], v4 offset:8192\n"
        "ds_read_b128 v[132:135], v4 offset:8448\n"
        "ds_read_b128 v[136:139], v4 offset:8704\n"
        "ds_read_b128 v[140:143], v4 offset:8960\n"
        "ds_read_b128 v[144:147], v4 offset:12288\n"
        "ds_read_b128 v[148:151], v4 offset:12544\n"
        "ds_read_b128 v[152:155], v4 offset:12800\n"
        "ds_read_b128 v[156:159], v4 offset:13056\n"
        "s_waitcnt lgkmcnt(6)\n"
        "v_mfma_f32_16x16x32_bf16 v[160:163], a[200:203], v[128:131], v[160:163]\n"
        "ds_read_b128 v[128:131], v4 offset:16384\n"
        "v_mfma_f32_16x16x32_bf16 v[164:167], a[200:203], v[132:135], v[164:167]\n"
        "ds_read_b128 v[132:135], v4 offset:16640\n"
        "s_add_u32 s54, s66, 524288\n"
        "buffer_load_dwordx2 v[248:249], v6, %[srd], s54 offen sc1\n"
        "s_waitcnt lgkmcnt(6)\n"
        "v_mfma_f32_16x16x32_bf16 v[168:171], a[200:203], v[136:139], v[168:171]\n"
        "ds_read_b128 v[136:139], v4 offset:16896\n"
        "v_mfma_f32_16x16x32_bf16 v[172:175], a[200:203], v[140:143], v[172:175]\n"
        "ds_read_b128 v[140:143], v4 offset:17152\n"
        "buffer_load_dwordx2 v[250:251], v6, %[srd], s54 offen offset:512 sc1\n"
        "s_waitcnt lgkmcnt(6)\n"
        "v_mfma_f32_16x16x32_bf16 v[160:163], a[204:207], v[144:147], v[160:163]\n"
        "ds_read_b128 v[144:147], v4 offset:20480\n"
        "v_mfma_f32_16x16x32_bf16 v[164:167], a[204:207], v[148:151], v[164:167]\n"
        "ds_read_b128 v[148:151], v4 offset:20736\n"
        "s_waitcnt lgkmcnt(6)\n"
        "v_mfma_f32_16x16x32_bf16 v[168:171], a[204:207], v[152:155], v[168:171]\n"
        "ds_read_b128 v[152:155], v4 offset:20992\n"
        "v_mfma_f32_16x16x32_bf16 v[172:175], a[204:207], v[156:159], v[172:175]\n"
        "ds_read_b128 v[156:159], v4 offset:21248\n"
        "s_waitcnt lgkmcnt(6)\n"
        "v_mfma_f32_16x16x32_bf16 v[160:163], a[208:211], v[128:131], v[160:163]\n"
        "ds_read_b128 v[128:131], v4 offset:24576\n"
        "v_mfma_f32_16x16x32_bf16 v[164:167], a[208:211], v[132:135], v[164:167]\n"
        "ds_read_b128 v[132:135], v4 offset:24832\n"
        "s_waitcnt lgkmcnt(6)\n"
        "v_mfma_f32_16x16x32_bf16 v[168:171], a[208:211], v[136:139], v[168:171]\n"
        "ds_read_b128 v[136:139], v4 offset:25088\n"
        "v_mfma_f32_16x16x32_bf16 v[172:175], a[208:211], v[140:143], v[172:175]\n"
        "ds_read_b128 v[140:143], v4 offset:25344\n"
        "s_waitcnt vmcnt(6)\n"
        "s_barrier\n"
        "s_waitcnt lgkmcnt(6)\n"
        "v_mfma_f32_16x16x32_bf16 v[160:163], a[212:215], v[144:147], v[160:163]\n"
        "ds_read_b128 v[144:147], v4 offset:28672\n"
        "v_mfma_f32_16x16x32_bf16 v[164:167], a[212:215], v[148:151], v[164:167]\n"
        "ds_read_b128 v[148:151], v4 offset:28928\n"
        "s_waitcnt lgkmcnt(6)\n"
        "v_mfma_f32_16x16x32_bf16 v[168:171], a[212:215], v[152:155], v[168:171]\n"
        "ds_read_b128 v[152:155], v4 offset:29184\n"
        "v_mfma_f32_16x16x32_bf16 v[172:175], a[212:215], v[156:159], v[172:175]\n"
        "ds_read_b128 v[156:159], v4 offset:29440\n"
        "s_waitcnt lgkmcnt(6)\n"
        "v_mfma_f32_16x16x32_bf16 v[160:163], a[216:219], v[128:131], v[160:163]\n"
        "ds_read_b128 v[128:131], v4 offset:32768\n"
        "v_mfma_f32_16x16x32_bf16 v[164:167], a[216:219], v[132:135], v[164:167]\n"
        "ds_read_b128 v[132:135], v4 offset:33024\n"
        "s_waitcnt lgkmcnt(6)\n"
        "v_mfma_f32_16x16x32_bf16 v[168:171], a[216:219], v[136:139], v[168:171]\n"
        "ds_read_b128 v[136:139], v4 offset:33280\n"
        "v_mfma_f32_16x16x32_bf16 v[172:175], a[216:219], v[140:143], v[172:175]\n"
        "ds_read_b128 v[140:143], v4 offset:33536\n"
        "s_waitcnt lgkmcnt(6)\n"
        "v_mfma_f32_16x16x32_bf16 v[160:163], a[220:223], v[144:147], v[160:163]\n"
        "ds_read_b128 v[144:147], v4 offset:36864\n"
        "v_mfma_f32_16x16x32_bf16 v[164:167], a[220:223], v[148:151], v[164:167]\n"
        "ds_read_b128 v[148:151], v4 offset:37120\n"
        "s_waitcnt lgkmcnt(6)\n"
        "v_mfma_f32_16x16x32_bf16 v[168:171], a[220:223], v[152:155], v[168:171]\n"
        "ds_read_b128 v[152:155], v4 offset:37376\n"
        "v_mfma_f32_16x16x32_bf16 v[172:175], a[220:223], v[156:159], v[172:175]\n"
        "ds_read_b128 v[156:159], v4 offset:37632\n"
        "s_waitcnt lgkmcnt(6)\n"
        "v_mfma_f32_16x16x32_bf16 v[160:163], a[224:227], v[128:131], v[160:163]\n"
        "ds_read_b128 v[128:131], v4 offset:40960\n"
        "v_mfma_f32_16x16x32_bf16 v[164:167], a[224:227], v[132:135], v[164:167]\n"
        "ds_read_b128 v[132:135], v4 offset:41216\n"
        "s_waitcnt lgkmcnt(6)\n"
        "v_mfma_f32_16x16x32_bf16 v[168:171], a[224:227], v[136:139], v[168:171]\n"
        "ds_read_b128 v[136:139], v4 offset:41472\n"
        "v_mfma_f32_16x16x32_bf16 v[172:175], a[224:227], v[140:143], v[172:175]\n"
        "ds_read_b128 v[140:143], v4 offset:41728\n"
        "s_waitcnt lgkmcnt(6)\n"
        "v_mfma_f32_16x16x32_bf16 v[160:163], a[228:231], v[144:147], v[160:163]\n"
        "ds_read_b128 v[144:147], v4 offset:45056\n"
        "v_mfma_f32_16x16x32_bf16 v[164:167], a[228:231], v[148:151], v[164:167]\n"
        "ds_read_b128 v[148:151], v4 offset:45312\n"
        "s_waitcnt lgkmcnt(6)\n"
        "v_mfma_f32_16x16x32_bf16 v[168:171], a[228:231], v[152:155], v[168:171]\n"
        "ds_read_b128 v[152:155], v4 offset:45568\n"
        "v_mfma_f32_16x16x32_bf16 v[172:175], a[228:231], v[156:159], v[172:175]\n"
        "ds_read_b128 v[156:159], v4 offset:45824\n"
        "s_cmp_eq_u32 s81, 49152\n"
        "s_cbranch_scc1 Le46_%=\n"
        "s_and_b32 s54, s86, 3\n"
        "s_lshl_b32 s54, s54, 8\n"
        "s_add_u32 s54, s54, s81\n"
        "v_add_u32 v192, s54, v4\n"
        "ds_read_b128 v[96:99], v192 offset:0\n"
        "ds_read_b128 v[100:103], v192 offset:4096\n"
        "ds_read_b128 v[104:107], v192 offset:8192\n"
        "ds_read_b128 v[108:111], v192 offset:12288\n"
        "s_lshr_b32 s54, s81, 4\n"
        "s_sub_u32 s54, s54, 8192\n"
        "v_add_u32 v193, s54, v228\n"
        "s_waitcnt lgkmcnt(0)\n"
        "s_cmp_lt_u32 s86, 4\n"
        "s_cbranch_scc0 Lv47_%=\n"
        "v_mfma_f32_16x16x32_bf16 v[176:179], a[64:67], v[96:99], 0\n"
        "v_mfma_f32_16x16x32_bf16 v[176:179], a[68:71], v[100:103], v[176:179]\n"
        "v_mfma_f32_16x16x32_bf16 v[176:179], a[72:75], v[104:107], v[176:179]\n"
        "v_mfma_f32_16x16x32_bf16 v[176:179], a[76:79], v[108:111], v[176:179]\n"
        "s_branch Lw48_%=\n"
        "Lv47_%=:\n"
        "v_mfma_f32_16x16x32_bf16 v[176:179], v[96:99], a[64:67], 0\n"
        "v_mfma_f32_16x16x32_bf16 v[176:179], v[100:103], a[68:71], v[176:179]\n"
        "v_mfma_f32_16x16x32_bf16 v[176:179], v[104:107], a[72:75], v[176:179]\n"
        "v_mfma_f32_16x16x32_bf16 v[176:179], v[108:111], a[76:79], v[176:179]\n"
        "Lw48_%=:\n"
        "s_nop 7\n"
        "ds_write_b128 v193, v[176:179]\n"
        "Le46_%=:\n"
        "s_waitcnt vmcnt(0)\n"
        "s_waitcnt lgkmcnt(4)\n"
        "v_mfma_f32_16x16x32_bf16 v[160:163], a[232:235], v[128:131], v[160:163]\n"
        "v_pk_add_f32 v[248:249], v[248:249], v[250:251]\n"
        "v_mov_b32 v253, v248\n"
        "v_mov_b32 v254, v249\n"
        "s_nop 0\n"
        "v_permlane16_swap_b32 v248, v253\n"
        "v_permlane16_swap_b32 v249, v254\n"
        "v_mfma_f32_16x16x32_bf16 v[164:167], a[232:235], v[132:135], v[164:167]\n"
        "v_add_f32 v248, v248, v253\n"
        "v_add_f32 v249, v249, v254\n"
        "v_mov_b32 v253, v248\n"
        "v_mov_b32 v254, v249\n"
        "s_nop 0\n"
        "v_permlane32_swap_b32 v248, v253\n"
        "v_mfma_f32_16x16x32_bf16 v[168:171], a[232:235], v[136:139], v[168:171]\n"
        "v_permlane32_swap_b32 v249, v254\n"
        "v_add_f32 v248, v248, v253\n"
        "v_add_f32 v249, v249, v254\n"
        "v_mul_f32 v248, 0x3b000000, v248\n"
        "v_mul_f32 v249, 0x3b000000, v249\n"
        "v_mfma_f32_16x16x32_bf16 v[172:175], a[232:235], v[140:143], v[172:175]\n"
        "v_fma_f32 v249, -v248, v248, v249\n"
        "v_and_b32 v249, 0x7fffffff, v249\n"
        "v_add_f32 v249, 0x3727c5ac, v249\n"
        "v_rsq_f32 v249, v249\n"
        "s_mov_b64 exec, 0xffff\n"
        "ds_write2_b32 v7, v248, v249 offset1:64\n"
        "s_mov_b64 exec, -1\n"
        "s_waitcnt lgkmcnt(0)\n"
        "s_barrier\n"
        "s_mov_b32 s53, 0\n"
        "s_cmp_eq_u32 s81, 49152\n"
        "s_cbranch_scc0 Lkvskip_%=\n"
        "s_and_b32 s54, s86, 3\n"
        "s_lshl_b32 s54, s54, 8\n"
        "s_add_u32 s54, s54, s81\n"
        "v_add_u32 v192, s54, v4\n"
        "ds_read_b128 v[96:99], v192 offset:0\n"
        "ds_read_b128 v[100:103], v192 offset:4096\n"
        "ds_read_b128 v[104:107], v192 offset:8192\n"
        "ds_read_b128 v[108:111], v192 offset:12288\n"
        "v_subrev_u32 v195, 8192, v228\n"
        "ds_read_b128 v[112:115], v195 offset:0\n"
        "ds_read_b128 v[116:119], v195 offset:1024\n"
        "ds_read_b128 v[120:123], v195 offset:2048\n"
        "s_and_b32 s55, s86, 3\n"
        "s_lshl_b32 s55, s55, 6\n"
        "v_add_u32 v193, s55, v8\n"
        "v_add_u32 v194, s55, v9\n"
        "ds_read2_b32 v[124:125], v193 offset1:64\n"
        "ds_read_b128 v[180:183], v194\n"
        "ds_read_b128 v[188:191], v194 offset:256\n"
        "s_waitcnt lgkmcnt(6)\n"
        "s_cmp_lt_u32 s86, 4\n"
        "s_cbranch_scc0 Lv44_%=\n"
        "v_mfma_f32_16x16x32_bf16 v[176:179], a[64:67], v[96:99], 0\n"
        "v_mfma_f32_16x16x32_bf16 v[176:179], a[68:71], v[100:103], v[176:179]\n"
        "v_mfma_f32_16x16x32_bf16 v[176:179], a[72:75], v[104:107], v[176:179]\n"
        "v_mfma_f32_16x16x32_bf16 v[176:179], a[76:79], v[108:111], v[176:179]\n"
        "s_branch Lw45_%=\n"
        "Lv44_%=:\n"
        "v_mfma_f32_16x16x32_bf16 v[176:179], v[96:99], a[64:67], 0\n"
        "v_mfma_f32_16x16x32_bf16 v[176:179], v[100:103], a[68:71], v[176:179]\n"
        "v_mfma_f32_16x16x32_bf16 v[176:179], v[104:107], a[72:75], v[176:179]\n"
        "v_mfma_f32_16x16x32_bf16 v[176:179], v[108:111], a[76:79], v[176:179]\n"
        "Lw45_%=:\n"
        "s_waitcnt lgkmcnt(0)\n"
        "v_pk_add_f32 v[112:113], v[112:113], v[116:117]\n"
        "v_pk_add_f32 v[114:115], v[114:115], v[118:119]\n"
        "v_pk_add_f32 v[112:113], v[112:113], v[120:121]\n"
        "v_pk_add_f32 v[114:115], v[114:115], v[122:123]\n"
        "s_nop 3\n"
        "v_pk_add_f32 v[176:177], v[176:177], v[112:113]\n"
        "v_pk_add_f32 v[178:179], v[178:179], v[114:115]\n"
        "s_cmp_lt_u32 s86, 4\n"
        "s_cbranch_scc0 Lv42_%=\n"
        "v_pk_fma_f32 v[176:177], v[124:125], v[236:237], v[176:177] op_sel_hi:[0,1,1] neg_lo:[1,0,0] neg_hi:[1,0,0]\n"
        "v_pk_fma_f32 v[178:179], v[124:125], v[238:239], v[178:179] op_sel_hi:[0,1,1] neg_lo:[1,0,0] neg_hi:[1,0,0]\n"
        "v_pk_mul_f32 v[176:177], v[124:125], v[176:177] op_sel:[1,0] op_sel_hi:[1,1]\n"
        "v_pk_mul_f32 v[178:179], v[124:125], v[178:179] op_sel:[1,0] op_sel_hi:[1,1]\n"
        "v_pk_mul_f32 v[184:185], v[176:177], v[176:177]\n"
        "v_pk_fma_f32 v[184:185], v[178:179], v[178:179], v[184:185]\n"
        "v_add_f32 v184, v184, v185\n"
        "v_mov_b32 v185, v184\n"
        "s_nop 1\n"
        "v_permlane16_swap_b32 v184, v185\n"
        "v_add_f32 v184, v184, v185\n"
        "v_mov_b32 v185, v184\n"
        "s_nop 1\n"
        "v_permlane32_swap_b32 v184, v185\n"
        "v_add_f32 v184, v184, v185\n"
        "s_branch Lw43_%=\n"
        "Lv42_%=:\n"
        "v_pk_fma_f32 v[176:177], v[180:181], v[236:237], v[176:177] op_sel_hi:[1,0,1] neg_lo:[1,0,0] neg_hi:[1,0,0]\n"
        "v_pk_fma_f32 v[178:179], v[182:183], v[236:237], v[178:179] op_sel_hi:[1,0,1] neg_lo:[1,0,0] neg_hi:[1,0,0]\n"
        "v_pk_mul_f32 v[176:177], v[188:189], v[176:177]\n"
        "v_pk_mul_f32 v[178:179], v[190:191], v[178:179]\n"
        "v_max3_f32 v184, |v176|, |v177|, |v178|\n"
        "v_max_f32 v184, v184, |v179|\n"
        "Lw43_%=:\n"
        "s_nop 1\n"
        "v_cvt_pk_bf16_f32 v186, v176, v177\n"
        "v_cvt_pk_bf16_f32 v187, v178, v179\n"
        "v_cmp_u_f32 vcc, v176, v177\n"
        "v_cmp_u_f32 s[62:63], v178, v179\n"
        "v_min_u32 v186, -2, v186\n"
        "v_min_u32 v187, -2, v187\n"
        "s_nop 1\n"
        "s_or_b64 vcc, vcc, s[62:63]\n"
        "s_cselect_b32 s53, -1, 0\n"
        "s_cbranch_vccnz Lkvskip_%=\n"
        "v_mov_b32 v188, v186\n"
        "v_mov_b32 v189, v187\n"
        "s_nop 0\n"
        "v_permlane16_swap_b32 v186, v188\n"
        "v_permlane16_swap_b32 v187, v189\n"
        "s_mov_b32 exec_lo, 0xffff\n"
        "s_mov_b32 exec_hi, 0xffff\n"
        "buffer_store_dwordx4 v[186:189], v226, %[srd], s74 offen sc1\n"
        "s_mov_b64 exec, -1\n"
        "v_max_u32_dpp v184, v184, v184 row_ror:8 row_mask:0xf bank_mask:0xf\n"
        "s_nop 1\n"
        "v_max_u32_dpp v184, v184, v184 row_ror:4 row_mask:0xf bank_mask:0xf\n"
        "s_nop 1\n"
        "v_max_u32_dpp v184, v184, v184 row_ror:2 row_mask:0xf bank_mask:0xf\n"
        "s_nop 1\n"
        "v_max_u32_dpp v184, v184, v184 row_ror:1 row_mask:0xf bank_mask:0xf\n"
        "v_mov_b32 v185, v184\n"
        "s_nop 1\n"
        "v_permlane16_swap_b32 v184, v185\n"
        "v_max_u32 v184, v184, v185\n"
        "v_mov_b32 v185, v184\n"
        "s_nop 1\n"
        "v_permlane32_swap_b32 v184, v185\n"
        "v_max_u32 v184, v184, v185\n"
        "v_min_u32 v184, -2, v184\n"
        "s_and_b32 s54, s76, 0x7f80\n"
        "s_add_u32 s54, s54, s74\n"
        "s_add_u32 s54, s54, 0x20000\n"
        "v_lshrrev_b32 v185, 2, v0\n"
        "s_mov_b32 exec_hi, 0\n"
        "buffer_store_dword v184, v185, %[srd], s54 offen sc1\n"
        "s_mov_b64 exec, -1\n"
        "Lkvskip_%=:\n"
        "ds_read_b128 v[128:131], v4 offset:49152\n"
        "ds_read_b128 v[132:135], v4 offset:49408\n"
        "ds_read_b128 v[136:139], v4 offset:49664\n"
        "ds_read_b128 v[140:143], v4 offset:49920\n"
        "s_waitcnt lgkmcnt(6)\n"
        "v_mfma_f32_16x16x32_bf16 v[160:163], a[236:239], v[144:147], v[160:163]\n"
        "ds_read_b128 v[144:147], v4 offset:53248\n"
        "v_mfma_f32_16x16x32_bf16 v[164:167], a[236:239], v[148:151], v[164:167]\n"
        "ds_read_b128 v[148:151], v4 offset:53504\n"
        "s_waitcnt lgkmcnt(6)\n"
        "v_mfma_f32_16x16x32_bf16 v[168:171], a[236:239], v[152:155], v[168:171]\n"
        "ds_read_b128 v[152:155], v4 offset:53760\n"
        "v_mfma_f32_16x16x32_bf16 v[172:175], a[236:239], v[156:159], v[172:175]\n"
        "ds_read_b128 v[156:159], v4 offset:54016\n"
        "s_waitcnt lgkmcnt(6)\n"
        "v_mfma_f32_16x16x32_bf16 v[160:163], a[240:243], v[128:131], v[160:163]\n"
        "ds_read_b128 v[128:131], v4 offset:57344\n"
        "v_mfma_f32_16x16x32_bf16 v[164:167], a[240:243], v[132:135], v[164:167]\n"
        "ds_read_b128 v[132:135], v4 offset:57600\n"
        "s_waitcnt lgkmcnt(6)\n"
        "v_mfma_f32_16x16x32_bf16 v[168:171], a[240:243], v[136:139], v[168:171]\n"
        "ds_read_b128 v[136:139], v4 offset:57856\n"
        "v_mfma_f32_16x16x32_bf16 v[172:175], a[240:243], v[140:143], v[172:175]\n"
        "ds_read_b128 v[140:143], v4 offset:58112\n"
        "s_waitcnt lgkmcnt(6)\n"
        "v_mfma_f32_16x16x32_bf16 v[160:163], a[244:247], v[144:147], v[160:163]\n"
        "ds_read_b128 v[144:147], v4 offset:61440\n"
        "v_mfma_f32_16x16x32_bf16 v[164:167], a[244:247], v[148:151], v[164:167]\n"
        "ds_read_b128 v[148:151], v4 offset:61696\n"
        "s_waitcnt lgkmcnt(6)\n"
        "v_mfma_f32_16x16x32_bf16 v[168:171], a[244:247], v[152:155], v[168:171]\n"
        "ds_read_b128 v[152:155], v4 offset:61952\n"
        "v_mfma_f32_16x16x32_bf16 v[172:175], a[244:247], v[156:159], v[172:175]\n"
        "ds_read_b128 v[156:159], v4 offset:62208\n"
        "s_waitcnt lgkmcnt(6)\n"
        "v_mfma_f32_16x16x32_bf16 v[160:163], a[248:251], v[128:131], v[160:163]\n"
        "ds_read2_b32 v[240:241], v8 offset0:0 offset1:64\n"
        "ds_read2_b32 v[242:243], v8 offset0:16 offset1:80\n"
        "v_mfma_f32_16x16x32_bf16 v[164:167], a[248:251], v[132:135], v[164:167]\n"
        "ds_read2_b32 v[244:245], v8 offset0:32 offset1:96\n"
        "ds_read2_b32 v[246:247], v8 offset0:48 offset1:112\n"
        "s_waitcnt lgkmcnt(8)\n"
        "v_mfma_f32_16x16x32_bf16 v[168:171], a[248:251], v[136:139], v[168:171]\n"
        "v_mfma_f32_16x16x32_bf16 v[172:175], a[248:251], v[140:143], v[172:175]\n"
        "s_waitcnt lgkmcnt(6)\n"
        "v_mfma_f32_16x16x32_bf16 v[160:163], a[252:255], v[144:147], v[160:163]\n"
        "v_mfma_f32_16x16x32_bf16 v[164:167], a[252:255], v[148:151], v[164:167]\n"
        "s_waitcnt lgkmcnt(4)\n"
        "v_mfma_f32_16x16x32_bf16 v[168:171], a[252:255], v[152:155], v[168:171]\n"
        "v_mfma_f32_16x16x32_bf16 v[172:175], a[252:255], v[156:159], v[172:175]\n"
        "s_waitcnt lgkmcnt(0)\n"
        "v_mul_f32 v241, 0x3eb8aa3b, v241\n"
        "v_mul_f32 v243, 0x3eb8aa3b, v243\n"
        "v_mul_f32 v245, 0x3eb8aa3b, v245\n"
        "v_mul_f32 v247, 0x3eb8aa3b, v247\n"
        "v_pk_fma_f32 v[160:161], v[240:241], v[232:233], v[160:161] op_sel_hi:[0,1,1] neg_lo:[1,0,0] neg_hi:[1,0,0]\n"
        "v_pk_fma_f32 v[162:163], v[240:241], v[234:235], v[162:163] op_sel_hi:[0,1,1] neg_lo:[1,0,0] neg_hi:[1,0,0]\n"
        "v_pk_fma_f32 v[164:165], v[242:243], v[232:233], v[164:165] op_sel_hi:[0,1,1] neg_lo:[1,0,0] neg_hi:[1,0,0]\n"
        "v_pk_fma_f32 v[166:167], v[242:243], v[234:235], v[166:167] op_sel_hi:[0,1,1] neg_lo:[1,0,0] neg_hi:[1,0,0]\n"
        "v_pk_fma_f32 v[168:169], v[244:245], v[232:233], v[168:169] op_sel_hi:[0,1,1] neg_lo:[1,0,0] neg_hi:[1,0,0]\n"
        "v_pk_fma_f32 v[170:171], v[244:245], v[234:235], v[170:171] op_sel_hi:[0,1,1] neg_lo:[1,0,0] neg_hi:[1,0,0]\n"
        "v_pk_fma_f32 v[172:173], v[246:247], v[232:233], v[172:173] op_sel_hi:[0,1,1] neg_lo:[1,0,0] neg_hi:[1,0,0]\n"
        "v_pk_fma_f32 v[174:175], v[246:247], v[234:235], v[174:175] op_sel_hi:[0,1,1] neg_lo:[1,0,0] neg_hi:[1,0,0]\n"
        "v_pk_mul_f32 v[160:161], v[240:241], v[160:161] op_sel:[1,0] op_sel_hi:[1,1]\n"
        "v_pk_mul_f32 v[162:163], v[240:241], v[162:163] op_sel:[1,0] op_sel_hi:[1,1]\n"
        "v_pk_mul_f32 v[164:165], v[242:243], v[164:165] op_sel:[1,0] op_sel_hi:[1,1]\n"
        "v_pk_mul_f32 v[166:167], v[242:243], v[166:167] op_sel:[1,0] op_sel_hi:[1,1]\n"
        "v_pk_mul_f32 v[168:169], v[244:245], v[168:169] op_sel:[1,0] op_sel_hi:[1,1]\n"
        "v_pk_mul_f32 v[170:171], v[244:245], v[170:171] op_sel:[1,0] op_sel_hi:[1,1]\n"
        "v_pk_mul_f32 v[172:173], v[246:247], v[172:173] op_sel:[1,0] op_sel_hi:[1,1]\n"
        "v_pk_mul_f32 v[174:175], v[246:247], v[174:175] op_sel:[1,0] op_sel_hi:[1,1]\n"
        "v_cmp_u_f32 vcc, v160, v164\n"
        "v_cmp_u_f32 s[62:63], v168, v172\n"
        "s_nop 3\n"
        "s_or_b64 vcc, vcc, s[62:63]\n"
        "s_or_b32 vcc_lo, vcc_lo, s53\n"
        "s_cbranch_vccnz Lsebad_%=\n"
        "; ---- stage Q, wait for the K/V tiles, next round\n"
        "Lseok_%=:\n"
        "v_cvt_pk_bf16_f32 v192, v160, v161\n"
        "v_cvt_pk_bf16_f32 v193, v162, v163\n"
        "v_cvt_pk_bf16_f32 v194, v164, v165\n"
        "v_cvt_pk_bf16_f32 v195, v166, v167\n"
        "v_cvt_pk_bf16_f32 v196, v168, v169\n"
        "v_cvt_pk_bf16_f32 v197, v170, v171\n"
        "v_cvt_pk_bf16_f32 v198, v172, v173\n"
        "v_cvt_pk_bf16_f32 v199, v174, v175\n"
        "v_permlane16_swap_b32 v192, v194\n"
        "v_permlane16_swap_b32 v193, v195\n"
        "v_permlane16_swap_b32 v196, v198\n"
        "v_permlane16_swap_b32 v197, v199\n"
        "v_pk_mul_f32 v[128:129], v[160:161], v[160:161]\n"
        "v_pk_mul_f32 v[130:131], v[164:165], v[164:165]\n"
        "v_pk_mul_f32 v[132:133], v[168:169], v[168:169]\n"
        "v_pk_mul_f32 v[134:135], v[172:173], v[172:173]\n"
        "v_pk_fma_f32 v[128:129], v[162:163], v[162:163], v[128:129]\n"
        "v_pk_fma_f32 v[130:131], v[166:167], v[166:167], v[130:131]\n"
        "v_pk_fma_f32 v[132:133], v[170:171], v[170:171], v[132:133]\n"
        "v_pk_fma_f32 v[134:135], v[174:175], v[174:175], v[134:135]\n"
        "v_add_f32 v128, v128, v129\n"
        "v_add_f32 v130, v130, v131\n"
        "v_add_f32 v132, v132, v133\n"
        "v_add_f32 v134, v134, v135\n"
        "s_nop 0\n"
        "v_permlane32_swap_b32 v128, v132\n"
        "v_permlane32_swap_b32 v130, v134\n"
        "v_add_f32 v128, v128, v132\n"
        "v_add_f32 v130, v130, v134\n"
        "s_nop 1\n"
        "v_permlane16_swap_b32 v128, v130\n"
        "v_add_f32 v128, v128, v130\n"
        "s_nop 1\n"
        "v_max_u32_dpp v128, v128, v128 row_ror:8 row_mask:0xf bank_mask:0xf\n"
        "s_nop 1\n"
        "v_max_u32_dpp v128, v128, v128 row_ror:4 row_mask:0xf bank_mask:0xf\n"
        "s_nop 1\n"
        "v_max_u32_dpp v128, v128, v128 row_ror:2 row_mask:0xf bank_mask:0xf\n"
        "s_nop 1\n"
        "v_max_u32_dpp v128, v128, v128 row_ror:1 row_mask:0xf bank_mask:0xf\n"
        "v_mov_b32 v129, v128\n"
        "s_nop 1\n"
        "v_permlane16_swap_b32 v128, v129\n"
        "v_max_u32 v128, v128, v129\n"
        "v_mov_b32 v129, v128\n"
        "s_nop 1\n"
        "v_permlane32_swap_b32 v128, v129\n"
        "v_max_u32 v128, v128, v129\n"
        "ds_write_b128 v227, v[192:195]\n"
        "ds_write_b128 v227, v[196:199] offset:1024\n"
        "s_lshr_b32 s54, s81, 12\n"
        "s_add_u32 s54, s54, s52\n"
        "v_mov_b32 v129, s54\n"
        "ds_write_b32 v129, v128 offset:512\n"
        "s_waitcnt lgkmcnt(0)\n"
        "s_barrier\n"
        "v_accvgpr_write_b32 a192, v14\n"
        "v_accvgpr_write_b32 a193, v14\n"
        "v_accvgpr_write_b32 a194, v14\n"
        "v_accvgpr_write_b32 a195, v14\n"
        "v_accvgpr_write_b32 a196, v14\n"
        "v_accvgpr_write_b32 a197, v14\n"
        "v_accvgpr_write_b32 a198, v14\n"
        "v_accvgpr_write_b32 a199, v14\n"
        "v_accvgpr_write_b32 a200, v14\n"
        "v_accvgpr_write_b32 a201, v14\n"
        "v_accvgpr_write_b32 a202, v14\n"
        "v_accvgpr_write_b32 a203, v14\n"
        "v_accvgpr_write_b32 a204, v14\n"
        "v_accvgpr_write_b32 a205, v14\n"
        "v_accvgpr_write_b32 a206, v14\n"
        "v_accvgpr_write_b32 a207, v14\n"
        "v_accvgpr_write_b32 a208, v14\n"
        "v_accvgpr_write_b32 a209, v14\n"
        "v_accvgpr_write_b32 a210, v14\n"
        "v_accvgpr_write_b32 a211, v14\n"
        "v_accvgpr_write_b32 a212, v14\n"
        "v_accvgpr_write_b32 a213, v14\n"
        "v_accvgpr_write_b32 a214, v14\n"
        "v_accvgpr_write_b32 a215, v14\n"
        "v_accvgpr_write_b32 a216, v14\n"
        "v_accvgpr_write_b32 a217, v14\n"
        "v_accvgpr_write_b32 a218, v14\n"
        "v_accvgpr_write_b32 a219, v14\n"
        "v_accvgpr_write_b32 a220, v14\n"
        "v_accvgpr_write_b32 a221, v14\n"
        "v_accvgpr_write_b32 a222, v14\n"
        "v_accvgpr_write_b32 a223, v14\n"
        "v_accvgpr_write_b32 a224, v14\n"
        "v_accvgpr_write_b32 a225, v14\n"
        "v_accvgpr_write_b32 a226, v14\n"
        "v_accvgpr_write_b32 a227, v14\n"
        "v_accvgpr_write_b32 a228, v14\n"
        "v_accvgpr_write_b32 a229, v14\n"
        "v_accvgpr_write_b32 a230, v14\n"
        "v_accvgpr_write_b32 a231, v14\n"
        "v_accvgpr_write_b32 a232, v14\n"
        "v_accvgpr_write_b32 a233, v14\n"
        "v_accvgpr_write_b32 a234, v14\n"
        "v_accvgpr_write_b32 a235, v14\n"
        "v_accvgpr_write_b32 a236, v14\n"
        "v_accvgpr_write_b32 a237, v14\n"
        "v_accvgpr_write_b32 a238, v14\n"
        "v_accvgpr_write_b32 a239, v14\n"
        "v_accvgpr_write_b32 a240, v14\n"
        "v_accvgpr_write_b32 a241, v14\n"
        "v_accvgpr_write_b32 a242, v14\n"
        "v_accvgpr_write_b32 a243, v14\n"
        "v_accvgpr_write_b32 a244, v14\n"
        "v_accvgpr_write_b32 a245, v14\n"
        "v_accvgpr_write_b32 a246, v14\n"
        "v_accvgpr_write_b32 a247, v14\n"
        "v_accvgpr_write_b32 a248, v14\n"
        "v_accvgpr_write_b32 a249, v14\n"
        "v_accvgpr_write_b32 a250, v14\n"
        "v_accvgpr_write_b32 a251, v14\n"
        "v_accvgpr_write_b32 a252, v14\n"
        "v_accvgpr_write_b32 a253, v14\n"
        "v_accvgpr_write_b32 a254, v14\n"
        "v_accvgpr_write_b32 a255, v14\n"
        "s_sleep 1\n"
        "s_lshr_b32 s54, s81, 1\n"
        "s_add_u32 s54, s54, s74\n"
        "s_add_u32 s54, s54, 131072\n"
        "s_mov_b32 s56, 0\n"
        "Lsp51_%=:\n"
        "s_add_u32 s56, s56, 1\n"
        "s_cmp_gt_u32 s56, 16384\n"
        "s_cbranch_scc1 Lsq52_%=\n"
        "buffer_load_dword v132, v15, %[srd], s54 offen sc1\n"
        "s_waitcnt vmcnt(0)\n"
        "v_cmp_eq_u32 vcc, -1, v132\n"
        "s_cbranch_vccnz Lsp51_%=\n"
        "Lsq52_%=:\n"
        "s_mov_b32 s58, 0xffff\n"
        "s_mov_b32 s59, 0xffff\n"
        "s_add_u32 s60, s74, s81\n"
        "s_add_u32 s60, s60, 0\n"
        "s_lshr_b32 s54, s81, 1\n"
        "s_add_u32 s54, s54, s74\n"
        "s_add_u32 s54, s54, 131072\n"
        "buffer_load_dword a0, v15, %[srd], s54 offen sc1\n"
        "s_add_u32 s54, s80, 0\n"
        "s_and_b32 s54, s54, 0x3fff\n"
        "s_add_u32 s54, s54, s60\n"
        "buffer_load_dwordx4 a[64:67], v0, %[srd], s54 offen sc1\n"
        "s_mov_b64 exec, s[58:59]\n"
        "buffer_load_dwordx4 a[128:131], v230, %[srd], s54 offen sc1\n"
        "buffer_load_dwordx4 a[132:135], v230, %[srd], s54 offen offset:512 sc1\n"
        "s_mov_b64 exec, -1\n"
        "s_add_u32 s54, s80, 1024\n"
        "s_and_b32 s54, s54, 0x3fff\n"
        "s_add_u32 s54, s54, s60\n"
        "buffer_load_dwordx4 a[68:71], v0, %[srd], s54 offen sc1\n"
        "s_mov_b64 exec, s[58:59]\n"
        "buffer_load_dwordx4 a[136:139], v230, %[srd], s54 offen sc1\n"
        "buffer_load_dwordx4 a[140:143], v230, %[srd], s54 offen offset:512 sc1\n"
        "s_mov_b64 exec, -1\n"
        "s_add_u32 s64, s64, 4194304\n"
        "s_add_u32 s65, s65, 4194304\n"
        "s_add_u32 s66, s66, 1048576\n"
        "s_add_u32 s67, s67, 1048576\n"
        "s_add_u32 s68, s68, 4194304\n"
        "s_add_u32 s70, s70, 131072\n"
        "s_add_u32 s71, s71, 131072\n"
        "s_add_u32 s74, s74, 163840\n"
        "s_add_u32 s75, s75, 32768\n"
        "s_add_u32 s51, s51, 1\n"
        "s_add_u32 s87, s87, 1\n"
        "s_add_u32 s34, s34, 524288\n"
        "s_branch Ltop_%=\n"
        "; ---- final output (whole lines through LDS), epoch\n"
        "Lfinal_%=:\n"
        "s_nop 1\n"
        "v_cvt_pk_bf16_f32 v128, v160, v161\n"
        "v_cvt_pk_bf16_f32 v129, v162, v163\n"
        "v_cvt_pk_bf16_f32 v130, v164, v165\n"
        "v_cvt_pk_bf16_f32 v131, v166, v167\n"
        "v_cvt_pk_bf16_f32 v132, v168, v169\n"
        "v_cvt_pk_bf16_f32 v133, v170, v171\n"
        "v_cvt_pk_bf16_f32 v134, v172, v173\n"
        "v_cvt_pk_bf16_f32 v135, v174, v175\n"
        "v_permlane16_swap_b32 v128, v130\n"
        "v_permlane16_swap_b32 v129, v131\n"
        "v_permlane16_swap_b32 v132, v134\n"
        "v_permlane16_swap_b32 v133, v135\n"
        "v_add_u32 v254, 0x10000, v2\n"
        "ds_write_b128 v254, v[128:131]\n"
        "ds_write_b128 v254, v[132:135] offset:512\n"
        "v_bfe_u32 v140, v0, 4, 3\n"
        "v_lshrrev_b32 v141, 7, v0\n"
        "s_lshr_b32 s54, s81, 6\n"
        "s_add_u32 s54, s54, s49\n"
        "s_add_u32 s54, s54, 0x10000\n"
        "v_lshlrev_b32 v142, 10, v140\n"
        "v_lshl_add_u32 v142, v141, 4, v142\n"
        "v_add_u32 v142, s54, v142\n"
        "v_lshlrev_b32 v140, 4, v140\n"
        "v_lshl_add_u32 v140, v141, 10, v140\n"
        "s_lshl_b32 s54, s69, 16\n"
        "s_add_u32 s54, s54, s81\n"
        "s_lshl_b32 s55, s86, 7\n"
        "s_add_u32 s54, s54, s55\n"
        "s_add_u32 s55, s54, 8192\n"
        "s_waitcnt lgkmcnt(0)\n"
        "s_barrier\n"
        "ds_read_b128 v[128:131], v142\n"
        "ds_read_b128 v[132:135], v142 offset:128\n"
        "s_waitcnt lgkmcnt(1)\n"
        "buffer_store_dwordx4 v[128:131], v140, %[sro], s54 offen sc1\n"
        "s_waitcnt lgkmcnt(0)\n"
        "buffer_store_dwordx4 v[132:135], v140, %[sro], s55 offen sc1\n"
        "s_cmp_eq_u32 s46, 0\n"
        "s_cbranch_scc0 Lfin2_%=\n"
        "v_mov_b32 v128, s47\n"
        "v_mov_b32 v129, s48\n"
        "s_mov_b64 exec, 1\n"
        "buffer_store_dwordx2 v[128:129], off, %[srd], 0 sc1\n"
        "s_mov_b64 exec, -1\n"
        "Lfin2_%=:\n"
        "s_endpgm\n"
        "Lf2spin_%=:\n"
        "s_mov_b32 s56, 0\n"
        "Lsp1_%=:\n"
        "s_add_u32 s56, s56, 1\n"
        "s_cmp_gt_u32 s56, 16384\n"
        "s_cbranch_scc1 Lsq2_%=\n"
        "s_mov_b32 s54, 0x8000\n"
        "buffer_load_dwordx4 v[28:31], v18, %[srd], s54 offen sc1\n"
        "s_waitcnt vmcnt(0)\n"
        "v_xor_b32 v28, v28, v19\n"
        "v_xor_b32 v29, v29, v19\n"
        "v_xor_b32 v30, v30, v19\n"
        "v_xor_b32 v31, v31, v19\n"
        "v_or3_b32 v28, v28, v29, v30\n"
        "v_or_b32 v28, v28, v31\n"
        "v_cmp_ne_u32 vcc, 0, v28\n"
        "s_cbranch_vccnz Lsp1_%=\n"
        "Lsq2_%=:\n"
        "s_branch Lf2back_%=\n"
        "; ---- K/V norm lines not yet there\n"
        "Lnavmiss_%=:\n"
        "s_nop 15\n"
        "s_lshr_b32 s54, s81, 1\n"
        "s_add_u32 s54, s54, s74\n"
        "s_add_u32 s54, s54, 4294934528\n"
        "s_mov_b32 s56, 0\n"
        "Lsp3_%=:\n"
        "s_add_u32 s56, s56, 1\n"
        "s_cmp_gt_u32 s56, 16384\n"
        "s_cbranch_scc1 Lsq4_%=\n"
        "buffer_load_dword v132, v15, %[srd], s54 offen sc1\n"
        "s_waitcnt vmcnt(0)\n"
        "v_cmp_eq_u32 vcc, -1, v132\n"
        "s_cbranch_vccnz Lsp3_%=\n"
        "Lsq4_%=:\n"
        "s_add_u32 s57, s57, 1\n"
        "s_cmp_gt_u32 s57, 8\n"
        "s_cbranch_scc1 LslowA_%=\n"
        "Lrehead_%=:\n"
        "s_waitcnt vmcnt(0) lgkmcnt(0)\n"
        "s_mov_b32 s58, 0xffff\n"
        "s_mov_b32 s59, 0xffff\n"
        "s_add_u32 s60, s74, s81\n"
        "s_add_u32 s60, s60, 4294803456\n"
        "s_lshr_b32 s54, s81, 1\n"
        "s_add_u32 s54, s54, s74\n"
        "s_add_u32 s54, s54, 4294934528\n"
        "buffer_load_dword a0, v15, %[srd], s54 offen sc1\n"
        "s_add_u32 s54, s80, 0\n"
        "s_and_b32 s54, s54, 0x3fff\n"
        "s_add_u32 s54, s54, s60\n"
        "buffer_load_dwordx4 a[64:67], v0, %[srd], s54 offen sc1\n"
        "s_mov_b64 exec, s[58:59]\n"
        "buffer_load_dwordx4 a[128:131], v230, %[srd], s54 offen sc1\n"
        "buffer_load_dwordx4 a[132:135], v230, %[srd], s54 offen offset:512 sc1\n"
        "s_mov_b64 exec, -1\n"
        "s_add_u32 s54, s80, 1024\n"
        "s_and_b32 s54, s54, 0x3fff\n"
        "s_add_u32 s54, s54, s60\n"
        "buffer_load_dwordx4 a[68:71], v0, %[srd], s54 offen sc1\n"
        "s_mov_b64 exec, s[58:59]\n"
        "buffer_load_dwordx4 a[136:139], v230, %[srd], s54 offen sc1\n"
        "buffer_load_dwordx4 a[140:143], v230, %[srd], s54 offen offset:512 sc1\n"
        "s_mov_b64 exec, -1\n"
        "s_branch Lsa_%=\n"
        "Lf3spin_%=:\n"
        "s_mov_b32 s56, 0\n"
        "Lsp7_%=:\n"
        "s_add_u32 s56, s56, 1\n"
        "s_cmp_gt_u32 s56, 16384\n"
        "s_cbranch_scc1 Lsq8_%=\n"
        "buffer_load_dwordx4 v[128:131], v241, %[srd], s75 offen sc1\n"
        "s_waitcnt vmcnt(0)\n"
        "v_xor_b32 v128, s51, v128\n"
        "v_xor_b32 v129, s51, v129\n"
        "v_xor_b32 v130, s51, v130\n"
        "v_xor_b32 v131, s51, v131\n"
        "v_or3_b32 v128, v128, v129, v130\n"
        "v_or_b32 v128, v128, v131\n"
        "v_cmp_ne_u32 vcc, 0, v128\n"
        "s_cbranch_vccnz Lsp7_%=\n"
        "Lsq8_%=:\n"
        "s_branch Lsb_%=\n"
        "; ---- window not yet published at the first look\n"
        "LwayB_%=:\n"
        "v_pk_mul_f32 v[40:41], v[160:161], v[160:161]\n"
        "v_pk_mul_f32 v[42:43], v[164:165], v[164:165]\n"
        "v_pk_mul_f32 v[44:45], v[168:169], v[168:169]\n"
        "v_pk_mul_f32 v[46:47], v[172:173], v[172:173]\n"
        "v_pk_fma_f32 v[40:41], v[162:163], v[162:163], v[40:41]\n"
        "v_pk_fma_f32 v[42:43], v[166:167], v[166:167], v[42:43]\n"
        "v_pk_fma_f32 v[44:45], v[170:171], v[170:171], v[44:45]\n"
        "v_pk_fma_f32 v[46:47], v[174:175], v[174:175], v[46:47]\n"
        "v_add_f32 v40, v40, v41\n"
        "v_add_f32 v42, v42, v43\n"
        "v_add_f32 v44, v44, v45\n"
        "v_add_f32 v46, v46, v47\n"
        "s_nop 0\n"
        "v_permlane32_swap_b32 v40, v44\n"
        "v_permlane32_swap_b32 v42, v46\n"
        "v_add_f32 v40, v40, v44\n"
        "v_add_f32 v42, v42, v46\n"
        "s_nop 1\n"
        "v_permlane16_swap_b32 v40, v42\n"
        "v_add_f32 v40, v40, v42\n"
        "s_nop 1\n"
        "v_max_u32_dpp v40, v40, v40 row_ror:8 row_mask:0xf bank_mask:0xf\n"
        "s_nop 1\n"
        "v_max_u32_dpp v40, v40, v40 row_ror:4 row_mask:0xf bank_mask:0xf\n"
        "s_nop 1\n"
        "v_max_u32_dpp v40, v40, v40 row_ror:2 row_mask:0xf bank_mask:0xf\n"
        "s_nop 1\n"
        "v_max_u32_dpp v40, v40, v40 row_ror:1 row_mask:0xf bank_mask:0xf\n"
        "v_mov_b32 v41, v40\n"
        "s_nop 1\n"
        "v_permlane16_swap_b32 v40, v41\n"
        "v_max_u32 v40, v40, v41\n"
        "v_mov_b32 v41, v40\n"
        "s_nop 1\n"
        "v_permlane32_swap_b32 v40, v41\n"
        "v_max_u32 v40, v40, v41\n"
        "v_xor_b32 v56, 0xff800000, v16\n"
        "v_xor_b32 v57, 0xff800000, v17\n"
        "v_xor_b32 v58, 0xff800000, v18\n"
        "v_xor_b32 v59, 0xff800000, v19\n"
        "v_xor_b32 v60, 0xff800000, v20\n"
        "v_xor_b32 v61, 0xff800000, v21\n"
        "v_xor_b32 v62, 0xff800000, v22\n"
        "v_xor_b32 v63, 0xff800000, v23\n"
        "v_xor_b32 v64, 0xff800000, v24\n"
        "v_xor_b32 v65, 0xff800000, v25\n"
        "v_xor_b32 v66, 0xff800000, v26\n"
        "v_xor_b32 v67, 0xff800000, v27\n"
        "v_xor_b32 v68, 0xff800000, v28\n"
        "v_xor_b32 v69, 0xff800000, v29\n"
        "v_xor_b32 v70, 0xff800000, v30\n"
        "v_xor_b32 v71, 0xff800000, v31\n"
        "buffer_load_dword v249, v224, %[srd], s70 offen sc1\n"
        "v_mfma_f32_32x32x16_bf16 v[160:175], v[32:35], v[32:35], 0\n"
        "v_mfma_f32_32x32x16_bf16 v[176:191], v[32:35], v[32:35], 0\n"
        "v_mfma_f32_32x32x16_bf16 v[128:143], v[200:203], v[192:195], v[16:31]\n"
        "s_nop 15\n"
        "v_exp_f32 v128, v128\n"
        "v_exp_f32 v129, v129\n"
        "v_exp_f32 v130, v130\n"
        "v_exp_f32 v131, v131\n"
        "v_exp_f32 v132, v132\n"
        "v_exp_f32 v133, v133\n"
        "v_mfma_f32_32x32x16_bf16 v[144:159], v[200:203], v[196:199], 0\n"
        "v_exp_f32 v134, v134\n"
        "v_exp_f32 v135, v135\n"
        "v_exp_f32 v136, v136\n"
        "v_exp_f32 v137, v137\n"
        "v_exp_f32 v138, v138\n"
        "v_smfmac_f32_32x32x32_bf16 v[160:175], v[208:211], v[128:135], v253\n"
        "v_exp_f32 v139, v139\n"
        "v_exp_f32 v140, v140\n"
        "v_exp_f32 v141, v141\n"
        "v_exp_f32 v142, v142\n"
        "v_exp_f32 v143, v143\n"
        "s_nop 0\n"
        "v_exp_f32 v144, v144\n"
        "v_exp_f32 v145, v145\n"
        "v_smfmac_f32_32x32x32_bf16 v[160:175], v[212:215], v[136:143], v253\n"
        "v_exp_f32 v146, v146\n"
        "v_exp_f32 v147, v147\n"
        "v_exp_f32 v148, v148\n"
        "v_exp_f32 v149, v149\n"
        "v_mfma_f32_32x32x16_bf16 v[128:143], v[204:207], v[196:199], v[16:31]\n"
        "v_exp_f32 v150, v150\n"
        "v_exp_f32 v151, v151\n"
        "v_exp_f32 v152, v152\n"
        "v_exp_f32 v153, v153\n"
        "v_exp_f32 v154, v154\n"
        "v_smfmac_f32_32x32x32_bf16 v[176:191], v[208:211], v[144:151], v253\n"
        "v_exp_f32 v155, v155\n"
        "v_exp_f32 v156, v156\n"
        "v_exp_f32 v157, v157\n"
        "v_exp_f32 v158, v158\n"
        "v_exp_f32 v159, v159\n"
        "s_nop 0\n"
        "v_exp_f32 v128, v128\n"
        "v_exp_f32 v129, v129\n"
        "v_smfmac_f32_32x32x32_bf16 v[176:191], v[212:215], v[152:159], v253\n"
        "v_exp_f32 v130, v130\n"
        "v_exp_f32 v131, v131\n"
        "v_exp_f32 v132, v132\n"
        "v_exp_f32 v133, v133\n"
        "v_exp_f32 v134, v134\n"
        "v_exp_f32 v135, v135\n"
        "v_exp_f32 v136, v136\n"
        "v_exp_f32 v137, v137\n"
        "v_exp_f32 v138, v138\n"
        "v_smfmac_f32_32x32x32_bf16 v[176:191], v[216:219], v[128:135], v253\n"
        "v_exp_f32 v139, v139\n"
        "v_exp_f32 v140, v140\n"
        "v_exp_f32 v141, v141\n"
        "v_exp_f32 v142, v142\n"
        "v_exp_f32 v143, v143\n"
        "s_nop 1\n"
        "v_smfmac_f32_32x32x32_bf16 v[176:191], v[220:223], v[136:143], v253\n"
        "s_waitcnt vmcnt(0)\n"
        "v_cmp_eq_u32 vcc, -1, v249\n"
        "s_cbranch_vccz Lk_wb_%=\n"
        "s_mov_b32 s56, 0\n"
        "Lsp24_%=:\n"
        "s_add_u32 s56, s56, 1\n"
        "s_cmp_gt_u32 s56, 16384\n"
        "s_cbranch_scc1 Lsq25_%=\n"
        "buffer_load_dword v249, v224, %[srd], s70 offen sc1\n"
        "s_waitcnt vmcnt(0)\n"
        "v_cmp_eq_u32 vcc, -1, v249\n"
        "s_cbranch_vccnz Lsp24_%=\n"
        "Lsq25_%=:\n"
        "Lk_wb_%=:\n"
        "s_add_u32 s54, s68, 4294443008\n"
        "s_cmp_ge_u32 s69, 4\n"
        "s_cselect_b32 s54, s54, 0x7f000000\n"
        "buffer_load_dwordx4 v[96:99], v0, %[srd], s54 offen sc1\n"
        "s_mov_b64 exec, s[58:59]\n"
        "buffer_load_dwordx4 a[0:3], v12, %[srd], s54 offen sc1\n"
        "buffer_load_dwordx4 a[4:7], v12, %[srd], s54 offen offset:512 sc1\n"
        "s_mov_b64 exec, -1\n"
        "s_add_u32 s54, s68, 4294508544\n"
        "s_cmp_ge_u32 s69, 4\n"
        "s_cselect_b32 s54, s54, 0x7f000000\n"
        "buffer_load_dwordx4 v[100:103], v0, %[srd], s54 offen sc1\n"
        "s_mov_b64 exec, s[58:59]\n"
        "buffer_load_dwordx4 a[8:11], v12, %[srd], s54 offen sc1\n"
        "buffer_load_dwordx4 a[12:15], v12, %[srd], s54 offen offset:512 sc1\n"
        "s_mov_b64 exec, -1\n"
        "s_add_u32 s54, s68, 4294574080\n"
        "s_cmp_ge_u32 s69, 3\n"
        "s_cselect_b32 s54, s54, 0x7f000000\n"
        "buffer_load_dwordx4 v[104:107], v0, %[srd], s54 offen sc1\n"
        "s_mov_b64 exec, s[58:59]\n"
        "buffer_load_dwordx4 a[16:19], v12, %[srd], s54 offen sc1\n"
        "buffer_load_dwordx4 a[20:23], v12, %[srd], s54 offen offset:512 sc1\n"
        "s_mov_b64 exec, -1\n"
        "v_max_u32_dpp v48, v249, v249 row_ror:2 row_mask:0xf bank_mask:0xf\n"
        "s_nop 1\n"
        "v_max_u32_dpp v48, v48, v48 row_ror:4 row_mask:0xf bank_mask:0xf\n"
        "s_nop 1\n"
        "v_max_u32_dpp v48, v48, v48 row_ror:8 row_mask:0xf bank_mask:0xf\n"
        "v_max_u32 v49, v48, v250\n"
        "v_max_u32 v48, v48, v251\n"
        "v_mul_f32 v49, v49, v40\n"
        "v_cmp_nge_f32 vcc, 0x46100000, v49\n"
        "s_nop 3\n"
        "s_and_b64 s[62:63], vcc, 1\n"
        "v_cmp_nge_f32 vcc, 0x47800000, v48\n"
        "s_nop 3\n"
        "s_and_b64 vcc, vcc, 2\n"
        "s_or_b64 vcc, vcc, s[62:63]\n"
        "s_cbranch_vccnz LslowB0a_%=\n"
        "s_waitcnt vmcnt(6)\n"
        "s_nop 1\n"
        "v_mfma_f32_32x32x16_bf16 v[128:143], v[96:99], v[192:195], v[56:71]\n"
        "s_nop 15\n"
        "v_exp_f32 v128, v128\n"
        "s_add_u32 s54, s68, 4294639616\n"
        "s_cmp_ge_u32 s69, 3\n"
        "s_cselect_b32 s54, s54, 0x7f000000\n"
        "v_exp_f32 v129, v129\n"
        "buffer_load_dwordx4 v[108:111], v0, %[srd], s54 offen sc1\n"
        "v_exp_f32 v130, v130\n"
        "v_exp_f32 v131, v131\n"
        "v_exp_f32 v132, v132\n"
        "v_exp_f32 v133, v133\n"
        "s_waitcnt vmcnt(4)\n"
        "v_mfma_f32_32x32x16_bf16 v[144:159], v[100:103], v[192:195], 0\n"
        "v_exp_f32 v134, v134\n"
        "v_exp_f32 v135, v135\n"
        "v_exp_f32 v136, v136\n"
        "v_exp_f32 v137, v137\n"
        "v_exp_f32 v138, v138\n"
        "v_smfmac_f32_32x32x32_bf16 v[160:175], a[0:3], v[128:135], v253\n"
        "s_mov_b64 exec, s[58:59]\n"
        "buffer_load_dwordx4 a[24:27], v12, %[srd], s54 offen sc1\n"
        "buffer_load_dwordx4 a[28:31], v12, %[srd], s54 offen offset:512 sc1\n"
        "s_mov_b64 exec, -1\n"
        "v_exp_f32 v139, v139\n"
        "v_exp_f32 v140, v140\n"
        "v_exp_f32 v141, v141\n"
        "v_exp_f32 v142, v142\n"
        "v_exp_f32 v143, v143\n"
        "v_exp_f32 v144, v144\n"
        "s_add_u32 s54, s68, 4294705152\n"
        "s_cmp_ge_u32 s69, 2\n"
        "s_cselect_b32 s54, s54, 0x7f000000\n"
        "v_exp_f32 v145, v145\n"
        "v_smfmac_f32_32x32x32_bf16 v[160:175], a[4:7], v[136:143], v253\n"
        "buffer_load_dwordx4 v[112:115], v0, %[srd], s54 offen sc1\n"
        "v_exp_f32 v146, v146\n"
        "v_exp_f32 v147, v147\n"
        "v_exp_f32 v148, v148\n"
        "v_exp_f32 v149, v149\n"
        "v_mfma_f32_32x32x16_bf16 v[128:143], v[100:103], v[196:199], v[56:71]\n"
        "v_exp_f32 v150, v150\n"
        "v_exp_f32 v151, v151\n"
        "v_exp_f32 v152, v152\n"
        "v_exp_f32 v153, v153\n"
        "v_exp_f32 v154, v154\n"
        "v_smfmac_f32_32x32x32_bf16 v[160:175], a[8:11], v[144:151], v253\n"
        "s_mov_b64 exec, s[58:59]\n"
        "buffer_load_dwordx4 a[32:35], v12, %[srd], s54 offen sc1\n"
        "buffer_load_dwordx4 a[36:39], v12, %[srd], s54 offen offset:512 sc1\n"
        "s_mov_b64 exec, -1\n"
        "v_exp_f32 v155, v155\n"
        "v_exp_f32 v156, v156\n"
        "v_exp_f32 v157, v157\n"
        "v_exp_f32 v158, v158\n"
        "v_exp_f32 v159, v159\n"
        "v_exp_f32 v128, v128\n"
        "s_add_u32 s54, s68, 4294770688\n"
        "s_cmp_ge_u32 s69, 2\n"
        "s_cselect_b32 s54, s54, 0x7f000000\n"
        "v_exp_f32 v129, v129\n"
        "v_smfmac_f32_32x32x32_bf16 v[160:175], a[12:15], v[152:159], v253\n"
        "buffer_load_dwordx4 v[116:119], v0, %[srd], s54 offen sc1\n"
        "v_exp_f32 v130, v130\n"
        "v_exp_f32 v131, v131\n"
        "v_exp_f32 v132, v132\n"
        "v_exp_f32 v133, v133\n"
        "s_waitcnt vmcnt(7)\n"
        "v_mfma_f32_32x32x16_bf16 v[144:159], v[104:107], v[192:195], 0\n"
        "v_exp_f32 v134, v134\n"
        "v_exp_f32 v135, v135\n"
        "v_exp_f32 v136, v136\n"
        "v_exp_f32 v137, v137\n"
        "v_exp_f32 v138, v138\n"
        "v_smfmac_f32_32x32x32_bf16 v[176:191], a[8:11], v[128:135], v253\n"
        "s_mov_b64 exec, s[58:59]\n"
        "buffer_load_dwordx4 a[40:43], v12, %[srd], s54 offen sc1\n"
        "buffer_load_dwordx4 a[44:47], v12, %[srd], s54 offen offset:512 sc1\n"
        "s_mov_b64 exec, -1\n"
        "v_exp_f32 v139, v139\n"
        "v_exp_f32 v140, v140\n"
        "v_exp_f32 v141, v141\n"
        "v_exp_f32 v142, v142\n"
        "v_exp_f32 v143, v143\n"
        "v_exp_f32 v144, v144\n"
        "s_add_u32 s54, s68, 4294836224\n"
        "s_cmp_ge_u32 s69, 1\n"
        "s_cselect_b32 s54, s54, 0x7f000000\n"
        "v_exp_f32 v145, v145\n"
        "v_smfmac_f32_32x32x32_bf16 v[176:191], a[12:15], v[136:143], v253\n"
        "buffer_load_dwordx4 v[120:123], v0, %[srd], s54 offen sc1\n"
        "v_exp_f32 v146, v146\n"
        "v_exp_f32 v147, v147\n"
        "v_exp_f32 v148, v148\n"
        "v_exp_f32 v149, v149\n"
        "v_mfma_f32_32x32x16_bf16 v[128:143], v[104:107], v[196:199], 0\n"
        "v_exp_f32 v150, v150\n"
        "v_exp_f32 v151, v151\n"
        "v_exp_f32 v152, v152\n"
        "v_exp_f32 v153, v153\n"
        "v_exp_f32 v154, v154\n"
        "v_smfmac_f32_32x32x32_bf16 v[160:175], a[16:19], v[144:151], v253\n"
        "s_mov_b64 exec, s[58:59]\n"
        "buffer_load_dwordx4 a[48:51], v12, %[srd], s54 offen sc1\n"
        "buffer_load_dwordx4 a[52:55], v12, %[srd], s54 offen offset:512 sc1\n"
        "s_mov_b64 exec, -1\n"
        "v_exp_f32 v155, v155\n"
        "v_exp_f32 v156, v156\n"
        "v_exp_f32 v157, v157\n"
        "v_exp_f32 v158, v158\n"
        "v_exp_f32 v159, v159\n"
        "v_exp_f32 v128, v128\n"
        "s_add_u32 s54, s68, 4294901760\n"
        "s_cmp_ge_u32 s69, 1\n"
        "s_cselect_b32 s54, s54, 0x7f000000\n"
        "v_exp_f32 v129, v129\n"
        "v_smfmac_f32_32x32x32_bf16 v[160:175], a[20:23], v[152:159], v253\n"
        "buffer_load_dwordx4 v[124:127], v0, %[srd], s54 offen sc1\n"
        "v_exp_f32 v130, v130\n"
        "v_exp_f32 v131, v131\n"
        "v_exp_f32 v132, v132\n"
        "v_exp_f32 v133, v133\n"
        "s_waitcnt vmcnt(10)\n"
        "v_mfma_f32_32x32x16_bf16 v[144:159], v[108:111], v[192:195], 0\n"
        "v_exp_f32 v134, v134\n"
        "v_exp_f32 v135, v135\n"
        "v_exp_f32 v136, v136\n"
        "v_exp_f32 v137, v137\n"
        "v_exp_f32 v138, v138\n"
        "v_smfmac_f32_32x32x32_bf16 v[176:191], a[16:19], v[128:135], v253\n"
        "s_mov_b64 exec, s[58:59]\n"
        "buffer_load_dwordx4 a[56:59], v12, %[srd], s54 offen sc1\n"
        "buffer_load_dwordx4 a[60:63], v12, %[srd], s54 offen offset:512 sc1\n"
        "s_mov_b64 exec, -1\n"
        "v_exp_f32 v139, v139\n"
        "v_exp_f32 v140, v140\n"
        "v_exp_f32 v141, v141\n"
        "v_exp_f32 v142, v142\n"
        "v_exp_f32 v143, v143\n"
        "v_exp_f32 v144, v144\n"
        "s_add_u32 s55, s72, 2097152\n"
        "v_exp_f32 v145, v145\n"
        "v_smfmac_f32_32x32x32_bf16 v[176:191], a[20:23], v[136:143], v253\n"
        "buffer_load_dwordx4 a[128:131], v0, %[srd], s55 offen offset:0 sc1\n"
        "v_exp_f32 v146, v146\n"
        "v_exp_f32 v147, v147\n"
        "v_exp_f32 v148, v148\n"
        "v_exp_f32 v149, v149\n"
        "v_mfma_f32_32x32x16_bf16 v[128:143], v[108:111], v[196:199], 0\n"
        "v_exp_f32 v150, v150\n"
        "v_exp_f32 v151, v151\n"
        "v_exp_f32 v152, v152\n"
        "v_exp_f32 v153, v153\n"
        "v_exp_f32 v154, v154\n"
        "v_smfmac_f32_32x32x32_bf16 v[160:175], a[24:27], v[144:151], v253\n"
        "buffer_load_dwordx4 a[132:135], v0, %[srd], s55 offen offset:1024 sc1\n"
        "v_exp_f32 v155, v155\n"
        "v_exp_f32 v156, v156\n"
        "v_exp_f32 v157, v157\n"
        "v_exp_f32 v158, v158\n"
        "v_exp_f32 v159, v159\n"
        "v_exp_f32 v128, v128\n"
        "v_exp_f32 v129, v129\n"
        "v_smfmac_f32_32x32x32_bf16 v[160:175], a[28:31], v[152:159], v253\n"
        "buffer_load_dwordx4 a[136:139], v0, %[srd], s55 offen offset:2048 sc1\n"
        "v_exp_f32 v130, v130\n"
        "v_exp_f32 v131, v131\n"
        "v_exp_f32 v132, v132\n"
        "v_exp_f32 v133, v133\n"
        "s_waitcnt vmcnt(12)\n"
        "v_mfma_f32_32x32x16_bf16 v[144:159], v[112:115], v[192:195], 0\n"
        "v_exp_f32 v134, v134\n"
        "v_exp_f32 v135, v135\n"
        "v_exp_f32 v136, v136\n"
        "v_exp_f32 v137, v137\n"
        "v_exp_f32 v138, v138\n"
        "v_smfmac_f32_32x32x32_bf16 v[176:191], a[24:27], v[128:135], v253\n"
        "buffer_load_dwordx4 a[140:143], v0, %[srd], s55 offen offset:3072 sc1\n"
        "v_exp_f32 v139, v139\n"
        "v_exp_f32 v140, v140\n"
        "v_exp_f32 v141, v141\n"
        "v_exp_f32 v142, v142\n"
        "v_exp_f32 v143, v143\n"
        "v_exp_f32 v144, v144\n"
        "s_add_u32 s55, s72, 2101248\n"
        "v_exp_f32 v145, v145\n"
        "v_smfmac_f32_32x32x32_bf16 v[176:191], a[28:31], v[136:143], v253\n"
        "buffer_load_dwordx4 a[144:147], v0, %[srd], s55 offen offset:0 sc1\n"
        "v_exp_f32 v146, v146\n"
        "v_exp_f32 v147, v147\n"
        "v_exp_f32 v148, v148\n"
        "v_exp_f32 v149, v149\n"
        "v_mfma_f32_32x32x16_bf16 v[128:143], v[112:115], v[196:199], 0\n"
        "v_exp_f32 v150, v150\n"
        "v_exp_f32 v151, v151\n"
        "v_exp_f32 v152, v152\n"
        "v_exp_f32 v153, v153\n"
        "v_exp_f32 v154, v154\n"
        "v_smfmac_f32_32x32x32_bf16 v[160:175], a[32:35], v[144:151], v253\n"
        "buffer_load_dwordx4 a[148:151], v0, %[srd], s55 offen offset:1024 sc1\n"
        "v_exp_f32 v155, v155\n"
        "v_exp_f32 v156, v156\n"
        "v_exp_f32 v157, v157\n"
        "v_exp_f32 v158, v158\n"
        "v_exp_f32 v159, v159\n"
        "buffer_load_dwordx4 a[152:155], v0, %[srd], s55 offen offset:2048 sc1\n"
        "v_exp_f32 v128, v128\n"
        "v_exp_f32 v129, v129\n"
        "v_smfmac_f32_32x32x32_bf16 v[160:175], a[36:39], v[152:159], v253\n"
        "buffer_load_dwordx4 a[156:159], v0, %[srd], s55 offen offset:3072 sc1\n"
        "v_exp_f32 v130, v130\n"
        "v_exp_f32 v131, v131\n"
        "v_exp_f32 v132, v132\n"
        "v_exp_f32 v133, v133\n"
        "s_waitcnt vmcnt(14)\n"
        "v_mfma_f32_32x32x16_bf16 v[144:159], v[116:119], v[192:195], 0\n"
        "v_exp_f32 v134, v134\n"
        "s_add_u32 s55, s72, 2105344\n"
        "v_exp_f32 v135, v135\n"
        "v_exp_f32 v136, v136\n"
        "v_exp_f32 v137, v137\n"
        "v_exp_f32 v138, v138\n"
        "v_smfmac_f32_32x32x32_bf16 v[176:191], a[32:35], v[128:135], v253\n"
        "buffer_load_dwordx4 a[160:163], v0, %[srd], s55 offen offset:0 sc1\n"
        "v_exp_f32 v139, v139\n"
        "v_exp_f32 v140, v140\n"
        "v_exp_f32 v141, v141\n"
        "v_exp_f32 v142, v142\n"
        "v_exp_f32 v143, v143\n"
        "buffer_load_dwordx4 a[164:167], v0, %[srd], s55 offen offset:1024 sc1\n"
        "v_exp_f32 v144, v144\n"
        "v_exp_f32 v145, v145\n"
        "v_smfmac_f32_32x32x32_bf16 v[176:191], a[36:39], v[136:143], v253\n"
        "buffer_load_dwordx4 a[168:171], v0, %[srd], s55 offen offset:2048 sc1\n"
        "v_exp_f32 v146, v146\n"
        "v_exp_f32 v147, v147\n"
        "v_exp_f32 v148, v148\n"
        "v_exp_f32 v149, v149\n"
        "v_mfma_f32_32x32x16_bf16 v[128:143], v[116:119], v[196:199], 0\n"
        "v_exp_f32 v150, v150\n"
        "v_exp_f32 v151, v151\n"
        "v_exp_f32 v152, v152\n"
        "v_exp_f32 v153, v153\n"
        "v_exp_f32 v154, v154\n"
        "v_smfmac_f32_32x32x32_bf16 v[160:175], a[40:43], v[144:151], v253\n"
        "buffer_load_dwordx4 a[172:175], v0, %[srd], s55 offen offset:3072 sc1\n"
        "v_exp_f32 v155, v155\n"
        "s_add_u32 s55, s72, 2109440\n"
        "v_exp_f32 v156, v156\n"
        "v_exp_f32 v157, v157\n"
        "v_exp_f32 v158, v158\n"
        "v_exp_f32 v159, v159\n"
        "buffer_load_dwordx4 a[176:179], v0, %[srd], s55 offen offset:0 sc1\n"
        "v_exp_f32 v128, v128\n"
        "v_exp_f32 v129, v129\n"
        "v_smfmac_f32_32x32x32_bf16 v[160:175], a[44:47], v[152:159], v253\n"
        "buffer_load_dwordx4 a[180:183], v0, %[srd], s55 offen offset:1024 sc1\n"
        "v_exp_f32 v130, v130\n"
        "v_exp_f32 v131, v131\n"
        "v_exp_f32 v132, v132\n"
        "v_exp_f32 v133, v133\n"
        "s_waitcnt vmcnt(17)\n"
        "v_mfma_f32_32x32x16_bf16 v[144:159], v[120:123], v[192:195], 0\n"
        "v_exp_f32 v134, v134\n"
        "v_exp_f32 v135, v135\n"
        "v_exp_f32 v136, v136\n"
        "v_exp_f32 v137, v137\n"
        "v_exp_f32 v138, v138\n"
        "v_smfmac_f32_32x32x32_bf16 v[176:191], a[40:43], v[128:135], v253\n"
        "buffer_load_dwordx4 a[184:187], v0, %[srd], s55 offen offset:2048 sc1\n"
        "v_exp_f32 v139, v139\n"
        "v_exp_f32 v140, v140\n"
        "v_exp_f32 v141, v141\n"
        "v_exp_f32 v142, v142\n"
        "v_exp_f32 v143, v143\n"
        "buffer_load_dwordx4 a[188:191], v0, %[srd], s55 offen offset:3072 sc1\n"
        "v_exp_f32 v144, v144\n"
        "s_add_u32 s54, s72, 2621440\n"
        "v_exp_f32 v145, v145\n"
        "v_smfmac_f32_32x32x32_bf16 v[176:191], a[44:47], v[136:143], v253\n"
        "buffer_load_dwordx4 a[192:195], v0, %[srd], s54 offen offset:0 sc1\n"
        "v_exp_f32 v146, v146\n"
        "v_exp_f32 v147, v147\n"
        "v_exp_f32 v148, v148\n"
        "v_exp_f32 v149, v149\n"
        "v_mfma_f32_32x32x16_bf16 v[128:143], v[120:123], v[196:199], 0\n"
        "v_exp_f32 v150, v150\n"
        "v_exp_f32 v151, v151\n"
        "v_exp_f32 v152, v152\n"
        "v_exp_f32 v153, v153\n"
        "v_exp_f32 v154, v154\n"
        "v_smfmac_f32_32x32x32_bf16 v[160:175], a[48:51], v[144:151], v253\n"
        "buffer_load_dwordx4 a[196:199], v0, %[srd], s54 offen offset:1024 sc1\n"
        "v_exp_f32 v155, v155\n"
        "v_exp_f32 v156, v156\n"
        "v_exp_f32 v157, v157\n"
        "v_exp_f32 v158, v158\n"
        "v_exp_f32 v159, v159\n"
        "buffer_load_dwordx4 a[200:203], v0, %[srd], s54 offen offset:2048 sc1\n"
        "v_exp_f32 v128, v128\n"
        "v_exp_f32 v129, v129\n"
        "v_smfmac_f32_32x32x32_bf16 v[160:175], a[52:55], v[152:159], v253\n"
        "buffer_load_dwordx4 a[204:207], v0, %[srd], s54 offen offset:3072 sc1\n"
        "v_exp_f32 v130, v130\n"
        "v_exp_f32 v131, v131\n"
        "v_exp_f32 v132, v132\n"
        "v_exp_f32 v133, v133\n"
        "s_waitcnt vmcnt(20)\n"
        "v_mfma_f32_32x32x16_bf16 v[144:159], v[124:127], v[192:195], 0\n"
        "v_exp_f32 v134, v134\n"
        "s_add_u32 s54, s72, 2625536\n"
        "v_exp_f32 v135, v135\n"
        "v_exp_f32 v136, v136\n"
        "v_exp_f32 v137, v137\n"
        "v_exp_f32 v138, v138\n"
        "v_smfmac_f32_32x32x32_bf16 v[176:191], a[48:51], v[128:135], v253\n"
        "buffer_load_dwordx4 a[208:211], v0, %[srd], s54 offen offset:0 sc1\n"
        "v_exp_f32 v139, v139\n"
        "v_exp_f32 v140, v140\n"
        "v_exp_f32 v141, v141\n"
        "v_exp_f32 v142, v142\n"
        "v_exp_f32 v143, v143\n"
        "buffer_load_dwordx4 a[212:215], v0, %[srd], s54 offen offset:1024 sc1\n"
        "v_exp_f32 v144, v144\n"
        "v_exp_f32 v145, v145\n"
        "v_smfmac_f32_32x32x32_bf16 v[176:191], a[52:55], v[136:143], v253\n"
        "buffer_load_dwordx4 a[216:219], v0, %[srd], s54 offen offset:2048 sc1\n"
        "v_exp_f32 v146, v146\n"
        "v_exp_f32 v147, v147\n"
        "v_exp_f32 v148, v148\n"
        "v_exp_f32 v149, v149\n"
        "v_mfma_f32_32x32x16_bf16 v[128:143], v[124:127], v[196:199], 0\n"
        "v_exp_f32 v150, v150\n"
        "v_exp_f32 v151, v151\n"
        "v_exp_f32 v152, v152\n"
        "v_exp_f32 v153, v153\n"
        "v_exp_f32 v154, v154\n"
        "v_smfmac_f32_32x32x32_bf16 v[160:175], a[56:59], v[144:151], v253\n"
        "buffer_load_dwordx4 a[220:223], v0, %[srd], s54 offen offset:3072 sc1\n"
        "v_exp_f32 v155, v155\n"
        "s_lshr_b32 s54, s81, 2\n"
        "v_exp_f32 v156, v156\n"
        "s_add_u32 s54, s54, s83\n"
        "v_exp_f32 v157, v157\n"
        "s_add_u32 s54, s54, 0\n"
        "v_exp_f32 v158, v158\n"
        "s_and_b32 s54, s54, 0x3fff\n"
        "s_add_u32 s54, s54, s84\n"
        "v_exp_f32 v159, v159\n"
        "buffer_load_dwordx4 a[64:67], v0, %[srd], s54 offen sc1\n"
        "v_exp_f32 v128, v128\n"
        "v_exp_f32 v129, v129\n"
        "v_smfmac_f32_32x32x32_bf16 v[160:175], a[60:63], v[152:159], v253\n"
        "buffer_load_dwordx4 a[68:71], v0, %[srd], s54 offen offset:1024 sc1\n"
        "v_exp_f32 v130, v130\n"
        "v_exp_f32 v131, v131\n"
        "v_exp_f32 v132, v132\n"
        "v_exp_f32 v133, v133\n"
        "v_exp_f32 v134, v134\n"
        "s_lshr_b32 s54, s81, 2\n"
        "v_exp_f32 v135, v135\n"
        "s_add_u32 s54, s54, s83\n"
        "v_exp_f32 v136, v136\n"
        "s_add_u32 s54, s54, 2048\n"
        "v_exp_f32 v137, v137\n"
        "s_and_b32 s54, s54, 0x3fff\n"
        "s_add_u32 s54, s54, s84\n"
        "v_exp_f32 v138, v138\n"
        "v_smfmac_f32_32x32x32_bf16 v[176:191], a[56:59], v[128:135], v253\n"
        "buffer_load_dwordx4 a[72:75], v0, %[srd], s54 offen sc1\n"
        "v_exp_f32 v139, v139\n"
        "v_exp_f32 v140, v140\n"
        "v_exp_f32 v141, v141\n"
        "v_exp_f32 v142, v142\n"
        "v_exp_f32 v143, v143\n"
        "buffer_load_dwordx4 a[76:79], v0, %[srd], s54 offen offset:1024 sc1\n"
        "s_nop 1\n"
        "v_smfmac_f32_32x32x32_bf16 v[176:191], a[60:63], v[136:143], v253\n"
        "s_branch Lbend_%=\n"
        "Lf3g_%=:\n"
        "s_lshr_b32 s54, s81, 1\n"
        "s_add_u32 s54, s54, s75\n"
        "s_mov_b32 s56, 0\n"
        "Lsp40_%=:\n"
        "s_add_u32 s56, s56, 1\n"
        "s_cmp_gt_u32 s56, 16384\n"
        "s_cbranch_scc1 Lsq41_%=\n"
        "buffer_load_dwordx4 v[192:195], v15, %[srd], s54 offen sc1\n"
        "s_waitcnt vmcnt(0)\n"
        "v_xor_b32 v192, s51, v192\n"
        "v_xor_b32 v193, s51, v193\n"
        "v_xor_b32 v194, s51, v194\n"
        "v_xor_b32 v195, s51, v195\n"
        "v_or3_b32 v192, v192, v193, v194\n"
        "v_or_b32 v192, v192, v195\n"
        "v_cmp_ne_u32 vcc, 0, v192\n"
        "s_cbranch_vccnz Lsp40_%=\n"
        "Lsq41_%=:\n"
        "s_branch Lse_%=\n"
        "Lf1spin_%=:\n"
        "s_mov_b32 s56, 0\n"
        "Lsp73_%=:\n"
        "s_add_u32 s56, s56, 1\n"
        "s_cmp_gt_u32 s56, 16384\n"
        "s_cbranch_scc1 Lsq74_%=\n"
        "s_sleep 2\n"
        "buffer_load_dwordx4 v[212:215], v15, %[srd], s55 offen sc1\n"
        "s_waitcnt vmcnt(0)\n"
        "v_xor_b32 v214, s37, v214\n"
        "v_xor_b32 v215, s36, v215\n"
        "v_or_b32 v214, v214, v215\n"
        "v_cmp_ne_u32 vcc, 0, v214\n"
        "s_cbranch_vccnz Lsp73_%=\n"
        "Lsq74_%=:\n"
        "s_branch Lf1ok_%=\n"
        "; ---- NaN after the first sweep: marker left in K/V (fetch again) or genuine\n"
        "Lp0bad_%=:\n"
        "s_nop 15\n"
        "s_waitcnt vmcnt(0)\n"
        "v_mov_b32 v132, 0\n"
        "v_accvgpr_read_b32 v133, a64\n"
        "v_max_u32 v132, v132, v133\n"
        "v_accvgpr_read_b32 v133, a65\n"
        "v_max_u32 v132, v132, v133\n"
        "v_accvgpr_read_b32 v133, a66\n"
        "v_max_u32 v132, v132, v133\n"
        "v_accvgpr_read_b32 v133, a67\n"
        "v_max_u32 v132, v132, v133\n"
        "v_accvgpr_read_b32 v133, a68\n"
        "v_max_u32 v132, v132, v133\n"
        "v_accvgpr_read_b32 v133, a69\n"
        "v_max_u32 v132, v132, v133\n"
        "v_accvgpr_read_b32 v133, a70\n"
        "v_max_u32 v132, v132, v133\n"
        "v_accvgpr_read_b32 v133, a71\n"
        "v_max_u32 v132, v132, v133\n"
        "v_accvgpr_read_b32 v133, a72\n"
        "v_max_u32 v132, v132, v133\n"
        "v_accvgpr_read_b32 v133, a73\n"
        "v_max_u32 v132, v132, v133\n"
        "v_accvgpr_read_b32 v133, a74\n"
        "v_max_u32 v132, v132, v133\n"
        "v_accvgpr_read_b32 v133, a75\n"
        "v_max_u32 v132, v132, v133\n"
        "v_accvgpr_read_b32 v133, a76\n"
        "v_max_u32 v132, v132, v133\n"
        "v_accvgpr_read_b32 v133, a77\n"
        "v_max_u32 v132, v132, v133\n"
        "v_accvgpr_read_b32 v133, a78\n"
        "v_max_u32 v132, v132, v133\n"
        "v_accvgpr_read_b32 v133, a79\n"
        "v_max_u32 v132, v132, v133\n"
        "v_accvgpr_read_b32 v133, a80\n"
        "v_max_u32 v132, v132, v133\n"
        "v_accvgpr_read_b32 v133, a81\n"
        "v_max_u32 v132, v132, v133\n"
        "v_accvgpr_read_b32 v133, a82\n"
        "v_max_u32 v132, v132, v133\n"
        "v_accvgpr_read_b32 v133, a83\n"
        "v_max_u32 v132, v132, v133\n"
        "v_accvgpr_read_b32 v133, a84\n"
        "v_max_u32 v132, v132, v133\n"
        "v_accvgpr_read_b32 v133, a85\n"
        "v_max_u32 v132, v132, v133\n"
        "v_accvgpr_read_b32 v133, a86\n"
        "v_max_u32 v132, v132, v133\n"
        "v_accvgpr_read_b32 v133, a87\n"
        "v_max_u32 v132, v132, v133\n"
        "v_accvgpr_read_b32 v133, a88\n"
        "v_max_u32 v132, v132, v133\n"
        "v_accvgpr_read_b32 v133, a89\n"
        "v_max_u32 v132, v132, v133\n"
        "v_accvgpr_read_b32 v133, a90\n"
        "v_max_u32 v132, v132, v133\n"
        "v_accvgpr_read_b32 v133, a91\n"
        "v_max_u32 v132, v132, v133\n"
        "v_accvgpr_read_b32 v133, a92\n"
        "v_max_u32 v132, v132, v133\n"
        "v_accvgpr_read_b32 v133, a93\n"
        "v_max_u32 v132, v132, v133\n"
        "v_accvgpr_read_b32 v133, a94\n"
        "v_max_u32 v132, v132, v133\n"
        "v_accvgpr_read_b32 v133, a95\n"
        "v_max_u32 v132, v132, v133\n"
        "v_accvgpr_read_b32 v133, a96\n"
        "v_max_u32 v132, v132, v133\n"
        "v_accvgpr_read_b32 v133, a97\n"
        "v_max_u32 v132, v132, v133\n"
        "v_accvgpr_read_b32 v133, a98\n"
        "v_max_u32 v132, v132, v133\n"
        "v_accvgpr_read_b32 v133, a99\n"
        "v_max_u32 v132, v132, v133\n"
        "v_accvgpr_read_b32 v133, a100\n"
        "v_max_u32 v132, v132, v133\n"
        "v_accvgpr_read_b32 v133, a101\n"
        "v_max_u32 v132, v132, v133\n"
        "v_accvgpr_read_b32 v133, a102\n"
        "v_max_u32 v132, v132, v133\n"
        "v_accvgpr_read_b32 v133, a103\n"
        "v_max_u32 v132, v132, v133\n"
        "v_accvgpr_read_b32 v133, a104\n"
        "v_max_u32 v132, v132, v133\n"
        "v_accvgpr_read_b32 v133, a105\n"
        "v_max_u32 v132, v132, v133\n"
        "v_accvgpr_read_b32 v133, a106\n"
        "v_max_u32 v132, v132, v133\n"
        "v_accvgpr_read_b32 v133, a107\n"
        "v_max_u32 v132, v132, v133\n"
        "v_accvgpr_read_b32 v133, a108\n"
        "v_max_u32 v132, v132, v133\n"
        "v_accvgpr_read_b32 v133, a109\n"
        "v_max_u32 v132, v132, v133\n"
        "v_accvgpr_read_b32 v133, a110\n"
        "v_max_u32 v132, v132, v133\n"
        "v_accvgpr_read_b32 v133, a111\n"
        "v_max_u32 v132, v132, v133\n"
        "v_accvgpr_read_b32 v133, a112\n"
        "v_max_u32 v132, v132, v133\n"
        "v_accvgpr_read_b32 v133, a113\n"
        "v_max_u32 v132, v132, v133\n"
        "v_accvgpr_read_b32 v133, a114\n"
        "v_max_u32 v132, v132, v133\n"
        "v_accvgpr_read_b32 v133, a115\n"
        "v_max_u32 v132, v132, v133\n"
        "v_accvgpr_read_b32 v133, a116\n"
        "v_max_u32 v132, v132, v133\n"
        "v_accvgpr_read_b32 v133, a117\n"
        "v_max_u32 v132, v132, v133\n"
        "v_accvgpr_read_b32 v133, a118\n"
        "v_max_u32 v132, v132, v133\n"
        "v_accvgpr_read_b32 v133, a119\n"
        "v_max_u32 v132, v132, v133\n"
        "v_accvgpr_read_b32 v133, a120\n"
        "v_max_u32 v132, v132, v133\n"
        "v_accvgpr_read_b32 v133, a121\n"
        "v_max_u32 v132, v132, v133\n"
        "v_accvgpr_read_b32 v133, a122\n"
        "v_max_u32 v132, v132, v133\n"
        "v_accvgpr_read_b32 v133, a123\n"
        "v_max_u32 v132, v132, v133\n"
        "v_accvgpr_read_b32 v133, a124\n"
        "v_max_u32 v132, v132, v133\n"
        "v_accvgpr_read_b32 v133, a125\n"
        "v_max_u32 v132, v132, v133\n"
        "v_accvgpr_read_b32 v133, a126\n"
        "v_max_u32 v132, v132, v133\n"
        "v_accvgpr_read_b32 v133, a127\n"
        "v_max_u32 v132, v132, v133\n"
        "v_accvgpr_read_b32 v133, a128\n"
        "v_max_u32 v132, v132, v133\n"
        "v_accvgpr_read_b32 v133, a129\n"
        "v_max_u32 v132, v132, v133\n"
        "v_accvgpr_read_b32 v133, a130\n"
        "v_max_u32 v132, v132, v133\n"
        "v_accvgpr_read_b32 v133, a131\n"
        "v_max_u32 v132, v132, v133\n"
        "v_accvgpr_read_b32 v133, a132\n"
        "v_max_u32 v132, v132, v133\n"
        "v_accvgpr_read_b32 v133, a133\n"
        "v_max_u32 v132, v132, v133\n"
        "v_accvgpr_read_b32 v133, a134\n"
        "v_max_u32 v132, v132, v133\n"
        "v_accvgpr_read_b32 v133, a135\n"
        "v_max_u32 v132, v132, v133\n"
        "v_accvgpr_read_b32 v133, a136\n"
        "v_max_u32 v132, v132, v133\n"
        "v_accvgpr_read_b32 v133, a137\n"
        "v_max_u32 v132, v132, v133\n"
        "v_accvgpr_read_b32 v133, a138\n"
        "v_max_u32 v132, v132, v133\n"
        "v_accvgpr_read_b32 v133, a139\n"
        "v_max_u32 v132, v132, v133\n"
        "v_accvgpr_read_b32 v133, a140\n"
        "v_max_u32 v132, v132, v133\n"
        "v_accvgpr_read_b32 v133, a141\n"
        "v_max_u32 v132, v132, v133\n"
        "v_accvgpr_read_b32 v133, a142\n"
        "v_max_u32 v132, v132, v133\n"
        "v_accvgpr_read_b32 v133, a143\n"
        "v_max_u32 v132, v132, v133\n"
        "v_accvgpr_read_b32 v133, a144\n"
        "v_max_u32 v132, v132, v133\n"
        "v_accvgpr_read_b32 v133, a145\n"
        "v_max_u32 v132, v132, v133\n"
        "v_accvgpr_read_b32 v133, a146\n"
        "v_max_u32 v132, v132, v133\n"
        "v_accvgpr_read_b32 v133, a147\n"
        "v_max_u32 v132, v132, v133\n"
        "v_accvgpr_read_b32 v133, a148\n"
        "v_max_u32 v132, v132, v133\n"
        "v_accvgpr_read_b32 v133, a149\n"
        "v_max_u32 v132, v132, v133\n"
        "v_accvgpr_read_b32 v133, a150\n"
        "v_max_u32 v132, v132, v133\n"
        "v_accvgpr_read_b32 v133, a151\n"
        "v_max_u32 v132, v132, v133\n"
        "v_accvgpr_read_b32 v133, a152\n"
        "v_max_u32 v132, v132, v133\n"
        "v_accvgpr_read_b32 v133, a153\n"
        "v_max_u32 v132, v132, v133\n"
        "v_accvgpr_read_b32 v133, a154\n"
        "v_max_u32 v132, v132, v133\n"
        "v_accvgpr_read_b32 v133, a155\n"
        "v_max_u32 v132, v132, v133\n"
        "v_accvgpr_read_b32 v133, a156\n"
        "v_max_u32 v132, v132, v133\n"
        "v_accvgpr_read_b32 v133, a157\n"
        "v_max_u32 v132, v132, v133\n"
        "v_accvgpr_read_b32 v133, a158\n"
        "v_max_u32 v132, v132, v133\n"
        "v_accvgpr_read_b32 v133, a159\n"
        "v_max_u32 v132, v132, v133\n"
        "v_accvgpr_read_b32 v133, a160\n"
        "v_max_u32 v132, v132, v133\n"
        "v_accvgpr_read_b32 v133, a161\n"
        "v_max_u32 v132, v132, v133\n"
        "v_accvgpr_read_b32 v133, a162\n"
        "v_max_u32 v132, v132, v133\n"
        "v_accvgpr_read_b32 v133, a163\n"
        "v_max_u32 v132, v132, v133\n"
        "v_accvgpr_read_b32 v133, a164\n"
        "v_max_u32 v132, v132, v133\n"
        "v_accvgpr_read_b32 v133, a165\n"
        "v_max_u32 v132, v132, v133\n"
        "v_accvgpr_read_b32 v133, a166\n"
        "v_max_u32 v132, v132, v133\n"
        "v_accvgpr_read_b32 v133, a167\n"
        "v_max_u32 v132, v132, v133\n"
        "v_accvgpr_read_b32 v133, a168\n"
        "v_max_u32 v132, v132, v133\n"
        "v_accvgpr_read_b32 v133, a169\n"
        "v_max_u32 v132, v132, v133\n"
        "v_accvgpr_read_b32 v133, a170\n"
        "v_max_u32 v132, v132, v133\n"
        "v_accvgpr_read_b32 v133, a171\n"
        "v_max_u32 v132, v132, v133\n"
        "v_accvgpr_read_b32 v133, a172\n"
        "v_max_u32 v132, v132, v133\n"
        "v_accvgpr_read_b32 v133, a173\n"
        "v_max_u32 v132, v132, v133\n"
        "v_accvgpr_read_b32 v133, a174\n"
        "v_max_u32 v132, v132, v133\n"
        "v_accvgpr_read_b32 v133, a175\n"
        "v_max_u32 v132, v132, v133\n"
        "v_accvgpr_read_b32 v133, a176\n"
        "v_max_u32 v132, v132, v133\n"
        "v_accvgpr_read_b32 v133, a177\n"
        "v_max_u32 v132, v132, v133\n"
        "v_accvgpr_read_b32 v133, a178\n"
        "v_max_u32 v132, v132, v133\n"
        "v_accvgpr_read_b32 v133, a179\n"
        "v_max_u32 v132, v132, v133\n"
        "v_accvgpr_read_b32 v133, a180\n"
        "v_max_u32 v132, v132, v133\n"
        "v_accvgpr_read_b32 v133, a181\n"
        "v_max_u32 v132, v132, v133\n"
        "v_accvgpr_read_b32 v133, a182\n"
        "v_max_u32 v132, v132, v133\n"
        "v_accvgpr_read_b32 v133, a183\n"
        "v_max_u32 v132, v132, v133\n"
        "v_accvgpr_read_b32 v133, a184\n"
        "v_max_u32 v132, v132, v133\n"
        "v_accvgpr_read_b32 v133, a185\n"
        "v_max_u32 v132, v132, v133\n"
        "v_accvgpr_read_b32 v133, a186\n"
        "v_max_u32 v132, v132, v133\n"
        "v_accvgpr_read_b32 v133, a187\n"
        "v_max_u32 v132, v132, v133\n"
        "v_accvgpr_read_b32 v133, a188\n"
        "v_max_u32 v132, v132, v133\n"
        "v_accvgpr_read_b32 v133, a189\n"
        "v_max_u32 v132, v132, v133\n"
        "v_accvgpr_read_b32 v133, a190\n"
        "v_max_u32 v132, v132, v133\n"
        "v_accvgpr_read_b32 v133, a191\n"
        "v_max_u32 v132, v132, v133\n"
        "v_accvgpr_read_b32 v133, a192\n"
        "v_max_u32 v132, v132, v133\n"
        "v_accvgpr_read_b32 v133, a193\n"
        "v_max_u32 v132, v132, v133\n"
        "v_accvgpr_read_b32 v133, a194\n"
        "v_max_u32 v132, v132, v133\n"
        "v_accvgpr_read_b32 v133, a195\n"
        "v_max_u32 v132, v132, v133\n"
        "v_accvgpr_read_b32 v133, a196\n"
        "v_max_u32 v132, v132, v133\n"
        "v_accvgpr_read_b32 v133, a197\n"
        "v_max_u32 v132, v132, v133\n"
        "v_accvgpr_read_b32 v133, a198\n"
        "v_max_u32 v132, v132, v133\n"
        "v_accvgpr_read_b32 v133, a199\n"
        "v_max_u32 v132, v132, v133\n"
        "v_accvgpr_read_b32 v133, a200\n"
        "v_max_u32 v132, v132, v133\n"
        "v_accvgpr_read_b32 v133, a201\n"
        "v_max_u32 v132, v132, v133\n"
        "v_accvgpr_read_b32 v133, a202\n"
        "v_max_u32 v132, v132, v133\n"
        "v_accvgpr_read_b32 v133, a203\n"
        "v_max_u32 v132, v132, v133\n"
        "v_accvgpr_read_b32 v133, a204\n"
        "v_max_u32 v132, v132, v133\n"
        "v_accvgpr_read_b32 v133, a205\n"
        "v_max_u32 v132, v132, v133\n"
        "v_accvgpr_read_b32 v133, a206\n"
        "v_max_u32 v132, v132, v133\n"
        "v_accvgpr_read_b32 v133, a207\n"
        "v_max_u32 v132, v132, v133\n"
        "v_accvgpr_read_b32 v133, a208\n"
        "v_max_u32 v132, v132, v133\n"
        "v_accvgpr_read_b32 v133, a209\n"
        "v_max_u32 v132, v132, v133\n"
        "v_accvgpr_read_b32 v133, a210\n"
        "v_max_u32 v132, v132, v133\n"
        "v_accvgpr_read_b32 v133, a211\n"
        "v_max_u32 v132, v132, v133\n"
        "v_accvgpr_read_b32 v133, a212\n"
        "v_max_u32 v132, v132, v133\n"
        "v_accvgpr_read_b32 v133, a213\n"
        "v_max_u32 v132, v132, v133\n"
        "v_accvgpr_read_b32 v133, a214\n"
        "v_max_u32 v132, v132, v133\n"
        "v_accvgpr_read_b32 v133, a215\n"
        "v_max_u32 v132, v132, v133\n"
        "v_accvgpr_read_b32 v133, a216\n"
        "v_max_u32 v132, v132, v133\n"
        "v_accvgpr_read_b32 v133, a217\n"
        "v_max_u32 v132, v132, v133\n"
        "v_accvgpr_read_b32 v133, a218\n"
        "v_max_u32 v132, v132, v133\n"
        "v_accvgpr_read_b32 v133, a219\n"
        "v_max_u32 v132, v132, v133\n"
        "v_accvgpr_read_b32 v133, a220\n"
        "v_max_u32 v132, v132, v133\n"
        "v_accvgpr_read_b32 v133, a221\n"
        "v_max_u32 v132, v132, v133\n"
        "v_accvgpr_read_b32 v133, a222\n"
        "v_max_u32 v132, v132, v133\n"
        "v_accvgpr_read_b32 v133, a223\n"
        "v_max_u32 v132, v132, v133\n"
        "v_accvgpr_read_b32 v133, a224\n"
        "v_max_u32 v132, v132, v133\n"
        "v_accvgpr_read_b32 v133, a225\n"
        "v_max_u32 v132, v132, v133\n"
        "v_accvgpr_read_b32 v133, a226\n"
        "v_max_u32 v132, v132, v133\n"
        "v_accvgpr_read_b32 v133, a227\n"
        "v_max_u32 v132, v132, v133\n"
        "v_accvgpr_read_b32 v133, a228\n"
        "v_max_u32 v132, v132, v133\n"
        "v_accvgpr_read_b32 v133, a229\n"
        "v_max_u32 v132, v132, v133\n"
        "v_accvgpr_read_b32 v133, a230\n"
        "v_max_u32 v132, v132, v133\n"
        "v_accvgpr_read_b32 v133, a231\n"
        "v_max_u32 v132, v132, v133\n"
        "v_accvgpr_read_b32 v133, a232\n"
        "v_max_u32 v132, v132, v133\n"
        "v_accvgpr_read_b32 v133, a233\n"
        "v_max_u32 v132, v132, v133\n"
        "v_accvgpr_read_b32 v133, a234\n"
        "v_max_u32 v132, v132, v133\n"
        "v_accvgpr_read_b32 v133, a235\n"
        "v_max_u32 v132, v132, v133\n"
        "v_accvgpr_read_b32 v133, a236\n"
        "v_max_u32 v132, v132, v133\n"
        "v_accvgpr_read_b32 v133, a237\n"
        "v_max_u32 v132, v132, v133\n"
        "v_accvgpr_read_b32 v133, a238\n"
        "v_max_u32 v132, v132, v133\n"
        "v_accvgpr_read_b32 v133, a239\n"
        "v_max_u32 v132, v132, v133\n"
        "v_accvgpr_read_b32 v133, a240\n"
        "v_max_u32 v132, v132, v133\n"
        "v_accvgpr_read_b32 v133, a241\n"
        "v_max_u32 v132, v132, v133\n"
        "v_accvgpr_read_b32 v133, a242\n"
        "v_max_u32 v132, v132, v133\n"
        "v_accvgpr_read_b32 v133, a243\n"
        "v_max_u32 v132, v132, v133\n"
        "v_accvgpr_read_b32 v133, a244\n"
        "v_max_u32 v132, v132, v133\n"
        "v_accvgpr_read_b32 v133, a245\n"
        "v_max_u32 v132, v132, v133\n"
        "v_accvgpr_read_b32 v133, a246\n"
        "v_max_u32 v132, v132, v133\n"
        "v_accvgpr_read_b32 v133, a247\n"
        "v_max_u32 v132, v132, v133\n"
        "v_accvgpr_read_b32 v133, a248\n"
        "v_max_u32 v132, v132, v133\n"
        "v_accvgpr_read_b32 v133, a249\n"
        "v_max_u32 v132, v132, v133\n"
        "v_accvgpr_read_b32 v133, a250\n"
        "v_max_u32 v132, v132, v133\n"
        "v_accvgpr_read_b32 v133, a251\n"
        "v_max_u32 v132, v132, v133\n"
        "v_accvgpr_read_b32 v133, a252\n"
        "v_max_u32 v132, v132, v133\n"
        "v_accvgpr_read_b32 v133, a253\n"
        "v_max_u32 v132, v132, v133\n"
        "v_accvgpr_read_b32 v133, a254\n"
        "v_max_u32 v132, v132, v133\n"
        "v_accvgpr_read_b32 v133, a255\n"
        "v_max_u32 v132, v132, v133\n"
        "v_cmp_eq_u32 vcc, -1, v132\n"
        "s_add_u32 s57, s57, 1\n"
        "s_cmp_gt_u32 s57, 64\n"
        "s_cselect_b64 s[62:63], 0, -1\n"
        "s_nop 0\n"
        "s_and_b64 vcc, vcc, s[62:63]\n"
        "s_cbranch_vccnz Lrehead_%=\n"
        "s_mov_b64 s[94:95], 0\n"
        "s_branch Lrehead_%=\n"
        "; ---- attention A with the true maxima of each wave's keys; merged with their maxima below\n"
        "LslowA_%=:\n"
        "s_nop 15\n"
        "s_waitcnt vmcnt(0)\n"
        "s_cmp_eq_u32 s87, 0\n"
        "s_cbranch_scc0 Lk72_%=\n"
        "s_barrier\n"
        "s_add_u32 s62, s72, 2621440\n"
        "s_add_u32 s63, s73, 40960\n"
        "s_cmp_lt_u32 s69, 16\n"
        "s_cbranch_scc0 Lp68k71_%=\n"
        "s_lshl_b32 s54, s69, 10\n"
        "s_sub_u32 s54, s54, s83\n"
        "s_and_b32 s54, s54, 0x3fff\n"
        "s_add_u32 s54, s54, s62\n"
        "buffer_store_dwordx4 v[124:127], v0, %[srd], s54 offen sc1\n"
        "s_branch Lpqdk71_%=\n"
        "Lp68k71_%=:\n"
        "s_cmp_eq_u32 s69, 16\n"
        "s_cbranch_scc0 Lpqdk71_%=\n"
        "v_lshrrev_b32 v120, 2, v0\n"
        "v_mov_b32 v121, 0\n"
        "s_mov_b32 exec_lo, 0xffff0000\n"
        "buffer_store_dword v121, v120, %[srd], s63 offen sc1\n"
        "s_mov_b64 exec, -1\n"
        "v_lshrrev_b32 v120, 4, v0\n"
        "s_mov_b32 exec_lo, 0x00010001\n"
        "s_mov_b32 exec_hi, 0x00010001\n"
        "buffer_store_dwordx4 v[112:115], v120, %[srd], s63 offen sc1\n"
        "s_mov_b64 exec, -1\n"
        "Lpqdk71_%=:\n"
        "v_add_u32 v122, 65536, v101\n"
        "ds_read_b64_tr_b16 v[64:65], v122 offset:0\n"
        "ds_read_b64_tr_b16 v[66:67], v122 offset:512\n"
        "ds_read_b64_tr_b16 v[68:69], v122 offset:4096\n"
        "ds_read_b64_tr_b16 v[70:71], v122 offset:4608\n"
        "ds_read_b64_tr_b16 v[72:73], v122 offset:8192\n"
        "ds_read_b64_tr_b16 v[74:75], v122 offset:8704\n"
        "ds_read_b64_tr_b16 v[76:77], v122 offset:12288\n"
        "ds_read_b64_tr_b16 v[78:79], v122 offset:12800\n"
        "s_nop 1\n"
        "s_waitcnt lgkmcnt(6)\n"
        "v_mfma_f32_16x16x32_bf16 v[104:107], v[116:119], v[64:67], 0\n"
        "s_waitcnt lgkmcnt(4)\n"
        "v_mfma_f32_16x16x32_bf16 v[104:107], v[116:119], v[68:71], v[104:107]\n"
        "s_waitcnt lgkmcnt(2)\n"
        "v_mfma_f32_16x16x32_bf16 v[104:107], v[116:119], v[72:75], v[104:107]\n"
        "s_waitcnt lgkmcnt(0)\n"
        "v_mfma_f32_16x16x32_bf16 v[104:107], v[116:119], v[76:79], v[104:107]\n"
        "s_nop 7\n"
        "s_cmp_eq_u32 s44, 0\n"
        "s_cbranch_scc1 Lk72_%=\n"
        "s_add_u32 s54, s35, 0\n"
        "s_and_b32 s54, s54, 0x3fff\n"
        "s_add_u32 s54, s54, s44\n"
        "buffer_store_dwordx4 v[64:67], v0, %[srd], s54 offen sc1\n"
        "s_add_u32 s54, s35, 1024\n"
        "s_and_b32 s54, s54, 0x3fff\n"
        "s_add_u32 s54, s54, s44\n"
        "buffer_store_dwordx4 v[68:71], v0, %[srd], s54 offen sc1\n"
        "s_add_u32 s54, s35, 2048\n"
        "s_and_b32 s54, s54, 0x3fff\n"
        "s_add_u32 s54, s54, s44\n"
        "buffer_store_dwordx4 v[72:75], v0, %[srd], s54 offen sc1\n"
        "s_add_u32 s54, s35, 3072\n"
        "s_and_b32 s54, s54, 0x3fff\n"
        "s_add_u32 s54, s54, s44\n"
        "buffer_store_dwordx4 v[76:79], v0, %[srd], s54 offen sc1\n"
        "v_lshrrev_b32 v120, 2, v0\n"
        "s_mov_b64 exec, 0xffff\n"
        "buffer_store_dword v104, v120, %[srd], s39 offen sc1\n"
        "s_mov_b64 exec, -1\n"
        "Lk72_%=:\n"
        "s_waitcnt vmcnt(0)\n"
        "s_add_u32 s54, s76, 0x8000\n"
        "s_mov_b64 exec, 1\n"
        "buffer_store_dword v19, off, %[srd], s54 sc1\n"
        "s_mov_b64 exec, -1\n"
        "s_waitcnt lgkmcnt(0)\n"
        "s_lshr_b32 s54, s81, 12\n"
        "v_add_u32 v252, s54, v250\n"
        "v_mov_b32 v253, 1\n"
        "ds_write_b32 v252, v253 offset:528\n"
        "s_waitcnt lgkmcnt(0)\n"
        "s_barrier\n"
        "LslowA2_%=:\n"
        "s_nop 15\n"
        "s_mov_b32 s57, 0\n"
        "Lsa1_%=:\n"
        "s_waitcnt vmcnt(0)\n"
        "s_mov_b32 s58, 0xffff\n"
        "s_mov_b32 s59, 0xffff\n"
        "s_add_u32 s60, s74, s81\n"
        "s_add_u32 s60, s60, 4294803456\n"
        "s_lshr_b32 s54, s81, 1\n"
        "s_add_u32 s54, s54, s74\n"
        "s_add_u32 s54, s54, 4294934528\n"
        "buffer_load_dword a0, v15, %[srd], s54 offen sc1\n"
        "s_add_u32 s54, s80, 0\n"
        "s_and_b32 s54, s54, 0x3fff\n"
        "s_add_u32 s54, s54, s60\n"
        "buffer_load_dwordx4 a[64:67], v0, %[srd], s54 offen sc1\n"
        "s_mov_b64 exec, s[58:59]\n"
        "buffer_load_dwordx4 a[128:131], v230, %[srd], s54 offen sc1\n"
        "buffer_load_dwordx4 a[132:135], v230, %[srd], s54 offen offset:512 sc1\n"
        "s_mov_b64 exec, -1\n"
        "s_add_u32 s54, s80, 1024\n"
        "s_and_b32 s54, s54, 0x3fff\n"
        "s_add_u32 s54, s54, s60\n"
        "buffer_load_dwordx4 a[68:71], v0, %[srd], s54 offen sc1\n"
        "s_mov_b64 exec, s[58:59]\n"
        "buffer_load_dwordx4 a[136:139], v230, %[srd], s54 offen sc1\n"
        "buffer_load_dwordx4 a[140:143], v230, %[srd], s54 offen offset:512 sc1\n"
        "s_mov_b64 exec, -1\n"
        "s_add_u32 s54, s80, 2048\n"
        "s_and_b32 s54, s54, 0x3fff\n"
        "s_add_u32 s54, s54, s60\n"
        "buffer_load_dwordx4 a[72:75], v0, %[srd], s54 offen sc1\n"
        "s_mov_b64 exec, s[58:59]\n"
        "buffer_load_dwordx4 a[144:147], v230, %[srd], s54 offen sc1\n"
        "buffer_load_dwordx4 a[148:151], v230, %[srd], s54 offen offset:512 sc1\n"
        "s_mov_b64 exec, -1\n"
        "s_add_u32 s54, s80, 3072\n"
        "s_and_b32 s54, s54, 0x3fff\n"
        "s_add_u32 s54, s54, s60\n"
        "buffer_load_dwordx4 a[76:79], v0, %[srd], s54 offen sc1\n"
        "s_mov_b64 exec, s[58:59]\n"
        "buffer_load_dwordx4 a[152:155], v230, %[srd], s54 offen sc1\n"
        "buffer_load_dwordx4 a[156:159], v230, %[srd], s54 offen offset:512 sc1\n"
        "s_mov_b64 exec, -1\n"
        "s_add_u32 s54, s80, 4096\n"
        "s_and_b32 s54, s54, 0x3fff\n"
        "s_add_u32 s54, s54, s60\n"
        "buffer_load_dwordx4 a[80:83], v0, %[srd], s54 offen sc1\n"
        "s_mov_b64 exec, s[58:59]\n"
        "buffer_load_dwordx4 a[160:163], v230, %[srd], s54 offen sc1\n"
        "buffer_load_dwordx4 a[164:167], v230, %[srd], s54 offen offset:512 sc1\n"
        "s_mov_b64 exec, -1\n"
        "s_add_u32 s54, s80, 5120\n"
        "s_and_b32 s54, s54, 0x3fff\n"
        "s_add_u32 s54, s54, s60\n"
        "buffer_load_dwordx4 a[84:87], v0, %[srd], s54 offen sc1\n"
        "s_mov_b64 exec, s[58:59]\n"
        "buffer_load_dwordx4 a[168:171], v230, %[srd], s54 offen sc1\n"
        "buffer_load_dwordx4 a[172:175], v230, %[srd], s54 offen offset:512 sc1\n"
        "s_mov_b64 exec, -1\n"
        "s_add_u32 s54, s80, 6144\n"
        "s_and_b32 s54, s54, 0x3fff\n"
        "s_add_u32 s54, s54, s60\n"
        "buffer_load_dwordx4 a[88:91], v0, %[srd], s54 offen sc1\n"
        "s_mov_b64 exec, s[58:59]\n"
        "buffer_load_dwordx4 a[176:179], v230, %[srd], s54 offen sc1\n"
        "buffer_load_dwordx4 a[180:183], v230, %[srd], s54 offen offset:512 sc1\n"
        "s_mov_b64 exec, -1\n"
        "s_add_u32 s54, s80, 7168\n"
        "s_and_b32 s54, s54, 0x3fff\n"
        "s_add_u32 s54, s54, s60\n"
        "buffer_load_dwordx4 a[92:95], v0, %[srd], s54 offen sc1\n"
        "s_mov_b64 exec, s[58:59]\n"
        "buffer_load_dwordx4 a[184:187], v230, %[srd], s54 offen sc1\n"
        "buffer_load_dwordx4 a[188:191], v230, %[srd], s54 offen offset:512 sc1\n"
        "s_mov_b64 exec, -1\n"
        "s_add_u32 s54, s80, 8192\n"
        "s_and_b32 s54, s54, 0x3fff\n"
        "s_add_u32 s54, s54, s60\n"
        "buffer_load_dwordx4 a[96:99], v0, %[srd], s54 offen sc1\n"
        "s_mov_b64 exec, s[58:59]\n"
        "buffer_load_dwordx4 a[192:195], v230, %[srd], s54 offen sc1\n"
        "buffer_load_dwordx4 a[196:199], v230, %[srd], s54 offen offset:512 sc1\n"
        "s_mov_b64 exec, -1\n"
        "s_add_u32 s54, s80, 9216\n"
        "s_and_b32 s54, s54, 0x3fff\n"
        "s_add_u32 s54, s54, s60\n"
        "buffer_load_dwordx4 a[100:103], v0, %[srd], s54 offen sc1\n"
        "s_mov_b64 exec, s[58:59]\n"
        "buffer_load_dwordx4 a[200:203], v230, %[srd], s54 offen sc1\n"
        "buffer_load_dwordx4 a[204:207], v230, %[srd], s54 offen offset:512 sc1\n"
        "s_mov_b64 exec, -1\n"
        "s_add_u32 s54, s80, 10240\n"
        "s_and_b32 s54, s54, 0x3fff\n"
        "s_add_u32 s54, s54, s60\n"
        "buffer_load_dwordx4 a[104:107], v0, %[srd], s54 offen sc1\n"
        "s_mov_b64 exec, s[58:59]\n"
        "buffer_load_dwordx4 a[208:211], v230, %[srd], s54 offen sc1\n"
        "buffer_load_dwordx4 a[212:215], v230, %[srd], s54 offen offset:512 sc1\n"
        "s_mov_b64 exec, -1\n"
        "s_add_u32 s54, s80, 11264\n"
        "s_and_b32 s54, s54, 0x3fff\n"
        "s_add_u32 s54, s54, s60\n"
        "buffer_load_dwordx4 a[108:111], v0, %[srd], s54 offen sc1\n"
        "s_mov_b64 exec, s[58:59]\n"
        "buffer_load_dwordx4 a[216:219], v230, %[srd], s54 offen sc1\n"
        "buffer_load_dwordx4 a[220:223], v230, %[srd], s54 offen offset:512 sc1\n"
        "s_mov_b64 exec, -1\n"
        "s_add_u32 s54, s80, 12288\n"
        "s_and_b32 s54, s54, 0x3fff\n"
        "s_add_u32 s54, s54, s60\n"
        "buffer_load_dwordx4 a[112:115], v0, %[srd], s54 offen sc1\n"
        "s_mov_b64 exec, s[58:59]\n"
        "buffer_load_dwordx4 a[224:227], v230, %[srd], s54 offen sc1\n"
        "buffer_load_dwordx4 a[228:231], v230, %[srd], s54 offen offset:512 sc1\n"
        "s_mov_b64 exec, -1\n"
        "s_add_u32 s54, s80, 13312\n"
        "s_and_b32 s54, s54, 0x3fff\n"
        "s_add_u32 s54, s54, s60\n"
        "buffer_load_dwordx4 a[116:119], v0, %[srd], s54 offen sc1\n"
        "s_mov_b64 exec, s[58:59]\n"
        "buffer_load_dwordx4 a[232:235], v230, %[srd], s54 offen sc1\n"
        "buffer_load_dwordx4 a[236:239], v230, %[srd], s54 offen offset:512 sc1\n"
        "s_mov_b64 exec, -1\n"
        "s_add_u32 s54, s80, 14336\n"
        "s_and_b32 s54, s54, 0x3fff\n"
        "s_add_u32 s54, s54, s60\n"
        "buffer_load_dwordx4 a[120:123], v0, %[srd], s54 offen sc1\n"
        "s_mov_b64 exec, s[58:59]\n"
        "buffer_load_dwordx4 a[240:243], v230, %[srd], s54 offen sc1\n"
        "buffer_load_dwordx4 a[244:247], v230, %[srd], s54 offen offset:512 sc1\n"
        "s_mov_b64 exec, -1\n"
        "s_add_u32 s54, s80, 15360\n"
        "s_and_b32 s54, s54, 0x3fff\n"
        "s_add_u32 s54, s54, s60\n"
        "buffer_load_dwordx4 a[124:127], v0, %[srd], s54 offen sc1\n"
        "s_mov_b64 exec, s[58:59]\n"
        "buffer_load_dwordx4 a[248:251], v230, %[srd], s54 offen sc1\n"
        "buffer_load_dwordx4 a[252:255], v230, %[srd], s54 offen offset:512 sc1\n"
        "s_mov_b64 exec, -1\n"
        "s_waitcnt vmcnt(0)\n"
        "v_mov_b32 v132, 0\n"
        "v_accvgpr_read_b32 v133, a64\n"
        "v_max_u32 v132, v132, v133\n"
        "v_accvgpr_read_b32 v133, a65\n"
        "v_max_u32 v132, v132, v133\n"
        "v_accvgpr_read_b32 v133, a66\n"
        "v_max_u32 v132, v132, v133\n"
        "v_accvgpr_read_b32 v133, a67\n"
        "v_max_u32 v132, v132, v133\n"
        "v_accvgpr_read_b32 v133, a68\n"
        "v_max_u32 v132, v132, v133\n"
        "v_accvgpr_read_b32 v133, a69\n"
        "v_max_u32 v132, v132, v133\n"
        "v_accvgpr_read_b32 v133, a70\n"
        "v_max_u32 v132, v132, v133\n"
        "v_accvgpr_read_b32 v133, a71\n"
        "v_max_u32 v132, v132, v133\n"
        "v_accvgpr_read_b32 v133, a72\n"
        "v_max_u32 v132, v132, v133\n"
        "v_accvgpr_read_b32 v133, a73\n"
        "v_max_u32 v132, v132, v133\n"
        "v_accvgpr_read_b32 v133, a74\n"
        "v_max_u32 v132, v132, v133\n"
        "v_accvgpr_read_b32 v133, a75\n"
        "v_max_u32 v132, v132, v133\n"
        "v_accvgpr_read_b32 v133, a76\n"
        "v_max_u32 v132, v132, v133\n"
        "v_accvgpr_read_b32 v133, a77\n"
        "v_max_u32 v132, v132, v133\n"
        "v_accvgpr_read_b32 v133, a78\n"
        "v_max_u32 v132, v132, v133\n"
        "v_accvgpr_read_b32 v133, a79\n"
        "v_max_u32 v132, v132, v133\n"
        "v_accvgpr_read_b32 v133, a80\n"
        "v_max_u32 v132, v132, v133\n"
        "v_accvgpr_read_b32 v133, a81\n"
        "v_max_u32 v132, v132, v133\n"
        "v_accvgpr_read_b32 v133, a82\n"
        "v_max_u32 v132, v132, v133\n"
        "v_accvgpr_read_b32 v133, a83\n"
        "v_max_u32 v132, v132, v133\n"
        "v_accvgpr_read_b32 v133, a84\n"
        "v_max_u32 v132, v132, v133\n"
        "v_accvgpr_read_b32 v133, a85\n"
        "v_max_u32 v132, v132, v133\n"
        "v_accvgpr_read_b32 v133, a86\n"
        "v_max_u32 v132, v132, v133\n"
        "v_accvgpr_read_b32 v133, a87\n"
        "v_max_u32 v132, v132, v133\n"
        "v_accvgpr_read_b32 v133, a88\n"
        "v_max_u32 v132, v132, v133\n"
        "v_accvgpr_read_b32 v133, a89\n"
        "v_max_u32 v132, v132, v133\n"
        "v_accvgpr_read_b32 v133, a90\n"
        "v_max_u32 v132, v132, v133\n"
        "v_accvgpr_read_b32 v133, a91\n"
        "v_max_u32 v132, v132, v133\n"
        "v_accvgpr_read_b32 v133, a92\n"
        "v_max_u32 v132, v132, v133\n"
        "v_accvgpr_read_b32 v133, a93\n"
        "v_max_u32 v132, v132, v133\n"
        "v_accvgpr_read_b32 v133, a94\n"
        "v_max_u32 v132, v132, v133\n"
        "v_accvgpr_read_b32 v133, a95\n"
        "v_max_u32 v132, v132, v133\n"
        "v_accvgpr_read_b32 v133, a96\n"
        "v_max_u32 v132, v132, v133\n"
        "v_accvgpr_read_b32 v133, a97\n"
        "v_max_u32 v132, v132, v133\n"
        "v_accvgpr_read_b32 v133, a98\n"
        "v_max_u32 v132, v132, v133\n"
        "v_accvgpr_read_b32 v133, a99\n"
        "v_max_u32 v132, v132, v133\n"
        "v_accvgpr_read_b32 v133, a100\n"
        "v_max_u32 v132, v132, v133\n"
        "v_accvgpr_read_b32 v133, a101\n"
        "v_max_u32 v132, v132, v133\n"
        "v_accvgpr_read_b32 v133, a102\n"
        "v_max_u32 v132, v132, v133\n"
        "v_accvgpr_read_b32 v133, a103\n"
        "v_max_u32 v132, v132, v133\n"
        "v_accvgpr_read_b32 v133, a104\n"
        "v_max_u32 v132, v132, v133\n"
        "v_accvgpr_read_b32 v133, a105\n"
        "v_max_u32 v132, v132, v133\n"
        "v_accvgpr_read_b32 v133, a106\n"
        "v_max_u32 v132, v132, v133\n"
        "v_accvgpr_read_b32 v133, a107\n"
        "v_max_u32 v132, v132, v133\n"
        "v_accvgpr_read_b32 v133, a108\n"
        "v_max_u32 v132, v132, v133\n"
        "v_accvgpr_read_b32 v133, a109\n"
        "v_max_u32 v132, v132, v133\n"
        "v_accvgpr_read_b32 v133, a110\n"
        "v_max_u32 v132, v132, v133\n"
        "v_accvgpr_read_b32 v133, a111\n"
        "v_max_u32 v132, v132, v133\n"
        "v_accvgpr_read_b32 v133, a112\n"
        "v_max_u32 v132, v132, v133\n"
        "v_accvgpr_read_b32 v133, a113\n"
        "v_max_u32 v132, v132, v133\n"
        "v_accvgpr_read_b32 v133, a114\n"
        "v_max_u32 v132, v132, v133\n"
        "v_accvgpr_read_b32 v133, a115\n"
        "v_max_u32 v132, v132, v133\n"
        "v_accvgpr_read_b32 v133, a116\n"
        "v_max_u32 v132, v132, v133\n"
        "v_accvgpr_read_b32 v133, a117\n"
        "v_max_u32 v132, v132, v133\n"
        "v_accvgpr_read_b32 v133, a118\n"
        "v_max_u32 v132, v132, v133\n"
        "v_accvgpr_read_b32 v133, a119\n"
        "v_max_u32 v132, v132, v133\n"
        "v_accvgpr_read_b32 v133, a120\n"
        "v_max_u32 v132, v132, v133\n"
        "v_accvgpr_read_b32 v133, a121\n"
        "v_max_u32 v132, v132, v133\n"
        "v_accvgpr_read_b32 v133, a122\n"
        "v_max_u32 v132, v132, v133\n"
        "v_accvgpr_read_b32 v133, a123\n"
        "v_max_u32 v132, v132, v133\n"
        "v_accvgpr_read_b32 v133, a124\n"
        "v_max_u32 v132, v132, v133\n"
        "v_accvgpr_read_b32 v133, a125\n"
        "v_max_u32 v132, v132, v133\n"
        "v_accvgpr_read_b32 v133, a126\n"
        "v_max_u32 v132, v132, v133\n"
        "v_accvgpr_read_b32 v133, a127\n"
        "v_max_u32 v132, v132, v133\n"
        "v_accvgpr_read_b32 v133, a128\n"
        "v_max_u32 v132, v132, v133\n"
        "v_accvgpr_read_b32 v133, a129\n"
        "v_max_u32 v132, v132, v133\n"
        "v_accvgpr_read_b32 v133, a130\n"
        "v_max_u32 v132, v132, v133\n"
        "v_accvgpr_read_b32 v133, a131\n"
        "v_max_u32 v132, v132, v133\n"
        "v_accvgpr_read_b32 v133, a132\n"
        "v_max_u32 v132, v132, v133\n"
        "v_accvgpr_read_b32 v133, a133\n"
        "v_max_u32 v132, v132, v133\n"
        "v_accvgpr_read_b32 v133, a134\n"
        "v_max_u32 v132, v132, v133\n"
        "v_accvgpr_read_b32 v133, a135\n"
        "v_max_u32 v132, v132, v133\n"
        "v_accvgpr_read_b32 v133, a136\n"
        "v_max_u32 v132, v132, v133\n"
        "v_accvgpr_read_b32 v133, a137\n"
        "v_max_u32 v132, v132, v133\n"
        "v_accvgpr_read_b32 v133, a138\n"
        "v_max_u32 v132, v132, v133\n"
        "v_accvgpr_read_b32 v133, a139\n"
        "v_max_u32 v132, v132, v133\n"
        "v_accvgpr_read_b32 v133, a140\n"
        "v_max_u32 v132, v132, v133\n"
        "v_accvgpr_read_b32 v133, a141\n"
        "v_max_u32 v132, v132, v133\n"
        "v_accvgpr_read_b32 v133, a142\n"
        "v_max_u32 v132, v132, v133\n"
        "v_accvgpr_read_b32 v133, a143\n"
        "v_max_u32 v132, v132, v133\n"
        "v_accvgpr_read_b32 v133, a144\n"
        "v_max_u32 v132, v132, v133\n"
        "v_accvgpr_read_b32 v133, a145\n"
        "v_max_u32 v132, v132, v133\n"
        "v_accvgpr_read_b32 v133, a146\n"
        "v_max_u32 v132, v132, v133\n"
        "v_accvgpr_read_b32 v133, a147\n"
        "v_max_u32 v132, v132, v133\n"
        "v_accvgpr_read_b32 v133, a148\n"
        "v_max_u32 v132, v132, v133\n"
        "v_accvgpr_read_b32 v133, a149\n"
        "v_max_u32 v132, v132, v133\n"
        "v_accvgpr_read_b32 v133, a150\n"
        "v_max_u32 v132, v132, v133\n"
        "v_accvgpr_read_b32 v133, a151\n"
        "v_max_u32 v132, v132, v133\n"
        "v_accvgpr_read_b32 v133, a152\n"
        "v_max_u32 v132, v132, v133\n"
        "v_accvgpr_read_b32 v133, a153\n"
        "v_max_u32 v132, v132, v133\n"
        "v_accvgpr_read_b32 v133, a154\n"
        "v_max_u32 v132, v132, v133\n"
        "v_accvgpr_read_b32 v133, a155\n"
        "v_max_u32 v132, v132, v133\n"
        "v_accvgpr_read_b32 v133, a156\n"
        "v_max_u32 v132, v132, v133\n"
        "v_accvgpr_read_b32 v133, a157\n"
        "v_max_u32 v132, v132, v133\n"
        "v_accvgpr_read_b32 v133, a158\n"
        "v_max_u32 v132, v132, v133\n"
        "v_accvgpr_read_b32 v133, a159\n"
        "v_max_u32 v132, v132, v133\n"
        "v_accvgpr_read_b32 v133, a160\n"
        "v_max_u32 v132, v132, v133\n"
        "v_accvgpr_read_b32 v133, a161\n"
        "v_max_u32 v132, v132, v133\n"
        "v_accvgpr_read_b32 v133, a162\n"
        "v_max_u32 v132, v132, v133\n"
        "v_accvgpr_read_b32 v133, a163\n"
        "v_max_u32 v132, v132, v133\n"
        "v_accvgpr_read_b32 v133, a164\n"
        "v_max_u32 v132, v132, v133\n"
        "v_accvgpr_read_b32 v133, a165\n"
        "v_max_u32 v132, v132, v133\n"
        "v_accvgpr_read_b32 v133, a166\n"
        "v_max_u32 v132, v132, v133\n"
        "v_accvgpr_read_b32 v133, a167\n"
        "v_max_u32 v132, v132, v133\n"
        "v_accvgpr_read_b32 v133, a168\n"
        "v_max_u32 v132, v132, v133\n"
        "v_accvgpr_read_b32 v133, a169\n"
        "v_max_u32 v132, v132, v133\n"
        "v_accvgpr_read_b32 v133, a170\n"
        "v_max_u32 v132, v132, v133\n"
        "v_accvgpr_read_b32 v133, a171\n"
        "v_max_u32 v132, v132, v133\n"
        "v_accvgpr_read_b32 v133, a172\n"
        "v_max_u32 v132, v132, v133\n"
        "v_accvgpr_read_b32 v133, a173\n"
        "v_max_u32 v132, v132, v133\n"
        "v_accvgpr_read_b32 v133, a174\n"
        "v_max_u32 v132, v132, v133\n"
        "v_accvgpr_read_b32 v133, a175\n"
        "v_max_u32 v132, v132, v133\n"
        "v_accvgpr_read_b32 v133, a176\n"
        "v_max_u32 v132, v132, v133\n"
        "v_accvgpr_read_b32 v133, a177\n"
        "v_max_u32 v132, v132, v133\n"
        "v_accvgpr_read_b32 v133, a178\n"
        "v_max_u32 v132, v132, v133\n"
        "v_accvgpr_read_b32 v133, a179\n"
        "v_max_u32 v132, v132, v133\n"
        "v_accvgpr_read_b32 v133, a180\n"
        "v_max_u32 v132, v132, v133\n"
        "v_accvgpr_read_b32 v133, a181\n"
        "v_max_u32 v132, v132, v133\n"
        "v_accvgpr_read_b32 v133, a182\n"
        "v_max_u32 v132, v132, v133\n"
        "v_accvgpr_read_b32 v133, a183\n"
        "v_max_u32 v132, v132, v133\n"
        "v_accvgpr_read_b32 v133, a184\n"
        "v_max_u32 v132, v132, v133\n"
        "v_accvgpr_read_b32 v133, a185\n"
        "v_max_u32 v132, v132, v133\n"
        "v_accvgpr_read_b32 v133, a186\n"
        "v_max_u32 v132, v132, v133\n"
        "v_accvgpr_read_b32 v133, a187\n"
        "v_max_u32 v132, v132, v133\n"
        "v_accvgpr_read_b32 v133, a188\n"
        "v_max_u32 v132, v132, v133\n"
        "v_accvgpr_read_b32 v133, a189\n"
        "v_max_u32 v132, v132, v133\n"
        "v_accvgpr_read_b32 v133, a190\n"
        "v_max_u32 v132, v132, v133\n"
        "v_accvgpr_read_b32 v133, a191\n"
        "v_max_u32 v132, v132, v133\n"
        "v_accvgpr_read_b32 v133, a192\n"
        "v_max_u32 v132, v132, v133\n"
        "v_accvgpr_read_b32 v133, a193\n"
        "v_max_u32 v132, v132, v133\n"
        "v_accvgpr_read_b32 v133, a194\n"
        "v_max_u32 v132, v132, v133\n"
        "v_accvgpr_read_b32 v133, a195\n"
        "v_max_u32 v132, v132, v133\n"
        "v_accvgpr_read_b32 v133, a196\n"
        "v_max_u32 v132, v132, v133\n"
        "v_accvgpr_read_b32 v133, a197\n"
        "v_max_u32 v132, v132, v133\n"
        "v_accvgpr_read_b32 v133, a198\n"
        "v_max_u32 v132, v132, v133\n"
        "v_accvgpr_read_b32 v133, a199\n"
        "v_max_u32 v132, v132, v133\n"
        "v_accvgpr_read_b32 v133, a200\n"
        "v_max_u32 v132, v132, v133\n"
        "v_accvgpr_read_b32 v133, a201\n"
        "v_max_u32 v132, v132, v133\n"
        "v_accvgpr_read_b32 v133, a202\n"
        "v_max_u32 v132, v132, v133\n"
        "v_accvgpr_read_b32 v133, a203\n"
        "v_max_u32 v132, v132, v133\n"
        "v_accvgpr_read_b32 v133, a204\n"
        "v_max_u32 v132, v132, v133\n"
        "v_accvgpr_read_b32 v133, a205\n"
        "v_max_u32 v132, v132, v133\n"
        "v_accvgpr_read_b32 v133, a206\n"
        "v_max_u32 v132, v132, v133\n"
        "v_accvgpr_read_b32 v133, a207\n"
        "v_max_u32 v132, v132, v133\n"
        "v_accvgpr_read_b32 v133, a208\n"
        "v_max_u32 v132, v132, v133\n"
        "v_accvgpr_read_b32 v133, a209\n"
        "v_max_u32 v132, v132, v133\n"
        "v_accvgpr_read_b32 v133, a210\n"
        "v_max_u32 v132, v132, v133\n"
        "v_accvgpr_read_b32 v133, a211\n"
        "v_max_u32 v132, v132, v133\n"
        "v_accvgpr_read_b32 v133, a212\n"
        "v_max_u32 v132, v132, v133\n"
        "v_accvgpr_read_b32 v133, a213\n"
        "v_max_u32 v132, v132, v133\n"
        "v_accvgpr_read_b32 v133, a214\n"
        "v_max_u32 v132, v132, v133\n"
        "v_accvgpr_read_b32 v133, a215\n"
        "v_max_u32 v132, v132, v133\n"
        "v_accvgpr_read_b32 v133, a216\n"
        "v_max_u32 v132, v132, v133\n"
        "v_accvgpr_read_b32 v133, a217\n"
        "v_max_u32 v132, v132, v133\n"
        "v_accvgpr_read_b32 v133, a218\n"
        "v_max_u32 v132, v132, v133\n"
        "v_accvgpr_read_b32 v133, a219\n"
        "v_max_u32 v132, v132, v133\n"
        "v_accvgpr_read_b32 v133, a220\n"
        "v_max_u32 v132, v132, v133\n"
        "v_accvgpr_read_b32 v133, a221\n"
        "v_max_u32 v132, v132, v133\n"
        "v_accvgpr_read_b32 v133, a222\n"
        "v_max_u32 v132, v132, v133\n"
        "v_accvgpr_read_b32 v133, a223\n"
        "v_max_u32 v132, v132, v133\n"
        "v_accvgpr_read_b32 v133, a224\n"
        "v_max_u32 v132, v132, v133\n"
        "v_accvgpr_read_b32 v133, a225\n"
        "v_max_u32 v132, v132, v133\n"
        "v_accvgpr_read_b32 v133, a226\n"
        "v_max_u32 v132, v132, v133\n"
        "v_accvgpr_read_b32 v133, a227\n"
        "v_max_u32 v132, v132, v133\n"
        "v_accvgpr_read_b32 v133, a228\n"
        "v_max_u32 v132, v132, v133\n"
        "v_accvgpr_read_b32 v133, a229\n"
        "v_max_u32 v132, v132, v133\n"
        "v_accvgpr_read_b32 v133, a230\n"
        "v_max_u32 v132, v132, v133\n"
        "v_accvgpr_read_b32 v133, a231\n"
        "v_max_u32 v132, v132, v133\n"
        "v_accvgpr_read_b32 v133, a232\n"
        "v_max_u32 v132, v132, v133\n"
        "v_accvgpr_read_b32 v133, a233\n"
        "v_max_u32 v132, v132, v133\n"
        "v_accvgpr_read_b32 v133, a234\n"
        "v_max_u32 v132, v132, v133\n"
        "v_accvgpr_read_b32 v133, a235\n"
        "v_max_u32 v132, v132, v133\n"
        "v_accvgpr_read_b32 v133, a236\n"
        "v_max_u32 v132, v132, v133\n"
        "v_accvgpr_read_b32 v133, a237\n"
        "v_max_u32 v132, v132, v133\n"
        "v_accvgpr_read_b32 v133, a238\n"
        "v_max_u32 v132, v132, v133\n"
        "v_accvgpr_read_b32 v133, a239\n"
        "v_max_u32 v132, v132, v133\n"
        "v_accvgpr_read_b32 v133, a240\n"
        "v_max_u32 v132, v132, v133\n"
        "v_accvgpr_read_b32 v133, a241\n"
        "v_max_u32 v132, v132, v133\n"
        "v_accvgpr_read_b32 v133, a242\n"
        "v_max_u32 v132, v132, v133\n"
        "v_accvgpr_read_b32 v133, a243\n"
        "v_max_u32 v132, v132, v133\n"
        "v_accvgpr_read_b32 v133, a244\n"
        "v_max_u32 v132, v132, v133\n"
        "v_accvgpr_read_b32 v133, a245\n"
        "v_max_u32 v132, v132, v133\n"
        "v_accvgpr_read_b32 v133, a246\n"
        "v_max_u32 v132, v132, v133\n"
        "v_accvgpr_read_b32 v133, a247\n"
        "v_max_u32 v132, v132, v133\n"
        "v_accvgpr_read_b32 v133, a248\n"
        "v_max_u32 v132, v132, v133\n"
        "v_accvgpr_read_b32 v133, a249\n"
        "v_max_u32 v132, v132, v133\n"
        "v_accvgpr_read_b32 v133, a250\n"
        "v_max_u32 v132, v132, v133\n"
        "v_accvgpr_read_b32 v133, a251\n"
        "v_max_u32 v132, v132, v133\n"
        "v_accvgpr_read_b32 v133, a252\n"
        "v_max_u32 v132, v132, v133\n"
        "v_accvgpr_read_b32 v133, a253\n"
        "v_max_u32 v132, v132, v133\n"
        "v_accvgpr_read_b32 v133, a254\n"
        "v_max_u32 v132, v132, v133\n"
        "v_accvgpr_read_b32 v133, a255\n"
        "v_max_u32 v132, v132, v133\n"
        "v_cmp_eq_u32 vcc, -1, v132\n"
        "s_add_u32 s57, s57, 1\n"
        "s_cmp_gt_u32 s57, 64\n"
        "s_cselect_b64 s[62:63], 0, -1\n"
        "s_nop 0\n"
        "s_and_b64 vcc, vcc, s[62:63]\n"
        "s_cbranch_vccnz Lsa1_%=\n"
        "s_and_b32 s61, s76, 0x7f80\n"
        "s_add_u32 s61, s61, s74\n"
        "s_add_u32 s54, s76, 0x8000\n"
        "s_mov_b64 exec, 1\n"
        "buffer_store_dword v19, off, %[srd], s54 sc1\n"
        "s_mov_b64 exec, -1\n"
        "buffer_store_dwordx4 v[24:27], v0, %[srd], s64 offen offset:0 sc1\n"
        "buffer_store_dwordx4 v[24:27], v0, %[srd], s64 offen offset:1024 sc1\n"
        "s_add_u32 s54, s64, 0x800000\n"
        "buffer_store_dwordx4 v[24:27], v0, %[srd], s54 offen offset:0 sc1\n"
        "buffer_store_dwordx4 v[24:27], v0, %[srd], s54 offen offset:1024 sc1\n"
        "buffer_store_dwordx4 v[24:27], v0, %[srd], s68 offen offset:0 sc1\n"
        "buffer_store_dwordx4 v[24:27], v0, %[srd], s68 offen offset:1024 sc1\n"
        "s_add_u32 s54, s68, 0x10000\n"
        "buffer_store_dwordx4 v[24:27], v0, %[srd], s54 offen offset:0 sc1\n"
        "buffer_store_dwordx4 v[24:27], v0, %[srd], s54 offen offset:1024 sc1\n"
        "s_add_u32 s54, s67, 0x80000\n"
        "s_mov_b64 exec, 0xffff\n"
        "buffer_store_dwordx2 v[24:25], v16, %[srd], s67 offen sc1\n"
        "buffer_store_dwordx2 v[24:25], v16, %[srd], s54 offen sc1\n"
        "s_mov_b64 exec, -1\n"
        "s_add_u32 s54, s64, 0x200000\n"
        "buffer_store_dwordx4 v[24:27], v0, %[srd], s54 offen offset:0 sc1\n"
        "buffer_store_dwordx4 v[24:27], v0, %[srd], s54 offen offset:1024 sc1\n"
        "s_add_u32 s54, s64, 0xa00000\n"
        "buffer_store_dwordx4 v[24:27], v0, %[srd], s54 offen offset:0 sc1\n"
        "buffer_store_dwordx4 v[24:27], v0, %[srd], s54 offen offset:1024 sc1\n"
        "s_add_u32 s54, s61, 0x20000\n"
        "s_mov_b64 exec, 0xff\n"
        "buffer_store_dwordx4 v[24:27], v0, %[srd], s71 offen offset:0 sc1\n"
        "buffer_store_dwordx4 v[24:27], v0, %[srd], s54 offen offset:0 sc1\n"
        "s_mov_b64 exec, -1\n"
        "buffer_store_dwordx2 v[24:25], v17, %[srd], s74 offen sc1\n"
        "v_mov_b32 v176, v228\n"
        "s_lshr_b32 s54, s81, 14\n"
        "s_mul_i32 s54, s54, 4608\n"
        "v_add_u32 v177, s54, v246\n"
        "v_add_u32 v178, s54, v247\n"
        "s_lshr_b32 s54, s81, 4\n"
        "s_add_u32 s54, s54, s49\n"
        "s_add_u32 s54, s54, 16384\n"
        "v_bfe_u32 v179, v0, 4, 5\n"
        "v_lshlrev_b32 v179, 2, v179\n"
        "v_add_u32 v179, s54, v179\n"
        "s_mov_b32 s57, 0\n"
        "Lsa2_%=:\n"
        "ds_read_b128 v[192:195], v176\n"
        "v_mov_b32 v180, 0xff7fffff\n"
        "s_waitcnt lgkmcnt(0)\n"
        "s_nop 1\n"
        "v_mfma_f32_32x32x16_bf16 v[128:143], a[64:67], v[192:195], 0\n"
        "s_nop 15\n"
        "s_nop 3\n"
        "v_max3_f32 v180, v180, v128, v129\n"
        "v_max3_f32 v180, v180, v130, v131\n"
        "v_max3_f32 v180, v180, v132, v133\n"
        "v_max3_f32 v180, v180, v134, v135\n"
        "v_max3_f32 v180, v180, v136, v137\n"
        "v_max3_f32 v180, v180, v138, v139\n"
        "v_max3_f32 v180, v180, v140, v141\n"
        "v_max3_f32 v180, v180, v142, v143\n"
        "s_nop 7\n"
        "v_mfma_f32_32x32x16_bf16 v[128:143], a[68:71], v[192:195], 0\n"
        "s_nop 15\n"
        "s_nop 3\n"
        "v_max3_f32 v180, v180, v128, v129\n"
        "v_max3_f32 v180, v180, v130, v131\n"
        "v_max3_f32 v180, v180, v132, v133\n"
        "v_max3_f32 v180, v180, v134, v135\n"
        "v_max3_f32 v180, v180, v136, v137\n"
        "v_max3_f32 v180, v180, v138, v139\n"
        "v_max3_f32 v180, v180, v140, v141\n"
        "v_max3_f32 v180, v180, v142, v143\n"
        "s_nop 7\n"
        "v_mfma_f32_32x32x16_bf16 v[128:143], a[72:75], v[192:195], 0\n"
        "s_nop 15\n"
        "s_nop 3\n"
        "v_max3_f32 v180, v180, v128, v129\n"
        "v_max3_f32 v180, v180, v130, v131\n"
        "v_max3_f32 v180, v180, v132, v133\n"
        "v_max3_f32 v180, v180, v134, v135\n"
        "v_max3_f32 v180, v180, v136, v137\n"
        "v_max3_f32 v180, v180, v138, v139\n"
        "v_max3_f32 v180, v180, v140, v141\n"
        "v_max3_f32 v180, v180, v142, v143\n"
        "s_nop 7\n"
        "v_mfma_f32_32x32x16_bf16 v[128:143], a[76:79], v[192:195], 0\n"
        "s_nop 15\n"
        "s_nop 3\n"
        "v_max3_f32 v180, v180, v128, v129\n"
        "v_max3_f32 v180, v180, v130, v131\n"
        "v_max3_f32 v180, v180, v132, v133\n"
        "v_max3_f32 v180, v180, v134, v135\n"
        "v_max3_f32 v180, v180, v136, v137\n"
        "v_max3_f32 v180, v180, v138, v139\n"
        "v_max3_f32 v180, v180, v140, v141\n"
        "v_max3_f32 v180, v180, v142, v143\n"
        "s_nop 7\n"
        "v_mfma_f32_32x32x16_bf16 v[128:143], a[80:83], v[192:195], 0\n"
        "s_nop 15\n"
        "s_nop 3\n"
        "v_max3_f32 v180, v180, v128, v129\n"
        "v_max3_f32 v180, v180, v130, v131\n"
        "v_max3_f32 v180, v180, v132, v133\n"
        "v_max3_f32 v180, v180, v134, v135\n"
        "v_max3_f32 v180, v180, v136, v137\n"
        "v_max3_f32 v180, v180, v138, v139\n"
        "v_max3_f32 v180, v180, v140, v141\n"
        "v_max3_f32 v180, v180, v142, v143\n"
        "s_nop 7\n"
        "v_mfma_f32_32x32x16_bf16 v[128:143], a[84:87], v[192:195], 0\n"
        "s_nop 15\n"
        "s_nop 3\n"
        "v_max3_f32 v180, v180, v128, v129\n"
        "v_max3_f32 v180, v180, v130, v131\n"
        "v_max3_f32 v180, v180, v132, v133\n"
        "v_max3_f32 v180, v180, v134, v135\n"
        "v_max3_f32 v180, v180, v136, v137\n"
        "v_max3_f32 v180, v180, v138, v139\n"
        "v_max3_f32 v180, v180, v140, v141\n"
        "v_max3_f32 v180, v180, v142, v143\n"
        "s_nop 7\n"
        "v_mfma_f32_32x32x16_bf16 v[128:143], a[88:91], v[192:195], 0\n"
        "s_nop 15\n"
        "s_nop 3\n"
        "v_max3_f32 v180, v180, v128, v129\n"
        "v_max3_f32 v180, v180, v130, v131\n"
        "v_max3_f32 v180, v180, v132, v133\n"
        "v_max3_f32 v180, v180, v134, v135\n"
        "v_max3_f32 v180, v180, v136, v137\n"
        "v_max3_f32 v180, v180, v138, v139\n"
        "v_max3_f32 v180, v180, v140, v141\n"
        "v_max3_f32 v180, v180, v142, v143\n"
        "s_nop 7\n"
        "v_mfma_f32_32x32x16_bf16 v[128:143], a[92:95], v[192:195], 0\n"
        "s_nop 15\n"
        "s_nop 3\n"
        "v_max3_f32 v180, v180, v128, v129\n"
        "v_max3_f32 v180, v180, v130, v131\n"
        "v_max3_f32 v180, v180, v132, v133\n"
        "v_max3_f32 v180, v180, v134, v135\n"
        "v_max3_f32 v180, v180, v136, v137\n"
        "v_max3_f32 v180, v180, v138, v139\n"
        "v_max3_f32 v180, v180, v140, v141\n"
        "v_max3_f32 v180, v180, v142, v143\n"
        "s_nop 7\n"
        "v_mfma_f32_32x32x16_bf16 v[128:143], a[96:99], v[192:195], 0\n"
        "s_nop 15\n"
        "s_nop 3\n"
        "v_max3_f32 v180, v180, v128, v129\n"
        "v_max3_f32 v180, v180, v130, v131\n"
        "v_max3_f32 v180, v180, v132, v133\n"
        "v_max3_f32 v180, v180, v134, v135\n"
        "v_max3_f32 v180, v180, v136, v137\n"
        "v_max3_f32 v180, v180, v138, v139\n"
        "v_max3_f32 v180, v180, v140, v141\n"
        "v_max3_f32 v180, v180, v142, v143\n"
        "s_nop 7\n"
        "v_mfma_f32_32x32x16_bf16 v[128:143], a[100:103], v[192:195], 0\n"
        "s_nop 15\n"
        "s_nop 3\n"
        "v_max3_f32 v180, v180, v128, v129\n"
        "v_max3_f32 v180, v180, v130, v131\n"
        "v_max3_f32 v180, v180, v132, v133\n"
        "v_max3_f32 v180, v180, v134, v135\n"
        "v_max3_f32 v180, v180, v136, v137\n"
        "v_max3_f32 v180, v180, v138, v139\n"
        "v_max3_f32 v180, v180, v140, v141\n"
        "v_max3_f32 v180, v180, v142, v143\n"
        "s_nop 7\n"
        "v_mfma_f32_32x32x16_bf16 v[128:143], a[104:107], v[192:195], 0\n"
        "s_nop 15\n"
        "s_nop 3\n"
        "v_max3_f32 v180, v180, v128, v129\n"
        "v_max3_f32 v180, v180, v130, v131\n"
        "v_max3_f32 v180, v180, v132, v133\n"
        "v_max3_f32 v180, v180, v134, v135\n"
        "v_max3_f32 v180, v180, v136, v137\n"
        "v_max3_f32 v180, v180, v138, v139\n"
        "v_max3_f32 v180, v180, v140, v141\n"
        "v_max3_f32 v180, v180, v142, v143\n"
        "s_nop 7\n"
        "v_mfma_f32_32x32x16_bf16 v[128:143], a[108:111], v[192:195], 0\n"
        "s_nop 15\n"
        "s_nop 3\n"
        "v_max3_f32 v180, v180, v128, v129\n"
        "v_max3_f32 v180, v180, v130, v131\n"
        "v_max3_f32 v180, v180, v132, v133\n"
        "v_max3_f32 v180, v180, v134, v135\n"
        "v_max3_f32 v180, v180, v136, v137\n"
        "v_max3_f32 v180, v180, v138, v139\n"
        "v_max3_f32 v180, v180, v140, v141\n"
        "v_max3_f32 v180, v180, v142, v143\n"
        "s_nop 7\n"
        "v_mfma_f32_32x32x16_bf16 v[128:143], a[112:115], v[192:195], 0\n"
        "s_nop 15\n"
        "s_nop 3\n"
        "v_max3_f32 v180, v180, v128, v129\n"
        "v_max3_f32 v180, v180, v130, v131\n"
        "v_max3_f32 v180, v180, v132, v133\n"
        "v_max3_f32 v180, v180, v134, v135\n"
        "v_max3_f32 v180, v180, v136, v137\n"
        "v_max3_f32 v180, v180, v138, v139\n"
        "v_max3_f32 v180, v180, v140, v141\n"
        "v_max3_f32 v180, v180, v142, v143\n"
        "s_nop 7\n"
        "v_mfma_f32_32x32x16_bf16 v[128:143], a[116:119], v[192:195], 0\n"
        "s_nop 15\n"
        "s_nop 3\n"
        "v_max3_f32 v180, v180, v128, v129\n"
        "v_max3_f32 v180, v180, v130, v131\n"
        "v_max3_f32 v180, v180, v132, v133\n"
        "v_max3_f32 v180, v180, v134, v135\n"
        "v_max3_f32 v180, v180, v136, v137\n"
        "v_max3_f32 v180, v180, v138, v139\n"
        "v_max3_f32 v180, v180, v140, v141\n"
        "v_max3_f32 v180, v180, v142, v143\n"
        "s_nop 7\n"
        "v_mfma_f32_32x32x16_bf16 v[128:143], a[120:123], v[192:195], 0\n"
        "s_nop 15\n"
        "s_nop 3\n"
        "v_max3_f32 v180, v180, v128, v129\n"
        "v_max3_f32 v180, v180, v130, v131\n"
        "v_max3_f32 v180, v180, v132, v133\n"
        "v_max3_f32 v180, v180, v134, v135\n"
        "v_max3_f32 v180, v180, v136, v137\n"
        "v_max3_f32 v180, v180, v138, v139\n"
        "v_max3_f32 v180, v180, v140, v141\n"
        "v_max3_f32 v180, v180, v142, v143\n"
        "s_nop 7\n"
        "v_mfma_f32_32x32x16_bf16 v[128:143], a[124:127], v[192:195], 0\n"
        "s_nop 15\n"
        "s_nop 3\n"
        "v_max3_f32 v180, v180, v128, v129\n"
        "v_max3_f32 v180, v180, v130, v131\n"
        "v_max3_f32 v180, v180, v132, v133\n"
        "v_max3_f32 v180, v180, v134, v135\n"
        "v_max3_f32 v180, v180, v136, v137\n"
        "v_max3_f32 v180, v180, v138, v139\n"
        "v_max3_f32 v180, v180, v140, v141\n"
        "v_max3_f32 v180, v180, v142, v143\n"
        "s_nop 7\n"
        "v_mov_b32 v181, v180\n"
        "s_nop 1\n"
        "v_permlane32_swap_b32 v180, v181\n"
        "v_max_f32 v180, v180, v181\n"
        "v_mfma_f32_32x32x16_bf16 v[160:175], v[220:223], v[220:223], 0\n"
        "s_nop 15\n"
        "s_nop 1\n"
        "v_mfma_f32_32x32x16_bf16 v[128:143], a[64:67], v[192:195], 0\n"
        "s_nop 15\n"
        "s_nop 3\n"
        "v_sub_f32 v128, v128, v180\n"
        "v_sub_f32 v129, v129, v180\n"
        "v_sub_f32 v130, v130, v180\n"
        "v_sub_f32 v131, v131, v180\n"
        "v_sub_f32 v132, v132, v180\n"
        "v_sub_f32 v133, v133, v180\n"
        "v_sub_f32 v134, v134, v180\n"
        "v_sub_f32 v135, v135, v180\n"
        "v_sub_f32 v136, v136, v180\n"
        "v_sub_f32 v137, v137, v180\n"
        "v_sub_f32 v138, v138, v180\n"
        "v_sub_f32 v139, v139, v180\n"
        "v_sub_f32 v140, v140, v180\n"
        "v_sub_f32 v141, v141, v180\n"
        "v_sub_f32 v142, v142, v180\n"
        "v_sub_f32 v143, v143, v180\n"
        "v_exp_f32 v128, v128\n"
        "v_exp_f32 v129, v129\n"
        "v_exp_f32 v130, v130\n"
        "v_exp_f32 v131, v131\n"
        "v_exp_f32 v132, v132\n"
        "v_exp_f32 v133, v133\n"
        "v_exp_f32 v134, v134\n"
        "v_exp_f32 v135, v135\n"
        "v_exp_f32 v136, v136\n"
        "v_exp_f32 v137, v137\n"
        "v_exp_f32 v138, v138\n"
        "v_exp_f32 v139, v139\n"
        "v_exp_f32 v140, v140\n"
        "v_exp_f32 v141, v141\n"
        "v_exp_f32 v142, v142\n"
        "v_exp_f32 v143, v143\n"
        "s_nop 3\n"
        "v_smfmac_f32_32x32x32_bf16 v[160:175], a[128:131], v[128:135], v245\n"
        "v_smfmac_f32_32x32x32_bf16 v[160:175], a[132:135], v[136:143], v245\n"
        "s_nop 15\n"
        "v_mfma_f32_32x32x16_bf16 v[128:143], a[68:71], v[192:195], 0\n"
        "s_nop 15\n"
        "s_nop 3\n"
        "v_sub_f32 v128, v128, v180\n"
        "v_sub_f32 v129, v129, v180\n"
        "v_sub_f32 v130, v130, v180\n"
        "v_sub_f32 v131, v131, v180\n"
        "v_sub_f32 v132, v132, v180\n"
        "v_sub_f32 v133, v133, v180\n"
        "v_sub_f32 v134, v134, v180\n"
        "v_sub_f32 v135, v135, v180\n"
        "v_sub_f32 v136, v136, v180\n"
        "v_sub_f32 v137, v137, v180\n"
        "v_sub_f32 v138, v138, v180\n"
        "v_sub_f32 v139, v139, v180\n"
        "v_sub_f32 v140, v140, v180\n"
        "v_sub_f32 v141, v141, v180\n"
        "v_sub_f32 v142, v142, v180\n"
        "v_sub_f32 v143, v143, v180\n"
        "v_exp_f32 v128, v128\n"
        "v_exp_f32 v129, v129\n"
        "v_exp_f32 v130, v130\n"
        "v_exp_f32 v131, v131\n"
        "v_exp_f32 v132, v132\n"
        "v_exp_f32 v133, v133\n"
        "v_exp_f32 v134, v134\n"
        "v_exp_f32 v135, v135\n"
        "v_exp_f32 v136, v136\n"
        "v_exp_f32 v137, v137\n"
        "v_exp_f32 v138, v138\n"
        "v_exp_f32 v139, v139\n"
        "v_exp_f32 v140, v140\n"
        "v_exp_f32 v141, v141\n"
        "v_exp_f32 v142, v142\n"
        "v_exp_f32 v143, v143\n"
        "s_nop 3\n"
        "v_smfmac_f32_32x32x32_bf16 v[160:175], a[136:139], v[128:135], v245\n"
        "v_smfmac_f32_32x32x32_bf16 v[160:175], a[140:143], v[136:143], v245\n"
        "s_nop 15\n"
        "v_mfma_f32_32x32x16_bf16 v[128:143], a[72:75], v[192:195], 0\n"
        "s_nop 15\n"
        "s_nop 3\n"
        "v_sub_f32 v128, v128, v180\n"
        "v_sub_f32 v129, v129, v180\n"
        "v_sub_f32 v130, v130, v180\n"
        "v_sub_f32 v131, v131, v180\n"
        "v_sub_f32 v132, v132, v180\n"
        "v_sub_f32 v133, v133, v180\n"
        "v_sub_f32 v134, v134, v180\n"
        "v_sub_f32 v135, v135, v180\n"
        "v_sub_f32 v136, v136, v180\n"
        "v_sub_f32 v137, v137, v180\n"
        "v_sub_f32 v138, v138, v180\n"
        "v_sub_f32 v139, v139, v180\n"
        "v_sub_f32 v140, v140, v180\n"
        "v_sub_f32 v141, v141, v180\n"
        "v_sub_f32 v142, v142, v180\n"
        "v_sub_f32 v143, v143, v180\n"
        "v_exp_f32 v128, v128\n"
        "v_exp_f32 v129, v129\n"
        "v_exp_f32 v130, v130\n"
        "v_exp_f32 v131, v131\n"
        "v_exp_f32 v132, v132\n"
        "v_exp_f32 v133, v133\n"
        "v_exp_f32 v134, v134\n"
        "v_exp_f32 v135, v135\n"
        "v_exp_f32 v136, v136\n"
        "v_exp_f32 v137, v137\n"
        "v_exp_f32 v138, v138\n"
        "v_exp_f32 v139, v139\n"
        "v_exp_f32 v140, v140\n"
        "v_exp_f32 v141, v141\n"
        "v_exp_f32 v142, v142\n"
        "v_exp_f32 v143, v143\n"
        "s_nop 3\n"
        "v_smfmac_f32_32x32x32_bf16 v[160:175], a[144:147], v[128:135], v245\n"
        "v_smfmac_f32_32x32x32_bf16 v[160:175], a[148:151], v[136:143], v245\n"
        "s_nop 15\n"
        "v_mfma_f32_32x32x16_bf16 v[128:143], a[76:79], v[192:195], 0\n"
        "s_nop 15\n"
        "s_nop 3\n"
        "v_sub_f32 v128, v128, v180\n"
        "v_sub_f32 v129, v129, v180\n"
        "v_sub_f32 v130, v130, v180\n"
        "v_sub_f32 v131, v131, v180\n"
        "v_sub_f32 v132, v132, v180\n"
        "v_sub_f32 v133, v133, v180\n"
        "v_sub_f32 v134, v134, v180\n"
        "v_sub_f32 v135, v135, v180\n"
        "v_sub_f32 v136, v136, v180\n"
        "v_sub_f32 v137, v137, v180\n"
        "v_sub_f32 v138, v138, v180\n"
        "v_sub_f32 v139, v139, v180\n"
        "v_sub_f32 v140, v140, v180\n"
        "v_sub_f32 v141, v141, v180\n"
        "v_sub_f32 v142, v142, v180\n"
        "v_sub_f32 v143, v143, v180\n"
        "v_exp_f32 v128, v128\n"
        "v_exp_f32 v129, v129\n"
        "v_exp_f32 v130, v130\n"
        "v_exp_f32 v131, v131\n"
        "v_exp_f32 v132, v132\n"
        "v_exp_f32 v133, v133\n"
        "v_exp_f32 v134, v134\n"
        "v_exp_f32 v135, v135\n"
        "v_exp_f32 v136, v136\n"
        "v_exp_f32 v137, v137\n"
        "v_exp_f32 v138, v138\n"
        "v_exp_f32 v139, v139\n"
        "v_exp_f32 v140, v140\n"
        "v_exp_f32 v141, v141\n"
        "v_exp_f32 v142, v142\n"
        "v_exp_f32 v143, v143\n"
        "s_nop 3\n"
        "v_smfmac_f32_32x32x32_bf16 v[160:175], a[152:155], v[128:135], v245\n"
        "v_smfmac_f32_32x32x32_bf16 v[160:175], a[156:159], v[136:143], v245\n"
        "s_nop 15\n"
        "v_mfma_f32_32x32x16_bf16 v[128:143], a[80:83], v[192:195], 0\n"
        "s_nop 15\n"
        "s_nop 3\n"
        "v_sub_f32 v128, v128, v180\n"
        "v_sub_f32 v129, v129, v180\n"
        "v_sub_f32 v130, v130, v180\n"
        "v_sub_f32 v131, v131, v180\n"
        "v_sub_f32 v132, v132, v180\n"
        "v_sub_f32 v133, v133, v180\n"
        "v_sub_f32 v134, v134, v180\n"
        "v_sub_f32 v135, v135, v180\n"
        "v_sub_f32 v136, v136, v180\n"
        "v_sub_f32 v137, v137, v180\n"
        "v_sub_f32 v138, v138, v180\n"
        "v_sub_f32 v139, v139, v180\n"
        "v_sub_f32 v140, v140, v180\n"
        "v_sub_f32 v141, v141, v180\n"
        "v_sub_f32 v142, v142, v180\n"
        "v_sub_f32 v143, v143, v180\n"
        "v_exp_f32 v128, v128\n"
        "v_exp_f32 v129, v129\n"
        "v_exp_f32 v130, v130\n"
        "v_exp_f32 v131, v131\n"
        "v_exp_f32 v132, v132\n"
        "v_exp_f32 v133, v133\n"
        "v_exp_f32 v134, v134\n"
        "v_exp_f32 v135, v135\n"
        "v_exp_f32 v136, v136\n"
        "v_exp_f32 v137, v137\n"
        "v_exp_f32 v138, v138\n"
        "v_exp_f32 v139, v139\n"
        "v_exp_f32 v140, v140\n"
        "v_exp_f32 v141, v141\n"
        "v_exp_f32 v142, v142\n"
        "v_exp_f32 v143, v143\n"
        "s_nop 3\n"
        "v_smfmac_f32_32x32x32_bf16 v[160:175], a[160:163], v[128:135], v245\n"
        "v_smfmac_f32_32x32x32_bf16 v[160:175], a[164:167], v[136:143], v245\n"
        "s_nop 15\n"
        "v_mfma_f32_32x32x16_bf16 v[128:143], a[84:87], v[192:195], 0\n"
        "s_nop 15\n"
        "s_nop 3\n"
        "v_sub_f32 v128, v128, v180\n"
        "v_sub_f32 v129, v129, v180\n"
        "v_sub_f32 v130, v130, v180\n"
        "v_sub_f32 v131, v131, v180\n"
        "v_sub_f32 v132, v132, v180\n"
        "v_sub_f32 v133, v133, v180\n"
        "v_sub_f32 v134, v134, v180\n"
        "v_sub_f32 v135, v135, v180\n"
        "v_sub_f32 v136, v136, v180\n"
        "v_sub_f32 v137, v137, v180\n"
        "v_sub_f32 v138, v138, v180\n"
        "v_sub_f32 v139, v139, v180\n"
        "v_sub_f32 v140, v140, v180\n"
        "v_sub_f32 v141, v141, v180\n"
        "v_sub_f32 v142, v142, v180\n"
        "v_sub_f32 v143, v143, v180\n"
        "v_exp_f32 v128, v128\n"
        "v_exp_f32 v129, v129\n"
        "v_exp_f32 v130, v130\n"
        "v_exp_f32 v131, v131\n"
        "v_exp_f32 v132, v132\n"
        "v_exp_f32 v133, v133\n"
        "v_exp_f32 v134, v134\n"
        "v_exp_f32 v135, v135\n"
        "v_exp_f32 v136, v136\n"
        "v_exp_f32 v137, v137\n"
        "v_exp_f32 v138, v138\n"
        "v_exp_f32 v139, v139\n"
        "v_exp_f32 v140, v140\n"
        "v_exp_f32 v141, v141\n"
        "v_exp_f32 v142, v142\n"
        "v_exp_f32 v143, v143\n"
        "s_nop 3\n"
        "v_smfmac_f32_32x32x32_bf16 v[160:175], a[168:171], v[128:135], v245\n"
        "v_smfmac_f32_32x32x32_bf16 v[160:175], a[172:175], v[136:143], v245\n"
        "s_nop 15\n"
        "v_mfma_f32_32x32x16_bf16 v[128:143], a[88:91], v[192:195], 0\n"
        "s_nop 15\n"
        "s_nop 3\n"
        "v_sub_f32 v128, v128, v180\n"
        "v_sub_f32 v129, v129, v180\n"
        "v_sub_f32 v130, v130, v180\n"
        "v_sub_f32 v131, v131, v180\n"
        "v_sub_f32 v132, v132, v180\n"
        "v_sub_f32 v133, v133, v180\n"
        "v_sub_f32 v134, v134, v180\n"
        "v_sub_f32 v135, v135, v180\n"
        "v_sub_f32 v136, v136, v180\n"
        "v_sub_f32 v137, v137, v180\n"
        "v_sub_f32 v138, v138, v180\n"
        "v_sub_f32 v139, v139, v180\n"
        "v_sub_f32 v140, v140, v180\n"
        "v_sub_f32 v141, v141, v180\n"
        "v_sub_f32 v142, v142, v180\n"
        "v_sub_f32 v143, v143, v180\n"
        "v_exp_f32 v128, v128\n"
        "v_exp_f32 v129, v129\n"
        "v_exp_f32 v130, v130\n"
        "v_exp_f32 v131, v131\n"
        "v_exp_f32 v132, v132\n"
        "v_exp_f32 v133, v133\n"
        "v_exp_f32 v134, v134\n"
        "v_exp_f32 v135, v135\n"
        "v_exp_f32 v136, v136\n"
        "v_exp_f32 v137, v137\n"
        "v_exp_f32 v138, v138\n"
        "v_exp_f32 v139, v139\n"
        "v_exp_f32 v140, v140\n"
        "v_exp_f32 v141, v141\n"
        "v_exp_f32 v142, v142\n"
        "v_exp_f32 v143, v143\n"
        "s_nop 3\n"
        "v_smfmac_f32_32x32x32_bf16 v[160:175], a[176:179], v[128:135], v245\n"
        "v_smfmac_f32_32x32x32_bf16 v[160:175], a[180:183], v[136:143], v245\n"
        "s_nop 15\n"
        "v_mfma_f32_32x32x16_bf16 v[128:143], a[92:95], v[192:195], 0\n"
        "s_nop 15\n"
        "s_nop 3\n"
        "v_sub_f32 v128, v128, v180\n"
        "v_sub_f32 v129, v129, v180\n"
        "v_sub_f32 v130, v130, v180\n"
        "v_sub_f32 v131, v131, v180\n"
        "v_sub_f32 v132, v132, v180\n"
        "v_sub_f32 v133, v133, v180\n"
        "v_sub_f32 v134, v134, v180\n"
        "v_sub_f32 v135, v135, v180\n"
        "v_sub_f32 v136, v136, v180\n"
        "v_sub_f32 v137, v137, v180\n"
        "v_sub_f32 v138, v138, v180\n"
        "v_sub_f32 v139, v139, v180\n"
        "v_sub_f32 v140, v140, v180\n"
        "v_sub_f32 v141, v141, v180\n"
        "v_sub_f32 v142, v142, v180\n"
        "v_sub_f32 v143, v143, v180\n"
        "v_exp_f32 v128, v128\n"
        "v_exp_f32 v129, v129\n"
        "v_exp_f32 v130, v130\n"
        "v_exp_f32 v131, v131\n"
        "v_exp_f32 v132, v132\n"
        "v_exp_f32 v133, v133\n"
        "v_exp_f32 v134, v134\n"
        "v_exp_f32 v135, v135\n"
        "v_exp_f32 v136, v136\n"
        "v_exp_f32 v137, v137\n"
        "v_exp_f32 v138, v138\n"
        "v_exp_f32 v139, v139\n"
        "v_exp_f32 v140, v140\n"
        "v_exp_f32 v141, v141\n"
        "v_exp_f32 v142, v142\n"
        "v_exp_f32 v143, v143\n"
        "s_nop 3\n"
        "v_smfmac_f32_32x32x32_bf16 v[160:175], a[184:187], v[128:135], v245\n"
        "v_smfmac_f32_32x32x32_bf16 v[160:175], a[188:191], v[136:143], v245\n"
        "s_nop 15\n"
        "v_mfma_f32_32x32x16_bf16 v[128:143], a[96:99], v[192:195], 0\n"
        "s_nop 15\n"
        "s_nop 3\n"
        "v_sub_f32 v128, v128, v180\n"
        "v_sub_f32 v129, v129, v180\n"
        "v_sub_f32 v130, v130, v180\n"
        "v_sub_f32 v131, v131, v180\n"
        "v_sub_f32 v132, v132, v180\n"
        "v_sub_f32 v133, v133, v180\n"
        "v_sub_f32 v134, v134, v180\n"
        "v_sub_f32 v135, v135, v180\n"
        "v_sub_f32 v136, v136, v180\n"
        "v_sub_f32 v137, v137, v180\n"
        "v_sub_f32 v138, v138, v180\n"
        "v_sub_f32 v139, v139, v180\n"
        "v_sub_f32 v140, v140, v180\n"
        "v_sub_f32 v141, v141, v180\n"
        "v_sub_f32 v142, v142, v180\n"
        "v_sub_f32 v143, v143, v180\n"
        "v_exp_f32 v128, v128\n"
        "v_exp_f32 v129, v129\n"
        "v_exp_f32 v130, v130\n"
        "v_exp_f32 v131, v131\n"
        "v_exp_f32 v132, v132\n"
        "v_exp_f32 v133, v133\n"
        "v_exp_f32 v134, v134\n"
        "v_exp_f32 v135, v135\n"
        "v_exp_f32 v136, v136\n"
        "v_exp_f32 v137, v137\n"
        "v_exp_f32 v138, v138\n"
        "v_exp_f32 v139, v139\n"
        "v_exp_f32 v140, v140\n"
        "v_exp_f32 v141, v141\n"
        "v_exp_f32 v142, v142\n"
        "v_exp_f32 v143, v143\n"
        "s_nop 3\n"
        "v_smfmac_f32_32x32x32_bf16 v[160:175], a[192:195], v[128:135], v245\n"
        "v_smfmac_f32_32x32x32_bf16 v[160:175], a[196:199], v[136:143], v245\n"
        "s_nop 15\n"
        "v_mfma_f32_32x32x16_bf16 v[128:143], a[100:103], v[192:195], 0\n"
        "s_nop 15\n"
        "s_nop 3\n"
        "v_sub_f32 v128, v128, v180\n"
        "v_sub_f32 v129, v129, v180\n"
        "v_sub_f32 v130, v130, v180\n"
        "v_sub_f32 v131, v131, v180\n"
        "v_sub_f32 v132, v132, v180\n"
        "v_sub_f32 v133, v133, v180\n"
        "v_sub_f32 v134, v134, v180\n"
        "v_sub_f32 v135, v135, v180\n"
        "v_sub_f32 v136, v136, v180\n"
        "v_sub_f32 v137, v137, v180\n"
        "v_sub_f32 v138, v138, v180\n"
        "v_sub_f32 v139, v139, v180\n"
        "v_sub_f32 v140, v140, v180\n"
        "v_sub_f32 v141, v141, v180\n"
        "v_sub_f32 v142, v142, v180\n"
        "v_sub_f32 v143, v143, v180\n"
        "v_exp_f32 v128, v128\n"
        "v_exp_f32 v129, v129\n"
        "v_exp_f32 v130, v130\n"
        "v_exp_f32 v131, v131\n"
        "v_exp_f32 v132, v132\n"
        "v_exp_f32 v133, v133\n"
        "v_exp_f32 v134, v134\n"
        "v_exp_f32 v135, v135\n"
        "v_exp_f32 v136, v136\n"
        "v_exp_f32 v137, v137\n"
        "v_exp_f32 v138, v138\n"
        "v_exp_f32 v139, v139\n"
        "v_exp_f32 v140, v140\n"
        "v_exp_f32 v141, v141\n"
        "v_exp_f32 v142, v142\n"
        "v_exp_f32 v143, v143\n"
        "s_nop 3\n"
        "v_smfmac_f32_32x32x32_bf16 v[160:175], a[200:203], v[128:135], v245\n"
        "v_smfmac_f32_32x32x32_bf16 v[160:175], a[204:207], v[136:143], v245\n"
        "s_nop 15\n"
        "v_mfma_f32_32x32x16_bf16 v[128:143], a[104:107], v[192:195], 0\n"
        "s_nop 15\n"
        "s_nop 3\n"
        "v_sub_f32 v128, v128, v180\n"
        "v_sub_f32 v129, v129, v180\n"
        "v_sub_f32 v130, v130, v180\n"
        "v_sub_f32 v131, v131, v180\n"
        "v_sub_f32 v132, v132, v180\n"
        "v_sub_f32 v133, v133, v180\n"
        "v_sub_f32 v134, v134, v180\n"
        "v_sub_f32 v135, v135, v180\n"
        "v_sub_f32 v136, v136, v180\n"
        "v_sub_f32 v137, v137, v180\n"
        "v_sub_f32 v138, v138, v180\n"
        "v_sub_f32 v139, v139, v180\n"
        "v_sub_f32 v140, v140, v180\n"
        "v_sub_f32 v141, v141, v180\n"
        "v_sub_f32 v142, v142, v180\n"
        "v_sub_f32 v143, v143, v180\n"
        "v_exp_f32 v128, v128\n"
        "v_exp_f32 v129, v129\n"
        "v_exp_f32 v130, v130\n"
        "v_exp_f32 v131, v131\n"
        "v_exp_f32 v132, v132\n"
        "v_exp_f32 v133, v133\n"
        "v_exp_f32 v134, v134\n"
        "v_exp_f32 v135, v135\n"
        "v_exp_f32 v136, v136\n"
        "v_exp_f32 v137, v137\n"
        "v_exp_f32 v138, v138\n"
        "v_exp_f32 v139, v139\n"
        "v_exp_f32 v140, v140\n"
        "v_exp_f32 v141, v141\n"
        "v_exp_f32 v142, v142\n"
        "v_exp_f32 v143, v143\n"
        "s_nop 3\n"
        "v_smfmac_f32_32x32x32_bf16 v[160:175], a[208:211], v[128:135], v245\n"
        "v_smfmac_f32_32x32x32_bf16 v[160:175], a[212:215], v[136:143], v245\n"
        "s_nop 15\n"
        "v_mfma_f32_32x32x16_bf16 v[128:143], a[108:111], v[192:195], 0\n"
        "s_nop 15\n"
        "s_nop 3\n"
        "v_sub_f32 v128, v128, v180\n"
        "v_sub_f32 v129, v129, v180\n"
        "v_sub_f32 v130, v130, v180\n"
        "v_sub_f32 v131, v131, v180\n"
        "v_sub_f32 v132, v132, v180\n"
        "v_sub_f32 v133, v133, v180\n"
        "v_sub_f32 v134, v134, v180\n"
        "v_sub_f32 v135, v135, v180\n"
        "v_sub_f32 v136, v136, v180\n"
        "v_sub_f32 v137, v137, v180\n"
        "v_sub_f32 v138, v138, v180\n"
        "v_sub_f32 v139, v139, v180\n"
        "v_sub_f32 v140, v140, v180\n"
        "v_sub_f32 v141, v141, v180\n"
        "v_sub_f32 v142, v142, v180\n"
        "v_sub_f32 v143, v143, v180\n"
        "v_exp_f32 v128, v128\n"
        "v_exp_f32 v129, v129\n"
        "v_exp_f32 v130, v130\n"
        "v_exp_f32 v131, v131\n"
        "v_exp_f32 v132, v132\n"
        "v_exp_f32 v133, v133\n"
        "v_exp_f32 v134, v134\n"
        "v_exp_f32 v135, v135\n"
        "v_exp_f32 v136, v136\n"
        "v_exp_f32 v137, v137\n"
        "v_exp_f32 v138, v138\n"
        "v_exp_f32 v139, v139\n"
        "v_exp_f32 v140, v140\n"
        "v_exp_f32 v141, v141\n"
        "v_exp_f32 v142, v142\n"
        "v_exp_f32 v143, v143\n"
        "s_nop 3\n"
        "v_smfmac_f32_32x32x32_bf16 v[160:175], a[216:219], v[128:135], v245\n"
        "v_smfmac_f32_32x32x32_bf16 v[160:175], a[220:223], v[136:143], v245\n"
        "s_nop 15\n"
        "v_mfma_f32_32x32x16_bf16 v[128:143], a[112:115], v[192:195], 0\n"
        "s_nop 15\n"
        "s_nop 3\n"
        "v_sub_f32 v128, v128, v180\n"
        "v_sub_f32 v129, v129, v180\n"
        "v_sub_f32 v130, v130, v180\n"
        "v_sub_f32 v131, v131, v180\n"
        "v_sub_f32 v132, v132, v180\n"
        "v_sub_f32 v133, v133, v180\n"
        "v_sub_f32 v134, v134, v180\n"
        "v_sub_f32 v135, v135, v180\n"
        "v_sub_f32 v136, v136, v180\n"
        "v_sub_f32 v137, v137, v180\n"
        "v_sub_f32 v138, v138, v180\n"
        "v_sub_f32 v139, v139, v180\n"
        "v_sub_f32 v140, v140, v180\n"
        "v_sub_f32 v141, v141, v180\n"
        "v_sub_f32 v142, v142, v180\n"
        "v_sub_f32 v143, v143, v180\n"
        "v_exp_f32 v128, v128\n"
        "v_exp_f32 v129, v129\n"
        "v_exp_f32 v130, v130\n"
        "v_exp_f32 v131, v131\n"
        "v_exp_f32 v132, v132\n"
        "v_exp_f32 v133, v133\n"
        "v_exp_f32 v134, v134\n"
        "v_exp_f32 v135, v135\n"
        "v_exp_f32 v136, v136\n"
        "v_exp_f32 v137, v137\n"
        "v_exp_f32 v138, v138\n"
        "v_exp_f32 v139, v139\n"
        "v_exp_f32 v140, v140\n"
        "v_exp_f32 v141, v141\n"
        "v_exp_f32 v142, v142\n"
        "v_exp_f32 v143, v143\n"
        "s_nop 3\n"
        "v_smfmac_f32_32x32x32_bf16 v[160:175], a[224:227], v[128:135], v245\n"
        "v_smfmac_f32_32x32x32_bf16 v[160:175], a[228:231], v[136:143], v245\n"
        "s_nop 15\n"
        "v_mfma_f32_32x32x16_bf16 v[128:143], a[116:119], v[192:195], 0\n"
        "s_nop 15\n"
        "s_nop 3\n"
        "v_sub_f32 v128, v128, v180\n"
        "v_sub_f32 v129, v129, v180\n"
        "v_sub_f32 v130, v130, v180\n"
        "v_sub_f32 v131, v131, v180\n"
        "v_sub_f32 v132, v132, v180\n"
        "v_sub_f32 v133, v133, v180\n"
        "v_sub_f32 v134, v134, v180\n"
        "v_sub_f32 v135, v135, v180\n"
        "v_sub_f32 v136, v136, v180\n"
        "v_sub_f32 v137, v137, v180\n"
        "v_sub_f32 v138, v138, v180\n"
        "v_sub_f32 v139, v139, v180\n"
        "v_sub_f32 v140, v140, v180\n"
        "v_sub_f32 v141, v141, v180\n"
        "v_sub_f32 v142, v142, v180\n"
        "v_sub_f32 v143, v143, v180\n"
        "v_exp_f32 v128, v128\n"
        "v_exp_f32 v129, v129\n"
        "v_exp_f32 v130, v130\n"
        "v_exp_f32 v131, v131\n"
        "v_exp_f32 v132, v132\n"
        "v_exp_f32 v133, v133\n"
        "v_exp_f32 v134, v134\n"
        "v_exp_f32 v135, v135\n"
        "v_exp_f32 v136, v136\n"
        "v_exp_f32 v137, v137\n"
        "v_exp_f32 v138, v138\n"
        "v_exp_f32 v139, v139\n"
        "v_exp_f32 v140, v140\n"
        "v_exp_f32 v141, v141\n"
        "v_exp_f32 v142, v142\n"
        "v_exp_f32 v143, v143\n"
        "s_nop 3\n"
        "v_smfmac_f32_32x32x32_bf16 v[160:175], a[232:235], v[128:135], v245\n"
        "v_smfmac_f32_32x32x32_bf16 v[160:175], a[236:239], v[136:143], v245\n"
        "s_nop 15\n"
        "v_mfma_f32_32x32x16_bf16 v[128:143], a[120:123], v[192:195], 0\n"
        "s_nop 15\n"
        "s_nop 3\n"
        "v_sub_f32 v128, v128, v180\n"
        "v_sub_f32 v129, v129, v180\n"
        "v_sub_f32 v130, v130, v180\n"
        "v_sub_f32 v131, v131, v180\n"
        "v_sub_f32 v132, v132, v180\n"
        "v_sub_f32 v133, v133, v180\n"
        "v_sub_f32 v134, v134, v180\n"
        "v_sub_f32 v135, v135, v180\n"
        "v_sub_f32 v136, v136, v180\n"
        "v_sub_f32 v137, v137, v180\n"
        "v_sub_f32 v138, v138, v180\n"
        "v_sub_f32 v139, v139, v180\n"
        "v_sub_f32 v140, v140, v180\n"
        "v_sub_f32 v141, v141, v180\n"
        "v_sub_f32 v142, v142, v180\n"
        "v_sub_f32 v143, v143, v180\n"
        "v_exp_f32 v128, v128\n"
        "v_exp_f32 v129, v129\n"
        "v_exp_f32 v130, v130\n"
        "v_exp_f32 v131, v131\n"
        "v_exp_f32 v132, v132\n"
        "v_exp_f32 v133, v133\n"
        "v_exp_f32 v134, v134\n"
        "v_exp_f32 v135, v135\n"
        "v_exp_f32 v136, v136\n"
        "v_exp_f32 v137, v137\n"
        "v_exp_f32 v138, v138\n"
        "v_exp_f32 v139, v139\n"
        "v_exp_f32 v140, v140\n"
        "v_exp_f32 v141, v141\n"
        "v_exp_f32 v142, v142\n"
        "v_exp_f32 v143, v143\n"
        "s_nop 3\n"
        "v_smfmac_f32_32x32x32_bf16 v[160:175], a[240:243], v[128:135], v245\n"
        "v_smfmac_f32_32x32x32_bf16 v[160:175], a[244:247], v[136:143], v245\n"
        "s_nop 15\n"
        "v_mfma_f32_32x32x16_bf16 v[128:143], a[124:127], v[192:195], 0\n"
        "s_nop 15\n"
        "s_nop 3\n"
        "v_sub_f32 v128, v128, v180\n"
        "v_sub_f32 v129, v129, v180\n"
        "v_sub_f32 v130, v130, v180\n"
        "v_sub_f32 v131, v131, v180\n"
        "v_sub_f32 v132, v132, v180\n"
        "v_sub_f32 v133, v133, v180\n"
        "v_sub_f32 v134, v134, v180\n"
        "v_sub_f32 v135, v135, v180\n"
        "v_sub_f32 v136, v136, v180\n"
        "v_sub_f32 v137, v137, v180\n"
        "v_sub_f32 v138, v138, v180\n"
        "v_sub_f32 v139, v139, v180\n"
        "v_sub_f32 v140, v140, v180\n"
        "v_sub_f32 v141, v141, v180\n"
        "v_sub_f32 v142, v142, v180\n"
        "v_sub_f32 v143, v143, v180\n"
        "v_exp_f32 v128, v128\n"
        "v_exp_f32 v129, v129\n"
        "v_exp_f32 v130, v130\n"
        "v_exp_f32 v131, v131\n"
        "v_exp_f32 v132, v132\n"
        "v_exp_f32 v133, v133\n"
        "v_exp_f32 v134, v134\n"
        "v_exp_f32 v135, v135\n"
        "v_exp_f32 v136, v136\n"
        "v_exp_f32 v137, v137\n"
        "v_exp_f32 v138, v138\n"
        "v_exp_f32 v139, v139\n"
        "v_exp_f32 v140, v140\n"
        "v_exp_f32 v141, v141\n"
        "v_exp_f32 v142, v142\n"
        "v_exp_f32 v143, v143\n"
        "s_nop 3\n"
        "v_smfmac_f32_32x32x32_bf16 v[160:175], a[248:251], v[128:135], v245\n"
        "v_smfmac_f32_32x32x32_bf16 v[160:175], a[252:255], v[136:143], v245\n"
        "s_nop 15\n"
        "s_nop 15\n"
        "s_nop 3\n"
        "ds_write_b128 v177, v[160:163]\n"
        "ds_write_b128 v177, v[164:167] offset:1024\n"
        "ds_write_b32 v178, v168\n"
        "ds_write_b32 v179, v180\n"
        "v_add_u32 v176, 1024, v176\n"
        "v_add_u32 v177, 2304, v177\n"
        "v_add_u32 v178, 2304, v178\n"
        "v_add_u32 v179, 128, v179\n"
        "s_add_u32 s57, s57, 1\n"
        "s_cmp_lt_u32 s57, 8\n"
        "s_cbranch_scc1 Lsa2_%=\n"
        "s_add_u32 s54, s76, s75\n"
        "s_waitcnt vmcnt(0)\n"
        "s_mov_b64 exec, 1\n"
        "buffer_store_dword v20, off, %[srd], s54 sc1\n"
        "s_mov_b64 exec, -1\n"
        "s_mov_b32 s56, 0\n"
        "Lsp5_%=:\n"
        "s_add_u32 s56, s56, 1\n"
        "s_cmp_gt_u32 s56, 16384\n"
        "s_cbranch_scc1 Lsq6_%=\n"
        "s_mov_b32 s54, 0x8000\n"
        "buffer_load_dwordx4 v[28:31], v18, %[srd], s54 offen sc1\n"
        "s_waitcnt vmcnt(0)\n"
        "v_xor_b32 v28, v28, v19\n"
        "v_xor_b32 v29, v29, v19\n"
        "v_xor_b32 v30, v30, v19\n"
        "v_xor_b32 v31, v31, v19\n"
        "v_or3_b32 v28, v28, v29, v30\n"
        "v_or_b32 v28, v28, v31\n"
        "v_cmp_ne_u32 vcc, 0, v28\n"
        "s_cbranch_vccnz Lsp5_%=\n"
        "Lsq6_%=:\n"
        "s_add_u32 s54, s73, 8192\n"
        "buffer_load_dwordx4 v[200:203], v242, %[srd], s54 offen sc1\n"
        "s_add_u32 s54, s73, 16384\n"
        "buffer_load_dwordx4 v[204:207], v242, %[srd], s54 offen sc1\n"
        "s_add_u32 s54, s73, 40960\n"
        "buffer_load_dwordx4 v[208:211], v242, %[srd], s54 offen sc1\n"
        "buffer_load_dwordx4 v[212:215], v244, %[srd], s85 offen sc1\n"
        "s_add_u32 s54, s73, 24576\n"
        "buffer_load_dword v216, v243, %[srd], s54 offen sc1\n"
        "s_add_u32 s54, s72, 0\n"
        "buffer_load_dwordx4 a[0:3], v0, %[srd], s54 offen offset:0 sc1\n"
        "buffer_load_dwordx4 a[4:7], v0, %[srd], s54 offen offset:1024 sc1\n"
        "buffer_load_dwordx4 a[8:11], v0, %[srd], s54 offen offset:2048 sc1\n"
        "buffer_load_dwordx4 a[12:15], v0, %[srd], s54 offen offset:3072 sc1\n"
        "s_add_u32 s54, s72, 4096\n"
        "buffer_load_dwordx4 a[16:19], v0, %[srd], s54 offen offset:0 sc1\n"
        "buffer_load_dwordx4 a[20:23], v0, %[srd], s54 offen offset:1024 sc1\n"
        "buffer_load_dwordx4 a[24:27], v0, %[srd], s54 offen offset:2048 sc1\n"
        "buffer_load_dwordx4 a[28:31], v0, %[srd], s54 offen offset:3072 sc1\n"
        "s_add_u32 s54, s72, 8192\n"
        "buffer_load_dwordx4 a[32:35], v0, %[srd], s54 offen offset:0 sc1\n"
        "buffer_load_dwordx4 a[36:39], v0, %[srd], s54 offen offset:1024 sc1\n"
        "buffer_load_dwordx4 a[40:43], v0, %[srd], s54 offen offset:2048 sc1\n"
        "buffer_load_dwordx4 a[44:47], v0, %[srd], s54 offen offset:3072 sc1\n"
        "s_add_u32 s54, s72, 12288\n"
        "buffer_load_dwordx4 a[48:51], v0, %[srd], s54 offen offset:0 sc1\n"
        "buffer_load_dwordx4 a[52:55], v0, %[srd], s54 offen offset:1024 sc1\n"
        "buffer_load_dwordx4 a[56:59], v0, %[srd], s54 offen offset:2048 sc1\n"
        "buffer_load_dwordx4 a[60:63], v0, %[srd], s54 offen offset:3072 sc1\n"
        "s_add_u32 s54, s72, 1048576\n"
        "buffer_load_dwordx4 v[96:99], v0, %[srd], s54 offen offset:0 sc1\n"
        "buffer_load_dwordx4 v[100:103], v0, %[srd], s54 offen offset:1024 sc1\n"
        "buffer_load_dwordx4 v[104:107], v0, %[srd], s54 offen offset:2048 sc1\n"
        "buffer_load_dwordx4 v[108:111], v0, %[srd], s54 offen offset:3072 sc1\n"
        "s_add_u32 s54, s72, 1052672\n"
        "buffer_load_dwordx4 v[112:115], v0, %[srd], s54 offen offset:0 sc1\n"
        "buffer_load_dwordx4 v[116:119], v0, %[srd], s54 offen offset:1024 sc1\n"
        "buffer_load_dwordx4 v[120:123], v0, %[srd], s54 offen offset:2048 sc1\n"
        "buffer_load_dwordx4 v[124:127], v0, %[srd], s54 offen offset:3072 sc1\n"
        "s_add_u32 s54, s72, 524288\n"
        "buffer_load_dwordx4 v[16:19], v0, %[srd], s54 offen offset:0 sc1\n"
        "buffer_load_dwordx4 v[20:23], v0, %[srd], s54 offen offset:1024 sc1\n"
        "buffer_load_dwordx4 v[24:27], v0, %[srd], s54 offen offset:2048 sc1\n"
        "buffer_load_dwordx4 v[28:31], v0, %[srd], s54 offen offset:3072 sc1\n"
        "s_add_u32 s54, s72, 528384\n"
        "buffer_load_dwordx4 v[32:35], v0, %[srd], s54 offen offset:0 sc1\n"
        "buffer_load_dwordx4 v[36:39], v0, %[srd], s54 offen offset:1024 sc1\n"
        "buffer_load_dwordx4 v[40:43], v0, %[srd], s54 offen offset:2048 sc1\n"
        "buffer_load_dwordx4 v[44:47], v0, %[srd], s54 offen offset:3072 sc1\n"
        "s_add_u32 s54, s72, 532480\n"
        "buffer_load_dwordx4 v[48:51], v0, %[srd], s54 offen offset:0 sc1\n"
        "buffer_load_dwordx4 v[52:55], v0, %[srd], s54 offen offset:1024 sc1\n"
        "buffer_load_dwordx4 v[56:59], v0, %[srd], s54 offen offset:2048 sc1\n"
        "buffer_load_dwordx4 v[60:63], v0, %[srd], s54 offen offset:3072 sc1\n"
        "s_add_u32 s54, s72, 536576\n"
        "buffer_load_dwordx4 v[64:67], v0, %[srd], s54 offen offset:0 sc1\n"
        "buffer_load_dwordx4 v[68:71], v0, %[srd], s54 offen offset:1024 sc1\n"
        "buffer_load_dwordx4 v[72:75], v0, %[srd], s54 offen offset:2048 sc1\n"
        "buffer_load_dwordx4 v[76:79], v0, %[srd], s54 offen offset:3072 sc1\n"
        "s_waitcnt vmcnt(0)\n"
        "buffer_load_dwordx4 a[64:67], v241, %[srd], s75 offen sc1\n"
        "s_add_u32 s54, s72, 1056768\n"
        "buffer_load_dwordx4 a[128:131], v0, %[srd], s54 offen offset:0 sc1\n"
        "buffer_load_dwordx4 a[132:135], v0, %[srd], s54 offen offset:1024 sc1\n"
        "buffer_load_dwordx4 a[136:139], v0, %[srd], s54 offen offset:2048 sc1\n"
        "buffer_load_dwordx4 a[140:143], v0, %[srd], s54 offen offset:3072 sc1\n"
        "s_add_u32 s54, s72, 1060864\n"
        "buffer_load_dwordx4 a[144:147], v0, %[srd], s54 offen offset:0 sc1\n"
        "buffer_load_dwordx4 a[148:151], v0, %[srd], s54 offen offset:1024 sc1\n"
        "buffer_load_dwordx4 a[152:155], v0, %[srd], s54 offen offset:2048 sc1\n"
        "buffer_load_dwordx4 a[156:159], v0, %[srd], s54 offen offset:3072 sc1\n"
        "s_add_u32 s54, s72, 1572864\n"
        "buffer_load_dwordx4 a[160:163], v240, %[srd], s54 offen offset:0 sc1\n"
        "buffer_load_dwordx4 a[164:167], v240, %[srd], s54 offen offset:1024 sc1\n"
        "buffer_load_dwordx4 a[168:171], v240, %[srd], s54 offen offset:2048 sc1\n"
        "buffer_load_dwordx4 a[172:175], v240, %[srd], s54 offen offset:3072 sc1\n"
        "s_add_u32 s54, s72, 1576960\n"
        "buffer_load_dwordx4 a[176:179], v240, %[srd], s54 offen offset:0 sc1\n"
        "buffer_load_dwordx4 a[180:183], v240, %[srd], s54 offen offset:1024 sc1\n"
        "buffer_load_dwordx4 a[184:187], v240, %[srd], s54 offen offset:2048 sc1\n"
        "buffer_load_dwordx4 a[188:191], v240, %[srd], s54 offen offset:3072 sc1\n"
        "s_add_u32 s54, s72, 1581056\n"
        "buffer_load_dwordx4 a[192:195], v240, %[srd], s54 offen offset:0 sc1\n"
        "buffer_load_dwordx4 a[196:199], v240, %[srd], s54 offen offset:1024 sc1\n"
        "buffer_load_dwordx4 a[200:203], v240, %[srd], s54 offen offset:2048 sc1\n"
        "buffer_load_dwordx4 a[204:207], v240, %[srd], s54 offen offset:3072 sc1\n"
        "s_add_u32 s54, s72, 1585152\n"
        "buffer_load_dwordx4 a[208:211], v240, %[srd], s54 offen offset:0 sc1\n"
        "buffer_load_dwordx4 a[212:215], v240, %[srd], s54 offen offset:1024 sc1\n"
        "buffer_load_dwordx4 a[216:219], v240, %[srd], s54 offen offset:2048 sc1\n"
        "buffer_load_dwordx4 a[220:223], v240, %[srd], s54 offen offset:3072 sc1\n"
        "s_waitcnt lgkmcnt(0)\n"
        "s_barrier\n"
        "s_lshr_b32 s54, s81, 14\n"
        "s_mul_i32 s55, s54, 4608\n"
        "s_add_u32 s55, s55, s49\n"
        "s_add_u32 s55, s55, 65536\n"
        "v_add_u32 v248, s55, v0\n"
        "v_lshrrev_b32 v249, 2, v0\n"
        "s_add_u32 s55, s55, 2048\n"
        "v_add_u32 v249, s55, v249\n"
        "s_lshr_b32 s54, s81, 6\n"
        "s_add_u32 s54, s54, s49\n"
        "s_add_u32 s54, s54, 16384\n"
        "v_bfe_u32 v199, v0, 4, 5\n"
        "v_lshlrev_b32 v199, 2, v199\n"
        "v_add_u32 v199, s54, v199\n"
        "ds_read_b32 v164, v199 offset:0\n"
        "ds_read_b32 v165, v199 offset:1024\n"
        "ds_read_b32 v166, v199 offset:2048\n"
        "ds_read_b32 v167, v199 offset:3072\n"
        "ds_read_b128 v[128:131], v248 offset:0\n"
        "ds_read_b128 v[132:135], v248 offset:1024\n"
        "ds_read_b128 v[136:139], v248 offset:18432\n"
        "ds_read_b128 v[140:143], v248 offset:19456\n"
        "ds_read_b128 v[144:147], v248 offset:36864\n"
        "ds_read_b128 v[148:151], v248 offset:37888\n"
        "ds_read_b128 v[152:155], v248 offset:55296\n"
        "ds_read_b128 v[156:159], v248 offset:56320\n"
        "s_waitcnt lgkmcnt(0)\n"
        "ds_read_b32 v160, v249 offset:0\n"
        "ds_read_b32 v161, v249 offset:18432\n"
        "ds_read_b32 v162, v249 offset:36864\n"
        "ds_read_b32 v163, v249 offset:55296\n"
        "v_max3_f32 v168, v164, v165, v166\n"
        "v_max_f32 v168, v168, v167\n"
        "v_sub_f32 v164, v164, v168\n"
        "v_sub_f32 v165, v165, v168\n"
        "v_sub_f32 v166, v166, v168\n"
        "v_sub_f32 v167, v167, v168\n"
        "v_exp_f32 v164, v164\n"
        "v_exp_f32 v165, v165\n"
        "v_exp_f32 v166, v166\n"
        "v_exp_f32 v167, v167\n"
        "s_waitcnt lgkmcnt(0)\n"
        "v_mul_f32 v128, v128, v164\n"
        "v_mul_f32 v129, v129, v164\n"
        "v_mul_f32 v130, v130, v164\n"
        "v_mul_f32 v131, v131, v164\n"
        "v_mul_f32 v132, v132, v164\n"
        "v_mul_f32 v133, v133, v164\n"
        "v_mul_f32 v134, v134, v164\n"
        "v_mul_f32 v135, v135, v164\n"
        "v_mul_f32 v160, v160, v164\n"
        "v_fmac_f32 v128, v136, v165\n"
        "v_fmac_f32 v129, v137, v165\n"
        "v_fmac_f32 v130, v138, v165\n"
        "v_fmac_f32 v131, v139, v165\n"
        "v_fmac_f32 v132, v140, v165\n"
        "v_fmac_f32 v133, v141, v165\n"
        "v_fmac_f32 v134, v142, v165\n"
        "v_fmac_f32 v135, v143, v165\n"
        "v_fmac_f32 v160, v161, v165\n"
        "v_fmac_f32 v128, v144, v166\n"
        "v_fmac_f32 v129, v145, v166\n"
        "v_fmac_f32 v130, v146, v166\n"
        "v_fmac_f32 v131, v147, v166\n"
        "v_fmac_f32 v132, v148, v166\n"
        "v_fmac_f32 v133, v149, v166\n"
        "v_fmac_f32 v134, v150, v166\n"
        "v_fmac_f32 v135, v151, v166\n"
        "v_fmac_f32 v160, v162, v166\n"
        "v_fmac_f32 v128, v152, v167\n"
        "v_fmac_f32 v129, v153, v167\n"
        "v_fmac_f32 v130, v154, v167\n"
        "v_fmac_f32 v131, v155, v167\n"
        "v_fmac_f32 v132, v156, v167\n"
        "v_fmac_f32 v133, v157, v167\n"
        "v_fmac_f32 v134, v158, v167\n"
        "v_fmac_f32 v135, v159, v167\n"
        "v_fmac_f32 v160, v163, v167\n"
        "v_rcp_f32 v160, v160\n"
        "s_nop 0\n"
        "v_mul_f32 v128, v128, v160\n"
        "v_mul_f32 v129, v129, v160\n"
        "v_mul_f32 v130, v130, v160\n"
        "v_mul_f32 v131, v131, v160\n"
        "v_mul_f32 v132, v132, v160\n"
        "v_mul_f32 v133, v133, v160\n"
        "v_mul_f32 v134, v134, v160\n"
        "v_mul_f32 v135, v135, v160\n"
        "v_cvt_pk_bf16_f32 v176, v128, v129\n"
        "v_cvt_pk_bf16_f32 v177, v130, v131\n"
        "v_cvt_pk_bf16_f32 v178, v132, v133\n"
        "v_cvt_pk_bf16_f32 v179, v134, v135\n"
        "ds_read_b32 v164, v199 offset:128\n"
        "ds_read_b32 v165, v199 offset:1152\n"
        "ds_read_b32 v166, v199 offset:2176\n"
        "ds_read_b32 v167, v199 offset:3200\n"
        "ds_read_b128 v[128:131], v248 offset:2304\n"
        "ds_read_b128 v[132:135], v248 offset:3328\n"
        "ds_read_b128 v[136:139], v248 offset:20736\n"
        "ds_read_b128 v[140:143], v248 offset:21760\n"
        "ds_read_b128 v[144:147], v248 offset:39168\n"
        "ds_read_b128 v[148:151], v248 offset:40192\n"
        "ds_read_b128 v[152:155], v248 offset:57600\n"
        "ds_read_b128 v[156:159], v248 offset:58624\n"
        "s_waitcnt lgkmcnt(0)\n"
        "ds_read_b32 v160, v249 offset:2304\n"
        "ds_read_b32 v161, v249 offset:20736\n"
        "ds_read_b32 v162, v249 offset:39168\n"
        "ds_read_b32 v163, v249 offset:57600\n"
        "v_max3_f32 v168, v164, v165, v166\n"
        "v_max_f32 v168, v168, v167\n"
        "v_sub_f32 v164, v164, v168\n"
        "v_sub_f32 v165, v165, v168\n"
        "v_sub_f32 v166, v166, v168\n"
        "v_sub_f32 v167, v167, v168\n"
        "v_exp_f32 v164, v164\n"
        "v_exp_f32 v165, v165\n"
        "v_exp_f32 v166, v166\n"
        "v_exp_f32 v167, v167\n"
        "s_waitcnt lgkmcnt(0)\n"
        "v_mul_f32 v128, v128, v164\n"
        "v_mul_f32 v129, v129, v164\n"
        "v_mul_f32 v130, v130, v164\n"
        "v_mul_f32 v131, v131, v164\n"
        "v_mul_f32 v132, v132, v164\n"
        "v_mul_f32 v133, v133, v164\n"
        "v_mul_f32 v134, v134, v164\n"
        "v_mul_f32 v135, v135, v164\n"
        "v_mul_f32 v160, v160, v164\n"
        "v_fmac_f32 v128, v136, v165\n"
        "v_fmac_f32 v129, v137, v165\n"
        "v_fmac_f32 v130, v138, v165\n"
        "v_fmac_f32 v131, v139, v165\n"
        "v_fmac_f32 v132, v140, v165\n"
        "v_fmac_f32 v133, v141, v165\n"
        "v_fmac_f32 v134, v142, v165\n"
        "v_fmac_f32 v135, v143, v165\n"
        "v_fmac_f32 v160, v161, v165\n"
        "v_fmac_f32 v128, v144, v166\n"
        "v_fmac_f32 v129, v145, v166\n"
        "v_fmac_f32 v130, v146, v166\n"
        "v_fmac_f32 v131, v147, v166\n"
        "v_fmac_f32 v132, v148, v166\n"
        "v_fmac_f32 v133, v149, v166\n"
        "v_fmac_f32 v134, v150, v166\n"
        "v_fmac_f32 v135, v151, v166\n"
        "v_fmac_f32 v160, v162, v166\n"
        "v_fmac_f32 v128, v152, v167\n"
        "v_fmac_f32 v129, v153, v167\n"
        "v_fmac_f32 v130, v154, v167\n"
        "v_fmac_f32 v131, v155, v167\n"
        "v_fmac_f32 v132, v156, v167\n"
        "v_fmac_f32 v133, v157, v167\n"
        "v_fmac_f32 v134, v158, v167\n"
        "v_fmac_f32 v135, v159, v167\n"
        "v_fmac_f32 v160, v163, v167\n"
        "v_rcp_f32 v160, v160\n"
        "s_nop 0\n"
        "v_mul_f32 v128, v128, v160\n"
        "v_mul_f32 v129, v129, v160\n"
        "v_mul_f32 v130, v130, v160\n"
        "v_mul_f32 v131, v131, v160\n"
        "v_mul_f32 v132, v132, v160\n"
        "v_mul_f32 v133, v133, v160\n"
        "v_mul_f32 v134, v134, v160\n"
        "v_mul_f32 v135, v135, v160\n"
        "v_cvt_pk_bf16_f32 v180, v128, v129\n"
        "v_cvt_pk_bf16_f32 v181, v130, v131\n"
        "v_cvt_pk_bf16_f32 v182, v132, v133\n"
        "v_cvt_pk_bf16_f32 v183, v134, v135\n"
        "v_min_u32 v176, -2, v176\n"
        "v_min_u32 v177, -2, v177\n"
        "v_min_u32 v178, -2, v178\n"
        "v_min_u32 v179, -2, v179\n"
        "v_min_u32 v180, -2, v180\n"
        "v_min_u32 v181, -2, v181\n"
        "v_min_u32 v182, -2, v182\n"
        "v_min_u32 v183, -2, v183\n"
        "s_mov_b32 s54, s64\n"
        "buffer_store_dwordx4 v[176:179], v1, %[srd], s54 offen sc1\n"
        "buffer_store_dwordx4 v[180:183], v1, %[srd], s54 offen offset:512 sc1\n"
        "s_lshr_b32 s54, s64, 4\n"
        "s_and_b32 s54, s54, 0xf80\n"
        "s_add_u32 s54, s54, s34\n"
        "v_mov_b32 v254, s50\n"
        "s_add_u32 s54, s54, 0\n"
        "v_lshrrev_b32 v252, 2, v0\n"
        "s_mov_b32 exec_hi, 0\n"
        "buffer_store_dword v254, v252, %[srd], s54 offen sc1\n"
        "s_mov_b64 exec, -1\n"
        "ds_write_b128 v2, v[176:179]\n"
        "ds_write_b128 v2, v[180:183] offset:512\n"
        "s_waitcnt vmcnt(0) lgkmcnt(0)\n"
        "s_barrier\n"
        "s_branch Lredone_%=\n"
        "; ---- NaN in a GEMM: marker left in the tile (fetch again) or genuine\n"
        "Lsbbad_%=:\n"
        "s_nop 15\n"
        "s_mov_b32 s57, 0\n"
        "Lr11_%=:\n"
        "s_waitcnt lgkmcnt(0)\n"
        "s_add_u32 s54, s49, 8192\n"
        "v_add_u32 v128, s54, v0\n"
        "v_mov_b32 v129, 0\n"
        "s_mov_b32 s56, 0\n"
        "Lr12_%=:\n"
        "ds_read_b128 v[132:135], v128 offset:0\n"
        "ds_read_b128 v[136:139], v128 offset:1024\n"
        "ds_read_b128 v[140:143], v128 offset:2048\n"
        "ds_read_b128 v[144:147], v128 offset:3072\n"
        "ds_read_b128 v[148:151], v128 offset:4096\n"
        "ds_read_b128 v[152:155], v128 offset:5120\n"
        "ds_read_b128 v[156:159], v128 offset:6144\n"
        "s_waitcnt lgkmcnt(0)\n"
        "v_max3_u32 v129, v129, v132, v133\n"
        "v_max3_u32 v129, v129, v134, v135\n"
        "v_max3_u32 v129, v129, v136, v137\n"
        "v_max3_u32 v129, v129, v138, v139\n"
        "v_max3_u32 v129, v129, v140, v141\n"
        "v_max3_u32 v129, v129, v142, v143\n"
        "v_max3_u32 v129, v129, v144, v145\n"
        "v_max3_u32 v129, v129, v146, v147\n"
        "v_max3_u32 v129, v129, v148, v149\n"
        "v_max3_u32 v129, v129, v150, v151\n"
        "v_max3_u32 v129, v129, v152, v153\n"
        "v_max3_u32 v129, v129, v154, v155\n"
        "v_max3_u32 v129, v129, v156, v157\n"
        "v_max3_u32 v129, v129, v158, v159\n"
        "v_add_u32 v128, 7168, v128\n"
        "s_add_u32 s56, s56, 1\n"
        "s_cmp_lt_u32 s56, 8\n"
        "s_cbranch_scc1 Lr12_%=\n"
        "v_cmp_eq_u32 vcc, -1, v129\n"
        "s_cbranch_vccz Lr13_%=\n"
        "s_add_u32 s57, s57, 1\n"
        "s_cmp_gt_u32 s57, 64\n"
        "s_cbranch_scc1 Lr13_%=\n"
        "s_barrier\n"
        "s_add_u32 s60, s65, 0\n"
        "s_add_u32 m0, s78, 0\n"
        "s_add_u32 s54, s77, 0\n"
        "s_and_b32 s54, s54, 0xffff\n"
        "s_add_u32 s54, s54, s60\n"
        "buffer_load_dwordx4 v0, %[srd], s54 offen sc1 lds\n"
        "s_add_u32 m0, m0, 4096\n"
        "s_add_u32 s54, s77, 4096\n"
        "s_and_b32 s54, s54, 0xffff\n"
        "s_add_u32 s54, s54, s60\n"
        "buffer_load_dwordx4 v0, %[srd], s54 offen sc1 lds\n"
        "s_add_u32 m0, m0, 4096\n"
        "s_add_u32 s54, s77, 8192\n"
        "s_and_b32 s54, s54, 0xffff\n"
        "s_add_u32 s54, s54, s60\n"
        "buffer_load_dwordx4 v0, %[srd], s54 offen sc1 lds\n"
        "s_add_u32 m0, m0, 4096\n"
        "s_add_u32 s54, s77, 12288\n"
        "s_and_b32 s54, s54, 0xffff\n"
        "s_add_u32 s54, s54, s60\n"
        "buffer_load_dwordx4 v0, %[srd], s54 offen sc1 lds\n"
        "s_add_u32 m0, m0, 4096\n"
        "s_add_u32 s54, s77, 16384\n"
        "s_and_b32 s54, s54, 0xffff\n"
        "s_add_u32 s54, s54, s60\n"
        "buffer_load_dwordx4 v0, %[srd], s54 offen sc1 lds\n"
        "s_add_u32 m0, m0, 4096\n"
        "s_add_u32 s54, s77, 20480\n"
        "s_and_b32 s54, s54, 0xffff\n"
        "s_add_u32 s54, s54, s60\n"
        "buffer_load_dwordx4 v0, %[srd], s54 offen sc1 lds\n"
        "s_add_u32 m0, m0, 4096\n"
        "s_add_u32 s54, s77, 24576\n"
        "s_and_b32 s54, s54, 0xffff\n"
        "s_add_u32 s54, s54, s60\n"
        "buffer_load_dwordx4 v0, %[srd], s54 offen sc1 lds\n"
        "s_add_u32 m0, m0, 4096\n"
        "s_add_u32 s54, s77, 28672\n"
        "s_and_b32 s54, s54, 0xffff\n"
        "s_add_u32 s54, s54, s60\n"
        "buffer_load_dwordx4 v0, %[srd], s54 offen sc1 lds\n"
        "s_add_u32 m0, m0, 4096\n"
        "s_add_u32 s54, s77, 32768\n"
        "s_and_b32 s54, s54, 0xffff\n"
        "s_add_u32 s54, s54, s60\n"
        "buffer_load_dwordx4 v0, %[srd], s54 offen sc1 lds\n"
        "s_add_u32 m0, m0, 4096\n"
        "s_add_u32 s54, s77, 36864\n"
        "s_and_b32 s54, s54, 0xffff\n"
        "s_add_u32 s54, s54, s60\n"
        "buffer_load_dwordx4 v0, %[srd], s54 offen sc1 lds\n"
        "s_add_u32 m0, m0, 4096\n"
        "s_add_u32 s54, s77, 40960\n"
        "s_and_b32 s54, s54, 0xffff\n"
        "s_add_u32 s54, s54, s60\n"
        "buffer_load_dwordx4 v0, %[srd], s54 offen sc1 lds\n"
        "s_add_u32 m0, m0, 4096\n"
        "s_add_u32 s54, s77, 45056\n"
        "s_and_b32 s54, s54, 0xffff\n"
        "s_add_u32 s54, s54, s60\n"
        "buffer_load_dwordx4 v0, %[srd], s54 offen sc1 lds\n"
        "s_add_u32 m0, m0, 4096\n"
        "s_add_u32 s54, s77, 49152\n"
        "s_and_b32 s54, s54, 0xffff\n"
        "s_add_u32 s54, s54, s60\n"
        "buffer_load_dwordx4 v0, %[srd], s54 offen sc1 lds\n"
        "s_add_u32 m0, m0, 4096\n"
        "s_add_u32 s54, s77, 53248\n"
        "s_and_b32 s54, s54, 0xffff\n"
        "s_add_u32 s54, s54, s60\n"
        "buffer_load_dwordx4 v0, %[srd], s54 offen sc1 lds\n"
        "s_add_u32 m0, m0, 4096\n"
        "s_waitcnt vmcnt(0)\n"
        "s_barrier\n"
        "s_branch Lr11_%=\n"
        "Lr13_%=:\n"
        "ds_read_b128 v[128:131], v3 offset:0\n"
        "ds_read_b128 v[132:135], v3 offset:256\n"
        "ds_read_b128 v[136:139], v3 offset:512\n"
        "ds_read_b128 v[140:143], v3 offset:768\n"
        "ds_read_b128 v[144:147], v3 offset:4096\n"
        "ds_read_b128 v[148:151], v3 offset:4352\n"
        "ds_read_b128 v[152:155], v3 offset:4608\n"
        "ds_read_b128 v[156:159], v3 offset:4864\n"
        "s_waitcnt lgkmcnt(6)\n"
        "v_mfma_f32_16x16x32_bf16 v[160:163], a[0:3], v[128:131], v[80:83]\n"
        "ds_read_b128 v[128:131], v3 offset:8192\n"
        "v_mfma_f32_16x16x32_bf16 v[164:167], a[0:3], v[132:135], v[84:87]\n"
        "ds_read_b128 v[132:135], v3 offset:8448\n"
        "s_waitcnt lgkmcnt(6)\n"
        "v_mfma_f32_16x16x32_bf16 v[168:171], a[0:3], v[136:139], v[88:91]\n"
        "ds_read_b128 v[136:139], v3 offset:8704\n"
        "v_mfma_f32_16x16x32_bf16 v[172:175], a[0:3], v[140:143], v[92:95]\n"
        "ds_read_b128 v[140:143], v3 offset:8960\n"
        "s_waitcnt lgkmcnt(6)\n"
        "v_mfma_f32_16x16x32_bf16 v[160:163], a[4:7], v[144:147], v[160:163]\n"
        "ds_read_b128 v[144:147], v3 offset:12288\n"
        "v_mfma_f32_16x16x32_bf16 v[164:167], a[4:7], v[148:151], v[164:167]\n"
        "ds_read_b128 v[148:151], v3 offset:12544\n"
        "s_waitcnt lgkmcnt(6)\n"
        "v_mfma_f32_16x16x32_bf16 v[168:171], a[4:7], v[152:155], v[168:171]\n"
        "ds_read_b128 v[152:155], v3 offset:12800\n"
        "v_mfma_f32_16x16x32_bf16 v[172:175], a[4:7], v[156:159], v[172:175]\n"
        "ds_read_b128 v[156:159], v3 offset:13056\n"
        "s_waitcnt lgkmcnt(6)\n"
        "v_mfma_f32_16x16x32_bf16 v[160:163], a[8:11], v[128:131], v[160:163]\n"
        "ds_read_b128 v[128:131], v3 offset:16384\n"
        "v_mfma_f32_16x16x32_bf16 v[164:167], a[8:11], v[132:135], v[164:167]\n"
        "ds_read_b128 v[132:135], v3 offset:16640\n"
        "s_waitcnt lgkmcnt(6)\n"
        "v_mfma_f32_16x16x32_bf16 v[168:171], a[8:11], v[136:139], v[168:171]\n"
        "ds_read_b128 v[136:139], v3 offset:16896\n"
        "v_mfma_f32_16x16x32_bf16 v[172:175], a[8:11], v[140:143], v[172:175]\n"
        "ds_read_b128 v[140:143], v3 offset:17152\n"
        "s_waitcnt lgkmcnt(6)\n"
        "v_mfma_f32_16x16x32_bf16 v[160:163], a[12:15], v[144:147], v[160:163]\n"
        "ds_read_b128 v[144:147], v3 offset:20480\n"
        "v_mfma_f32_16x16x32_bf16 v[164:167], a[12:15], v[148:151], v[164:167]\n"
        "ds_read_b128 v[148:151], v3 offset:20736\n"
        "s_waitcnt lgkmcnt(6)\n"
        "v_mfma_f32_16x16x32_bf16 v[168:171], a[12:15], v[152:155], v[168:171]\n"
        "ds_read_b128 v[152:155], v3 offset:20992\n"
        "v_mfma_f32_16x16x32_bf16 v[172:175], a[12:15], v[156:159], v[172:175]\n"
        "ds_read_b128 v[156:159], v3 offset:21248\n"
        "s_waitcnt lgkmcnt(6)\n"
        "v_mfma_f32_16x16x32_bf16 v[160:163], a[16:19], v[128:131], v[160:163]\n"
        "ds_read_b128 v[128:131], v3 offset:24576\n"
        "v_mfma_f32_16x16x32_bf16 v[164:167], a[16:19], v[132:135], v[164:167]\n"
        "ds_read_b128 v[132:135], v3 offset:24832\n"
        "s_waitcnt lgkmcnt(6)\n"
        "v_mfma_f32_16x16x32_bf16 v[168:171], a[16:19], v[136:139], v[168:171]\n"
        "ds_read_b128 v[136:139], v3 offset:25088\n"
        "v_mfma_f32_16x16x32_bf16 v[172:175], a[16:19], v[140:143], v[172:175]\n"
        "ds_read_b128 v[140:143], v3 offset:25344\n"
        "s_waitcnt lgkmcnt(6)\n"
        "v_mfma_f32_16x16x32_bf16 v[160:163], a[20:23], v[144:147], v[160:163]\n"
        "ds_read_b128 v[144:147], v3 offset:28672\n"
        "v_mfma_f32_16x16x32_bf16 v[164:167], a[20:23], v[148:151], v[164:167]\n"
        "ds_read_b128 v[148:151], v3 offset:28928\n"
        "s_waitcnt lgkmcnt(6)\n"
        "v_mfma_f32_16x16x32_bf16 v[168:171], a[20:23], v[152:155], v[168:171]\n"
        "ds_read_b128 v[152:155], v3 offset:29184\n"
        "v_mfma_f32_16x16x32_bf16 v[172:175], a[20:23], v[156:159], v[172:175]\n"
        "ds_read_b128 v[156:159], v3 offset:29440\n"
        "s_waitcnt lgkmcnt(6)\n"
        "v_mfma_f32_16x16x32_bf16 v[160:163], a[24:27], v[128:131], v[160:163]\n"
        "ds_read_b128 v[128:131], v3 offset:32768\n"
        "v_mfma_f32_16x16x32_bf16 v[164:167], a[24:27], v[132:135], v[164:167]\n"
        "ds_read_b128 v[132:135], v3 offset:33024\n"
        "s_waitcnt lgkmcnt(6)\n"
        "v_mfma_f32_16x16x32_bf16 v[168:171], a[24:27], v[136:139], v[168:171]\n"
        "ds_read_b128 v[136:139], v3 offset:33280\n"
        "v_mfma_f32_16x16x32_bf16 v[172:175], a[24:27], v[140:143], v[172:175]\n"
        "ds_read_b128 v[140:143], v3 offset:33536\n"
        "s_waitcnt lgkmcnt(6)\n"
        "v_mfma_f32_16x16x32_bf16 v[160:163], a[28:31], v[144:147], v[160:163]\n"
        "ds_read_b128 v[144:147], v3 offset:36864\n"
        "v_mfma_f32_16x16x32_bf16 v[164:167], a[28:31], v[148:151], v[164:167]\n"
        "ds_read_b128 v[148:151], v3 offset:37120\n"
        "s_waitcnt lgkmcnt(6)\n"
        "v_mfma_f32_16x16x32_bf16 v[168:171], a[28:31], v[152:155], v[168:171]\n"
        "ds_read_b128 v[152:155], v3 offset:37376\n"
        "v_mfma_f32_16x16x32_bf16 v[172:175], a[28:31], v[156:159], v[172:175]\n"
        "ds_read_b128 v[156:159], v3 offset:37632\n"
        "s_waitcnt lgkmcnt(6)\n"
        "v_mfma_f32_16x16x32_bf16 v[160:163], a[32:35], v[128:131], v[160:163]\n"
        "ds_read_b128 v[128:131], v3 offset:40960\n"
        "v_mfma_f32_16x16x32_bf16 v[164:167], a[32:35], v[132:135], v[164:167]\n"
        "ds_read_b128 v[132:135], v3 offset:41216\n"
        "s_waitcnt lgkmcnt(6)\n"
        "v_mfma_f32_16x16x32_bf16 v[168:171], a[32:35], v[136:139], v[168:171]\n"
        "ds_read_b128 v[136:139], v3 offset:41472\n"
        "v_mfma_f32_16x16x32_bf16 v[172:175], a[32:35], v[140:143], v[172:175]\n"
        "ds_read_b128 v[140:143], v3 offset:41728\n"
        "s_waitcnt lgkmcnt(6)\n"
        "v_mfma_f32_16x16x32_bf16 v[160:163], a[36:39], v[144:147], v[160:163]\n"
        "ds_read_b128 v[144:147], v3 offset:45056\n"
        "v_mfma_f32_16x16x32_bf16 v[164:167], a[36:39], v[148:151], v[164:167]\n"
        "ds_read_b128 v[148:151], v3 offset:45312\n"
        "s_waitcnt lgkmcnt(6)\n"
        "v_mfma_f32_16x16x32_bf16 v[168:171], a[36:39], v[152:155], v[168:171]\n"
        "ds_read_b128 v[152:155], v3 offset:45568\n"
        "v_mfma_f32_16x16x32_bf16 v[172:175], a[36:39], v[156:159], v[172:175]\n"
        "ds_read_b128 v[156:159], v3 offset:45824\n"
        "s_waitcnt lgkmcnt(6)\n"
        "v_mfma_f32_16x16x32_bf16 v[160:163], a[40:43], v[128:131], v[160:163]\n"
        "ds_read_b128 v[128:131], v3 offset:49152\n"
        "v_mfma_f32_16x16x32_bf16 v[164:167], a[40:43], v[132:135], v[164:167]\n"
        "ds_read_b128 v[132:135], v3 offset:49408\n"
        "s_waitcnt lgkmcnt(6)\n"
        "v_mfma_f32_16x16x32_bf16 v[168:171], a[40:43], v[136:139], v[168:171]\n"
        "ds_read_b128 v[136:139], v3 offset:49664\n"
        "v_mfma_f32_16x16x32_bf16 v[172:175], a[40:43], v[140:143], v[172:175]\n"
        "ds_read_b128 v[140:143], v3 offset:49920\n"
        "s_waitcnt lgkmcnt(6)\n"
        "v_mfma_f32_16x16x32_bf16 v[160:163], a[44:47], v[144:147], v[160:163]\n"
        "ds_read_b128 v[144:147], v3 offset:53248\n"
        "v_mfma_f32_16x16x32_bf16 v[164:167], a[44:47], v[148:151], v[164:167]\n"
        "ds_read_b128 v[148:151], v3 offset:53504\n"
        "s_waitcnt lgkmcnt(6)\n"
        "v_mfma_f32_16x16x32_bf16 v[168:171], a[44:47], v[152:155], v[168:171]\n"
        "ds_read_b128 v[152:155], v3 offset:53760\n"
        "v_mfma_f32_16x16x32_bf16 v[172:175], a[44:47], v[156:159], v[172:175]\n"
        "ds_read_b128 v[156:159], v3 offset:54016\n"
        "s_waitcnt lgkmcnt(6)\n"
        "v_mfma_f32_16x16x32_bf16 v[160:163], a[48:51], v[128:131], v[160:163]\n"
        "ds_read_b128 v[128:131], v3 offset:57344\n"
        "v_mfma_f32_16x16x32_bf16 v[164:167], a[48:51], v[132:135], v[164:167]\n"
        "ds_read_b128 v[132:135], v3 offset:57600\n"
        "s_waitcnt lgkmcnt(6)\n"
        "v_mfma_f32_16x16x32_bf16 v[168:171], a[48:51], v[136:139], v[168:171]\n"
        "ds_read_b128 v[136:139], v3 offset:57856\n"
        "v_mfma_f32_16x16x32_bf16 v[172:175], a[48:51], v[140:143], v[172:175]\n"
        "ds_read_b128 v[140:143], v3 offset:58112\n"
        "s_waitcnt lgkmcnt(6)\n"
        "v_mfma_f32_16x16x32_bf16 v[160:163], a[52:55], v[144:147], v[160:163]\n"
        "ds_read_b128 v[144:147], v3 offset:61440\n"
        "v_mfma_f32_16x16x32_bf16 v[164:167], a[52:55], v[148:151], v[164:167]\n"
        "ds_read_b128 v[148:151], v3 offset:61696\n"
        "s_waitcnt lgkmcnt(6)\n"
        "v_mfma_f32_16x16x32_bf16 v[168:171], a[52:55], v[152:155], v[168:171]\n"
        "ds_read_b128 v[152:155], v3 offset:61952\n"
        "v_mfma_f32_16x16x32_bf16 v[172:175], a[52:55], v[156:159], v[172:175]\n"
        "ds_read_b128 v[156:159], v3 offset:62208\n"
        "s_waitcnt lgkmcnt(6)\n"
        "v_mfma_f32_16x16x32_bf16 v[160:163], a[56:59], v[128:131], v[160:163]\n"
        "v_mfma_f32_16x16x32_bf16 v[164:167], a[56:59], v[132:135], v[164:167]\n"
        "s_waitcnt lgkmcnt(4)\n"
        "v_mfma_f32_16x16x32_bf16 v[168:171], a[56:59], v[136:139], v[168:171]\n"
        "v_mfma_f32_16x16x32_bf16 v[172:175], a[56:59], v[140:143], v[172:175]\n"
        "s_waitcnt lgkmcnt(2)\n"
        "v_mfma_f32_16x16x32_bf16 v[160:163], a[60:63], v[144:147], v[160:163]\n"
        "v_mfma_f32_16x16x32_bf16 v[164:167], a[60:63], v[148:151], v[164:167]\n"
        "s_waitcnt lgkmcnt(0)\n"
        "v_mfma_f32_16x16x32_bf16 v[168:171], a[60:63], v[152:155], v[168:171]\n"
        "v_mfma_f32_16x16x32_bf16 v[172:175], a[60:63], v[156:159], v[172:175]\n"
        "s_nop 7\n"
        "s_nop 1\n"
        "s_branch Lsbok_%=\n"
        "Lscbad_%=:\n"
        "s_nop 15\n"
        "s_mov_b32 s57, 0\n"
        "Lr17_%=:\n"
        "s_waitcnt lgkmcnt(0)\n"
        "s_add_u32 s54, s49, 73728\n"
        "v_add_u32 v128, s54, v0\n"
        "v_mov_b32 v129, 0\n"
        "s_mov_b32 s56, 0\n"
        "Lr18_%=:\n"
        "ds_read_b128 v[132:135], v128 offset:0\n"
        "ds_read_b128 v[136:139], v128 offset:1024\n"
        "ds_read_b128 v[140:143], v128 offset:2048\n"
        "ds_read_b128 v[144:147], v128 offset:3072\n"
        "ds_read_b128 v[148:151], v128 offset:4096\n"
        "ds_read_b128 v[152:155], v128 offset:5120\n"
        "ds_read_b128 v[156:159], v128 offset:6144\n"
        "s_waitcnt lgkmcnt(0)\n"
        "v_max3_u32 v129, v129, v132, v133\n"
        "v_max3_u32 v129, v129, v134, v135\n"
        "v_max3_u32 v129, v129, v136, v137\n"
        "v_max3_u32 v129, v129, v138, v139\n"
        "v_max3_u32 v129, v129, v140, v141\n"
        "v_max3_u32 v129, v129, v142, v143\n"
        "v_max3_u32 v129, v129, v144, v145\n"
        "v_max3_u32 v129, v129, v146, v147\n"
        "v_max3_u32 v129, v129, v148, v149\n"
        "v_max3_u32 v129, v129, v150, v151\n"
        "v_max3_u32 v129, v129, v152, v153\n"
        "v_max3_u32 v129, v129, v154, v155\n"
        "v_max3_u32 v129, v129, v156, v157\n"
        "v_max3_u32 v129, v129, v158, v159\n"
        "v_add_u32 v128, 7168, v128\n"
        "s_add_u32 s56, s56, 1\n"
        "s_cmp_lt_u32 s56, 8\n"
        "s_cbranch_scc1 Lr18_%=\n"
        "v_cmp_eq_u32 vcc, -1, v129\n"
        "s_cbranch_vccz Lr19_%=\n"
        "s_add_u32 s57, s57, 1\n"
        "s_cmp_gt_u32 s57, 64\n"
        "s_cbranch_scc1 Lr19_%=\n"
        "s_barrier\n"
        "s_add_u32 s60, s65, 8388608\n"
        "s_add_u32 m0, s78, 65536\n"
        "s_add_u32 s54, s77, 0\n"
        "s_and_b32 s54, s54, 0xffff\n"
        "s_add_u32 s54, s54, s60\n"
        "buffer_load_dwordx4 v0, %[srd], s54 offen sc1 lds\n"
        "s_add_u32 m0, m0, 4096\n"
        "s_add_u32 s54, s77, 4096\n"
        "s_and_b32 s54, s54, 0xffff\n"
        "s_add_u32 s54, s54, s60\n"
        "buffer_load_dwordx4 v0, %[srd], s54 offen sc1 lds\n"
        "s_add_u32 m0, m0, 4096\n"
        "s_add_u32 s54, s77, 8192\n"
        "s_and_b32 s54, s54, 0xffff\n"
        "s_add_u32 s54, s54, s60\n"
        "buffer_load_dwordx4 v0, %[srd], s54 offen sc1 lds\n"
        "s_add_u32 m0, m0, 4096\n"
        "s_add_u32 s54, s77, 12288\n"
        "s_and_b32 s54, s54, 0xffff\n"
        "s_add_u32 s54, s54, s60\n"
        "buffer_load_dwordx4 v0, %[srd], s54 offen sc1 lds\n"
        "s_add_u32 m0, m0, 4096\n"
        "s_add_u32 s54, s77, 16384\n"
        "s_and_b32 s54, s54, 0xffff\n"
        "s_add_u32 s54, s54, s60\n"
        "buffer_load_dwordx4 v0, %[srd], s54 offen sc1 lds\n"
        "s_add_u32 m0, m0, 4096\n"
        "s_add_u32 s54, s77, 20480\n"
        "s_and_b32 s54, s54, 0xffff\n"
        "s_add_u32 s54, s54, s60\n"
        "buffer_load_dwordx4 v0, %[srd], s54 offen sc1 lds\n"
        "s_add_u32 m0, m0, 4096\n"
        "s_add_u32 s54, s77, 24576\n"
        "s_and_b32 s54, s54, 0xffff\n"
        "s_add_u32 s54, s54, s60\n"
        "buffer_load_dwordx4 v0, %[srd], s54 offen sc1 lds\n"
        "s_add_u32 m0, m0, 4096\n"
        "s_add_u32 s54, s77, 28672\n"
        "s_and_b32 s54, s54, 0xffff\n"
        "s_add_u32 s54, s54, s60\n"
        "buffer_load_dwordx4 v0, %[srd], s54 offen sc1 lds\n"
        "s_add_u32 m0, m0, 4096\n"
        "s_add_u32 s54, s77, 32768\n"
        "s_and_b32 s54, s54, 0xffff\n"
        "s_add_u32 s54, s54, s60\n"
        "buffer_load_dwordx4 v0, %[srd], s54 offen sc1 lds\n"
        "s_add_u32 m0, m0, 4096\n"
        "s_add_u32 s54, s77, 36864\n"
        "s_and_b32 s54, s54, 0xffff\n"
        "s_add_u32 s54, s54, s60\n"
        "buffer_load_dwordx4 v0, %[srd], s54 offen sc1 lds\n"
        "s_add_u32 m0, m0, 4096\n"
        "s_add_u32 s54, s77, 40960\n"
        "s_and_b32 s54, s54, 0xffff\n"
        "s_add_u32 s54, s54, s60\n"
        "buffer_load_dwordx4 v0, %[srd], s54 offen sc1 lds\n"
        "s_add_u32 m0, m0, 4096\n"
        "s_add_u32 s54, s77, 45056\n"
        "s_and_b32 s54, s54, 0xffff\n"
        "s_add_u32 s54, s54, s60\n"
        "buffer_load_dwordx4 v0, %[srd], s54 offen sc1 lds\n"
        "s_add_u32 m0, m0, 4096\n"
        "s_add_u32 s54, s77, 49152\n"
        "s_and_b32 s54, s54, 0xffff\n"
        "s_add_u32 s54, s54, s60\n"
        "buffer_load_dwordx4 v0, %[srd], s54 offen sc1 lds\n"
        "s_add_u32 m0, m0, 4096\n"
        "s_add_u32 s54, s77, 53248\n"
        "s_and_b32 s54, s54, 0xffff\n"
        "s_add_u32 s54, s54, s60\n"
        "buffer_load_dwordx4 v0, %[srd], s54 offen sc1 lds\n"
        "s_add_u32 m0, m0, 4096\n"
        "s_waitcnt vmcnt(0)\n"
        "s_barrier\n"
        "s_branch Lr17_%=\n"
        "Lr19_%=:\n"
        "v_lshrrev_b32 v128, 2, v0\n"
        "v_add_u32 v128, s52, v128\n"
        "ds_read_b32 v128, v128 offset:256\n"
        "s_waitcnt lgkmcnt(0)\n"
        "v_cmp_u_f32 vcc, v128, v128\n"
        "s_cbranch_vccz Lf21_%=\n"
        "s_barrier\n"
        "s_add_u32 s54, s66, 0\n"
        "s_mov_b32 s56, 0\n"
        "Lsp22_%=:\n"
        "s_add_u32 s56, s56, 1\n"
        "s_cmp_gt_u32 s56, 16384\n"
        "s_cbranch_scc1 Lsq23_%=\n"
        "buffer_load_dwordx2 v[248:249], v6, %[srd], s54 offen sc1\n"
        "buffer_load_dwordx2 v[250:251], v6, %[srd], s54 offen offset:512 sc1\n"
        "s_waitcnt vmcnt(0)\n"
        "v_max3_u32 v128, v248, v249, v250\n"
        "v_max_u32 v128, v128, v251\n"
        "v_cmp_eq_u32 vcc, -1, v128\n"
        "s_cbranch_vccnz Lsp22_%=\n"
        "Lsq23_%=:\n"
        "v_pk_add_f32 v[248:249], v[248:249], v[250:251]\n"
        "v_mov_b32 v253, v248\n"
        "v_mov_b32 v254, v249\n"
        "s_nop 0\n"
        "v_permlane16_swap_b32 v248, v253\n"
        "v_permlane16_swap_b32 v249, v254\n"
        "v_add_f32 v248, v248, v253\n"
        "v_add_f32 v249, v249, v254\n"
        "v_mov_b32 v253, v248\n"
        "v_mov_b32 v254, v249\n"
        "s_nop 0\n"
        "v_permlane32_swap_b32 v248, v253\n"
        "v_permlane32_swap_b32 v249, v254\n"
        "v_add_f32 v248, v248, v253\n"
        "v_add_f32 v249, v249, v254\n"
        "v_mul_f32 v248, 0x3b000000, v248\n"
        "v_mul_f32 v249, 0x3b000000, v249\n"
        "v_fma_f32 v249, -v248, v248, v249\n"
        "v_and_b32 v249, 0x7fffffff, v249\n"
        "v_add_f32 v249, 0x3727c5ac, v249\n"
        "v_rsq_f32 v249, v249\n"
        "s_mov_b64 exec, 0xffff\n"
        "ds_write2_b32 v7, v248, v249 offset1:64\n"
        "s_mov_b64 exec, -1\n"
        "s_waitcnt lgkmcnt(0)\n"
        "s_barrier\n"
        "Lf21_%=:\n"
        "s_add_u32 s54, s72, 1048576\n"
        "buffer_load_dwordx4 v[96:99], v0, %[srd], s54 offen offset:0 sc1\n"
        "buffer_load_dwordx4 v[100:103], v0, %[srd], s54 offen offset:1024 sc1\n"
        "buffer_load_dwordx4 v[104:107], v0, %[srd], s54 offen offset:2048 sc1\n"
        "buffer_load_dwordx4 v[108:111], v0, %[srd], s54 offen offset:3072 sc1\n"
        "s_add_u32 s54, s72, 1052672\n"
        "buffer_load_dwordx4 v[112:115], v0, %[srd], s54 offen offset:0 sc1\n"
        "buffer_load_dwordx4 v[116:119], v0, %[srd], s54 offen offset:1024 sc1\n"
        "buffer_load_dwordx4 v[120:123], v0, %[srd], s54 offen offset:2048 sc1\n"
        "buffer_load_dwordx4 v[124:127], v0, %[srd], s54 offen offset:3072 sc1\n"
        "s_waitcnt vmcnt(0)\n"
        "ds_read_b128 v[128:131], v4 offset:0\n"
        "ds_read_b128 v[132:135], v4 offset:256\n"
        "ds_read_b128 v[136:139], v4 offset:512\n"
        "ds_read_b128 v[140:143], v4 offset:768\n"
        "ds_read_b128 v[144:147], v4 offset:4096\n"
        "ds_read_b128 v[148:151], v4 offset:4352\n"
        "ds_read_b128 v[152:155], v4 offset:4608\n"
        "ds_read_b128 v[156:159], v4 offset:4864\n"
        "s_waitcnt lgkmcnt(7)\n"
        "v_mfma_f32_16x16x32_bf16 v[160:163], v[96:99], v[128:131], 0\n"
        "v_mfma_f32_16x16x32_bf16 v[176:179], v[128:131], a[160:163], 0\n"
        "ds_read_b128 v[128:131], v4 offset:8192\n"
        "s_waitcnt lgkmcnt(7)\n"
        "v_mfma_f32_16x16x32_bf16 v[164:167], v[96:99], v[132:135], 0\n"
        "v_mfma_f32_16x16x32_bf16 v[180:183], v[132:135], a[160:163], 0\n"
        "ds_read_b128 v[132:135], v4 offset:8448\n"
        "s_waitcnt lgkmcnt(7)\n"
        "v_mfma_f32_16x16x32_bf16 v[168:171], v[96:99], v[136:139], 0\n"
        "v_mfma_f32_16x16x32_bf16 v[184:187], v[136:139], a[160:163], 0\n"
        "ds_read_b128 v[136:139], v4 offset:8704\n"
        "s_waitcnt lgkmcnt(7)\n"
        "v_mfma_f32_16x16x32_bf16 v[172:175], v[96:99], v[140:143], 0\n"
        "v_mfma_f32_16x16x32_bf16 v[188:191], v[140:143], a[160:163], 0\n"
        "ds_read_b128 v[140:143], v4 offset:8960\n"
        "s_waitcnt lgkmcnt(7)\n"
        "v_mfma_f32_16x16x32_bf16 v[160:163], v[100:103], v[144:147], v[160:163]\n"
        "v_mfma_f32_16x16x32_bf16 v[176:179], v[144:147], a[164:167], v[176:179]\n"
        "ds_read_b128 v[144:147], v4 offset:12288\n"
        "s_waitcnt lgkmcnt(7)\n"
        "v_mfma_f32_16x16x32_bf16 v[164:167], v[100:103], v[148:151], v[164:167]\n"
        "v_mfma_f32_16x16x32_bf16 v[180:183], v[148:151], a[164:167], v[180:183]\n"
        "ds_read_b128 v[148:151], v4 offset:12544\n"
        "s_waitcnt lgkmcnt(7)\n"
        "v_mfma_f32_16x16x32_bf16 v[168:171], v[100:103], v[152:155], v[168:171]\n"
        "v_mfma_f32_16x16x32_bf16 v[184:187], v[152:155], a[164:167], v[184:187]\n"
        "ds_read_b128 v[152:155], v4 offset:12800\n"
        "s_waitcnt lgkmcnt(7)\n"
        "v_mfma_f32_16x16x32_bf16 v[172:175], v[100:103], v[156:159], v[172:175]\n"
        "v_mfma_f32_16x16x32_bf16 v[188:191], v[156:159], a[164:167], v[188:191]\n"
        "ds_read_b128 v[156:159], v4 offset:13056\n"
        "s_waitcnt lgkmcnt(7)\n"
        "v_mfma_f32_16x16x32_bf16 v[160:163], v[104:107], v[128:131], v[160:163]\n"
        "v_mfma_f32_16x16x32_bf16 v[176:179], v[128:131], a[168:171], v[176:179]\n"
        "ds_read_b128 v[128:131], v4 offset:16384\n"
        "s_waitcnt lgkmcnt(7)\n"
        "v_mfma_f32_16x16x32_bf16 v[164:167], v[104:107], v[132:135], v[164:167]\n"
        "v_mfma_f32_16x16x32_bf16 v[180:183], v[132:135], a[168:171], v[180:183]\n"
        "ds_read_b128 v[132:135], v4 offset:16640\n"
        "s_waitcnt lgkmcnt(7)\n"
        "v_mfma_f32_16x16x32_bf16 v[168:171], v[104:107], v[136:139], v[168:171]\n"
        "v_mfma_f32_16x16x32_bf16 v[184:187], v[136:139], a[168:171], v[184:187]\n"
        "ds_read_b128 v[136:139], v4 offset:16896\n"
        "s_waitcnt lgkmcnt(7)\n"
        "v_mfma_f32_16x16x32_bf16 v[172:175], v[104:107], v[140:143], v[172:175]\n"
        "v_mfma_f32_16x16x32_bf16 v[188:191], v[140:143], a[168:171], v[188:191]\n"
        "ds_read_b128 v[140:143], v4 offset:17152\n"
        "s_waitcnt lgkmcnt(7)\n"
        "v_mfma_f32_16x16x32_bf16 v[160:163], v[108:111], v[144:147], v[160:163]\n"
        "v_mfma_f32_16x16x32_bf16 v[176:179], v[144:147], a[172:175], v[176:179]\n"
        "ds_read_b128 v[144:147], v4 offset:20480\n"
        "s_waitcnt lgkmcnt(7)\n"
        "v_mfma_f32_16x16x32_bf16 v[164:167], v[108:111], v[148:151], v[164:167]\n"
        "v_mfma_f32_16x16x32_bf16 v[180:183], v[148:151], a[172:175], v[180:183]\n"
        "ds_read_b128 v[148:151], v4 offset:20736\n"
        "s_waitcnt lgkmcnt(7)\n"
        "v_mfma_f32_16x16x32_bf16 v[168:171], v[108:111], v[152:155], v[168:171]\n"
        "v_mfma_f32_16x16x32_bf16 v[184:187], v[152:155], a[172:175], v[184:187]\n"
        "ds_read_b128 v[152:155], v4 offset:20992\n"
        "s_waitcnt lgkmcnt(7)\n"
        "v_mfma_f32_16x16x32_bf16 v[172:175], v[108:111], v[156:159], v[172:175]\n"
        "v_mfma_f32_16x16x32_bf16 v[188:191], v[156:159], a[172:175], v[188:191]\n"
        "ds_read_b128 v[156:159], v4 offset:21248\n"
        "s_waitcnt lgkmcnt(7)\n"
        "v_mfma_f32_16x16x32_bf16 v[160:163], v[112:115], v[128:131], v[160:163]\n"
        "v_mfma_f32_16x16x32_bf16 v[176:179], v[128:131], a[176:179], v[176:179]\n"
        "ds_read_b128 v[128:131], v4 offset:24576\n"
        "s_waitcnt lgkmcnt(7)\n"
        "v_mfma_f32_16x16x32_bf16 v[164:167], v[112:115], v[132:135], v[164:167]\n"
        "v_mfma_f32_16x16x32_bf16 v[180:183], v[132:135], a[176:179], v[180:183]\n"
        "ds_read_b128 v[132:135], v4 offset:24832\n"
        "s_waitcnt lgkmcnt(7)\n"
        "v_mfma_f32_16x16x32_bf16 v[168:171], v[112:115], v[136:139], v[168:171]\n"
        "v_mfma_f32_16x16x32_bf16 v[184:187], v[136:139], a[176:179], v[184:187]\n"
        "ds_read_b128 v[136:139], v4 offset:25088\n"
        "s_waitcnt lgkmcnt(7)\n"
        "v_mfma_f32_16x16x32_bf16 v[172:175], v[112:115], v[140:143], v[172:175]\n"
        "v_mfma_f32_16x16x32_bf16 v[188:191], v[140:143], a[176:179], v[188:191]\n"
        "ds_read_b128 v[140:143], v4 offset:25344\n"
        "s_waitcnt lgkmcnt(7)\n"
        "v_mfma_f32_16x16x32_bf16 v[160:163], v[116:119], v[144:147], v[160:163]\n"
        "v_mfma_f32_16x16x32_bf16 v[176:179], v[144:147], a[180:183], v[176:179]\n"
        "ds_read_b128 v[144:147], v4 offset:28672\n"
        "s_waitcnt lgkmcnt(7)\n"
        "v_mfma_f32_16x16x32_bf16 v[164:167], v[116:119], v[148:151], v[164:167]\n"
        "v_mfma_f32_16x16x32_bf16 v[180:183], v[148:151], a[180:183], v[180:183]\n"
        "ds_read_b128 v[148:151], v4 offset:28928\n"
        "s_waitcnt lgkmcnt(7)\n"
        "v_mfma_f32_16x16x32_bf16 v[168:171], v[116:119], v[152:155], v[168:171]\n"
        "v_mfma_f32_16x16x32_bf16 v[184:187], v[152:155], a[180:183], v[184:187]\n"
        "ds_read_b128 v[152:155], v4 offset:29184\n"
        "s_waitcnt lgkmcnt(7)\n"
        "v_mfma_f32_16x16x32_bf16 v[172:175], v[116:119], v[156:159], v[172:175]\n"
        "v_mfma_f32_16x16x32_bf16 v[188:191], v[156:159], a[180:183], v[188:191]\n"
        "ds_read_b128 v[156:159], v4 offset:29440\n"
        "s_waitcnt lgkmcnt(7)\n"
        "v_mfma_f32_16x16x32_bf16 v[160:163], v[120:123], v[128:131], v[160:163]\n"
        "v_mfma_f32_16x16x32_bf16 v[176:179], v[128:131], a[184:187], v[176:179]\n"
        "ds_read_b128 v[128:131], v4 offset:32768\n"
        "s_waitcnt lgkmcnt(7)\n"
        "v_mfma_f32_16x16x32_bf16 v[164:167], v[120:123], v[132:135], v[164:167]\n"
        "v_mfma_f32_16x16x32_bf16 v[180:183], v[132:135], a[184:187], v[180:183]\n"
        "ds_read_b128 v[132:135], v4 offset:33024\n"
        "s_waitcnt lgkmcnt(7)\n"
        "v_mfma_f32_16x16x32_bf16 v[168:171], v[120:123], v[136:139], v[168:171]\n"
        "v_mfma_f32_16x16x32_bf16 v[184:187], v[136:139], a[184:187], v[184:187]\n"
        "ds_read_b128 v[136:139], v4 offset:33280\n"
        "s_waitcnt lgkmcnt(7)\n"
        "v_mfma_f32_16x16x32_bf16 v[172:175], v[120:123], v[140:143], v[172:175]\n"
        "v_mfma_f32_16x16x32_bf16 v[188:191], v[140:143], a[184:187], v[188:191]\n"
        "ds_read_b128 v[140:143], v4 offset:33536\n"
        "s_waitcnt lgkmcnt(7)\n"
        "v_mfma_f32_16x16x32_bf16 v[160:163], v[124:127], v[144:147], v[160:163]\n"
        "v_mfma_f32_16x16x32_bf16 v[176:179], v[144:147], a[188:191], v[176:179]\n"
        "ds_read_b128 v[144:147], v4 offset:36864\n"
        "s_waitcnt lgkmcnt(7)\n"
        "v_mfma_f32_16x16x32_bf16 v[164:167], v[124:127], v[148:151], v[164:167]\n"
        "v_mfma_f32_16x16x32_bf16 v[180:183], v[148:151], a[188:191], v[180:183]\n"
        "ds_read_b128 v[148:151], v4 offset:37120\n"
        "s_waitcnt lgkmcnt(7)\n"
        "v_mfma_f32_16x16x32_bf16 v[168:171], v[124:127], v[152:155], v[168:171]\n"
        "v_mfma_f32_16x16x32_bf16 v[184:187], v[152:155], a[188:191], v[184:187]\n"
        "ds_read_b128 v[152:155], v4 offset:37376\n"
        "s_waitcnt lgkmcnt(7)\n"
        "v_mfma_f32_16x16x32_bf16 v[172:175], v[124:127], v[156:159], v[172:175]\n"
        "v_mfma_f32_16x16x32_bf16 v[188:191], v[156:159], a[188:191], v[188:191]\n"
        "ds_read_b128 v[156:159], v4 offset:37632\n"
        "s_waitcnt lgkmcnt(7)\n"
        "v_mfma_f32_16x16x32_bf16 v[160:163], a[128:131], v[128:131], v[160:163]\n"
        "v_mfma_f32_16x16x32_bf16 v[176:179], v[128:131], a[192:195], v[176:179]\n"
        "ds_read_b128 v[128:131], v4 offset:40960\n"
        "s_waitcnt lgkmcnt(7)\n"
        "v_mfma_f32_16x16x32_bf16 v[164:167], a[128:131], v[132:135], v[164:167]\n"
        "v_mfma_f32_16x16x32_bf16 v[180:183], v[132:135], a[192:195], v[180:183]\n"
        "ds_read_b128 v[132:135], v4 offset:41216\n"
        "s_waitcnt lgkmcnt(7)\n"
        "v_mfma_f32_16x16x32_bf16 v[168:171], a[128:131], v[136:139], v[168:171]\n"
        "v_mfma_f32_16x16x32_bf16 v[184:187], v[136:139], a[192:195], v[184:187]\n"
        "ds_read_b128 v[136:139], v4 offset:41472\n"
        "s_waitcnt lgkmcnt(7)\n"
        "v_mfma_f32_16x16x32_bf16 v[172:175], a[128:131], v[140:143], v[172:175]\n"
        "v_mfma_f32_16x16x32_bf16 v[188:191], v[140:143], a[192:195], v[188:191]\n"
        "ds_read_b128 v[140:143], v4 offset:41728\n"
        "s_waitcnt lgkmcnt(7)\n"
        "v_mfma_f32_16x16x32_bf16 v[160:163], a[132:135], v[144:147], v[160:163]\n"
        "v_mfma_f32_16x16x32_bf16 v[176:179], v[144:147], a[196:199], v[176:179]\n"
        "ds_read_b128 v[144:147], v4 offset:45056\n"
        "s_waitcnt lgkmcnt(7)\n"
        "v_mfma_f32_16x16x32_bf16 v[164:167], a[132:135], v[148:151], v[164:167]\n"
        "v_mfma_f32_16x16x32_bf16 v[180:183], v[148:151], a[196:199], v[180:183]\n"
        "ds_read_b128 v[148:151], v4 offset:45312\n"
        "s_waitcnt lgkmcnt(7)\n"
        "v_mfma_f32_16x16x32_bf16 v[168:171], a[132:135], v[152:155], v[168:171]\n"
        "v_mfma_f32_16x16x32_bf16 v[184:187], v[152:155], a[196:199], v[184:187]\n"
        "ds_read_b128 v[152:155], v4 offset:45568\n"
        "s_waitcnt lgkmcnt(7)\n"
        "v_mfma_f32_16x16x32_bf16 v[172:175], a[132:135], v[156:159], v[172:175]\n"
        "v_mfma_f32_16x16x32_bf16 v[188:191], v[156:159], a[196:199], v[188:191]\n"
        "ds_read_b128 v[156:159], v4 offset:45824\n"
        "s_waitcnt lgkmcnt(7)\n"
        "v_mfma_f32_16x16x32_bf16 v[160:163], a[136:139], v[128:131], v[160:163]\n"
        "v_mfma_f32_16x16x32_bf16 v[176:179], v[128:131], a[200:203], v[176:179]\n"
        "ds_read_b128 v[128:131], v4 offset:49152\n"
        "s_waitcnt lgkmcnt(7)\n"
        "v_mfma_f32_16x16x32_bf16 v[164:167], a[136:139], v[132:135], v[164:167]\n"
        "v_mfma_f32_16x16x32_bf16 v[180:183], v[132:135], a[200:203], v[180:183]\n"
        "ds_read_b128 v[132:135], v4 offset:49408\n"
        "s_waitcnt lgkmcnt(7)\n"
        "v_mfma_f32_16x16x32_bf16 v[168:171], a[136:139], v[136:139], v[168:171]\n"
        "v_mfma_f32_16x16x32_bf16 v[184:187], v[136:139], a[200:203], v[184:187]\n"
        "ds_read_b128 v[136:139], v4 offset:49664\n"
        "s_waitcnt lgkmcnt(7)\n"
        "v_mfma_f32_16x16x32_bf16 v[172:175], a[136:139], v[140:143], v[172:175]\n"
        "v_mfma_f32_16x16x32_bf16 v[188:191], v[140:143], a[200:203], v[188:191]\n"
        "ds_read_b128 v[140:143], v4 offset:49920\n"
        "s_waitcnt lgkmcnt(7)\n"
        "v_mfma_f32_16x16x32_bf16 v[160:163], a[140:143], v[144:147], v[160:163]\n"
        "v_mfma_f32_16x16x32_bf16 v[176:179], v[144:147], a[204:207], v[176:179]\n"
        "ds_read_b128 v[144:147], v4 offset:53248\n"
        "s_waitcnt lgkmcnt(7)\n"
        "v_mfma_f32_16x16x32_bf16 v[164:167], a[140:143], v[148:151], v[164:167]\n"
        "v_mfma_f32_16x16x32_bf16 v[180:183], v[148:151], a[204:207], v[180:183]\n"
        "ds_read_b128 v[148:151], v4 offset:53504\n"
        "s_waitcnt lgkmcnt(7)\n"
        "v_mfma_f32_16x16x32_bf16 v[168:171], a[140:143], v[152:155], v[168:171]\n"
        "v_mfma_f32_16x16x32_bf16 v[184:187], v[152:155], a[204:207], v[184:187]\n"
        "ds_read_b128 v[152:155], v4 offset:53760\n"
        "s_waitcnt lgkmcnt(7)\n"
        "v_mfma_f32_16x16x32_bf16 v[172:175], a[140:143], v[156:159], v[172:175]\n"
        "v_mfma_f32_16x16x32_bf16 v[188:191], v[156:159], a[204:207], v[188:191]\n"
        "ds_read_b128 v[156:159], v4 offset:54016\n"
        "s_waitcnt lgkmcnt(7)\n"
        "v_mfma_f32_16x16x32_bf16 v[160:163], a[144:147], v[128:131], v[160:163]\n"
        "v_mfma_f32_16x16x32_bf16 v[176:179], v[128:131], a[208:211], v[176:179]\n"
        "ds_read_b128 v[128:131], v4 offset:57344\n"
        "s_waitcnt lgkmcnt(7)\n"
        "v_mfma_f32_16x16x32_bf16 v[164:167], a[144:147], v[132:135], v[164:167]\n"
        "v_mfma_f32_16x16x32_bf16 v[180:183], v[132:135], a[208:211], v[180:183]\n"
        "ds_read_b128 v[132:135], v4 offset:57600\n"
        "s_waitcnt lgkmcnt(7)\n"
        "v_mfma_f32_16x16x32_bf16 v[168:171], a[144:147], v[136:139], v[168:171]\n"
        "v_mfma_f32_16x16x32_bf16 v[184:187], v[136:139], a[208:211], v[184:187]\n"
        "ds_read_b128 v[136:139], v4 offset:57856\n"
        "s_waitcnt lgkmcnt(7)\n"
        "v_mfma_f32_16x16x32_bf16 v[172:175], a[144:147], v[140:143], v[172:175]\n"
        "v_mfma_f32_16x16x32_bf16 v[188:191], v[140:143], a[208:211], v[188:191]\n"
        "ds_read_b128 v[140:143], v4 offset:58112\n"
        "s_waitcnt lgkmcnt(7)\n"
        "v_mfma_f32_16x16x32_bf16 v[160:163], a[148:151], v[144:147], v[160:163]\n"
        "v_mfma_f32_16x16x32_bf16 v[176:179], v[144:147], a[212:215], v[176:179]\n"
        "ds_read_b128 v[144:147], v4 offset:61440\n"
        "s_waitcnt lgkmcnt(7)\n"
        "v_mfma_f32_16x16x32_bf16 v[164:167], a[148:151], v[148:151], v[164:167]\n"
        "v_mfma_f32_16x16x32_bf16 v[180:183], v[148:151], a[212:215], v[180:183]\n"
        "ds_read_b128 v[148:151], v4 offset:61696\n"
        "s_waitcnt lgkmcnt(7)\n"
        "v_mfma_f32_16x16x32_bf16 v[168:171], a[148:151], v[152:155], v[168:171]\n"
        "v_mfma_f32_16x16x32_bf16 v[184:187], v[152:155], a[212:215], v[184:187]\n"
        "ds_read_b128 v[152:155], v4 offset:61952\n"
        "s_waitcnt lgkmcnt(7)\n"
        "v_mfma_f32_16x16x32_bf16 v[172:175], a[148:151], v[156:159], v[172:175]\n"
        "v_mfma_f32_16x16x32_bf16 v[188:191], v[156:159], a[212:215], v[188:191]\n"
        "ds_read_b128 v[156:159], v4 offset:62208\n"
        "s_waitcnt lgkmcnt(7)\n"
        "v_mfma_f32_16x16x32_bf16 v[160:163], a[152:155], v[128:131], v[160:163]\n"
        "v_mfma_f32_16x16x32_bf16 v[176:179], v[128:131], a[216:219], v[176:179]\n"
        "s_waitcnt lgkmcnt(6)\n"
        "v_mfma_f32_16x16x32_bf16 v[164:167], a[152:155], v[132:135], v[164:167]\n"
        "v_mfma_f32_16x16x32_bf16 v[180:183], v[132:135], a[216:219], v[180:183]\n"
        "s_waitcnt lgkmcnt(5)\n"
        "v_mfma_f32_16x16x32_bf16 v[168:171], a[152:155], v[136:139], v[168:171]\n"
        "v_mfma_f32_16x16x32_bf16 v[184:187], v[136:139], a[216:219], v[184:187]\n"
        "s_waitcnt lgkmcnt(4)\n"
        "v_mfma_f32_16x16x32_bf16 v[172:175], a[152:155], v[140:143], v[172:175]\n"
        "v_mfma_f32_16x16x32_bf16 v[188:191], v[140:143], a[216:219], v[188:191]\n"
        "s_waitcnt lgkmcnt(3)\n"
        "v_mfma_f32_16x16x32_bf16 v[160:163], a[156:159], v[144:147], v[160:163]\n"
        "v_mfma_f32_16x16x32_bf16 v[176:179], v[144:147], a[220:223], v[176:179]\n"
        "s_waitcnt lgkmcnt(2)\n"
        "v_mfma_f32_16x16x32_bf16 v[164:167], a[156:159], v[148:151], v[164:167]\n"
        "v_mfma_f32_16x16x32_bf16 v[180:183], v[148:151], a[220:223], v[180:183]\n"
        "s_waitcnt lgkmcnt(1)\n"
        "v_mfma_f32_16x16x32_bf16 v[168:171], a[156:159], v[152:155], v[168:171]\n"
        "v_mfma_f32_16x16x32_bf16 v[184:187], v[152:155], a[220:223], v[184:187]\n"
        "s_waitcnt lgkmcnt(0)\n"
        "v_mfma_f32_16x16x32_bf16 v[172:175], a[156:159], v[156:159], v[172:175]\n"
        "v_mfma_f32_16x16x32_bf16 v[188:191], v[156:159], a[220:223], v[188:191]\n"
        "s_nop 7\n"
        "s_nop 1\n"
        "ds_read2_b32 v[240:241], v8 offset0:0 offset1:64\n"
        "ds_read2_b32 v[242:243], v8 offset0:16 offset1:80\n"
        "ds_read2_b32 v[244:245], v8 offset0:32 offset1:96\n"
        "ds_read2_b32 v[246:247], v8 offset0:48 offset1:112\n"
        "ds_read_b128 v[96:99], v9 offset:0\n"
        "ds_read_b128 v[100:103], v9 offset:256\n"
        "ds_read_b128 v[104:107], v9 offset:64\n"
        "ds_read_b128 v[108:111], v9 offset:320\n"
        "ds_read_b128 v[112:115], v9 offset:128\n"
        "ds_read_b128 v[116:119], v9 offset:384\n"
        "ds_read_b128 v[120:123], v9 offset:192\n"
        "ds_read_b128 v[124:127], v9 offset:448\n"
        "s_waitcnt lgkmcnt(0)\n"
        "v_pk_fma_f32 v[160:161], v[240:241], v[204:205], v[160:161] op_sel_hi:[0,1,1] neg_lo:[1,0,0] neg_hi:[1,0,0]\n"
        "v_pk_fma_f32 v[162:163], v[240:241], v[206:207], v[162:163] op_sel_hi:[0,1,1] neg_lo:[1,0,0] neg_hi:[1,0,0]\n"
        "v_pk_fma_f32 v[164:165], v[242:243], v[204:205], v[164:165] op_sel_hi:[0,1,1] neg_lo:[1,0,0] neg_hi:[1,0,0]\n"
        "v_pk_fma_f32 v[166:167], v[242:243], v[206:207], v[166:167] op_sel_hi:[0,1,1] neg_lo:[1,0,0] neg_hi:[1,0,0]\n"
        "v_pk_fma_f32 v[168:169], v[244:245], v[204:205], v[168:169] op_sel_hi:[0,1,1] neg_lo:[1,0,0] neg_hi:[1,0,0]\n"
        "v_pk_fma_f32 v[170:171], v[244:245], v[206:207], v[170:171] op_sel_hi:[0,1,1] neg_lo:[1,0,0] neg_hi:[1,0,0]\n"
        "v_pk_fma_f32 v[172:173], v[246:247], v[204:205], v[172:173] op_sel_hi:[0,1,1] neg_lo:[1,0,0] neg_hi:[1,0,0]\n"
        "v_pk_fma_f32 v[174:175], v[246:247], v[206:207], v[174:175] op_sel_hi:[0,1,1] neg_lo:[1,0,0] neg_hi:[1,0,0]\n"
        "v_pk_mul_f32 v[160:161], v[240:241], v[160:161] op_sel:[1,0] op_sel_hi:[1,1]\n"
        "v_pk_mul_f32 v[162:163], v[240:241], v[162:163] op_sel:[1,0] op_sel_hi:[1,1]\n"
        "v_pk_mul_f32 v[164:165], v[242:243], v[164:165] op_sel:[1,0] op_sel_hi:[1,1]\n"
        "v_pk_mul_f32 v[166:167], v[242:243], v[166:167] op_sel:[1,0] op_sel_hi:[1,1]\n"
        "v_pk_mul_f32 v[168:169], v[244:245], v[168:169] op_sel:[1,0] op_sel_hi:[1,1]\n"
        "v_pk_mul_f32 v[170:171], v[244:245], v[170:171] op_sel:[1,0] op_sel_hi:[1,1]\n"
        "v_pk_mul_f32 v[172:173], v[246:247], v[172:173] op_sel:[1,0] op_sel_hi:[1,1]\n"
        "v_pk_mul_f32 v[174:175], v[246:247], v[174:175] op_sel:[1,0] op_sel_hi:[1,1]\n"
        "v_pk_fma_f32 v[176:177], v[96:97], v[216:217], v[176:177] op_sel_hi:[1,0,1] neg_lo:[1,0,0] neg_hi:[1,0,0]\n"
        "v_pk_fma_f32 v[178:179], v[98:99], v[216:217], v[178:179] op_sel_hi:[1,0,1] neg_lo:[1,0,0] neg_hi:[1,0,0]\n"
        "v_pk_fma_f32 v[180:181], v[104:105], v[216:217], v[180:181] op_sel_hi:[1,0,1] neg_lo:[1,0,0] neg_hi:[1,0,0]\n"
        "v_pk_fma_f32 v[182:183], v[106:107], v[216:217], v[182:183] op_sel_hi:[1,0,1] neg_lo:[1,0,0] neg_hi:[1,0,0]\n"
        "v_pk_fma_f32 v[184:185], v[112:113], v[216:217], v[184:185] op_sel_hi:[1,0,1] neg_lo:[1,0,0] neg_hi:[1,0,0]\n"
        "v_pk_fma_f32 v[186:187], v[114:115], v[216:217], v[186:187] op_sel_hi:[1,0,1] neg_lo:[1,0,0] neg_hi:[1,0,0]\n"
        "v_pk_fma_f32 v[188:189], v[120:121], v[216:217], v[188:189] op_sel_hi:[1,0,1] neg_lo:[1,0,0] neg_hi:[1,0,0]\n"
        "v_pk_fma_f32 v[190:191], v[122:123], v[216:217], v[190:191] op_sel_hi:[1,0,1] neg_lo:[1,0,0] neg_hi:[1,0,0]\n"
        "s_branch Lscok_%=\n"
        "LslowB0a_%=:\n"
        "s_waitcnt vmcnt(0)\n"
        "s_mov_b32 s58, 0xffff\n"
        "s_mov_b32 s59, 0xffff\n"
        "s_add_u32 s54, s68, 4294443008\n"
        "s_cmp_ge_u32 s69, 4\n"
        "s_cselect_b32 s54, s54, 0x7f000000\n"
        "buffer_load_dwordx4 v[96:99], v0, %[srd], s54 offen sc1\n"
        "s_mov_b64 exec, s[58:59]\n"
        "buffer_load_dwordx4 a[0:3], v12, %[srd], s54 offen sc1\n"
        "buffer_load_dwordx4 a[4:7], v12, %[srd], s54 offen offset:512 sc1\n"
        "s_mov_b64 exec, -1\n"
        "s_add_u32 s54, s68, 4294508544\n"
        "s_cmp_ge_u32 s69, 4\n"
        "s_cselect_b32 s54, s54, 0x7f000000\n"
        "buffer_load_dwordx4 v[100:103], v0, %[srd], s54 offen sc1\n"
        "s_mov_b64 exec, s[58:59]\n"
        "buffer_load_dwordx4 a[8:11], v12, %[srd], s54 offen sc1\n"
        "buffer_load_dwordx4 a[12:15], v12, %[srd], s54 offen offset:512 sc1\n"
        "s_mov_b64 exec, -1\n"
        "s_add_u32 s54, s68, 4294574080\n"
        "s_cmp_ge_u32 s69, 3\n"
        "s_cselect_b32 s54, s54, 0x7f000000\n"
        "buffer_load_dwordx4 v[104:107], v0, %[srd], s54 offen sc1\n"
        "s_mov_b64 exec, s[58:59]\n"
        "buffer_load_dwordx4 a[16:19], v12, %[srd], s54 offen sc1\n"
        "buffer_load_dwordx4 a[20:23], v12, %[srd], s54 offen offset:512 sc1\n"
        "s_mov_b64 exec, -1\n"
        "s_add_u32 s54, s68, 4294639616\n"
        "s_cmp_ge_u32 s69, 3\n"
        "s_cselect_b32 s54, s54, 0x7f000000\n"
        "buffer_load_dwordx4 v[108:111], v0, %[srd], s54 offen sc1\n"
        "s_mov_b64 exec, s[58:59]\n"
        "buffer_load_dwordx4 a[24:27], v12, %[srd], s54 offen sc1\n"
        "buffer_load_dwordx4 a[28:31], v12, %[srd], s54 offen offset:512 sc1\n"
        "s_mov_b64 exec, -1\n"
        "s_add_u32 s54, s68, 4294705152\n"
        "s_cmp_ge_u32 s69, 2\n"
        "s_cselect_b32 s54, s54, 0x7f000000\n"
        "buffer_load_dwordx4 v[112:115], v0, %[srd], s54 offen sc1\n"
        "s_mov_b64 exec, s[58:59]\n"
        "buffer_load_dwordx4 a[32:35], v12, %[srd], s54 offen sc1\n"
        "buffer_load_dwordx4 a[36:39], v12, %[srd], s54 offen offset:512 sc1\n"
        "s_mov_b64 exec, -1\n"
        "s_add_u32 s54, s68, 4294770688\n"
        "s_cmp_ge_u32 s69, 2\n"
        "s_cselect_b32 s54, s54, 0x7f000000\n"
        "buffer_load_dwordx4 v[116:119], v0, %[srd], s54 offen sc1\n"
        "s_mov_b64 exec, s[58:59]\n"
        "buffer_load_dwordx4 a[40:43], v12, %[srd], s54 offen sc1\n"
        "buffer_load_dwordx4 a[44:47], v12, %[srd], s54 offen offset:512 sc1\n"
        "s_mov_b64 exec, -1\n"
        "s_add_u32 s54, s68, 4294836224\n"
        "s_cmp_ge_u32 s69, 1\n"
        "s_cselect_b32 s54, s54, 0x7f000000\n"
        "buffer_load_dwordx4 v[120:123], v0, %[srd], s54 offen sc1\n"
        "s_mov_b64 exec, s[58:59]\n"
        "buffer_load_dwordx4 a[48:51], v12, %[srd], s54 offen sc1\n"
        "buffer_load_dwordx4 a[52:55], v12, %[srd], s54 offen offset:512 sc1\n"
        "s_mov_b64 exec, -1\n"
        "s_add_u32 s54, s68, 4294901760\n"
        "s_cmp_ge_u32 s69, 1\n"
        "s_cselect_b32 s54, s54, 0x7f000000\n"
        "buffer_load_dwordx4 v[124:127], v0, %[srd], s54 offen sc1\n"
        "s_mov_b64 exec, s[58:59]\n"
        "buffer_load_dwordx4 a[56:59], v12, %[srd], s54 offen sc1\n"
        "buffer_load_dwordx4 a[60:63], v12, %[srd], s54 offen offset:512 sc1\n"
        "s_mov_b64 exec, -1\n"
        "buffer_load_dword v249, v224, %[srd], s70 offen sc1\n"
        "; ---- attention B with the true row maxima\n"
        "LslowB0_%=:\n"
        "s_add_u32 s55, s72, 2097152\n"
        "buffer_load_dwordx4 a[128:131], v0, %[srd], s55 offen offset:0 sc1\n"
        "buffer_load_dwordx4 a[132:135], v0, %[srd], s55 offen offset:1024 sc1\n"
        "buffer_load_dwordx4 a[136:139], v0, %[srd], s55 offen offset:2048 sc1\n"
        "buffer_load_dwordx4 a[140:143], v0, %[srd], s55 offen offset:3072 sc1\n"
        "s_add_u32 s55, s72, 2101248\n"
        "buffer_load_dwordx4 a[144:147], v0, %[srd], s55 offen offset:0 sc1\n"
        "buffer_load_dwordx4 a[148:151], v0, %[srd], s55 offen offset:1024 sc1\n"
        "buffer_load_dwordx4 a[152:155], v0, %[srd], s55 offen offset:2048 sc1\n"
        "buffer_load_dwordx4 a[156:159], v0, %[srd], s55 offen offset:3072 sc1\n"
        "s_add_u32 s55, s72, 2105344\n"
        "buffer_load_dwordx4 a[160:163], v0, %[srd], s55 offen offset:0 sc1\n"
        "buffer_load_dwordx4 a[164:167], v0, %[srd], s55 offen offset:1024 sc1\n"
        "buffer_load_dwordx4 a[168:171], v0, %[srd], s55 offen offset:2048 sc1\n"
        "buffer_load_dwordx4 a[172:175], v0, %[srd], s55 offen offset:3072 sc1\n"
        "s_add_u32 s55, s72, 2109440\n"
        "buffer_load_dwordx4 a[176:179], v0, %[srd], s55 offen offset:0 sc1\n"
        "buffer_load_dwordx4 a[180:183], v0, %[srd], s55 offen offset:1024 sc1\n"
        "buffer_load_dwordx4 a[184:187], v0, %[srd], s55 offen offset:2048 sc1\n"
        "buffer_load_dwordx4 a[188:191], v0, %[srd], s55 offen offset:3072 sc1\n"
        "s_add_u32 s54, s72, 2621440\n"
        "buffer_load_dwordx4 a[192:195], v0, %[srd], s54 offen offset:0 sc1\n"
        "buffer_load_dwordx4 a[196:199], v0, %[srd], s54 offen offset:1024 sc1\n"
        "buffer_load_dwordx4 a[200:203], v0, %[srd], s54 offen offset:2048 sc1\n"
        "buffer_load_dwordx4 a[204:207], v0, %[srd], s54 offen offset:3072 sc1\n"
        "s_add_u32 s54, s72, 2625536\n"
        "buffer_load_dwordx4 a[208:211], v0, %[srd], s54 offen offset:0 sc1\n"
        "buffer_load_dwordx4 a[212:215], v0, %[srd], s54 offen offset:1024 sc1\n"
        "buffer_load_dwordx4 a[216:219], v0, %[srd], s54 offen offset:2048 sc1\n"
        "buffer_load_dwordx4 a[220:223], v0, %[srd], s54 offen offset:3072 sc1\n"
        "s_lshr_b32 s54, s81, 2\n"
        "s_add_u32 s54, s54, s83\n"
        "s_add_u32 s54, s54, 0\n"
        "s_and_b32 s54, s54, 0x3fff\n"
        "s_add_u32 s54, s54, s84\n"
        "buffer_load_dwordx4 a[64:67], v0, %[srd], s54 offen sc1\n"
        "buffer_load_dwordx4 a[68:71], v0, %[srd], s54 offen offset:1024 sc1\n"
        "s_lshr_b32 s54, s81, 2\n"
        "s_add_u32 s54, s54, s83\n"
        "s_add_u32 s54, s54, 2048\n"
        "s_and_b32 s54, s54, 0x3fff\n"
        "s_add_u32 s54, s54, s84\n"
        "buffer_load_dwordx4 a[72:75], v0, %[srd], s54 offen sc1\n"
        "buffer_load_dwordx4 a[76:79], v0, %[srd], s54 offen offset:1024 sc1\n"
        "s_mov_b32 s57, 0\n"
        "Lsb1_%=:\n"
        "s_waitcnt vmcnt(0)\n"
        "v_mov_b32 v50, 0\n"
        "v_accvgpr_read_b32 v51, a0\n"
        "v_max_u32 v50, v50, v51\n"
        "v_accvgpr_read_b32 v51, a1\n"
        "v_max_u32 v50, v50, v51\n"
        "v_accvgpr_read_b32 v51, a2\n"
        "v_max_u32 v50, v50, v51\n"
        "v_accvgpr_read_b32 v51, a3\n"
        "v_max_u32 v50, v50, v51\n"
        "v_accvgpr_read_b32 v51, a4\n"
        "v_max_u32 v50, v50, v51\n"
        "v_accvgpr_read_b32 v51, a5\n"
        "v_max_u32 v50, v50, v51\n"
        "v_accvgpr_read_b32 v51, a6\n"
        "v_max_u32 v50, v50, v51\n"
        "v_accvgpr_read_b32 v51, a7\n"
        "v_max_u32 v50, v50, v51\n"
        "v_accvgpr_read_b32 v51, a8\n"
        "v_max_u32 v50, v50, v51\n"
        "v_accvgpr_read_b32 v51, a9\n"
        "v_max_u32 v50, v50, v51\n"
        "v_accvgpr_read_b32 v51, a10\n"
        "v_max_u32 v50, v50, v51\n"
        "v_accvgpr_read_b32 v51, a11\n"
        "v_max_u32 v50, v50, v51\n"
        "v_accvgpr_read_b32 v51, a12\n"
        "v_max_u32 v50, v50, v51\n"
        "v_accvgpr_read_b32 v51, a13\n"
        "v_max_u32 v50, v50, v51\n"
        "v_accvgpr_read_b32 v51, a14\n"
        "v_max_u32 v50, v50, v51\n"
        "v_accvgpr_read_b32 v51, a15\n"
        "v_max_u32 v50, v50, v51\n"
        "v_accvgpr_read_b32 v51, a16\n"
        "v_max_u32 v50, v50, v51\n"
        "v_accvgpr_read_b32 v51, a17\n"
        "v_max_u32 v50, v50, v51\n"
        "v_accvgpr_read_b32 v51, a18\n"
        "v_max_u32 v50, v50, v51\n"
        "v_accvgpr_read_b32 v51, a19\n"
        "v_max_u32 v50, v50, v51\n"
        "v_accvgpr_read_b32 v51, a20\n"
        "v_max_u32 v50, v50, v51\n"
        "v_accvgpr_read_b32 v51, a21\n"
        "v_max_u32 v50, v50, v51\n"
        "v_accvgpr_read_b32 v51, a22\n"
        "v_max_u32 v50, v50, v51\n"
        "v_accvgpr_read_b32 v51, a23\n"
        "v_max_u32 v50, v50, v51\n"
        "v_accvgpr_read_b32 v51, a24\n"
        "v_max_u32 v50, v50, v51\n"
        "v_accvgpr_read_b32 v51, a25\n"
        "v_max_u32 v50, v50, v51\n"
        "v_accvgpr_read_b32 v51, a26\n"
        "v_max_u32 v50, v50, v51\n"
        "v_accvgpr_read_b32 v51, a27\n"
        "v_max_u32 v50, v50, v51\n"
        "v_accvgpr_read_b32 v51, a28\n"
        "v_max_u32 v50, v50, v51\n"
        "v_accvgpr_read_b32 v51, a29\n"
        "v_max_u32 v50, v50, v51\n"
        "v_accvgpr_read_b32 v51, a30\n"
        "v_max_u32 v50, v50, v51\n"
        "v_accvgpr_read_b32 v51, a31\n"
        "v_max_u32 v50, v50, v51\n"
        "v_accvgpr_read_b32 v51, a32\n"
        "v_max_u32 v50, v50, v51\n"
        "v_accvgpr_read_b32 v51, a33\n"
        "v_max_u32 v50, v50, v51\n"
        "v_accvgpr_read_b32 v51, a34\n"
        "v_max_u32 v50, v50, v51\n"
        "v_accvgpr_read_b32 v51, a35\n"
        "v_max_u32 v50, v50, v51\n"
        "v_accvgpr_read_b32 v51, a36\n"
        "v_max_u32 v50, v50, v51\n"
        "v_accvgpr_read_b32 v51, a37\n"
        "v_max_u32 v50, v50, v51\n"
        "v_accvgpr_read_b32 v51, a38\n"
        "v_max_u32 v50, v50, v51\n"
        "v_accvgpr_read_b32 v51, a39\n"
        "v_max_u32 v50, v50, v51\n"
        "v_accvgpr_read_b32 v51, a40\n"
        "v_max_u32 v50, v50, v51\n"
        "v_accvgpr_read_b32 v51, a41\n"
        "v_max_u32 v50, v50, v51\n"
        "v_accvgpr_read_b32 v51, a42\n"
        "v_max_u32 v50, v50, v51\n"
        "v_accvgpr_read_b32 v51, a43\n"
        "v_max_u32 v50, v50, v51\n"
        "v_accvgpr_read_b32 v51, a44\n"
        "v_max_u32 v50, v50, v51\n"
        "v_accvgpr_read_b32 v51, a45\n"
        "v_max_u32 v50, v50, v51\n"
        "v_accvgpr_read_b32 v51, a46\n"
        "v_max_u32 v50, v50, v51\n"
        "v_accvgpr_read_b32 v51, a47\n"
        "v_max_u32 v50, v50, v51\n"
        "v_accvgpr_read_b32 v51, a48\n"
        "v_max_u32 v50, v50, v51\n"
        "v_accvgpr_read_b32 v51, a49\n"
        "v_max_u32 v50, v50, v51\n"
        "v_accvgpr_read_b32 v51, a50\n"
        "v_max_u32 v50, v50, v51\n"
        "v_accvgpr_read_b32 v51, a51\n"
        "v_max_u32 v50, v50, v51\n"
        "v_accvgpr_read_b32 v51, a52\n"
        "v_max_u32 v50, v50, v51\n"
        "v_accvgpr_read_b32 v51, a53\n"
        "v_max_u32 v50, v50, v51\n"
        "v_accvgpr_read_b32 v51, a54\n"
        "v_max_u32 v50, v50, v51\n"
        "v_accvgpr_read_b32 v51, a55\n"
        "v_max_u32 v50, v50, v51\n"
        "v_accvgpr_read_b32 v51, a56\n"
        "v_max_u32 v50, v50, v51\n"
        "v_accvgpr_read_b32 v51, a57\n"
        "v_max_u32 v50, v50, v51\n"
        "v_accvgpr_read_b32 v51, a58\n"
        "v_max_u32 v50, v50, v51\n"
        "v_accvgpr_read_b32 v51, a59\n"
        "v_max_u32 v50, v50, v51\n"
        "v_accvgpr_read_b32 v51, a60\n"
        "v_max_u32 v50, v50, v51\n"
        "v_accvgpr_read_b32 v51, a61\n"
        "v_max_u32 v50, v50, v51\n"
        "v_accvgpr_read_b32 v51, a62\n"
        "v_max_u32 v50, v50, v51\n"
        "v_accvgpr_read_b32 v51, a63\n"
        "v_max_u32 v50, v50, v51\n"
        "v_max3_u32 v50, v50, v96, v97\n"
        "v_max3_u32 v50, v50, v98, v99\n"
        "v_max3_u32 v50, v50, v100, v101\n"
        "v_max3_u32 v50, v50, v102, v103\n"
        "v_max3_u32 v50, v50, v104, v105\n"
        "v_max3_u32 v50, v50, v106, v107\n"
        "v_max3_u32 v50, v50, v108, v109\n"
        "v_max3_u32 v50, v50, v110, v111\n"
        "v_max3_u32 v50, v50, v112, v113\n"
        "v_max3_u32 v50, v50, v114, v115\n"
        "v_max3_u32 v50, v50, v116, v117\n"
        "v_max3_u32 v50, v50, v118, v119\n"
        "v_max3_u32 v50, v50, v120, v121\n"
        "v_max3_u32 v50, v50, v122, v123\n"
        "v_max3_u32 v50, v50, v124, v125\n"
        "v_max3_u32 v50, v50, v126, v127\n"
        "v_cmp_eq_u32 vcc, -1, v50\n"
        "s_cbranch_vccz Lsb2_%=\n"
        "s_add_u32 s57, s57, 1\n"
        "s_cmp_gt_u32 s57, 64\n"
        "s_cbranch_scc1 Lsb2_%=\n"
        "s_mov_b32 s58, 0xffff\n"
        "s_mov_b32 s59, 0xffff\n"
        "s_add_u32 s54, s68, 4294443008\n"
        "s_cmp_ge_u32 s69, 4\n"
        "s_cselect_b32 s54, s54, 0x7f000000\n"
        "buffer_load_dwordx4 v[96:99], v0, %[srd], s54 offen sc1\n"
        "s_mov_b64 exec, s[58:59]\n"
        "buffer_load_dwordx4 a[0:3], v12, %[srd], s54 offen sc1\n"
        "buffer_load_dwordx4 a[4:7], v12, %[srd], s54 offen offset:512 sc1\n"
        "s_mov_b64 exec, -1\n"
        "s_add_u32 s54, s68, 4294508544\n"
        "s_cmp_ge_u32 s69, 4\n"
        "s_cselect_b32 s54, s54, 0x7f000000\n"
        "buffer_load_dwordx4 v[100:103], v0, %[srd], s54 offen sc1\n"
        "s_mov_b64 exec, s[58:59]\n"
        "buffer_load_dwordx4 a[8:11], v12, %[srd], s54 offen sc1\n"
        "buffer_load_dwordx4 a[12:15], v12, %[srd], s54 offen offset:512 sc1\n"
        "s_mov_b64 exec, -1\n"
        "s_add_u32 s54, s68, 4294574080\n"
        "s_cmp_ge_u32 s69, 3\n"
        "s_cselect_b32 s54, s54, 0x7f000000\n"
        "buffer_load_dwordx4 v[104:107], v0, %[srd], s54 offen sc1\n"
        "s_mov_b64 exec, s[58:59]\n"
        "buffer_load_dwordx4 a[16:19], v12, %[srd], s54 offen sc1\n"
        "buffer_load_dwordx4 a[20:23], v12, %[srd], s54 offen offset:512 sc1\n"
        "s_mov_b64 exec, -1\n"
        "s_add_u32 s54, s68, 4294639616\n"
        "s_cmp_ge_u32 s69, 3\n"
        "s_cselect_b32 s54, s54, 0x7f000000\n"
        "buffer_load_dwordx4 v[108:111], v0, %[srd], s54 offen sc1\n"
        "s_mov_b64 exec, s[58:59]\n"
        "buffer_load_dwordx4 a[24:27], v12, %[srd], s54 offen sc1\n"
        "buffer_load_dwordx4 a[28:31], v12, %[srd], s54 offen offset:512 sc1\n"
        "s_mov_b64 exec, -1\n"
        "s_add_u32 s54, s68, 4294705152\n"
        "s_cmp_ge_u32 s69, 2\n"
        "s_cselect_b32 s54, s54, 0x7f000000\n"
        "buffer_load_dwordx4 v[112:115], v0, %[srd], s54 offen sc1\n"
        "s_mov_b64 exec, s[58:59]\n"
        "buffer_load_dwordx4 a[32:35], v12, %[srd], s54 offen sc1\n"
        "buffer_load_dwordx4 a[36:39], v12, %[srd], s54 offen offset:512 sc1\n"
        "s_mov_b64 exec, -1\n"
        "s_add_u32 s54, s68, 4294770688\n"
        "s_cmp_ge_u32 s69, 2\n"
        "s_cselect_b32 s54, s54, 0x7f000000\n"
        "buffer_load_dwordx4 v[116:119], v0, %[srd], s54 offen sc1\n"
        "s_mov_b64 exec, s[58:59]\n"
        "buffer_load_dwordx4 a[40:43], v12, %[srd], s54 offen sc1\n"
        "buffer_load_dwordx4 a[44:47], v12, %[srd], s54 offen offset:512 sc1\n"
        "s_mov_b64 exec, -1\n"
        "s_add_u32 s54, s68, 4294836224\n"
        "s_cmp_ge_u32 s69, 1\n"
        "s_cselect_b32 s54, s54, 0x7f000000\n"
        "buffer_load_dwordx4 v[120:123], v0, %[srd], s54 offen sc1\n"
        "s_mov_b64 exec, s[58:59]\n"
        "buffer_load_dwordx4 a[48:51], v12, %[srd], s54 offen sc1\n"
        "buffer_load_dwordx4 a[52:55], v12, %[srd], s54 offen offset:512 sc1\n"
        "s_mov_b64 exec, -1\n"
        "s_add_u32 s54, s68, 4294901760\n"
        "s_cmp_ge_u32 s69, 1\n"
        "s_cselect_b32 s54, s54, 0x7f000000\n"
        "buffer_load_dwordx4 v[124:127], v0, %[srd], s54 offen sc1\n"
        "s_mov_b64 exec, s[58:59]\n"
        "buffer_load_dwordx4 a[56:59], v12, %[srd], s54 offen sc1\n"
        "buffer_load_dwordx4 a[60:63], v12, %[srd], s54 offen offset:512 sc1\n"
        "s_mov_b64 exec, -1\n"
        "buffer_load_dword v249, v224, %[srd], s70 offen sc1\n"
        "s_branch Lsb1_%=\n"
        "; ---- NaN after the window steps: marker left (fetch again, same steps) or genuine\n"
        "LslowB1_%=:\n"
        "s_nop 15\n"
        "s_waitcnt vmcnt(0)\n"
        "v_mov_b32 v50, 0\n"
        "v_accvgpr_read_b32 v51, a0\n"
        "v_max_u32 v50, v50, v51\n"
        "v_accvgpr_read_b32 v51, a1\n"
        "v_max_u32 v50, v50, v51\n"
        "v_accvgpr_read_b32 v51, a2\n"
        "v_max_u32 v50, v50, v51\n"
        "v_accvgpr_read_b32 v51, a3\n"
        "v_max_u32 v50, v50, v51\n"
        "v_accvgpr_read_b32 v51, a4\n"
        "v_max_u32 v50, v50, v51\n"
        "v_accvgpr_read_b32 v51, a5\n"
        "v_max_u32 v50, v50, v51\n"
        "v_accvgpr_read_b32 v51, a6\n"
        "v_max_u32 v50, v50, v51\n"
        "v_accvgpr_read_b32 v51, a7\n"
        "v_max_u32 v50, v50, v51\n"
        "v_accvgpr_read_b32 v51, a8\n"
        "v_max_u32 v50, v50, v51\n"
        "v_accvgpr_read_b32 v51, a9\n"
        "v_max_u32 v50, v50, v51\n"
        "v_accvgpr_read_b32 v51, a10\n"
        "v_max_u32 v50, v50, v51\n"
        "v_accvgpr_read_b32 v51, a11\n"
        "v_max_u32 v50, v50, v51\n"
        "v_accvgpr_read_b32 v51, a12\n"
        "v_max_u32 v50, v50, v51\n"
        "v_accvgpr_read_b32 v51, a13\n"
        "v_max_u32 v50, v50, v51\n"
        "v_accvgpr_read_b32 v51, a14\n"
        "v_max_u32 v50, v50, v51\n"
        "v_accvgpr_read_b32 v51, a15\n"
        "v_max_u32 v50, v50, v51\n"
        "v_accvgpr_read_b32 v51, a16\n"
        "v_max_u32 v50, v50, v51\n"
        "v_accvgpr_read_b32 v51, a17\n"
        "v_max_u32 v50, v50, v51\n"
        "v_accvgpr_read_b32 v51, a18\n"
        "v_max_u32 v50, v50, v51\n"
        "v_accvgpr_read_b32 v51, a19\n"
        "v_max_u32 v50, v50, v51\n"
        "v_accvgpr_read_b32 v51, a20\n"
        "v_max_u32 v50, v50, v51\n"
        "v_accvgpr_read_b32 v51, a21\n"
        "v_max_u32 v50, v50, v51\n"
        "v_accvgpr_read_b32 v51, a22\n"
        "v_max_u32 v50, v50, v51\n"
        "v_accvgpr_read_b32 v51, a23\n"
        "v_max_u32 v50, v50, v51\n"
        "v_accvgpr_read_b32 v51, a24\n"
        "v_max_u32 v50, v50, v51\n"
        "v_accvgpr_read_b32 v51, a25\n"
        "v_max_u32 v50, v50, v51\n"
        "v_accvgpr_read_b32 v51, a26\n"
        "v_max_u32 v50, v50, v51\n"
        "v_accvgpr_read_b32 v51, a27\n"
        "v_max_u32 v50, v50, v51\n"
        "v_accvgpr_read_b32 v51, a28\n"
        "v_max_u32 v50, v50, v51\n"
        "v_accvgpr_read_b32 v51, a29\n"
        "v_max_u32 v50, v50, v51\n"
        "v_accvgpr_read_b32 v51, a30\n"
        "v_max_u32 v50, v50, v51\n"
        "v_accvgpr_read_b32 v51, a31\n"
        "v_max_u32 v50, v50, v51\n"
        "v_accvgpr_read_b32 v51, a32\n"
        "v_max_u32 v50, v50, v51\n"
        "v_accvgpr_read_b32 v51, a33\n"
        "v_max_u32 v50, v50, v51\n"
        "v_accvgpr_read_b32 v51, a34\n"
        "v_max_u32 v50, v50, v51\n"
        "v_accvgpr_read_b32 v51, a35\n"
        "v_max_u32 v50, v50, v51\n"
        "v_accvgpr_read_b32 v51, a36\n"
        "v_max_u32 v50, v50, v51\n"
        "v_accvgpr_read_b32 v51, a37\n"
        "v_max_u32 v50, v50, v51\n"
        "v_accvgpr_read_b32 v51, a38\n"
        "v_max_u32 v50, v50, v51\n"
        "v_accvgpr_read_b32 v51, a39\n"
        "v_max_u32 v50, v50, v51\n"
        "v_accvgpr_read_b32 v51, a40\n"
        "v_max_u32 v50, v50, v51\n"
        "v_accvgpr_read_b32 v51, a41\n"
        "v_max_u32 v50, v50, v51\n"
        "v_accvgpr_read_b32 v51, a42\n"
        "v_max_u32 v50, v50, v51\n"
        "v_accvgpr_read_b32 v51, a43\n"
        "v_max_u32 v50, v50, v51\n"
        "v_accvgpr_read_b32 v51, a44\n"
        "v_max_u32 v50, v50, v51\n"
        "v_accvgpr_read_b32 v51, a45\n"
        "v_max_u32 v50, v50, v51\n"
        "v_accvgpr_read_b32 v51, a46\n"
        "v_max_u32 v50, v50, v51\n"
        "v_accvgpr_read_b32 v51, a47\n"
        "v_max_u32 v50, v50, v51\n"
        "v_accvgpr_read_b32 v51, a48\n"
        "v_max_u32 v50, v50, v51\n"
        "v_accvgpr_read_b32 v51, a49\n"
        "v_max_u32 v50, v50, v51\n"
        "v_accvgpr_read_b32 v51, a50\n"
        "v_max_u32 v50, v50, v51\n"
        "v_accvgpr_read_b32 v51, a51\n"
        "v_max_u32 v50, v50, v51\n"
        "v_accvgpr_read_b32 v51, a52\n"
        "v_max_u32 v50, v50, v51\n"
        "v_accvgpr_read_b32 v51, a53\n"
        "v_max_u32 v50, v50, v51\n"
        "v_accvgpr_read_b32 v51, a54\n"
        "v_max_u32 v50, v50, v51\n"
        "v_accvgpr_read_b32 v51, a55\n"
        "v_max_u32 v50, v50, v51\n"
        "v_accvgpr_read_b32 v51, a56\n"
        "v_max_u32 v50, v50, v51\n"
        "v_accvgpr_read_b32 v51, a57\n"
        "v_max_u32 v50, v50, v51\n"
        "v_accvgpr_read_b32 v51, a58\n"
        "v_max_u32 v50, v50, v51\n"
        "v_accvgpr_read_b32 v51, a59\n"
        "v_max_u32 v50, v50, v51\n"
        "v_accvgpr_read_b32 v51, a60\n"
        "v_max_u32 v50, v50, v51\n"
        "v_accvgpr_read_b32 v51, a61\n"
        "v_max_u32 v50, v50, v51\n"
        "v_accvgpr_read_b32 v51, a62\n"
        "v_max_u32 v50, v50, v51\n"
        "v_accvgpr_read_b32 v51, a63\n"
        "v_max_u32 v50, v50, v51\n"
        "v_max3_u32 v50, v50, v96, v97\n"
        "v_max3_u32 v50, v50, v98, v99\n"
        "v_max3_u32 v50, v50, v100, v101\n"
        "v_max3_u32 v50, v50, v102, v103\n"
        "v_max3_u32 v50, v50, v104, v105\n"
        "v_max3_u32 v50, v50, v106, v107\n"
        "v_max3_u32 v50, v50, v108, v109\n"
        "v_max3_u32 v50, v50, v110, v111\n"
        "v_max3_u32 v50, v50, v112, v113\n"
        "v_max3_u32 v50, v50, v114, v115\n"
        "v_max3_u32 v50, v50, v116, v117\n"
        "v_max3_u32 v50, v50, v118, v119\n"
        "v_max3_u32 v50, v50, v120, v121\n"
        "v_max3_u32 v50, v50, v122, v123\n"
        "v_max3_u32 v50, v50, v124, v125\n"
        "v_max3_u32 v50, v50, v126, v127\n"
        "v_cmp_eq_u32 vcc, -1, v50\n"
        "s_cbranch_vccz Lsb2_%=\n"
        "s_add_u32 s57, s57, 1\n"
        "s_cmp_gt_u32 s57, 64\n"
        "s_cbranch_scc1 Lsb2_%=\n"
        "s_mov_b32 s58, 0xffff\n"
        "s_mov_b32 s59, 0xffff\n"
        "s_add_u32 s54, s68, 4294443008\n"
        "s_cmp_ge_u32 s69, 4\n"
        "s_cselect_b32 s54, s54, 0x7f000000\n"
        "buffer_load_dwordx4 v[96:99], v0, %[srd], s54 offen sc1\n"
        "s_mov_b64 exec, s[58:59]\n"
        "buffer_load_dwordx4 a[0:3], v12, %[srd], s54 offen sc1\n"
        "buffer_load_dwordx4 a[4:7], v12, %[srd], s54 offen offset:512 sc1\n"
        "s_mov_b64 exec, -1\n"
        "s_add_u32 s54, s68, 4294508544\n"
        "s_cmp_ge_u32 s69, 4\n"
        "s_cselect_b32 s54, s54, 0x7f000000\n"
        "buffer_load_dwordx4 v[100:103], v0, %[srd], s54 offen sc1\n"
        "s_mov_b64 exec, s[58:59]\n"
        "buffer_load_dwordx4 a[8:11], v12, %[srd], s54 offen sc1\n"
        "buffer_load_dwordx4 a[12:15], v12, %[srd], s54 offen offset:512 sc1\n"
        "s_mov_b64 exec, -1\n"
        "s_add_u32 s54, s68, 4294574080\n"
        "s_cmp_ge_u32 s69, 3\n"
        "s_cselect_b32 s54, s54, 0x7f000000\n"
        "buffer_load_dwordx4 v[104:107], v0, %[srd], s54 offen sc1\n"
        "s_mov_b64 exec, s[58:59]\n"
        "buffer_load_dwordx4 a[16:19], v12, %[srd], s54 offen sc1\n"
        "buffer_load_dwordx4 a[20:23], v12, %[srd], s54 offen offset:512 sc1\n"
        "s_mov_b64 exec, -1\n"
        "s_add_u32 s54, s68, 4294639616\n"
        "s_cmp_ge_u32 s69, 3\n"
        "s_cselect_b32 s54, s54, 0x7f000000\n"
        "buffer_load_dwordx4 v[108:111], v0, %[srd], s54 offen sc1\n"
        "s_mov_b64 exec, s[58:59]\n"
        "buffer_load_dwordx4 a[24:27], v12, %[srd], s54 offen sc1\n"
        "buffer_load_dwordx4 a[28:31], v12, %[srd], s54 offen offset:512 sc1\n"
        "s_mov_b64 exec, -1\n"
        "s_add_u32 s54, s68, 4294705152\n"
        "s_cmp_ge_u32 s69, 2\n"
        "s_cselect_b32 s54, s54, 0x7f000000\n"
        "buffer_load_dwordx4 v[112:115], v0, %[srd], s54 offen sc1\n"
        "s_mov_b64 exec, s[58:59]\n"
        "buffer_load_dwordx4 a[32:35], v12, %[srd], s54 offen sc1\n"
        "buffer_load_dwordx4 a[36:39], v12, %[srd], s54 offen offset:512 sc1\n"
        "s_mov_b64 exec, -1\n"
        "s_add_u32 s54, s68, 4294770688\n"
        "s_cmp_ge_u32 s69, 2\n"
        "s_cselect_b32 s54, s54, 0x7f000000\n"
        "buffer_load_dwordx4 v[116:119], v0, %[srd], s54 offen sc1\n"
        "s_mov_b64 exec, s[58:59]\n"
        "buffer_load_dwordx4 a[40:43], v12, %[srd], s54 offen sc1\n"
        "buffer_load_dwordx4 a[44:47], v12, %[srd], s54 offen offset:512 sc1\n"
        "s_mov_b64 exec, -1\n"
        "s_add_u32 s54, s68, 4294836224\n"
        "s_cmp_ge_u32 s69, 1\n"
        "s_cselect_b32 s54, s54, 0x7f000000\n"
        "buffer_load_dwordx4 v[120:123], v0, %[srd], s54 offen sc1\n"
        "s_mov_b64 exec, s[58:59]\n"
        "buffer_load_dwordx4 a[48:51], v12, %[srd], s54 offen sc1\n"
        "buffer_load_dwordx4 a[52:55], v12, %[srd], s54 offen offset:512 sc1\n"
        "s_mov_b64 exec, -1\n"
        "s_add_u32 s54, s68, 4294901760\n"
        "s_cmp_ge_u32 s69, 1\n"
        "s_cselect_b32 s54, s54, 0x7f000000\n"
        "buffer_load_dwordx4 v[124:127], v0, %[srd], s54 offen sc1\n"
        "s_mov_b64 exec, s[58:59]\n"
        "buffer_load_dwordx4 a[56:59], v12, %[srd], s54 offen sc1\n"
        "buffer_load_dwordx4 a[60:63], v12, %[srd], s54 offen offset:512 sc1\n"
        "s_mov_b64 exec, -1\n"
        "buffer_load_dword v249, v224, %[srd], s70 offen sc1\n"
        "s_waitcnt vmcnt(0)\n"
        "v_mfma_f32_32x32x16_bf16 v[160:175], v[32:35], v[32:35], 0\n"
        "v_mfma_f32_32x32x16_bf16 v[176:191], v[32:35], v[32:35], 0\n"
        "v_mfma_f32_32x32x16_bf16 v[128:143], v[200:203], v[192:195], v[16:31]\n"
        "s_nop 15\n"
        "v_exp_f32 v128, v128\n"
        "v_exp_f32 v129, v129\n"
        "v_exp_f32 v130, v130\n"
        "v_exp_f32 v131, v131\n"
        "v_exp_f32 v132, v132\n"
        "v_exp_f32 v133, v133\n"
        "v_mfma_f32_32x32x16_bf16 v[144:159], v[200:203], v[196:199], 0\n"
        "v_exp_f32 v134, v134\n"
        "v_exp_f32 v135, v135\n"
        "v_exp_f32 v136, v136\n"
        "v_exp_f32 v137, v137\n"
        "v_exp_f32 v138, v138\n"
        "v_smfmac_f32_32x32x32_bf16 v[160:175], v[208:211], v[128:135], v253\n"
        "v_exp_f32 v139, v139\n"
        "v_exp_f32 v140, v140\n"
        "v_exp_f32 v141, v141\n"
        "v_exp_f32 v142, v142\n"
        "v_exp_f32 v143, v143\n"
        "s_nop 0\n"
        "v_exp_f32 v144, v144\n"
        "v_exp_f32 v145, v145\n"
        "v_smfmac_f32_32x32x32_bf16 v[160:175], v[212:215], v[136:143], v253\n"
        "v_exp_f32 v146, v146\n"
        "v_exp_f32 v147, v147\n"
        "v_exp_f32 v148, v148\n"
        "v_exp_f32 v149, v149\n"
        "v_mfma_f32_32x32x16_bf16 v[128:143], v[204:207], v[196:199], v[16:31]\n"
        "v_exp_f32 v150, v150\n"
        "v_exp_f32 v151, v151\n"
        "v_exp_f32 v152, v152\n"
        "v_exp_f32 v153, v153\n"
        "v_exp_f32 v154, v154\n"
        "v_smfmac_f32_32x32x32_bf16 v[176:191], v[208:211], v[144:151], v253\n"
        "v_exp_f32 v155, v155\n"
        "v_exp_f32 v156, v156\n"
        "v_exp_f32 v157, v157\n"
        "v_exp_f32 v158, v158\n"
        "v_exp_f32 v159, v159\n"
        "s_nop 0\n"
        "v_exp_f32 v128, v128\n"
        "v_exp_f32 v129, v129\n"
        "v_smfmac_f32_32x32x32_bf16 v[176:191], v[212:215], v[152:159], v253\n"
        "v_exp_f32 v130, v130\n"
        "v_exp_f32 v131, v131\n"
        "v_exp_f32 v132, v132\n"
        "v_exp_f32 v133, v133\n"
        "v_exp_f32 v134, v134\n"
        "v_exp_f32 v135, v135\n"
        "v_exp_f32 v136, v136\n"
        "v_exp_f32 v137, v137\n"
        "v_exp_f32 v138, v138\n"
        "v_smfmac_f32_32x32x32_bf16 v[176:191], v[216:219], v[128:135], v253\n"
        "v_exp_f32 v139, v139\n"
        "v_exp_f32 v140, v140\n"
        "v_exp_f32 v141, v141\n"
        "v_exp_f32 v142, v142\n"
        "v_exp_f32 v143, v143\n"
        "s_nop 1\n"
        "v_smfmac_f32_32x32x32_bf16 v[176:191], v[220:223], v[136:143], v253\n"
        "s_waitcnt vmcnt(21)\n"
        "s_nop 1\n"
        "v_mfma_f32_32x32x16_bf16 v[128:143], v[96:99], v[192:195], v[56:71]\n"
        "s_nop 15\n"
        "v_exp_f32 v128, v128\n"
        "s_add_u32 s55, s72, 2097152\n"
        "v_exp_f32 v129, v129\n"
        "buffer_load_dwordx4 a[128:131], v0, %[srd], s55 offen offset:0 sc1\n"
        "v_exp_f32 v130, v130\n"
        "v_exp_f32 v131, v131\n"
        "v_exp_f32 v132, v132\n"
        "v_exp_f32 v133, v133\n"
        "s_waitcnt vmcnt(19)\n"
        "v_mfma_f32_32x32x16_bf16 v[144:159], v[100:103], v[192:195], 0\n"
        "v_exp_f32 v134, v134\n"
        "v_exp_f32 v135, v135\n"
        "v_exp_f32 v136, v136\n"
        "v_exp_f32 v137, v137\n"
        "v_exp_f32 v138, v138\n"
        "v_smfmac_f32_32x32x32_bf16 v[160:175], a[0:3], v[128:135], v253\n"
        "buffer_load_dwordx4 a[132:135], v0, %[srd], s55 offen offset:1024 sc1\n"
        "v_exp_f32 v139, v139\n"
        "v_exp_f32 v140, v140\n"
        "v_exp_f32 v141, v141\n"
        "v_exp_f32 v142, v142\n"
        "v_exp_f32 v143, v143\n"
        "v_exp_f32 v144, v144\n"
        "v_exp_f32 v145, v145\n"
        "v_smfmac_f32_32x32x32_bf16 v[160:175], a[4:7], v[136:143], v253\n"
        "buffer_load_dwordx4 a[136:139], v0, %[srd], s55 offen offset:2048 sc1\n"
        "v_exp_f32 v146, v146\n"
        "v_exp_f32 v147, v147\n"
        "v_exp_f32 v148, v148\n"
        "v_exp_f32 v149, v149\n"
        "v_mfma_f32_32x32x16_bf16 v[128:143], v[100:103], v[196:199], v[56:71]\n"
        "v_exp_f32 v150, v150\n"
        "v_exp_f32 v151, v151\n"
        "v_exp_f32 v152, v152\n"
        "v_exp_f32 v153, v153\n"
        "v_exp_f32 v154, v154\n"
        "v_smfmac_f32_32x32x32_bf16 v[160:175], a[8:11], v[144:151], v253\n"
        "buffer_load_dwordx4 a[140:143], v0, %[srd], s55 offen offset:3072 sc1\n"
        "v_exp_f32 v155, v155\n"
        "v_exp_f32 v156, v156\n"
        "v_exp_f32 v157, v157\n"
        "v_exp_f32 v158, v158\n"
        "v_exp_f32 v159, v159\n"
        "v_exp_f32 v128, v128\n"
        "s_add_u32 s55, s72, 2101248\n"
        "v_exp_f32 v129, v129\n"
        "v_smfmac_f32_32x32x32_bf16 v[160:175], a[12:15], v[152:159], v253\n"
        "buffer_load_dwordx4 a[144:147], v0, %[srd], s55 offen offset:0 sc1\n"
        "v_exp_f32 v130, v130\n"
        "v_exp_f32 v131, v131\n"
        "v_exp_f32 v132, v132\n"
        "v_exp_f32 v133, v133\n"
        "s_waitcnt vmcnt(20)\n"
        "v_mfma_f32_32x32x16_bf16 v[144:159], v[104:107], v[192:195], 0\n"
        "v_exp_f32 v134, v134\n"
        "v_exp_f32 v135, v135\n"
        "v_exp_f32 v136, v136\n"
        "v_exp_f32 v137, v137\n"
        "v_exp_f32 v138, v138\n"
        "v_smfmac_f32_32x32x32_bf16 v[176:191], a[8:11], v[128:135], v253\n"
        "buffer_load_dwordx4 a[148:151], v0, %[srd], s55 offen offset:1024 sc1\n"
        "v_exp_f32 v139, v139\n"
        "v_exp_f32 v140, v140\n"
        "v_exp_f32 v141, v141\n"
        "v_exp_f32 v142, v142\n"
        "v_exp_f32 v143, v143\n"
        "v_exp_f32 v144, v144\n"
        "v_exp_f32 v145, v145\n"
        "v_smfmac_f32_32x32x32_bf16 v[176:191], a[12:15], v[136:143], v253\n"
        "buffer_load_dwordx4 a[152:155], v0, %[srd], s55 offen offset:2048 sc1\n"
        "v_exp_f32 v146, v146\n"
        "v_exp_f32 v147, v147\n"
        "v_exp_f32 v148, v148\n"
        "v_exp_f32 v149, v149\n"
        "v_mfma_f32_32x32x16_bf16 v[128:143], v[104:107], v[196:199], 0\n"
        "v_exp_f32 v150, v150\n"
        "v_exp_f32 v151, v151\n"
        "v_exp_f32 v152, v152\n"
        "v_exp_f32 v153, v153\n"
        "v_exp_f32 v154, v154\n"
        "v_smfmac_f32_32x32x32_bf16 v[160:175], a[16:19], v[144:151], v253\n"
        "buffer_load_dwordx4 a[156:159], v0, %[srd], s55 offen offset:3072 sc1\n"
        "v_exp_f32 v155, v155\n"
        "v_exp_f32 v156, v156\n"
        "v_exp_f32 v157, v157\n"
        "v_exp_f32 v158, v158\n"
        "v_exp_f32 v159, v159\n"
        "v_exp_f32 v128, v128\n"
        "s_add_u32 s55, s72, 2105344\n"
        "v_exp_f32 v129, v129\n"
        "v_smfmac_f32_32x32x32_bf16 v[160:175], a[20:23], v[152:159], v253\n"
        "buffer_load_dwordx4 a[160:163], v0, %[srd], s55 offen offset:0 sc1\n"
        "v_exp_f32 v130, v130\n"
        "v_exp_f32 v131, v131\n"
        "v_exp_f32 v132, v132\n"
        "v_exp_f32 v133, v133\n"
        "s_waitcnt vmcnt(21)\n"
        "v_mfma_f32_32x32x16_bf16 v[144:159], v[108:111], v[192:195], 0\n"
        "v_exp_f32 v134, v134\n"
        "v_exp_f32 v135, v135\n"
        "v_exp_f32 v136, v136\n"
        "v_exp_f32 v137, v137\n"
        "v_exp_f32 v138, v138\n"
        "v_smfmac_f32_32x32x32_bf16 v[176:191], a[16:19], v[128:135], v253\n"
        "buffer_load_dwordx4 a[164:167], v0, %[srd], s55 offen offset:1024 sc1\n"
        "v_exp_f32 v139, v139\n"
        "v_exp_f32 v140, v140\n"
        "v_exp_f32 v141, v141\n"
        "v_exp_f32 v142, v142\n"
        "v_exp_f32 v143, v143\n"
        "v_exp_f32 v144, v144\n"
        "v_exp_f32 v145, v145\n"
        "v_smfmac_f32_32x32x32_bf16 v[176:191], a[20:23], v[136:143], v253\n"
        "buffer_load_dwordx4 a[168:171], v0, %[srd], s55 offen offset:2048 sc1\n"
        "v_exp_f32 v146, v146\n"
        "v_exp_f32 v147, v147\n"
        "v_exp_f32 v148, v148\n"
        "v_exp_f32 v149, v149\n"
        "v_mfma_f32_32x32x16_bf16 v[128:143], v[108:111], v[196:199], 0\n"
        "v_exp_f32 v150, v150\n"
        "v_exp_f32 v151, v151\n"
        "v_exp_f32 v152, v152\n"
        "v_exp_f32 v153, v153\n"
        "v_exp_f32 v154, v154\n"
        "v_smfmac_f32_32x32x32_bf16 v[160:175], a[24:27], v[144:151], v253\n"
        "buffer_load_dwordx4 a[172:175], v0, %[srd], s55 offen offset:3072 sc1\n"
        "v_exp_f32 v155, v155\n"
        "v_exp_f32 v156, v156\n"
        "v_exp_f32 v157, v157\n"
        "v_exp_f32 v158, v158\n"
        "v_exp_f32 v159, v159\n"
        "v_exp_f32 v128, v128\n"
        "s_add_u32 s55, s72, 2109440\n"
        "v_exp_f32 v129, v129\n"
        "v_smfmac_f32_32x32x32_bf16 v[160:175], a[28:31], v[152:159], v253\n"
        "buffer_load_dwordx4 a[176:179], v0, %[srd], s55 offen offset:0 sc1\n"
        "v_exp_f32 v130, v130\n"
        "v_exp_f32 v131, v131\n"
        "v_exp_f32 v132, v132\n"
        "v_exp_f32 v133, v133\n"
        "s_waitcnt vmcnt(22)\n"
        "v_mfma_f32_32x32x16_bf16 v[144:159], v[112:115], v[192:195], 0\n"
        "v_exp_f32 v134, v134\n"
        "v_exp_f32 v135, v135\n"
        "v_exp_f32 v136, v136\n"
        "v_exp_f32 v137, v137\n"
        "v_exp_f32 v138, v138\n"
        "v_smfmac_f32_32x32x32_bf16 v[176:191], a[24:27], v[128:135], v253\n"
        "buffer_load_dwordx4 a[180:183], v0, %[srd], s55 offen offset:1024 sc1\n"
        "v_exp_f32 v139, v139\n"
        "v_exp_f32 v140, v140\n"
        "v_exp_f32 v141, v141\n"
        "v_exp_f32 v142, v142\n"
        "v_exp_f32 v143, v143\n"
        "v_exp_f32 v144, v144\n"
        "v_exp_f32 v145, v145\n"
        "v_smfmac_f32_32x32x32_bf16 v[176:191], a[28:31], v[136:143], v253\n"
        "buffer_load_dwordx4 a[184:187], v0, %[srd], s55 offen offset:2048 sc1\n"
        "v_exp_f32 v146, v146\n"
        "v_exp_f32 v147, v147\n"
        "v_exp_f32 v148, v148\n"
        "v_exp_f32 v149, v149\n"
        "v_mfma_f32_32x32x16_bf16 v[128:143], v[112:115], v[196:199], 0\n"
        "v_exp_f32 v150, v150\n"
        "v_exp_f32 v151, v151\n"
        "v_exp_f32 v152, v152\n"
        "v_exp_f32 v153, v153\n"
        "v_exp_f32 v154, v154\n"
        "v_smfmac_f32_32x32x32_bf16 v[160:175], a[32:35], v[144:151], v253\n"
        "buffer_load_dwordx4 a[188:191], v0, %[srd], s55 offen offset:3072 sc1\n"
        "v_exp_f32 v155, v155\n"
        "v_exp_f32 v156, v156\n"
        "v_exp_f32 v157, v157\n"
        "v_exp_f32 v158, v158\n"
        "v_exp_f32 v159, v159\n"
        "v_exp_f32 v128, v128\n"
        "s_add_u32 s54, s72, 2621440\n"
        "v_exp_f32 v129, v129\n"
        "v_smfmac_f32_32x32x32_bf16 v[160:175], a[36:39], v[152:159], v253\n"
        "buffer_load_dwordx4 a[192:195], v0, %[srd], s54 offen offset:0 sc1\n"
        "v_exp_f32 v130, v130\n"
        "v_exp_f32 v131, v131\n"
        "v_exp_f32 v132, v132\n"
        "v_exp_f32 v133, v133\n"
        "s_waitcnt vmcnt(23)\n"
        "v_mfma_f32_32x32x16_bf16 v[144:159], v[116:119], v[192:195], 0\n"
        "v_exp_f32 v134, v134\n"
        "v_exp_f32 v135, v135\n"
        "v_exp_f32 v136, v136\n"
        "v_exp_f32 v137, v137\n"
        "v_exp_f32 v138, v138\n"
        "v_smfmac_f32_32x32x32_bf16 v[176:191], a[32:35], v[128:135], v253\n"
        "buffer_load_dwordx4 a[196:199], v0, %[srd], s54 offen offset:1024 sc1\n"
        "v_exp_f32 v139, v139\n"
        "v_exp_f32 v140, v140\n"
        "v_exp_f32 v141, v141\n"
        "v_exp_f32 v142, v142\n"
        "v_exp_f32 v143, v143\n"
        "v_exp_f32 v144, v144\n"
        "v_exp_f32 v145, v145\n"
        "v_smfmac_f32_32x32x32_bf16 v[176:191], a[36:39], v[136:143], v253\n"
        "buffer_load_dwordx4 a[200:203], v0, %[srd], s54 offen offset:2048 sc1\n"
        "v_exp_f32 v146, v146\n"
        "v_exp_f32 v147, v147\n"
        "v_exp_f32 v148, v148\n"
        "v_exp_f32 v149, v149\n"
        "v_mfma_f32_32x32x16_bf16 v[128:143], v[116:119], v[196:199], 0\n"
        "v_exp_f32 v150, v150\n"
        "v_exp_f32 v151, v151\n"
        "v_exp_f32 v152, v152\n"
        "v_exp_f32 v153, v153\n"
        "v_exp_f32 v154, v154\n"
        "v_smfmac_f32_32x32x32_bf16 v[160:175], a[40:43], v[144:151], v253\n"
        "buffer_load_dwordx4 a[204:207], v0, %[srd], s54 offen offset:3072 sc1\n"
        "v_exp_f32 v155, v155\n"
        "v_exp_f32 v156, v156\n"
        "v_exp_f32 v157, v157\n"
        "v_exp_f32 v158, v158\n"
        "v_exp_f32 v159, v159\n"
        "v_exp_f32 v128, v128\n"
        "s_add_u32 s54, s72, 2625536\n"
        "v_exp_f32 v129, v129\n"
        "v_smfmac_f32_32x32x32_bf16 v[160:175], a[44:47], v[152:159], v253\n"
        "buffer_load_dwordx4 a[208:211], v0, %[srd], s54 offen offset:0 sc1\n"
        "v_exp_f32 v130, v130\n"
        "v_exp_f32 v131, v131\n"
        "v_exp_f32 v132, v132\n"
        "v_exp_f32 v133, v133\n"
        "s_waitcnt vmcnt(24)\n"
        "v_mfma_f32_32x32x16_bf16 v[144:159], v[120:123], v[192:195], 0\n"
        "v_exp_f32 v134, v134\n"
        "v_exp_f32 v135, v135\n"
        "v_exp_f32 v136, v136\n"
        "v_exp_f32 v137, v137\n"
        "v_exp_f32 v138, v138\n"
        "v_smfmac_f32_32x32x32_bf16 v[176:191], a[40:43], v[128:135], v253\n"
        "buffer_load_dwordx4 a[212:215], v0, %[srd], s54 offen offset:1024 sc1\n"
        "v_exp_f32 v139, v139\n"
        "v_exp_f32 v140, v140\n"
        "v_exp_f32 v141, v141\n"
        "v_exp_f32 v142, v142\n"
        "v_exp_f32 v143, v143\n"
        "v_exp_f32 v144, v144\n"
        "v_exp_f32 v145, v145\n"
        "v_smfmac_f32_32x32x32_bf16 v[176:191], a[44:47], v[136:143], v253\n"
        "buffer_load_dwordx4 a[216:219], v0, %[srd], s54 offen offset:2048 sc1\n"
        "v_exp_f32 v146, v146\n"
        "v_exp_f32 v147, v147\n"
        "v_exp_f32 v148, v148\n"
        "v_exp_f32 v149, v149\n"
        "v_mfma_f32_32x32x16_bf16 v[128:143], v[120:123], v[196:199], 0\n"
        "v_exp_f32 v150, v150\n"
        "v_exp_f32 v151, v151\n"
        "v_exp_f32 v152, v152\n"
        "v_exp_f32 v153, v153\n"
        "v_exp_f32 v154, v154\n"
        "v_smfmac_f32_32x32x32_bf16 v[160:175], a[48:51], v[144:151], v253\n"
        "buffer_load_dwordx4 a[220:223], v0, %[srd], s54 offen offset:3072 sc1\n"
        "v_exp_f32 v155, v155\n"
        "v_exp_f32 v156, v156\n"
        "v_exp_f32 v157, v157\n"
        "v_exp_f32 v158, v158\n"
        "v_exp_f32 v159, v159\n"
        "v_exp_f32 v128, v128\n"
        "s_lshr_b32 s54, s81, 2\n"
        "s_add_u32 s54, s54, s83\n"
        "s_add_u32 s54, s54, 0\n"
        "s_and_b32 s54, s54, 0x3fff\n"
        "s_add_u32 s54, s54, s84\n"
        "v_exp_f32 v129, v129\n"
        "v_smfmac_f32_32x32x32_bf16 v[160:175], a[52:55], v[152:159], v253\n"
        "buffer_load_dwordx4 a[64:67], v0, %[srd], s54 offen sc1\n"
        "v_exp_f32 v130, v130\n"
        "v_exp_f32 v131, v131\n"
        "v_exp_f32 v132, v132\n"
        "v_exp_f32 v133, v133\n"
        "s_waitcnt vmcnt(25)\n"
        "v_mfma_f32_32x32x16_bf16 v[144:159], v[124:127], v[192:195], 0\n"
        "v_exp_f32 v134, v134\n"
        "v_exp_f32 v135, v135\n"
        "v_exp_f32 v136, v136\n"
        "v_exp_f32 v137, v137\n"
        "v_exp_f32 v138, v138\n"
        "v_smfmac_f32_32x32x32_bf16 v[176:191], a[48:51], v[128:135], v253\n"
        "buffer_load_dwordx4 a[68:71], v0, %[srd], s54 offen offset:1024 sc1\n"
        "v_exp_f32 v139, v139\n"
        "v_exp_f32 v140, v140\n"
        "v_exp_f32 v141, v141\n"
        "v_exp_f32 v142, v142\n"
        "v_exp_f32 v143, v143\n"
        "v_exp_f32 v144, v144\n"
        "s_lshr_b32 s54, s81, 2\n"
        "s_add_u32 s54, s54, s83\n"
        "s_add_u32 s54, s54, 2048\n"
        "s_and_b32 s54, s54, 0x3fff\n"
        "s_add_u32 s54, s54, s84\n"
        "v_exp_f32 v145, v145\n"
        "v_smfmac_f32_32x32x32_bf16 v[176:191], a[52:55], v[136:143], v253\n"
        "buffer_load_dwordx4 a[72:75], v0, %[srd], s54 offen sc1\n"
        "v_exp_f32 v146, v146\n"
        "v_exp_f32 v147, v147\n"
        "v_exp_f32 v148, v148\n"
        "v_exp_f32 v149, v149\n"
        "v_mfma_f32_32x32x16_bf16 v[128:143], v[124:127], v[196:199], 0\n"
        "v_exp_f32 v150, v150\n"
        "v_exp_f32 v151, v151\n"
        "v_exp_f32 v152, v152\n"
        "v_exp_f32 v153, v153\n"
        "v_exp_f32 v154, v154\n"
        "v_smfmac_f32_32x32x32_bf16 v[160:175], a[56:59], v[144:151], v253\n"
        "buffer_load_dwordx4 a[76:79], v0, %[srd], s54 offen offset:1024 sc1\n"
        "v_exp_f32 v155, v155\n"
        "v_exp_f32 v156, v156\n"
        "v_exp_f32 v157, v157\n"
        "v_exp_f32 v158, v158\n"
        "v_exp_f32 v159, v159\n"
        "v_exp_f32 v128, v128\n"
        "v_exp_f32 v129, v129\n"
        "v_smfmac_f32_32x32x32_bf16 v[160:175], a[60:63], v[152:159], v253\n"
        "v_exp_f32 v130, v130\n"
        "v_exp_f32 v131, v131\n"
        "v_exp_f32 v132, v132\n"
        "v_exp_f32 v133, v133\n"
        "v_exp_f32 v134, v134\n"
        "v_exp_f32 v135, v135\n"
        "v_exp_f32 v136, v136\n"
        "v_exp_f32 v137, v137\n"
        "v_exp_f32 v138, v138\n"
        "v_smfmac_f32_32x32x32_bf16 v[176:191], a[56:59], v[128:135], v253\n"
        "v_exp_f32 v139, v139\n"
        "v_exp_f32 v140, v140\n"
        "v_exp_f32 v141, v141\n"
        "v_exp_f32 v142, v142\n"
        "v_exp_f32 v143, v143\n"
        "s_nop 1\n"
        "v_smfmac_f32_32x32x32_bf16 v[176:191], a[60:63], v[136:143], v253\n"
        "s_branch Lbend_%=\n"
        "Lsb2_%=:\n"
        "v_mov_b32 v50, 0xff7fffff\n"
        "v_mov_b32 v51, 0xff7fffff\n"
        "s_cmp_lt_u32 s69, 4\n"
        "s_cbranch_scc1 Lg26_%=\n"
        "s_nop 1\n"
        "v_mfma_f32_32x32x16_bf16 v[128:143], v[96:99], v[192:195], v[56:71]\n"
        "s_nop 15\n"
        "s_nop 3\n"
        "v_max3_f32 v50, v50, v128, v129\n"
        "v_max3_f32 v50, v50, v130, v131\n"
        "v_max3_f32 v50, v50, v132, v133\n"
        "v_max3_f32 v50, v50, v134, v135\n"
        "v_max3_f32 v50, v50, v136, v137\n"
        "v_max3_f32 v50, v50, v138, v139\n"
        "v_max3_f32 v50, v50, v140, v141\n"
        "v_max3_f32 v50, v50, v142, v143\n"
        "s_nop 7\n"
        "v_mfma_f32_32x32x16_bf16 v[128:143], v[100:103], v[192:195], 0\n"
        "s_nop 15\n"
        "s_nop 3\n"
        "v_max3_f32 v50, v50, v128, v129\n"
        "v_max3_f32 v50, v50, v130, v131\n"
        "v_max3_f32 v50, v50, v132, v133\n"
        "v_max3_f32 v50, v50, v134, v135\n"
        "v_max3_f32 v50, v50, v136, v137\n"
        "v_max3_f32 v50, v50, v138, v139\n"
        "v_max3_f32 v50, v50, v140, v141\n"
        "v_max3_f32 v50, v50, v142, v143\n"
        "s_nop 7\n"
        "v_mfma_f32_32x32x16_bf16 v[128:143], v[100:103], v[196:199], v[56:71]\n"
        "s_nop 15\n"
        "s_nop 3\n"
        "v_max3_f32 v51, v51, v128, v129\n"
        "v_max3_f32 v51, v51, v130, v131\n"
        "v_max3_f32 v51, v51, v132, v133\n"
        "v_max3_f32 v51, v51, v134, v135\n"
        "v_max3_f32 v51, v51, v136, v137\n"
        "v_max3_f32 v51, v51, v138, v139\n"
        "v_max3_f32 v51, v51, v140, v141\n"
        "v_max3_f32 v51, v51, v142, v143\n"
        "s_nop 7\n"
        "Lg26_%=:\n"
        "s_cmp_lt_u32 s69, 3\n"
        "s_cbranch_scc1 Lg27_%=\n"
        "s_nop 1\n"
        "v_mfma_f32_32x32x16_bf16 v[128:143], v[104:107], v[192:195], 0\n"
        "s_nop 15\n"
        "s_nop 3\n"
        "v_max3_f32 v50, v50, v128, v129\n"
        "v_max3_f32 v50, v50, v130, v131\n"
        "v_max3_f32 v50, v50, v132, v133\n"
        "v_max3_f32 v50, v50, v134, v135\n"
        "v_max3_f32 v50, v50, v136, v137\n"
        "v_max3_f32 v50, v50, v138, v139\n"
        "v_max3_f32 v50, v50, v140, v141\n"
        "v_max3_f32 v50, v50, v142, v143\n"
        "s_nop 7\n"
        "v_mfma_f32_32x32x16_bf16 v[128:143], v[104:107], v[196:199], 0\n"
        "s_nop 15\n"
        "s_nop 3\n"
        "v_max3_f32 v51, v51, v128, v129\n"
        "v_max3_f32 v51, v51, v130, v131\n"
        "v_max3_f32 v51, v51, v132, v133\n"
        "v_max3_f32 v51, v51, v134, v135\n"
        "v_max3_f32 v51, v51, v136, v137\n"
        "v_max3_f32 v51, v51, v138, v139\n"
        "v_max3_f32 v51, v51, v140, v141\n"
        "v_max3_f32 v51, v51, v142, v143\n"
        "s_nop 7\n"
        "v_mfma_f32_32x32x16_bf16 v[128:143], v[108:111], v[192:195], 0\n"
        "s_nop 15\n"
        "s_nop 3\n"
        "v_max3_f32 v50, v50, v128, v129\n"
        "v_max3_f32 v50, v50, v130, v131\n"
        "v_max3_f32 v50, v50, v132, v133\n"
        "v_max3_f32 v50, v50, v134, v135\n"
        "v_max3_f32 v50, v50, v136, v137\n"
        "v_max3_f32 v50, v50, v138, v139\n"
        "v_max3_f32 v50, v50, v140, v141\n"
        "v_max3_f32 v50, v50, v142, v143\n"
        "s_nop 7\n"
        "v_mfma_f32_32x32x16_bf16 v[128:143], v[108:111], v[196:199], 0\n"
        "s_nop 15\n"
        "s_nop 3\n"
        "v_max3_f32 v51, v51, v128, v129\n"
        "v_max3_f32 v51, v51, v130, v131\n"
        "v_max3_f32 v51, v51, v132, v133\n"
        "v_max3_f32 v51, v51, v134, v135\n"
        "v_max3_f32 v51, v51, v136, v137\n"
        "v_max3_f32 v51, v51, v138, v139\n"
        "v_max3_f32 v51, v51, v140, v141\n"
        "v_max3_f32 v51, v51, v142, v143\n"
        "s_nop 7\n"
        "Lg27_%=:\n"
        "s_cmp_lt_u32 s69, 2\n"
        "s_cbranch_scc1 Lg28_%=\n"
        "s_nop 1\n"
        "v_mfma_f32_32x32x16_bf16 v[128:143], v[112:115], v[192:195], 0\n"
        "s_nop 15\n"
        "s_nop 3\n"
        "v_max3_f32 v50, v50, v128, v129\n"
        "v_max3_f32 v50, v50, v130, v131\n"
        "v_max3_f32 v50, v50, v132, v133\n"
        "v_max3_f32 v50, v50, v134, v135\n"
        "v_max3_f32 v50, v50, v136, v137\n"
        "v_max3_f32 v50, v50, v138, v139\n"
        "v_max3_f32 v50, v50, v140, v141\n"
        "v_max3_f32 v50, v50, v142, v143\n"
        "s_nop 7\n"
        "v_mfma_f32_32x32x16_bf16 v[128:143], v[112:115], v[196:199], 0\n"
        "s_nop 15\n"
        "s_nop 3\n"
        "v_max3_f32 v51, v51, v128, v129\n"
        "v_max3_f32 v51, v51, v130, v131\n"
        "v_max3_f32 v51, v51, v132, v133\n"
        "v_max3_f32 v51, v51, v134, v135\n"
        "v_max3_f32 v51, v51, v136, v137\n"
        "v_max3_f32 v51, v51, v138, v139\n"
        "v_max3_f32 v51, v51, v140, v141\n"
        "v_max3_f32 v51, v51, v142, v143\n"
        "s_nop 7\n"
        "v_mfma_f32_32x32x16_bf16 v[128:143], v[116:119], v[192:195], 0\n"
        "s_nop 15\n"
        "s_nop 3\n"
        "v_max3_f32 v50, v50, v128, v129\n"
        "v_max3_f32 v50, v50, v130, v131\n"
        "v_max3_f32 v50, v50, v132, v133\n"
        "v_max3_f32 v50, v50, v134, v135\n"
        "v_max3_f32 v50, v50, v136, v137\n"
        "v_max3_f32 v50, v50, v138, v139\n"
        "v_max3_f32 v50, v50, v140, v141\n"
        "v_max3_f32 v50, v50, v142, v143\n"
        "s_nop 7\n"
        "v_mfma_f32_32x32x16_bf16 v[128:143], v[116:119], v[196:199], 0\n"
        "s_nop 15\n"
        "s_nop 3\n"
        "v_max3_f32 v51, v51, v128, v129\n"
        "v_max3_f32 v51, v51, v130, v131\n"
        "v_max3_f32 v51, v51, v132, v133\n"
        "v_max3_f32 v51, v51, v134, v135\n"
        "v_max3_f32 v51, v51, v136, v137\n"
        "v_max3_f32 v51, v51, v138, v139\n"
        "v_max3_f32 v51, v51, v140, v141\n"
        "v_max3_f32 v51, v51, v142, v143\n"
        "s_nop 7\n"
        "Lg28_%=:\n"
        "s_cmp_lt_u32 s69, 1\n"
        "s_cbranch_scc1 Lg29_%=\n"
        "s_nop 1\n"
        "v_mfma_f32_32x32x16_bf16 v[128:143], v[120:123], v[192:195], 0\n"
        "s_nop 15\n"
        "s_nop 3\n"
        "v_max3_f32 v50, v50, v128, v129\n"
        "v_max3_f32 v50, v50, v130, v131\n"
        "v_max3_f32 v50, v50, v132, v133\n"
        "v_max3_f32 v50, v50, v134, v135\n"
        "v_max3_f32 v50, v50, v136, v137\n"
        "v_max3_f32 v50, v50, v138, v139\n"
        "v_max3_f32 v50, v50, v140, v141\n"
        "v_max3_f32 v50, v50, v142, v143\n"
        "s_nop 7\n"
        "v_mfma_f32_32x32x16_bf16 v[128:143], v[120:123], v[196:199], 0\n"
        "s_nop 15\n"
        "s_nop 3\n"
        "v_max3_f32 v51, v51, v128, v129\n"
        "v_max3_f32 v51, v51, v130, v131\n"
        "v_max3_f32 v51, v51, v132, v133\n"
        "v_max3_f32 v51, v51, v134, v135\n"
        "v_max3_f32 v51, v51, v136, v137\n"
        "v_max3_f32 v51, v51, v138, v139\n"
        "v_max3_f32 v51, v51, v140, v141\n"
        "v_max3_f32 v51, v51, v142, v143\n"
        "s_nop 7\n"
        "v_mfma_f32_32x32x16_bf16 v[128:143], v[124:127], v[192:195], 0\n"
        "s_nop 15\n"
        "s_nop 3\n"
        "v_max3_f32 v50, v50, v128, v129\n"
        "v_max3_f32 v50, v50, v130, v131\n"
        "v_max3_f32 v50, v50, v132, v133\n"
        "v_max3_f32 v50, v50, v134, v135\n"
        "v_max3_f32 v50, v50, v136, v137\n"
        "v_max3_f32 v50, v50, v138, v139\n"
        "v_max3_f32 v50, v50, v140, v141\n"
        "v_max3_f32 v50, v50, v142, v143\n"
        "s_nop 7\n"
        "v_mfma_f32_32x32x16_bf16 v[128:143], v[124:127], v[196:199], 0\n"
        "s_nop 15\n"
        "s_nop 3\n"
        "v_max3_f32 v51, v51, v128, v129\n"
        "v_max3_f32 v51, v51, v130, v131\n"
        "v_max3_f32 v51, v51, v132, v133\n"
        "v_max3_f32 v51, v51, v134, v135\n"
        "v_max3_f32 v51, v51, v136, v137\n"
        "v_max3_f32 v51, v51, v138, v139\n"
        "v_max3_f32 v51, v51, v140, v141\n"
        "v_max3_f32 v51, v51, v142, v143\n"
        "s_nop 7\n"
        "Lg29_%=:\n"
        "s_nop 1\n"
        "v_mfma_f32_32x32x16_bf16 v[128:143], v[200:203], v[192:195], v[16:31]\n"
        "s_nop 15\n"
        "s_nop 3\n"
        "v_max3_f32 v50, v50, v128, v129\n"
        "v_max3_f32 v50, v50, v130, v131\n"
        "v_max3_f32 v50, v50, v132, v133\n"
        "v_max3_f32 v50, v50, v134, v135\n"
        "v_max3_f32 v50, v50, v136, v137\n"
        "v_max3_f32 v50, v50, v138, v139\n"
        "v_max3_f32 v50, v50, v140, v141\n"
        "v_max3_f32 v50, v50, v142, v143\n"
        "s_nop 7\n"
        "v_mfma_f32_32x32x16_bf16 v[128:143], v[200:203], v[196:199], 0\n"
        "s_nop 15\n"
        "s_nop 3\n"
        "v_max3_f32 v51, v51, v128, v129\n"
        "v_max3_f32 v51, v51, v130, v131\n"
        "v_max3_f32 v51, v51, v132, v133\n"
        "v_max3_f32 v51, v51, v134, v135\n"
        "v_max3_f32 v51, v51, v136, v137\n"
        "v_max3_f32 v51, v51, v138, v139\n"
        "v_max3_f32 v51, v51, v140, v141\n"
        "v_max3_f32 v51, v51, v142, v143\n"
        "s_nop 7\n"
        "v_mfma_f32_32x32x16_bf16 v[128:143], v[204:207], v[196:199], v[16:31]\n"
        "s_nop 15\n"
        "s_nop 3\n"
        "v_max3_f32 v51, v51, v128, v129\n"
        "v_max3_f32 v51, v51, v130, v131\n"
        "v_max3_f32 v51, v51, v132, v133\n"
        "v_max3_f32 v51, v51, v134, v135\n"
        "v_max3_f32 v51, v51, v136, v137\n"
        "v_max3_f32 v51, v51, v138, v139\n"
        "v_max3_f32 v51, v51, v140, v141\n"
        "v_max3_f32 v51, v51, v142, v143\n"
        "s_nop 7\n"
        "v_mov_b32 v52, v50\n"
        "v_mov_b32 v53, v51\n"
        "s_nop 0\n"
        "v_permlane32_swap_b32 v50, v52\n"
        "v_permlane32_swap_b32 v51, v53\n"
        "v_max_f32 v50, v50, v52\n"
        "v_max_f32 v51, v51, v53\n"
        "v_mfma_f32_32x32x16_bf16 v[160:175], v[32:35], v[32:35], 0\n"
        "v_mfma_f32_32x32x16_bf16 v[176:191], v[32:35], v[32:35], 0\n"
        "s_nop 15\n"
        "s_nop 1\n"
        "v_mfma_f32_32x32x16_bf16 v[128:143], v[200:203], v[192:195], v[16:31]\n"
        "s_nop 15\n"
        "s_nop 3\n"
        "v_sub_f32 v128, v128, v50\n"
        "v_sub_f32 v129, v129, v50\n"
        "v_sub_f32 v130, v130, v50\n"
        "v_sub_f32 v131, v131, v50\n"
        "v_sub_f32 v132, v132, v50\n"
        "v_sub_f32 v133, v133, v50\n"
        "v_sub_f32 v134, v134, v50\n"
        "v_sub_f32 v135, v135, v50\n"
        "v_sub_f32 v136, v136, v50\n"
        "v_sub_f32 v137, v137, v50\n"
        "v_sub_f32 v138, v138, v50\n"
        "v_sub_f32 v139, v139, v50\n"
        "v_sub_f32 v140, v140, v50\n"
        "v_sub_f32 v141, v141, v50\n"
        "v_sub_f32 v142, v142, v50\n"
        "v_sub_f32 v143, v143, v50\n"
        "v_exp_f32 v128, v128\n"
        "v_exp_f32 v129, v129\n"
        "v_exp_f32 v130, v130\n"
        "v_exp_f32 v131, v131\n"
        "v_exp_f32 v132, v132\n"
        "v_exp_f32 v133, v133\n"
        "v_exp_f32 v134, v134\n"
        "v_exp_f32 v135, v135\n"
        "v_exp_f32 v136, v136\n"
        "v_exp_f32 v137, v137\n"
        "v_exp_f32 v138, v138\n"
        "v_exp_f32 v139, v139\n"
        "v_exp_f32 v140, v140\n"
        "v_exp_f32 v141, v141\n"
        "v_exp_f32 v142, v142\n"
        "v_exp_f32 v143, v143\n"
        "s_nop 3\n"
        "v_smfmac_f32_32x32x32_bf16 v[160:175], v[208:211], v[128:135], v253\n"
        "v_smfmac_f32_32x32x32_bf16 v[160:175], v[212:215], v[136:143], v253\n"
        "s_nop 15\n"
        "v_mfma_f32_32x32x16_bf16 v[128:143], v[200:203], v[196:199], 0\n"
        "s_nop 15\n"
        "s_nop 3\n"
        "v_sub_f32 v128, v128, v51\n"
        "v_sub_f32 v129, v129, v51\n"
        "v_sub_f32 v130, v130, v51\n"
        "v_sub_f32 v131, v131, v51\n"
        "v_sub_f32 v132, v132, v51\n"
        "v_sub_f32 v133, v133, v51\n"
        "v_sub_f32 v134, v134, v51\n"
        "v_sub_f32 v135, v135, v51\n"
        "v_sub_f32 v136, v136, v51\n"
        "v_sub_f32 v137, v137, v51\n"
        "v_sub_f32 v138, v138, v51\n"
        "v_sub_f32 v139, v139, v51\n"
        "v_sub_f32 v140, v140, v51\n"
        "v_sub_f32 v141, v141, v51\n"
        "v_sub_f32 v142, v142, v51\n"
        "v_sub_f32 v143, v143, v51\n"
        "v_exp_f32 v128, v128\n"
        "v_exp_f32 v129, v129\n"
        "v_exp_f32 v130, v130\n"
        "v_exp_f32 v131, v131\n"
        "v_exp_f32 v132, v132\n"
        "v_exp_f32 v133, v133\n"
        "v_exp_f32 v134, v134\n"
        "v_exp_f32 v135, v135\n"
        "v_exp_f32 v136, v136\n"
        "v_exp_f32 v137, v137\n"
        "v_exp_f32 v138, v138\n"
        "v_exp_f32 v139, v139\n"
        "v_exp_f32 v140, v140\n"
        "v_exp_f32 v141, v141\n"
        "v_exp_f32 v142, v142\n"
        "v_exp_f32 v143, v143\n"
        "s_nop 3\n"
        "v_smfmac_f32_32x32x32_bf16 v[176:191], v[208:211], v[128:135], v253\n"
        "v_smfmac_f32_32x32x32_bf16 v[176:191], v[212:215], v[136:143], v253\n"
        "s_nop 15\n"
        "v_mfma_f32_32x32x16_bf16 v[128:143], v[204:207], v[196:199], v[16:31]\n"
        "s_nop 15\n"
        "s_nop 3\n"
        "v_sub_f32 v128, v128, v51\n"
        "v_sub_f32 v129, v129, v51\n"
        "v_sub_f32 v130, v130, v51\n"
        "v_sub_f32 v131, v131, v51\n"
        "v_sub_f32 v132, v132, v51\n"
        "v_sub_f32 v133, v133, v51\n"
        "v_sub_f32 v134, v134, v51\n"
        "v_sub_f32 v135, v135, v51\n"
        "v_sub_f32 v136, v136, v51\n"
        "v_sub_f32 v137, v137, v51\n"
        "v_sub_f32 v138, v138, v51\n"
        "v_sub_f32 v139, v139, v51\n"
        "v_sub_f32 v140, v140, v51\n"
        "v_sub_f32 v141, v141, v51\n"
        "v_sub_f32 v142, v142, v51\n"
        "v_sub_f32 v143, v143, v51\n"
        "v_exp_f32 v128, v128\n"
        "v_exp_f32 v129, v129\n"
        "v_exp_f32 v130, v130\n"
        "v_exp_f32 v131, v131\n"
        "v_exp_f32 v132, v132\n"
        "v_exp_f32 v133, v133\n"
        "v_exp_f32 v134, v134\n"
        "v_exp_f32 v135, v135\n"
        "v_exp_f32 v136, v136\n"
        "v_exp_f32 v137, v137\n"
        "v_exp_f32 v138, v138\n"
        "v_exp_f32 v139, v139\n"
        "v_exp_f32 v140, v140\n"
        "v_exp_f32 v141, v141\n"
        "v_exp_f32 v142, v142\n"
        "v_exp_f32 v143, v143\n"
        "s_nop 3\n"
        "v_smfmac_f32_32x32x32_bf16 v[176:191], v[216:219], v[128:135], v253\n"
        "v_smfmac_f32_32x32x32_bf16 v[176:191], v[220:223], v[136:143], v253\n"
        "s_nop 15\n"
        "s_nop 15\n"
        "s_nop 3\n"
        "s_cmp_lt_u32 s69, 4\n"
        "s_cbranch_scc1 Lg30_%=\n"
        "s_nop 1\n"
        "v_mfma_f32_32x32x16_bf16 v[128:143], v[96:99], v[192:195], v[56:71]\n"
        "s_nop 15\n"
        "s_nop 3\n"
        "v_sub_f32 v128, v128, v50\n"
        "v_sub_f32 v129, v129, v50\n"
        "v_sub_f32 v130, v130, v50\n"
        "v_sub_f32 v131, v131, v50\n"
        "v_sub_f32 v132, v132, v50\n"
        "v_sub_f32 v133, v133, v50\n"
        "v_sub_f32 v134, v134, v50\n"
        "v_sub_f32 v135, v135, v50\n"
        "v_sub_f32 v136, v136, v50\n"
        "v_sub_f32 v137, v137, v50\n"
        "v_sub_f32 v138, v138, v50\n"
        "v_sub_f32 v139, v139, v50\n"
        "v_sub_f32 v140, v140, v50\n"
        "v_sub_f32 v141, v141, v50\n"
        "v_sub_f32 v142, v142, v50\n"
        "v_sub_f32 v143, v143, v50\n"
        "v_exp_f32 v128, v128\n"
        "v_exp_f32 v129, v129\n"
        "v_exp_f32 v130, v130\n"
        "v_exp_f32 v131, v131\n"
        "v_exp_f32 v132, v132\n"
        "v_exp_f32 v133, v133\n"
        "v_exp_f32 v134, v134\n"
        "v_exp_f32 v135, v135\n"
        "v_exp_f32 v136, v136\n"
        "v_exp_f32 v137, v137\n"
        "v_exp_f32 v138, v138\n"
        "v_exp_f32 v139, v139\n"
        "v_exp_f32 v140, v140\n"
        "v_exp_f32 v141, v141\n"
        "v_exp_f32 v142, v142\n"
        "v_exp_f32 v143, v143\n"
        "s_nop 3\n"
        "v_smfmac_f32_32x32x32_bf16 v[160:175], a[0:3], v[128:135], v253\n"
        "v_smfmac_f32_32x32x32_bf16 v[160:175], a[4:7], v[136:143], v253\n"
        "s_nop 15\n"
        "v_mfma_f32_32x32x16_bf16 v[128:143], v[100:103], v[192:195], 0\n"
        "s_nop 15\n"
        "s_nop 3\n"
        "v_sub_f32 v128, v128, v50\n"
        "v_sub_f32 v129, v129, v50\n"
        "v_sub_f32 v130, v130, v50\n"
        "v_sub_f32 v131, v131, v50\n"
        "v_sub_f32 v132, v132, v50\n"
        "v_sub_f32 v133, v133, v50\n"
        "v_sub_f32 v134, v134, v50\n"
        "v_sub_f32 v135, v135, v50\n"
        "v_sub_f32 v136, v136, v50\n"
        "v_sub_f32 v137, v137, v50\n"
        "v_sub_f32 v138, v138, v50\n"
        "v_sub_f32 v139, v139, v50\n"
        "v_sub_f32 v140, v140, v50\n"
        "v_sub_f32 v141, v141, v50\n"
        "v_sub_f32 v142, v142, v50\n"
        "v_sub_f32 v143, v143, v50\n"
        "v_exp_f32 v128, v128\n"
        "v_exp_f32 v129, v129\n"
        "v_exp_f32 v130, v130\n"
        "v_exp_f32 v131, v131\n"
        "v_exp_f32 v132, v132\n"
        "v_exp_f32 v133, v133\n"
        "v_exp_f32 v134, v134\n"
        "v_exp_f32 v135, v135\n"
        "v_exp_f32 v136, v136\n"
        "v_exp_f32 v137, v137\n"
        "v_exp_f32 v138, v138\n"
        "v_exp_f32 v139, v139\n"
        "v_exp_f32 v140, v140\n"
        "v_exp_f32 v141, v141\n"
        "v_exp_f32 v142, v142\n"
        "v_exp_f32 v143, v143\n"
        "s_nop 3\n"
        "v_smfmac_f32_32x32x32_bf16 v[160:175], a[8:11], v[128:135], v253\n"
        "v_smfmac_f32_32x32x32_bf16 v[160:175], a[12:15], v[136:143], v253\n"
        "s_nop 15\n"
        "v_mfma_f32_32x32x16_bf16 v[128:143], v[100:103], v[196:199], v[56:71]\n"
        "s_nop 15\n"
        "s_nop 3\n"
        "v_sub_f32 v128, v128, v51\n"
        "v_sub_f32 v129, v129, v51\n"
        "v_sub_f32 v130, v130, v51\n"
        "v_sub_f32 v131, v131, v51\n"
        "v_sub_f32 v132, v132, v51\n"
        "v_sub_f32 v133, v133, v51\n"
        "v_sub_f32 v134, v134, v51\n"
        "v_sub_f32 v135, v135, v51\n"
        "v_sub_f32 v136, v136, v51\n"
        "v_sub_f32 v137, v137, v51\n"
        "v_sub_f32 v138, v138, v51\n"
        "v_sub_f32 v139, v139, v51\n"
        "v_sub_f32 v140, v140, v51\n"
        "v_sub_f32 v141, v141, v51\n"
        "v_sub_f32 v142, v142, v51\n"
        "v_sub_f32 v143, v143, v51\n"
        "v_exp_f32 v128, v128\n"
        "v_exp_f32 v129, v129\n"
        "v_exp_f32 v130, v130\n"
        "v_exp_f32 v131, v131\n"
        "v_exp_f32 v132, v132\n"
        "v_exp_f32 v133, v133\n"
        "v_exp_f32 v134, v134\n"
        "v_exp_f32 v135, v135\n"
        "v_exp_f32 v136, v136\n"
        "v_exp_f32 v137, v137\n"
        "v_exp_f32 v138, v138\n"
        "v_exp_f32 v139, v139\n"
        "v_exp_f32 v140, v140\n"
        "v_exp_f32 v141, v141\n"
        "v_exp_f32 v142, v142\n"
        "v_exp_f32 v143, v143\n"
        "s_nop 3\n"
        "v_smfmac_f32_32x32x32_bf16 v[176:191], a[8:11], v[128:135], v253\n"
        "v_smfmac_f32_32x32x32_bf16 v[176:191], a[12:15], v[136:143], v253\n"
        "s_nop 15\n"
        "s_nop 15\n"
        "s_nop 3\n"
        "Lg30_%=:\n"
        "s_cmp_lt_u32 s69, 3\n"
        "s_cbranch_scc1 Lg31_%=\n"
        "s_nop 1\n"
        "v_mfma_f32_32x32x16_bf16 v[128:143], v[104:107], v[192:195], 0\n"
        "s_nop 15\n"
        "s_nop 3\n"
        "v_sub_f32 v128, v128, v50\n"
        "v_sub_f32 v129, v129, v50\n"
        "v_sub_f32 v130, v130, v50\n"
        "v_sub_f32 v131, v131, v50\n"
        "v_sub_f32 v132, v132, v50\n"
        "v_sub_f32 v133, v133, v50\n"
        "v_sub_f32 v134, v134, v50\n"
        "v_sub_f32 v135, v135, v50\n"
        "v_sub_f32 v136, v136, v50\n"
        "v_sub_f32 v137, v137, v50\n"
        "v_sub_f32 v138, v138, v50\n"
        "v_sub_f32 v139, v139, v50\n"
        "v_sub_f32 v140, v140, v50\n"
        "v_sub_f32 v141, v141, v50\n"
        "v_sub_f32 v142, v142, v50\n"
        "v_sub_f32 v143, v143, v50\n"
        "v_exp_f32 v128, v128\n"
        "v_exp_f32 v129, v129\n"
        "v_exp_f32 v130, v130\n"
        "v_exp_f32 v131, v131\n"
        "v_exp_f32 v132, v132\n"
        "v_exp_f32 v133, v133\n"
        "v_exp_f32 v134, v134\n"
        "v_exp_f32 v135, v135\n"
        "v_exp_f32 v136, v136\n"
        "v_exp_f32 v137, v137\n"
        "v_exp_f32 v138, v138\n"
        "v_exp_f32 v139, v139\n"
        "v_exp_f32 v140, v140\n"
        "v_exp_f32 v141, v141\n"
        "v_exp_f32 v142, v142\n"
        "v_exp_f32 v143, v143\n"
        "s_nop 3\n"
        "v_smfmac_f32_32x32x32_bf16 v[160:175], a[16:19], v[128:135], v253\n"
        "v_smfmac_f32_32x32x32_bf16 v[160:175], a[20:23], v[136:143], v253\n"
        "s_nop 15\n"
        "v_mfma_f32_32x32x16_bf16 v[128:143], v[104:107], v[196:199], 0\n"
        "s_nop 15\n"
        "s_nop 3\n"
        "v_sub_f32 v128, v128, v51\n"
        "v_sub_f32 v129, v129, v51\n"
        "v_sub_f32 v130, v130, v51\n"
        "v_sub_f32 v131, v131, v51\n"
        "v_sub_f32 v132, v132, v51\n"
        "v_sub_f32 v133, v133, v51\n"
        "v_sub_f32 v134, v134, v51\n"
        "v_sub_f32 v135, v135, v51\n"
        "v_sub_f32 v136, v136, v51\n"
        "v_sub_f32 v137, v137, v51\n"
        "v_sub_f32 v138, v138, v51\n"
        "v_sub_f32 v139, v139, v51\n"
        "v_sub_f32 v140, v140, v51\n"
        "v_sub_f32 v141, v141, v51\n"
        "v_sub_f32 v142, v142, v51\n"
        "v_sub_f32 v143, v143, v51\n"
        "v_exp_f32 v128, v128\n"
        "v_exp_f32 v129, v129\n"
        "v_exp_f32 v130, v130\n"
        "v_exp_f32 v131, v131\n"
        "v_exp_f32 v132, v132\n"
        "v_exp_f32 v133, v133\n"
        "v_exp_f32 v134, v134\n"
        "v_exp_f32 v135, v135\n"
        "v_exp_f32 v136, v136\n"
        "v_exp_f32 v137, v137\n"
        "v_exp_f32 v138, v138\n"
        "v_exp_f32 v139, v139\n"
        "v_exp_f32 v140, v140\n"
        "v_exp_f32 v141, v141\n"
        "v_exp_f32 v142, v142\n"
        "v_exp_f32 v143, v143\n"
        "s_nop 3\n"
        "v_smfmac_f32_32x32x32_bf16 v[176:191], a[16:19], v[128:135], v253\n"
        "v_smfmac_f32_32x32x32_bf16 v[176:191], a[20:23], v[136:143], v253\n"
        "s_nop 15\n"
        "v_mfma_f32_32x32x16_bf16 v[128:143], v[108:111], v[192:195], 0\n"
        "s_nop 15\n"
        "s_nop 3\n"
        "v_sub_f32 v128, v128, v50\n"
        "v_sub_f32 v129, v129, v50\n"
        "v_sub_f32 v130, v130, v50\n"
        "v_sub_f32 v131, v131, v50\n"
        "v_sub_f32 v132, v132, v50\n"
        "v_sub_f32 v133, v133, v50\n"
        "v_sub_f32 v134, v134, v50\n"
        "v_sub_f32 v135, v135, v50\n"
        "v_sub_f32 v136, v136, v50\n"
        "v_sub_f32 v137, v137, v50\n"
        "v_sub_f32 v138, v138, v50\n"
        "v_sub_f32 v139, v139, v50\n"
        "v_sub_f32 v140, v140, v50\n"
        "v_sub_f32 v141, v141, v50\n"
        "v_sub_f32 v142, v142, v50\n"
        "v_sub_f32 v143, v143, v50\n"
        "v_exp_f32 v128, v128\n"
        "v_exp_f32 v129, v129\n"
        "v_exp_f32 v130, v130\n"
        "v_exp_f32 v131, v131\n"
        "v_exp_f32 v132, v132\n"
        "v_exp_f32 v133, v133\n"
        "v_exp_f32 v134, v134\n"
        "v_exp_f32 v135, v135\n"
        "v_exp_f32 v136, v136\n"
        "v_exp_f32 v137, v137\n"
        "v_exp_f32 v138, v138\n"
        "v_exp_f32 v139, v139\n"
        "v_exp_f32 v140, v140\n"
        "v_exp_f32 v141, v141\n"
        "v_exp_f32 v142, v142\n"
        "v_exp_f32 v143, v143\n"
        "s_nop 3\n"
        "v_smfmac_f32_32x32x32_bf16 v[160:175], a[24:27], v[128:135], v253\n"
        "v_smfmac_f32_32x32x32_bf16 v[160:175], a[28:31], v[136:143], v253\n"
        "s_nop 15\n"
        "v_mfma_f32_32x32x16_bf16 v[128:143], v[108:111], v[196:199], 0\n"
        "s_nop 15\n"
        "s_nop 3\n"
        "v_sub_f32 v128, v128, v51\n"
        "v_sub_f32 v129, v129, v51\n"
        "v_sub_f32 v130, v130, v51\n"
        "v_sub_f32 v131, v131, v51\n"
        "v_sub_f32 v132, v132, v51\n"
        "v_sub_f32 v133, v133, v51\n"
        "v_sub_f32 v134, v134, v51\n"
        "v_sub_f32 v135, v135, v51\n"
        "v_sub_f32 v136, v136, v51\n"
        "v_sub_f32 v137, v137, v51\n"
        "v_sub_f32 v138, v138, v51\n"
        "v_sub_f32 v139, v139, v51\n"
        "v_sub_f32 v140, v140, v51\n"
        "v_sub_f32 v141, v141, v51\n"
        "v_sub_f32 v142, v142, v51\n"
        "v_sub_f32 v143, v143, v51\n"
        "v_exp_f32 v128, v128\n"
        "v_exp_f32 v129, v129\n"
        "v_exp_f32 v130, v130\n"
        "v_exp_f32 v131, v131\n"
        "v_exp_f32 v132, v132\n"
        "v_exp_f32 v133, v133\n"
        "v_exp_f32 v134, v134\n"
        "v_exp_f32 v135, v135\n"
        "v_exp_f32 v136, v136\n"
        "v_exp_f32 v137, v137\n"
        "v_exp_f32 v138, v138\n"
        "v_exp_f32 v139, v139\n"
        "v_exp_f32 v140, v140\n"
        "v_exp_f32 v141, v141\n"
        "v_exp_f32 v142, v142\n"
        "v_exp_f32 v143, v143\n"
        "s_nop 3\n"
        "v_smfmac_f32_32x32x32_bf16 v[176:191], a[24:27], v[128:135], v253\n"
        "v_smfmac_f32_32x32x32_bf16 v[176:191], a[28:31], v[136:143], v253\n"
        "s_nop 15\n"
        "s_nop 15\n"
        "s_nop 3\n"
        "Lg31_%=:\n"
        "s_cmp_lt_u32 s69, 2\n"
        "s_cbranch_scc1 Lg32_%=\n"
        "s_nop 1\n"
        "v_mfma_f32_32x32x16_bf16 v[128:143], v[112:115], v[192:195], 0\n"
        "s_nop 15\n"
        "s_nop 3\n"
        "v_sub_f32 v128, v128, v50\n"
        "v_sub_f32 v129, v129, v50\n"
        "v_sub_f32 v130, v130, v50\n"
        "v_sub_f32 v131, v131, v50\n"
        "v_sub_f32 v132, v132, v50\n"
        "v_sub_f32 v133, v133, v50\n"
        "v_sub_f32 v134, v134, v50\n"
        "v_sub_f32 v135, v135, v50\n"
        "v_sub_f32 v136, v136, v50\n"
        "v_sub_f32 v137, v137, v50\n"
        "v_sub_f32 v138, v138, v50\n"
        "v_sub_f32 v139, v139, v50\n"
        "v_sub_f32 v140, v140, v50\n"
        "v_sub_f32 v141, v141, v50\n"
        "v_sub_f32 v142, v142, v50\n"
        "v_sub_f32 v143, v143, v50\n"
        "v_exp_f32 v128, v128\n"
        "v_exp_f32 v129, v129\n"
        "v_exp_f32 v130, v130\n"
        "v_exp_f32 v131, v131\n"
        "v_exp_f32 v132, v132\n"
        "v_exp_f32 v133, v133\n"
        "v_exp_f32 v134, v134\n"
        "v_exp_f32 v135, v135\n"
        "v_exp_f32 v136, v136\n"
        "v_exp_f32 v137, v137\n"
        "v_exp_f32 v138, v138\n"
        "v_exp_f32 v139, v139\n"
        "v_exp_f32 v140, v140\n"
        "v_exp_f32 v141, v141\n"
        "v_exp_f32 v142, v142\n"
        "v_exp_f32 v143, v143\n"
        "s_nop 3\n"
        "v_smfmac_f32_32x32x32_bf16 v[160:175], a[32:35], v[128:135], v253\n"
        "v_smfmac_f32_32x32x32_bf16 v[160:175], a[36:39], v[136:143], v253\n"
        "s_nop 15\n"
        "v_mfma_f32_32x32x16_bf16 v[128:143], v[112:115], v[196:199], 0\n"
        "s_nop 15\n"
        "s_nop 3\n"
        "v_sub_f32 v128, v128, v51\n"
        "v_sub_f32 v129, v129, v51\n"
        "v_sub_f32 v130, v130, v51\n"
        "v_sub_f32 v131, v131, v51\n"
        "v_sub_f32 v132, v132, v51\n"
        "v_sub_f32 v133, v133, v51\n"
        "v_sub_f32 v134, v134, v51\n"
        "v_sub_f32 v135, v135, v51\n"
        "v_sub_f32 v136, v136, v51\n"
        "v_sub_f32 v137, v137, v51\n"
        "v_sub_f32 v138, v138, v51\n"
        "v_sub_f32 v139, v139, v51\n"
        "v_sub_f32 v140, v140, v51\n"
        "v_sub_f32 v141, v141, v51\n"
        "v_sub_f32 v142, v142, v51\n"
        "v_sub_f32 v143, v143, v51\n"
        "v_exp_f32 v128, v128\n"
        "v_exp_f32 v129, v129\n"
        "v_exp_f32 v130, v130\n"
        "v_exp_f32 v131, v131\n"
        "v_exp_f32 v132, v132\n"
        "v_exp_f32 v133, v133\n"
        "v_exp_f32 v134, v134\n"
        "v_exp_f32 v135, v135\n"
        "v_exp_f32 v136, v136\n"
        "v_exp_f32 v137, v137\n"
        "v_exp_f32 v138, v138\n"
        "v_exp_f32 v139, v139\n"
        "v_exp_f32 v140, v140\n"
        "v_exp_f32 v141, v141\n"
        "v_exp_f32 v142, v142\n"
        "v_exp_f32 v143, v143\n"
        "s_nop 3\n"
        "v_smfmac_f32_32x32x32_bf16 v[176:191], a[32:35], v[128:135], v253\n"
        "v_smfmac_f32_32x32x32_bf16 v[176:191], a[36:39], v[136:143], v253\n"
        "s_nop 15\n"
        "v_mfma_f32_32x32x16_bf16 v[128:143], v[116:119], v[192:195], 0\n"
        "s_nop 15\n"
        "s_nop 3\n"
        "v_sub_f32 v128, v128, v50\n"
        "v_sub_f32 v129, v129, v50\n"
        "v_sub_f32 v130, v130, v50\n"
        "v_sub_f32 v131, v131, v50\n"
        "v_sub_f32 v132, v132, v50\n"
        "v_sub_f32 v133, v133, v50\n"
        "v_sub_f32 v134, v134, v50\n"
        "v_sub_f32 v135, v135, v50\n"
        "v_sub_f32 v136, v136, v50\n"
        "v_sub_f32 v137, v137, v50\n"
        "v_sub_f32 v138, v138, v50\n"
        "v_sub_f32 v139, v139, v50\n"
        "v_sub_f32 v140, v140, v50\n"
        "v_sub_f32 v141, v141, v50\n"
        "v_sub_f32 v142, v142, v50\n"
        "v_sub_f32 v143, v143, v50\n"
        "v_exp_f32 v128, v128\n"
        "v_exp_f32 v129, v129\n"
        "v_exp_f32 v130, v130\n"
        "v_exp_f32 v131, v131\n"
        "v_exp_f32 v132, v132\n"
        "v_exp_f32 v133, v133\n"
        "v_exp_f32 v134, v134\n"
        "v_exp_f32 v135, v135\n"
        "v_exp_f32 v136, v136\n"
        "v_exp_f32 v137, v137\n"
        "v_exp_f32 v138, v138\n"
        "v_exp_f32 v139, v139\n"
        "v_exp_f32 v140, v140\n"
        "v_exp_f32 v141, v141\n"
        "v_exp_f32 v142, v142\n"
        "v_exp_f32 v143, v143\n"
        "s_nop 3\n"
        "v_smfmac_f32_32x32x32_bf16 v[160:175], a[40:43], v[128:135], v253\n"
        "v_smfmac_f32_32x32x32_bf16 v[160:175], a[44:47], v[136:143], v253\n"
        "s_nop 15\n"
        "v_mfma_f32_32x32x16_bf16 v[128:143], v[116:119], v[196:199], 0\n"
        "s_nop 15\n"
        "s_nop 3\n"
        "v_sub_f32 v128, v128, v51\n"
        "v_sub_f32 v129, v129, v51\n"
        "v_sub_f32 v130, v130, v51\n"
        "v_sub_f32 v131, v131, v51\n"
        "v_sub_f32 v132, v132, v51\n"
        "v_sub_f32 v133, v133, v51\n"
        "v_sub_f32 v134, v134, v51\n"
        "v_sub_f32 v135, v135, v51\n"
        "v_sub_f32 v136, v136, v51\n"
        "v_sub_f32 v137, v137, v51\n"
        "v_sub_f32 v138, v138, v51\n"
        "v_sub_f32 v139, v139, v51\n"
        "v_sub_f32 v140, v140, v51\n"
        "v_sub_f32 v141, v141, v51\n"
        "v_sub_f32 v142, v142, v51\n"
        "v_sub_f32 v143, v143, v51\n"
        "v_exp_f32 v128, v128\n"
        "v_exp_f32 v129, v129\n"
        "v_exp_f32 v130, v130\n"
        "v_exp_f32 v131, v131\n"
        "v_exp_f32 v132, v132\n"
        "v_exp_f32 v133, v133\n"
        "v_exp_f32 v134, v134\n"
        "v_exp_f32 v135, v135\n"
        "v_exp_f32 v136, v136\n"
        "v_exp_f32 v137, v137\n"
        "v_exp_f32 v138, v138\n"
        "v_exp_f32 v139, v139\n"
        "v_exp_f32 v140, v140\n"
        "v_exp_f32 v141, v141\n"
        "v_exp_f32 v142, v142\n"
        "v_exp_f32 v143, v143\n"
        "s_nop 3\n"
        "v_smfmac_f32_32x32x32_bf16 v[176:191], a[40:43], v[128:135], v253\n"
        "v_smfmac_f32_32x32x32_bf16 v[176:191], a[44:47], v[136:143], v253\n"
        "s_nop 15\n"
        "s_nop 15\n"
        "s_nop 3\n"
        "Lg32_%=:\n"
        "s_cmp_lt_u32 s69, 1\n"
        "s_cbranch_scc1 Lg33_%=\n"
        "s_nop 1\n"
        "v_mfma_f32_32x32x16_bf16 v[128:143], v[120:123], v[192:195], 0\n"
        "s_nop 15\n"
        "s_nop 3\n"
        "v_sub_f32 v128, v128, v50\n"
        "v_sub_f32 v129, v129, v50\n"
        "v_sub_f32 v130, v130, v50\n"
        "v_sub_f32 v131, v131, v50\n"
        "v_sub_f32 v132, v132, v50\n"
        "v_sub_f32 v133, v133, v50\n"
        "v_sub_f32 v134, v134, v50\n"
        "v_sub_f32 v135, v135, v50\n"
        "v_sub_f32 v136, v136, v50\n"
        "v_sub_f32 v137, v137, v50\n"
        "v_sub_f32 v138, v138, v50\n"
        "v_sub_f32 v139, v139, v50\n"
        "v_sub_f32 v140, v140, v50\n"
        "v_sub_f32 v141, v141, v50\n"
        "v_sub_f32 v142, v142, v50\n"
        "v_sub_f32 v143, v143, v50\n"
        "v_exp_f32 v128, v128\n"
        "v_exp_f32 v129, v129\n"
        "v_exp_f32 v130, v130\n"
        "v_exp_f32 v131, v131\n"
        "v_exp_f32 v132, v132\n"
        "v_exp_f32 v133, v133\n"
        "v_exp_f32 v134, v134\n"
        "v_exp_f32 v135, v135\n"
        "v_exp_f32 v136, v136\n"
        "v_exp_f32 v137, v137\n"
        "v_exp_f32 v138, v138\n"
        "v_exp_f32 v139, v139\n"
        "v_exp_f32 v140, v140\n"
        "v_exp_f32 v141, v141\n"
        "v_exp_f32 v142, v142\n"
        "v_exp_f32 v143, v143\n"
        "s_nop 3\n"
        "v_smfmac_f32_32x32x32_bf16 v[160:175], a[48:51], v[128:135], v253\n"
        "v_smfmac_f32_32x32x32_bf16 v[160:175], a[52:55], v[136:143], v253\n"
        "s_nop 15\n"
        "v_mfma_f32_32x32x16_bf16 v[128:143], v[120:123], v[196:199], 0\n"
        "s_nop 15\n"
        "s_nop 3\n"
        "v_sub_f32 v128, v128, v51\n"
        "v_sub_f32 v129, v129, v51\n"
        "v_sub_f32 v130, v130, v51\n"
        "v_sub_f32 v131, v131, v51\n"
        "v_sub_f32 v132, v132, v51\n"
        "v_sub_f32 v133, v133, v51\n"
        "v_sub_f32 v134, v134, v51\n"
        "v_sub_f32 v135, v135, v51\n"
        "v_sub_f32 v136, v136, v51\n"
        "v_sub_f32 v137, v137, v51\n"
        "v_sub_f32 v138, v138, v51\n"
        "v_sub_f32 v139, v139, v51\n"
        "v_sub_f32 v140, v140, v51\n"
        "v_sub_f32 v141, v141, v51\n"
        "v_sub_f32 v142, v142, v51\n"
        "v_sub_f32 v143, v143, v51\n"
        "v_exp_f32 v128, v128\n"
        "v_exp_f32 v129, v129\n"
        "v_exp_f32 v130, v130\n"
        "v_exp_f32 v131, v131\n"
        "v_exp_f32 v132, v132\n"
        "v_exp_f32 v133, v133\n"
        "v_exp_f32 v134, v134\n"
        "v_exp_f32 v135, v135\n"
        "v_exp_f32 v136, v136\n"
        "v_exp_f32 v137, v137\n"
        "v_exp_f32 v138, v138\n"
        "v_exp_f32 v139, v139\n"
        "v_exp_f32 v140, v140\n"
        "v_exp_f32 v141, v141\n"
        "v_exp_f32 v142, v142\n"
        "v_exp_f32 v143, v143\n"
        "s_nop 3\n"
        "v_smfmac_f32_32x32x32_bf16 v[176:191], a[48:51], v[128:135], v253\n"
        "v_smfmac_f32_32x32x32_bf16 v[176:191], a[52:55], v[136:143], v253\n"
        "s_nop 15\n"
        "v_mfma_f32_32x32x16_bf16 v[128:143], v[124:127], v[192:195], 0\n"
        "s_nop 15\n"
        "s_nop 3\n"
        "v_sub_f32 v128, v128, v50\n"
        "v_sub_f32 v129, v129, v50\n"
        "v_sub_f32 v130, v130, v50\n"
        "v_sub_f32 v131, v131, v50\n"
        "v_sub_f32 v132, v132, v50\n"
        "v_sub_f32 v133, v133, v50\n"
        "v_sub_f32 v134, v134, v50\n"
        "v_sub_f32 v135, v135, v50\n"
        "v_sub_f32 v136, v136, v50\n"
        "v_sub_f32 v137, v137, v50\n"
        "v_sub_f32 v138, v138, v50\n"
        "v_sub_f32 v139, v139, v50\n"
        "v_sub_f32 v140, v140, v50\n"
        "v_sub_f32 v141, v141, v50\n"
        "v_sub_f32 v142, v142, v50\n"
        "v_sub_f32 v143, v143, v50\n"
        "v_exp_f32 v128, v128\n"
        "v_exp_f32 v129, v129\n"
        "v_exp_f32 v130, v130\n"
        "v_exp_f32 v131, v131\n"
        "v_exp_f32 v132, v132\n"
        "v_exp_f32 v133, v133\n"
        "v_exp_f32 v134, v134\n"
        "v_exp_f32 v135, v135\n"
        "v_exp_f32 v136, v136\n"
        "v_exp_f32 v137, v137\n"
        "v_exp_f32 v138, v138\n"
        "v_exp_f32 v139, v139\n"
        "v_exp_f32 v140, v140\n"
        "v_exp_f32 v141, v141\n"
        "v_exp_f32 v142, v142\n"
        "v_exp_f32 v143, v143\n"
        "s_nop 3\n"
        "v_smfmac_f32_32x32x32_bf16 v[160:175], a[56:59], v[128:135], v253\n"
        "v_smfmac_f32_32x32x32_bf16 v[160:175], a[60:63], v[136:143], v253\n"
        "s_nop 15\n"
        "v_mfma_f32_32x32x16_bf16 v[128:143], v[124:127], v[196:199], 0\n"
        "s_nop 15\n"
        "s_nop 3\n"
        "v_sub_f32 v128, v128, v51\n"
        "v_sub_f32 v129, v129, v51\n"
        "v_sub_f32 v130, v130, v51\n"
        "v_sub_f32 v131, v131, v51\n"
        "v_sub_f32 v132, v132, v51\n"
        "v_sub_f32 v133, v133, v51\n"
        "v_sub_f32 v134, v134, v51\n"
        "v_sub_f32 v135, v135, v51\n"
        "v_sub_f32 v136, v136, v51\n"
        "v_sub_f32 v137, v137, v51\n"
        "v_sub_f32 v138, v138, v51\n"
        "v_sub_f32 v139, v139, v51\n"
        "v_sub_f32 v140, v140, v51\n"
        "v_sub_f32 v141, v141, v51\n"
        "v_sub_f32 v142, v142, v51\n"
        "v_sub_f32 v143, v143, v51\n"
        "v_exp_f32 v128, v128\n"
        "v_exp_f32 v129, v129\n"
        "v_exp_f32 v130, v130\n"
        "v_exp_f32 v131, v131\n"
        "v_exp_f32 v132, v132\n"
        "v_exp_f32 v133, v133\n"
        "v_exp_f32 v134, v134\n"
        "v_exp_f32 v135, v135\n"
        "v_exp_f32 v136, v136\n"
        "v_exp_f32 v137, v137\n"
        "v_exp_f32 v138, v138\n"
        "v_exp_f32 v139, v139\n"
        "v_exp_f32 v140, v140\n"
        "v_exp_f32 v141, v141\n"
        "v_exp_f32 v142, v142\n"
        "v_exp_f32 v143, v143\n"
        "s_nop 3\n"
        "v_smfmac_f32_32x32x32_bf16 v[176:191], a[56:59], v[128:135], v253\n"
        "v_smfmac_f32_32x32x32_bf16 v[176:191], a[60:63], v[136:143], v253\n"
        "s_nop 15\n"
        "s_nop 15\n"
        "s_nop 3\n"
        "Lg33_%=:\n"
        "s_nop 15\n"
        "v_rcp_f32 v168, v168\n"
        "s_nop 1\n"
        "s_branch Lbok_%=\n"
        "Lsdbad_%=:\n"
        "s_nop 15\n"
        "s_mov_b32 s57, 0\n"
        "Lr36_%=:\n"
        "s_waitcnt lgkmcnt(0)\n"
        "s_add_u32 s54, s49, 8192\n"
        "v_add_u32 v128, s54, v0\n"
        "v_mov_b32 v129, 0\n"
        "s_mov_b32 s56, 0\n"
        "Lr37_%=:\n"
        "ds_read_b128 v[132:135], v128 offset:0\n"
        "ds_read_b128 v[136:139], v128 offset:1024\n"
        "ds_read_b128 v[140:143], v128 offset:2048\n"
        "ds_read_b128 v[144:147], v128 offset:3072\n"
        "ds_read_b128 v[148:151], v128 offset:4096\n"
        "ds_read_b128 v[152:155], v128 offset:5120\n"
        "ds_read_b128 v[156:159], v128 offset:6144\n"
        "s_waitcnt lgkmcnt(0)\n"
        "v_max3_u32 v129, v129, v132, v133\n"
        "v_max3_u32 v129, v129, v134, v135\n"
        "v_max3_u32 v129, v129, v136, v137\n"
        "v_max3_u32 v129, v129, v138, v139\n"
        "v_max3_u32 v129, v129, v140, v141\n"
        "v_max3_u32 v129, v129, v142, v143\n"
        "v_max3_u32 v129, v129, v144, v145\n"
        "v_max3_u32 v129, v129, v146, v147\n"
        "v_max3_u32 v129, v129, v148, v149\n"
        "v_max3_u32 v129, v129, v150, v151\n"
        "v_max3_u32 v129, v129, v152, v153\n"
        "v_max3_u32 v129, v129, v154, v155\n"
        "v_max3_u32 v129, v129, v156, v157\n"
        "v_max3_u32 v129, v129, v158, v159\n"
        "v_add_u32 v128, 7168, v128\n"
        "s_add_u32 s56, s56, 1\n"
        "s_cmp_lt_u32 s56, 8\n"
        "s_cbranch_scc1 Lr37_%=\n"
        "v_cmp_eq_u32 vcc, -1, v129\n"
        "s_cbranch_vccz Lr38_%=\n"
        "s_add_u32 s57, s57, 1\n"
        "s_cmp_gt_u32 s57, 64\n"
        "s_cbranch_scc1 Lr38_%=\n"
        "s_barrier\n"
        "s_add_u32 s60, s65, 2097152\n"
        "s_add_u32 m0, s78, 0\n"
        "s_add_u32 s54, s77, 0\n"
        "s_and_b32 s54, s54, 0xffff\n"
        "s_add_u32 s54, s54, s60\n"
        "buffer_load_dwordx4 v0, %[srd], s54 offen sc1 lds\n"
        "s_add_u32 m0, m0, 4096\n"
        "s_add_u32 s54, s77, 4096\n"
        "s_and_b32 s54, s54, 0xffff\n"
        "s_add_u32 s54, s54, s60\n"
        "buffer_load_dwordx4 v0, %[srd], s54 offen sc1 lds\n"
        "s_add_u32 m0, m0, 4096\n"
        "s_add_u32 s54, s77, 8192\n"
        "s_and_b32 s54, s54, 0xffff\n"
        "s_add_u32 s54, s54, s60\n"
        "buffer_load_dwordx4 v0, %[srd], s54 offen sc1 lds\n"
        "s_add_u32 m0, m0, 4096\n"
        "s_add_u32 s54, s77, 12288\n"
        "s_and_b32 s54, s54, 0xffff\n"
        "s_add_u32 s54, s54, s60\n"
        "buffer_load_dwordx4 v0, %[srd], s54 offen sc1 lds\n"
        "s_add_u32 m0, m0, 4096\n"
        "s_add_u32 s54, s77, 16384\n"
        "s_and_b32 s54, s54, 0xffff\n"
        "s_add_u32 s54, s54, s60\n"
        "buffer_load_dwordx4 v0, %[srd], s54 offen sc1 lds\n"
        "s_add_u32 m0, m0, 4096\n"
        "s_add_u32 s54, s77, 20480\n"
        "s_and_b32 s54, s54, 0xffff\n"
        "s_add_u32 s54, s54, s60\n"
        "buffer_load_dwordx4 v0, %[srd], s54 offen sc1 lds\n"
        "s_add_u32 m0, m0, 4096\n"
        "s_add_u32 s54, s77, 24576\n"
        "s_and_b32 s54, s54, 0xffff\n"
        "s_add_u32 s54, s54, s60\n"
        "buffer_load_dwordx4 v0, %[srd], s54 offen sc1 lds\n"
        "s_add_u32 m0, m0, 4096\n"
        "s_add_u32 s54, s77, 28672\n"
        "s_and_b32 s54, s54, 0xffff\n"
        "s_add_u32 s54, s54, s60\n"
        "buffer_load_dwordx4 v0, %[srd], s54 offen sc1 lds\n"
        "s_add_u32 m0, m0, 4096\n"
        "s_add_u32 s54, s77, 32768\n"
        "s_and_b32 s54, s54, 0xffff\n"
        "s_add_u32 s54, s54, s60\n"
        "buffer_load_dwordx4 v0, %[srd], s54 offen sc1 lds\n"
        "s_add_u32 m0, m0, 4096\n"
        "s_add_u32 s54, s77, 36864\n"
        "s_and_b32 s54, s54, 0xffff\n"
        "s_add_u32 s54, s54, s60\n"
        "buffer_load_dwordx4 v0, %[srd], s54 offen sc1 lds\n"
        "s_add_u32 m0, m0, 4096\n"
        "s_add_u32 s54, s77, 40960\n"
        "s_and_b32 s54, s54, 0xffff\n"
        "s_add_u32 s54, s54, s60\n"
        "buffer_load_dwordx4 v0, %[srd], s54 offen sc1 lds\n"
        "s_add_u32 m0, m0, 4096\n"
        "s_add_u32 s54, s77, 45056\n"
        "s_and_b32 s54, s54, 0xffff\n"
        "s_add_u32 s54, s54, s60\n"
        "buffer_load_dwordx4 v0, %[srd], s54 offen sc1 lds\n"
        "s_add_u32 m0, m0, 4096\n"
        "s_add_u32 s54, s77, 49152\n"
        "s_and_b32 s54, s54, 0xffff\n"
        "s_add_u32 s54, s54, s60\n"
        "buffer_load_dwordx4 v0, %[srd], s54 offen sc1 lds\n"
        "s_add_u32 m0, m0, 4096\n"
        "s_add_u32 s54, s77, 53248\n"
        "s_and_b32 s54, s54, 0xffff\n"
        "s_add_u32 s54, s54, s60\n"
        "buffer_load_dwordx4 v0, %[srd], s54 offen sc1 lds\n"
        "s_add_u32 m0, m0, 4096\n"
        "s_waitcnt vmcnt(0)\n"
        "s_barrier\n"
        "s_branch Lr36_%=\n"
        "Lr38_%=:\n"
        "ds_read_b128 v[128:131], v3 offset:0\n"
        "ds_read_b128 v[132:135], v3 offset:256\n"
        "ds_read_b128 v[136:139], v3 offset:512\n"
        "ds_read_b128 v[140:143], v3 offset:768\n"
        "ds_read_b128 v[144:147], v3 offset:4096\n"
        "ds_read_b128 v[148:151], v3 offset:4352\n"
        "ds_read_b128 v[152:155], v3 offset:4608\n"
        "ds_read_b128 v[156:159], v3 offset:4864\n"
        "s_waitcnt lgkmcnt(6)\n"
        "v_mfma_f32_16x16x32_bf16 v[160:163], a[128:131], v[128:131], v[80:83]\n"
        "ds_read_b128 v[128:131], v3 offset:8192\n"
        "v_mfma_f32_16x16x32_bf16 v[164:167], a[128:131], v[132:135], v[84:87]\n"
        "ds_read_b128 v[132:135], v3 offset:8448\n"
        "s_waitcnt lgkmcnt(6)\n"
        "v_mfma_f32_16x16x32_bf16 v[168:171], a[128:131], v[136:139], v[88:91]\n"
        "ds_read_b128 v[136:139], v3 offset:8704\n"
        "v_mfma_f32_16x16x32_bf16 v[172:175], a[128:131], v[140:143], v[92:95]\n"
        "ds_read_b128 v[140:143], v3 offset:8960\n"
        "s_waitcnt lgkmcnt(6)\n"
        "v_mfma_f32_16x16x32_bf16 v[160:163], a[132:135], v[144:147], v[160:163]\n"
        "ds_read_b128 v[144:147], v3 offset:12288\n"
        "v_mfma_f32_16x16x32_bf16 v[164:167], a[132:135], v[148:151], v[164:167]\n"
        "ds_read_b128 v[148:151], v3 offset:12544\n"
        "s_waitcnt lgkmcnt(6)\n"
        "v_mfma_f32_16x16x32_bf16 v[168:171], a[132:135], v[152:155], v[168:171]\n"
        "ds_read_b128 v[152:155], v3 offset:12800\n"
        "v_mfma_f32_16x16x32_bf16 v[172:175], a[132:135], v[156:159], v[172:175]\n"
        "ds_read_b128 v[156:159], v3 offset:13056\n"
        "s_waitcnt lgkmcnt(6)\n"
        "v_mfma_f32_16x16x32_bf16 v[160:163], a[136:139], v[128:131], v[160:163]\n"
        "ds_read_b128 v[128:131], v3 offset:16384\n"
        "v_mfma_f32_16x16x32_bf16 v[164:167], a[136:139], v[132:135], v[164:167]\n"
        "ds_read_b128 v[132:135], v3 offset:16640\n"
        "s_waitcnt lgkmcnt(6)\n"
        "v_mfma_f32_16x16x32_bf16 v[168:171], a[136:139], v[136:139], v[168:171]\n"
        "ds_read_b128 v[136:139], v3 offset:16896\n"
        "v_mfma_f32_16x16x32_bf16 v[172:175], a[136:139], v[140:143], v[172:175]\n"
        "ds_read_b128 v[140:143], v3 offset:17152\n"
        "s_waitcnt lgkmcnt(6)\n"
        "v_mfma_f32_16x16x32_bf16 v[160:163], a[140:143], v[144:147], v[160:163]\n"
        "ds_read_b128 v[144:147], v3 offset:20480\n"
        "v_mfma_f32_16x16x32_bf16 v[164:167], a[140:143], v[148:151], v[164:167]\n"
        "ds_read_b128 v[148:151], v3 offset:20736\n"
        "s_waitcnt lgkmcnt(6)\n"
        "v_mfma_f32_16x16x32_bf16 v[168:171], a[140:143], v[152:155], v[168:171]\n"
        "ds_read_b128 v[152:155], v3 offset:20992\n"
        "v_mfma_f32_16x16x32_bf16 v[172:175], a[140:143], v[156:159], v[172:175]\n"
        "ds_read_b128 v[156:159], v3 offset:21248\n"
        "s_waitcnt lgkmcnt(6)\n"
        "v_mfma_f32_16x16x32_bf16 v[160:163], a[144:147], v[128:131], v[160:163]\n"
        "ds_read_b128 v[128:131], v3 offset:24576\n"
        "v_mfma_f32_16x16x32_bf16 v[164:167], a[144:147], v[132:135], v[164:167]\n"
        "ds_read_b128 v[132:135], v3 offset:24832\n"
        "s_waitcnt lgkmcnt(6)\n"
        "v_mfma_f32_16x16x32_bf16 v[168:171], a[144:147], v[136:139], v[168:171]\n"
        "ds_read_b128 v[136:139], v3 offset:25088\n"
        "v_mfma_f32_16x16x32_bf16 v[172:175], a[144:147], v[140:143], v[172:175]\n"
        "ds_read_b128 v[140:143], v3 offset:25344\n"
        "s_waitcnt lgkmcnt(6)\n"
        "v_mfma_f32_16x16x32_bf16 v[160:163], a[148:151], v[144:147], v[160:163]\n"
        "ds_read_b128 v[144:147], v3 offset:28672\n"
        "v_mfma_f32_16x16x32_bf16 v[164:167], a[148:151], v[148:151], v[164:167]\n"
        "ds_read_b128 v[148:151], v3 offset:28928\n"
        "s_waitcnt lgkmcnt(6)\n"
        "v_mfma_f32_16x16x32_bf16 v[168:171], a[148:151], v[152:155], v[168:171]\n"
        "ds_read_b128 v[152:155], v3 offset:29184\n"
        "v_mfma_f32_16x16x32_bf16 v[172:175], a[148:151], v[156:159], v[172:175]\n"
        "ds_read_b128 v[156:159], v3 offset:29440\n"
        "s_waitcnt lgkmcnt(6)\n"
        "v_mfma_f32_16x16x32_bf16 v[160:163], a[152:155], v[128:131], v[160:163]\n"
        "ds_read_b128 v[128:131], v3 offset:32768\n"
        "v_mfma_f32_16x16x32_bf16 v[164:167], a[152:155], v[132:135], v[164:167]\n"
        "ds_read_b128 v[132:135], v3 offset:33024\n"
        "s_waitcnt lgkmcnt(6)\n"
        "v_mfma_f32_16x16x32_bf16 v[168:171], a[152:155], v[136:139], v[168:171]\n"
        "ds_read_b128 v[136:139], v3 offset:33280\n"
        "v_mfma_f32_16x16x32_bf16 v[172:175], a[152:155], v[140:143], v[172:175]\n"
        "ds_read_b128 v[140:143], v3 offset:33536\n"
        "s_waitcnt lgkmcnt(6)\n"
        "v_mfma_f32_16x16x32_bf16 v[160:163], a[156:159], v[144:147], v[160:163]\n"
        "ds_read_b128 v[144:147], v3 offset:36864\n"
        "v_mfma_f32_16x16x32_bf16 v[164:167], a[156:159], v[148:151], v[164:167]\n"
        "ds_read_b128 v[148:151], v3 offset:37120\n"
        "s_waitcnt lgkmcnt(6)\n"
        "v_mfma_f32_16x16x32_bf16 v[168:171], a[156:159], v[152:155], v[168:171]\n"
        "ds_read_b128 v[152:155], v3 offset:37376\n"
        "v_mfma_f32_16x16x32_bf16 v[172:175], a[156:159], v[156:159], v[172:175]\n"
        "ds_read_b128 v[156:159], v3 offset:37632\n"
        "s_waitcnt lgkmcnt(6)\n"
        "v_mfma_f32_16x16x32_bf16 v[160:163], a[160:163], v[128:131], v[160:163]\n"
        "ds_read_b128 v[128:131], v3 offset:40960\n"
        "v_mfma_f32_16x16x32_bf16 v[164:167], a[160:163], v[132:135], v[164:167]\n"
        "ds_read_b128 v[132:135], v3 offset:41216\n"
        "s_waitcnt lgkmcnt(6)\n"
        "v_mfma_f32_16x16x32_bf16 v[168:171], a[160:163], v[136:139], v[168:171]\n"
        "ds_read_b128 v[136:139], v3 offset:41472\n"
        "v_mfma_f32_16x16x32_bf16 v[172:175], a[160:163], v[140:143], v[172:175]\n"
        "ds_read_b128 v[140:143], v3 offset:41728\n"
        "s_waitcnt lgkmcnt(6)\n"
        "v_mfma_f32_16x16x32_bf16 v[160:163], a[164:167], v[144:147], v[160:163]\n"
        "ds_read_b128 v[144:147], v3 offset:45056\n"
        "v_mfma_f32_16x16x32_bf16 v[164:167], a[164:167], v[148:151], v[164:167]\n"
        "ds_read_b128 v[148:151], v3 offset:45312\n"
        "s_waitcnt lgkmcnt(6)\n"
        "v_mfma_f32_16x16x32_bf16 v[168:171], a[164:167], v[152:155], v[168:171]\n"
        "ds_read_b128 v[152:155], v3 offset:45568\n"
        "v_mfma_f32_16x16x32_bf16 v[172:175], a[164:167], v[156:159], v[172:175]\n"
        "ds_read_b128 v[156:159], v3 offset:45824\n"
        "s_waitcnt lgkmcnt(6)\n"
        "v_mfma_f32_16x16x32_bf16 v[160:163], a[168:171], v[128:131], v[160:163]\n"
        "ds_read_b128 v[128:131], v3 offset:49152\n"
        "v_mfma_f32_16x16x32_bf16 v[164:167], a[168:171], v[132:135], v[164:167]\n"
        "ds_read_b128 v[132:135], v3 offset:49408\n"
        "s_waitcnt lgkmcnt(6)\n"
        "v_mfma_f32_16x16x32_bf16 v[168:171], a[168:171], v[136:139], v[168:171]\n"
        "ds_read_b128 v[136:139], v3 offset:49664\n"
        "v_mfma_f32_16x16x32_bf16 v[172:175], a[168:171], v[140:143], v[172:175]\n"
        "ds_read_b128 v[140:143], v3 offset:49920\n"
        "s_waitcnt lgkmcnt(6)\n"
        "v_mfma_f32_16x16x32_bf16 v[160:163], a[172:175], v[144:147], v[160:163]\n"
        "ds_read_b128 v[144:147], v3 offset:53248\n"
        "v_mfma_f32_16x16x32_bf16 v[164:167], a[172:175], v[148:151], v[164:167]\n"
        "ds_read_b128 v[148:151], v3 offset:53504\n"
        "s_waitcnt lgkmcnt(6)\n"
        "v_mfma_f32_16x16x32_bf16 v[168:171], a[172:175], v[152:155], v[168:171]\n"
        "ds_read_b128 v[152:155], v3 offset:53760\n"
        "v_mfma_f32_16x16x32_bf16 v[172:175], a[172:175], v[156:159], v[172:175]\n"
        "ds_read_b128 v[156:159], v3 offset:54016\n"
        "s_waitcnt lgkmcnt(6)\n"
        "v_mfma_f32_16x16x32_bf16 v[160:163], a[176:179], v[128:131], v[160:163]\n"
        "ds_read_b128 v[128:131], v3 offset:57344\n"
        "v_mfma_f32_16x16x32_bf16 v[164:167], a[176:179], v[132:135], v[164:167]\n"
        "ds_read_b128 v[132:135], v3 offset:57600\n"
        "s_waitcnt lgkmcnt(6)\n"
        "v_mfma_f32_16x16x32_bf16 v[168:171], a[176:179], v[136:139], v[168:171]\n"
        "ds_read_b128 v[136:139], v3 offset:57856\n"
        "v_mfma_f32_16x16x32_bf16 v[172:175], a[176:179], v[140:143], v[172:175]\n"
        "ds_read_b128 v[140:143], v3 offset:58112\n"
        "s_waitcnt lgkmcnt(6)\n"
        "v_mfma_f32_16x16x32_bf16 v[160:163], a[180:183], v[144:147], v[160:163]\n"
        "ds_read_b128 v[144:147], v3 offset:61440\n"
        "v_mfma_f32_16x16x32_bf16 v[164:167], a[180:183], v[148:151], v[164:167]\n"
        "ds_read_b128 v[148:151], v3 offset:61696\n"
        "s_waitcnt lgkmcnt(6)\n"
        "v_mfma_f32_16x16x32_bf16 v[168:171], a[180:183], v[152:155], v[168:171]\n"
        "ds_read_b128 v[152:155], v3 offset:61952\n"
        "v_mfma_f32_16x16x32_bf16 v[172:175], a[180:183], v[156:159], v[172:175]\n"
        "ds_read_b128 v[156:159], v3 offset:62208\n"
        "s_waitcnt lgkmcnt(6)\n"
        "v_mfma_f32_16x16x32_bf16 v[160:163], a[184:187], v[128:131], v[160:163]\n"
        "v_mfma_f32_16x16x32_bf16 v[164:167], a[184:187], v[132:135], v[164:167]\n"
        "s_waitcnt lgkmcnt(4)\n"
        "v_mfma_f32_16x16x32_bf16 v[168:171], a[184:187], v[136:139], v[168:171]\n"
        "v_mfma_f32_16x16x32_bf16 v[172:175], a[184:187], v[140:143], v[172:175]\n"
        "s_waitcnt lgkmcnt(2)\n"
        "v_mfma_f32_16x16x32_bf16 v[160:163], a[188:191], v[144:147], v[160:163]\n"
        "v_mfma_f32_16x16x32_bf16 v[164:167], a[188:191], v[148:151], v[164:167]\n"
        "s_waitcnt lgkmcnt(0)\n"
        "v_mfma_f32_16x16x32_bf16 v[168:171], a[188:191], v[152:155], v[168:171]\n"
        "v_mfma_f32_16x16x32_bf16 v[172:175], a[188:191], v[156:159], v[172:175]\n"
        "s_nop 7\n"
        "s_nop 1\n"
        "s_branch Lsdok_%=\n"
        "Lsebad_%=:\n"
        "s_nop 15\n"
        "s_mov_b32 s57, 0\n"
        "Lr56_%=:\n"
        "s_waitcnt lgkmcnt(0)\n"
        "s_add_u32 s54, s49, 73728\n"
        "v_add_u32 v128, s54, v0\n"
        "v_mov_b32 v129, 0\n"
        "s_mov_b32 s56, 0\n"
        "Lr57_%=:\n"
        "ds_read_b128 v[132:135], v128 offset:0\n"
        "ds_read_b128 v[136:139], v128 offset:1024\n"
        "ds_read_b128 v[140:143], v128 offset:2048\n"
        "ds_read_b128 v[144:147], v128 offset:3072\n"
        "ds_read_b128 v[148:151], v128 offset:4096\n"
        "ds_read_b128 v[152:155], v128 offset:5120\n"
        "ds_read_b128 v[156:159], v128 offset:6144\n"
        "s_waitcnt lgkmcnt(0)\n"
        "v_max3_u32 v129, v129, v132, v133\n"
        "v_max3_u32 v129, v129, v134, v135\n"
        "v_max3_u32 v129, v129, v136, v137\n"
        "v_max3_u32 v129, v129, v138, v139\n"
        "v_max3_u32 v129, v129, v140, v141\n"
        "v_max3_u32 v129, v129, v142, v143\n"
        "v_max3_u32 v129, v129, v144, v145\n"
        "v_max3_u32 v129, v129, v146, v147\n"
        "v_max3_u32 v129, v129, v148, v149\n"
        "v_max3_u32 v129, v129, v150, v151\n"
        "v_max3_u32 v129, v129, v152, v153\n"
        "v_max3_u32 v129, v129, v154, v155\n"
        "v_max3_u32 v129, v129, v156, v157\n"
        "v_max3_u32 v129, v129, v158, v159\n"
        "v_add_u32 v128, 7168, v128\n"
        "s_add_u32 s56, s56, 1\n"
        "s_cmp_lt_u32 s56, 8\n"
        "s_cbranch_scc1 Lr57_%=\n"
        "v_cmp_eq_u32 vcc, -1, v129\n"
        "s_cbranch_vccz Lr58_%=\n"
        "s_add_u32 s57, s57, 1\n"
        "s_cmp_gt_u32 s57, 64\n"
        "s_cbranch_scc1 Lr58_%=\n"
        "s_barrier\n"
        "s_add_u32 s60, s65, 10485760\n"
        "s_add_u32 m0, s78, 65536\n"
        "s_add_u32 s54, s77, 0\n"
        "s_and_b32 s54, s54, 0xffff\n"
        "s_add_u32 s54, s54, s60\n"
        "buffer_load_dwordx4 v0, %[srd], s54 offen sc1 lds\n"
        "s_add_u32 m0, m0, 4096\n"
        "s_add_u32 s54, s77, 4096\n"
        "s_and_b32 s54, s54, 0xffff\n"
        "s_add_u32 s54, s54, s60\n"
        "buffer_load_dwordx4 v0, %[srd], s54 offen sc1 lds\n"
        "s_add_u32 m0, m0, 4096\n"
        "s_add_u32 s54, s77, 8192\n"
        "s_and_b32 s54, s54, 0xffff\n"
        "s_add_u32 s54, s54, s60\n"
        "buffer_load_dwordx4 v0, %[srd], s54 offen sc1 lds\n"
        "s_add_u32 m0, m0, 4096\n"
        "s_add_u32 s54, s77, 12288\n"
        "s_and_b32 s54, s54, 0xffff\n"
        "s_add_u32 s54, s54, s60\n"
        "buffer_load_dwordx4 v0, %[srd], s54 offen sc1 lds\n"
        "s_add_u32 m0, m0, 4096\n"
        "s_add_u32 s54, s77, 16384\n"
        "s_and_b32 s54, s54, 0xffff\n"
        "s_add_u32 s54, s54, s60\n"
        "buffer_load_dwordx4 v0, %[srd], s54 offen sc1 lds\n"
        "s_add_u32 m0, m0, 4096\n"
        "s_add_u32 s54, s77, 20480\n"
        "s_and_b32 s54, s54, 0xffff\n"
        "s_add_u32 s54, s54, s60\n"
        "buffer_load_dwordx4 v0, %[srd], s54 offen sc1 lds\n"
        "s_add_u32 m0, m0, 4096\n"
        "s_add_u32 s54, s77, 24576\n"
        "s_and_b32 s54, s54, 0xffff\n"
        "s_add_u32 s54, s54, s60\n"
        "buffer_load_dwordx4 v0, %[srd], s54 offen sc1 lds\n"
        "s_add_u32 m0, m0, 4096\n"
        "s_add_u32 s54, s77, 28672\n"
        "s_and_b32 s54, s54, 0xffff\n"
        "s_add_u32 s54, s54, s60\n"
        "buffer_load_dwordx4 v0, %[srd], s54 offen sc1 lds\n"
        "s_add_u32 m0, m0, 4096\n"
        "s_add_u32 s54, s77, 32768\n"
        "s_and_b32 s54, s54, 0xffff\n"
        "s_add_u32 s54, s54, s60\n"
        "buffer_load_dwordx4 v0, %[srd], s54 offen sc1 lds\n"
        "s_add_u32 m0, m0, 4096\n"
        "s_add_u32 s54, s77, 36864\n"
        "s_and_b32 s54, s54, 0xffff\n"
        "s_add_u32 s54, s54, s60\n"
        "buffer_load_dwordx4 v0, %[srd], s54 offen sc1 lds\n"
        "s_add_u32 m0, m0, 4096\n"
        "s_add_u32 s54, s77, 40960\n"
        "s_and_b32 s54, s54, 0xffff\n"
        "s_add_u32 s54, s54, s60\n"
        "buffer_load_dwordx4 v0, %[srd], s54 offen sc1 lds\n"
        "s_add_u32 m0, m0, 4096\n"
        "s_add_u32 s54, s77, 45056\n"
        "s_and_b32 s54, s54, 0xffff\n"
        "s_add_u32 s54, s54, s60\n"
        "buffer_load_dwordx4 v0, %[srd], s54 offen sc1 lds\n"
        "s_add_u32 m0, m0, 4096\n"
        "s_add_u32 s54, s77, 49152\n"
        "s_and_b32 s54, s54, 0xffff\n"
        "s_add_u32 s54, s54, s60\n"
        "buffer_load_dwordx4 v0, %[srd], s54 offen sc1 lds\n"
        "s_add_u32 m0, m0, 4096\n"
        "s_add_u32 s54, s77, 53248\n"
        "s_and_b32 s54, s54, 0xffff\n"
        "s_add_u32 s54, s54, s60\n"
        "buffer_load_dwordx4 v0, %[srd], s54 offen sc1 lds\n"
        "s_add_u32 m0, m0, 4096\n"
        "s_waitcnt vmcnt(0)\n"
        "s_barrier\n"
        "s_cmp_eq_u32 s81, 49152\n"
        "s_cbranch_scc1 Le53_%=\n"
        "s_and_b32 s54, s86, 3\n"
        "s_lshl_b32 s54, s54, 8\n"
        "s_add_u32 s54, s54, s81\n"
        "v_add_u32 v192, s54, v4\n"
        "ds_read_b128 v[96:99], v192 offset:0\n"
        "ds_read_b128 v[100:103], v192 offset:4096\n"
        "ds_read_b128 v[104:107], v192 offset:8192\n"
        "ds_read_b128 v[108:111], v192 offset:12288\n"
        "s_lshr_b32 s54, s81, 4\n"
        "s_sub_u32 s54, s54, 8192\n"
        "v_add_u32 v193, s54, v228\n"
        "s_waitcnt lgkmcnt(0)\n"
        "s_cmp_lt_u32 s86, 4\n"
        "s_cbranch_scc0 Lv54_%=\n"
        "v_mfma_f32_16x16x32_bf16 v[176:179], a[64:67], v[96:99], 0\n"
        "v_mfma_f32_16x16x32_bf16 v[176:179], a[68:71], v[100:103], v[176:179]\n"
        "v_mfma_f32_16x16x32_bf16 v[176:179], a[72:75], v[104:107], v[176:179]\n"
        "v_mfma_f32_16x16x32_bf16 v[176:179], a[76:79], v[108:111], v[176:179]\n"
        "s_branch Lw55_%=\n"
        "Lv54_%=:\n"
        "v_mfma_f32_16x16x32_bf16 v[176:179], v[96:99], a[64:67], 0\n"
        "v_mfma_f32_16x16x32_bf16 v[176:179], v[100:103], a[68:71], v[176:179]\n"
        "v_mfma_f32_16x16x32_bf16 v[176:179], v[104:107], a[72:75], v[176:179]\n"
        "v_mfma_f32_16x16x32_bf16 v[176:179], v[108:111], a[76:79], v[176:179]\n"
        "Lw55_%=:\n"
        "s_nop 7\n"
        "ds_write_b128 v193, v[176:179]\n"
        "Le53_%=:\n"
        "s_waitcnt lgkmcnt(0)\n"
        "s_barrier\n"
        "s_branch Lr56_%=\n"
        "Lr58_%=:\n"
        "v_lshrrev_b32 v128, 2, v0\n"
        "v_add_u32 v128, s52, v128\n"
        "ds_read_b32 v128, v128 offset:256\n"
        "s_waitcnt lgkmcnt(0)\n"
        "v_cmp_u_f32 vcc, v128, v128\n"
        "s_cbranch_vccz Lf60_%=\n"
        "s_barrier\n"
        "s_add_u32 s54, s66, 524288\n"
        "s_mov_b32 s56, 0\n"
        "Lsp61_%=:\n"
        "s_add_u32 s56, s56, 1\n"
        "s_cmp_gt_u32 s56, 16384\n"
        "s_cbranch_scc1 Lsq62_%=\n"
        "buffer_load_dwordx2 v[248:249], v6, %[srd], s54 offen sc1\n"
        "buffer_load_dwordx2 v[250:251], v6, %[srd], s54 offen offset:512 sc1\n"
        "s_waitcnt vmcnt(0)\n"
        "v_max3_u32 v128, v248, v249, v250\n"
        "v_max_u32 v128, v128, v251\n"
        "v_cmp_eq_u32 vcc, -1, v128\n"
        "s_cbranch_vccnz Lsp61_%=\n"
        "Lsq62_%=:\n"
        "v_pk_add_f32 v[248:249], v[248:249], v[250:251]\n"
        "v_mov_b32 v253, v248\n"
        "v_mov_b32 v254, v249\n"
        "s_nop 0\n"
        "v_permlane16_swap_b32 v248, v253\n"
        "v_permlane16_swap_b32 v249, v254\n"
        "v_add_f32 v248, v248, v253\n"
        "v_add_f32 v249, v249, v254\n"
        "v_mov_b32 v253, v248\n"
        "v_mov_b32 v254, v249\n"
        "s_nop 0\n"
        "v_permlane32_swap_b32 v248, v253\n"
        "v_permlane32_swap_b32 v249, v254\n"
        "v_add_f32 v248, v248, v253\n"
        "v_add_f32 v249, v249, v254\n"
        "v_mul_f32 v248, 0x3b000000, v248\n"
        "v_mul_f32 v249, 0x3b000000, v249\n"
        "v_fma_f32 v249, -v248, v248, v249\n"
        "v_and_b32 v249, 0x7fffffff, v249\n"
        "v_add_f32 v249, 0x3727c5ac, v249\n"
        "v_rsq_f32 v249, v249\n"
        "s_mov_b64 exec, 0xffff\n"
        "ds_write2_b32 v7, v248, v249 offset1:64\n"
        "s_mov_b64 exec, -1\n"
        "s_waitcnt lgkmcnt(0)\n"
        "s_barrier\n"
        "Lf60_%=:\n"
        "ds_read_b128 v[128:131], v4 offset:0\n"
        "ds_read_b128 v[132:135], v4 offset:256\n"
        "ds_read_b128 v[136:139], v4 offset:512\n"
        "ds_read_b128 v[140:143], v4 offset:768\n"
        "ds_read_b128 v[144:147], v4 offset:4096\n"
        "ds_read_b128 v[148:151], v4 offset:4352\n"
        "ds_read_b128 v[152:155], v4 offset:4608\n"
        "ds_read_b128 v[156:159], v4 offset:4864\n"
        "s_waitcnt lgkmcnt(6)\n"
        "v_mfma_f32_16x16x32_bf16 v[160:163], a[192:195], v[128:131], 0\n"
        "ds_read_b128 v[128:131], v4 offset:8192\n"
        "v_mfma_f32_16x16x32_bf16 v[164:167], a[192:195], v[132:135], 0\n"
        "ds_read_b128 v[132:135], v4 offset:8448\n"
        "s_waitcnt lgkmcnt(6)\n"
        "v_mfma_f32_16x16x32_bf16 v[168:171], a[192:195], v[136:139], 0\n"
        "ds_read_b128 v[136:139], v4 offset:8704\n"
        "v_mfma_f32_16x16x32_bf16 v[172:175], a[192:195], v[140:143], 0\n"
        "ds_read_b128 v[140:143], v4 offset:8960\n"
        "s_waitcnt lgkmcnt(6)\n"
        "v_mfma_f32_16x16x32_bf16 v[160:163], a[196:199], v[144:147], v[160:163]\n"
        "ds_read_b128 v[144:147], v4 offset:12288\n"
        "v_mfma_f32_16x16x32_bf16 v[164:167], a[196:199], v[148:151], v[164:167]\n"
        "ds_read_b128 v[148:151], v4 offset:12544\n"
        "s_waitcnt lgkmcnt(6)\n"
        "v_mfma_f32_16x16x32_bf16 v[168:171], a[196:199], v[152:155], v[168:171]\n"
        "ds_read_b128 v[152:155], v4 offset:12800\n"
        "v_mfma_f32_16x16x32_bf16 v[172:175], a[196:199], v[156:159], v[172:175]\n"
        "ds_read_b128 v[156:159], v4 offset:13056\n"
        "s_waitcnt lgkmcnt(6)\n"
        "v_mfma_f32_16x16x32_bf16 v[160:163], a[200:203], v[128:131], v[160:163]\n"
        "ds_read_b128 v[128:131], v4 offset:16384\n"
        "v_mfma_f32_16x16x32_bf16 v[164:167], a[200:203], v[132:135], v[164:167]\n"
        "ds_read_b128 v[132:135], v4 offset:16640\n"
        "s_waitcnt lgkmcnt(6)\n"
        "v_mfma_f32_16x16x32_bf16 v[168:171], a[200:203], v[136:139], v[168:171]\n"
        "ds_read_b128 v[136:139], v4 offset:16896\n"
        "v_mfma_f32_16x16x32_bf16 v[172:175], a[200:203], v[140:143], v[172:175]\n"
        "ds_read_b128 v[140:143], v4 offset:17152\n"
        "s_waitcnt lgkmcnt(6)\n"
        "v_mfma_f32_16x16x32_bf16 v[160:163], a[204:207], v[144:147], v[160:163]\n"
        "ds_read_b128 v[144:147], v4 offset:20480\n"
        "v_mfma_f32_16x16x32_bf16 v[164:167], a[204:207], v[148:151], v[164:167]\n"
        "ds_read_b128 v[148:151], v4 offset:20736\n"
        "s_waitcnt lgkmcnt(6)\n"
        "v_mfma_f32_16x16x32_bf16 v[168:171], a[204:207], v[152:155], v[168:171]\n"
        "ds_read_b128 v[152:155], v4 offset:20992\n"
        "v_mfma_f32_16x16x32_bf16 v[172:175], a[204:207], v[156:159], v[172:175]\n"
        "ds_read_b128 v[156:159], v4 offset:21248\n"
        "s_waitcnt lgkmcnt(6)\n"
        "v_mfma_f32_16x16x32_bf16 v[160:163], a[208:211], v[128:131], v[160:163]\n"
        "ds_read_b128 v[128:131], v4 offset:24576\n"
        "v_mfma_f32_16x16x32_bf16 v[164:167], a[208:211], v[132:135], v[164:167]\n"
        "ds_read_b128 v[132:135], v4 offset:24832\n"
        "s_waitcnt lgkmcnt(6)\n"
        "v_mfma_f32_16x16x32_bf16 v[168:171], a[208:211], v[136:139], v[168:171]\n"
        "ds_read_b128 v[136:139], v4 offset:25088\n"
        "v_mfma_f32_16x16x32_bf16 v[172:175], a[208:211], v[140:143], v[172:175]\n"
        "ds_read_b128 v[140:143], v4 offset:25344\n"
        "s_waitcnt lgkmcnt(6)\n"
        "v_mfma_f32_16x16x32_bf16 v[160:163], a[212:215], v[144:147], v[160:163]\n"
        "ds_read_b128 v[144:147], v4 offset:28672\n"
        "v_mfma_f32_16x16x32_bf16 v[164:167], a[212:215], v[148:151], v[164:167]\n"
        "ds_read_b128 v[148:151], v4 offset:28928\n"
        "s_waitcnt lgkmcnt(6)\n"
        "v_mfma_f32_16x16x32_bf16 v[168:171], a[212:215], v[152:155], v[168:171]\n"
        "ds_read_b128 v[152:155], v4 offset:29184\n"
        "v_mfma_f32_16x16x32_bf16 v[172:175], a[212:215], v[156:159], v[172:175]\n"
        "ds_read_b128 v[156:159], v4 offset:29440\n"
        "s_waitcnt lgkmcnt(6)\n"
        "v_mfma_f32_16x16x32_bf16 v[160:163], a[216:219], v[128:131], v[160:163]\n"
        "ds_read_b128 v[128:131], v4 offset:32768\n"
        "v_mfma_f32_16x16x32_bf16 v[164:167], a[216:219], v[132:135], v[164:167]\n"
        "ds_read_b128 v[132:135], v4 offset:33024\n"
        "s_waitcnt lgkmcnt(6)\n"
        "v_mfma_f32_16x16x32_bf16 v[168:171], a[216:219], v[136:139], v[168:171]\n"
        "ds_read_b128 v[136:139], v4 offset:33280\n"
        "v_mfma_f32_16x16x32_bf16 v[172:175], a[216:219], v[140:143], v[172:175]\n"
        "ds_read_b128 v[140:143], v4 offset:33536\n"
        "s_waitcnt lgkmcnt(6)\n"
        "v_mfma_f32_16x16x32_bf16 v[160:163], a[220:223], v[144:147], v[160:163]\n"
        "ds_read_b128 v[144:147], v4 offset:36864\n"
        "v_mfma_f32_16x16x32_bf16 v[164:167], a[220:223], v[148:151], v[164:167]\n"
        "ds_read_b128 v[148:151], v4 offset:37120\n"
        "s_waitcnt lgkmcnt(6)\n"
        "v_mfma_f32_16x16x32_bf16 v[168:171], a[220:223], v[152:155], v[168:171]\n"
        "ds_read_b128 v[152:155], v4 offset:37376\n"
        "v_mfma_f32_16x16x32_bf16 v[172:175], a[220:223], v[156:159], v[172:175]\n"
        "ds_read_b128 v[156:159], v4 offset:37632\n"
        "s_waitcnt lgkmcnt(6)\n"
        "v_mfma_f32_16x16x32_bf16 v[160:163], a[224:227], v[128:131], v[160:163]\n"
        "ds_read_b128 v[128:131], v4 offset:40960\n"
        "v_mfma_f32_16x16x32_bf16 v[164:167], a[224:227], v[132:135], v[164:167]\n"
        "ds_read_b128 v[132:135], v4 offset:41216\n"
        "s_waitcnt lgkmcnt(6)\n"
        "v_mfma_f32_16x16x32_bf16 v[168:171], a[224:227], v[136:139], v[168:171]\n"
        "ds_read_b128 v[136:139], v4 offset:41472\n"
        "v_mfma_f32_16x16x32_bf16 v[172:175], a[224:227], v[140:143], v[172:175]\n"
        "ds_read_b128 v[140:143], v4 offset:41728\n"
        "s_waitcnt lgkmcnt(6)\n"
        "v_mfma_f32_16x16x32_bf16 v[160:163], a[228:231], v[144:147], v[160:163]\n"
        "ds_read_b128 v[144:147], v4 offset:45056\n"
        "v_mfma_f32_16x16x32_bf16 v[164:167], a[228:231], v[148:151], v[164:167]\n"
        "ds_read_b128 v[148:151], v4 offset:45312\n"
        "s_waitcnt lgkmcnt(6)\n"
        "v_mfma_f32_16x16x32_bf16 v[168:171], a[228:231], v[152:155], v[168:171]\n"
        "ds_read_b128 v[152:155], v4 offset:45568\n"
        "v_mfma_f32_16x16x32_bf16 v[172:175], a[228:231], v[156:159], v[172:175]\n"
        "ds_read_b128 v[156:159], v4 offset:45824\n"
        "s_waitcnt lgkmcnt(6)\n"
        "v_mfma_f32_16x16x32_bf16 v[160:163], a[232:235], v[128:131], v[160:163]\n"
        "ds_read_b128 v[128:131], v4 offset:49152\n"
        "v_mfma_f32_16x16x32_bf16 v[164:167], a[232:235], v[132:135], v[164:167]\n"
        "ds_read_b128 v[132:135], v4 offset:49408\n"
        "s_waitcnt lgkmcnt(6)\n"
        "v_mfma_f32_16x16x32_bf16 v[168:171], a[232:235], v[136:139], v[168:171]\n"
        "ds_read_b128 v[136:139], v4 offset:49664\n"
        "v_mfma_f32_16x16x32_bf16 v[172:175], a[232:235], v[140:143], v[172:175]\n"
        "ds_read_b128 v[140:143], v4 offset:49920\n"
        "s_waitcnt lgkmcnt(6)\n"
        "v_mfma_f32_16x16x32_bf16 v[160:163], a[236:239], v[144:147], v[160:163]\n"
        "ds_read_b128 v[144:147], v4 offset:53248\n"
        "v_mfma_f32_16x16x32_bf16 v[164:167], a[236:239], v[148:151], v[164:167]\n"
        "ds_read_b128 v[148:151], v4 offset:53504\n"
        "s_waitcnt lgkmcnt(6)\n"
        "v_mfma_f32_16x16x32_bf16 v[168:171], a[236:239], v[152:155], v[168:171]\n"
        "ds_read_b128 v[152:155], v4 offset:53760\n"
        "v_mfma_f32_16x16x32_bf16 v[172:175], a[236:239], v[156:159], v[172:175]\n"
        "ds_read_b128 v[156:159], v4 offset:54016\n"
        "s_waitcnt lgkmcnt(6)\n"
        "v_mfma_f32_16x16x32_bf16 v[160:163], a[240:243], v[128:131], v[160:163]\n"
        "ds_read_b128 v[128:131], v4 offset:57344\n"
        "v_mfma_f32_16x16x32_bf16 v[164:167], a[240:243], v[132:135], v[164:167]\n"
        "ds_read_b128 v[132:135], v4 offset:57600\n"
        "s_waitcnt lgkmcnt(6)\n"
        "v_mfma_f32_16x16x32_bf16 v[168:171], a[240:243], v[136:139], v[168:171]\n"
        "ds_read_b128 v[136:139], v4 offset:57856\n"
        "v_mfma_f32_16x16x32_bf16 v[172:175], a[240:243], v[140:143], v[172:175]\n"
        "ds_read_b128 v[140:143], v4 offset:58112\n"
        "s_waitcnt lgkmcnt(6)\n"
        "v_mfma_f32_16x16x32_bf16 v[160:163], a[244:247], v[144:147], v[160:163]\n"
        "ds_read_b128 v[144:147], v4 offset:61440\n"
        "v_mfma_f32_16x16x32_bf16 v[164:167], a[244:247], v[148:151], v[164:167]\n"
        "ds_read_b128 v[148:151], v4 offset:61696\n"
        "s_waitcnt lgkmcnt(6)\n"
        "v_mfma_f32_16x16x32_bf16 v[168:171], a[244:247], v[152:155], v[168:171]\n"
        "ds_read_b128 v[152:155], v4 offset:61952\n"
        "v_mfma_f32_16x16x32_bf16 v[172:175], a[244:247], v[156:159], v[172:175]\n"
        "ds_read_b128 v[156:159], v4 offset:62208\n"
        "s_waitcnt lgkmcnt(6)\n"
        "v_mfma_f32_16x16x32_bf16 v[160:163], a[248:251], v[128:131], v[160:163]\n"
        "v_mfma_f32_16x16x32_bf16 v[164:167], a[248:251], v[132:135], v[164:167]\n"
        "s_waitcnt lgkmcnt(4)\n"
        "v_mfma_f32_16x16x32_bf16 v[168:171], a[248:251], v[136:139], v[168:171]\n"
        "v_mfma_f32_16x16x32_bf16 v[172:175], a[248:251], v[140:143], v[172:175]\n"
        "s_waitcnt lgkmcnt(2)\n"
        "v_mfma_f32_16x16x32_bf16 v[160:163], a[252:255], v[144:147], v[160:163]\n"
        "v_mfma_f32_16x16x32_bf16 v[164:167], a[252:255], v[148:151], v[164:167]\n"
        "s_waitcnt lgkmcnt(0)\n"
        "v_mfma_f32_16x16x32_bf16 v[168:171], a[252:255], v[152:155], v[168:171]\n"
        "v_mfma_f32_16x16x32_bf16 v[172:175], a[252:255], v[156:159], v[172:175]\n"
        "s_nop 7\n"
        "s_nop 1\n"
        "ds_read2_b32 v[240:241], v8 offset0:0 offset1:64\n"
        "ds_read2_b32 v[242:243], v8 offset0:16 offset1:80\n"
        "ds_read2_b32 v[244:245], v8 offset0:32 offset1:96\n"
        "ds_read2_b32 v[246:247], v8 offset0:48 offset1:112\n"
        "s_cmp_eq_u32 s81, 49152\n"
        "s_cbranch_scc0 Lkvs2_%=\n"
        "s_and_b32 s54, s86, 3\n"
        "s_lshl_b32 s54, s54, 8\n"
        "s_add_u32 s54, s54, s81\n"
        "v_add_u32 v192, s54, v4\n"
        "ds_read_b128 v[96:99], v192 offset:0\n"
        "ds_read_b128 v[100:103], v192 offset:4096\n"
        "ds_read_b128 v[104:107], v192 offset:8192\n"
        "ds_read_b128 v[108:111], v192 offset:12288\n"
        "v_subrev_u32 v195, 8192, v228\n"
        "ds_read_b128 v[112:115], v195 offset:0\n"
        "ds_read_b128 v[116:119], v195 offset:1024\n"
        "ds_read_b128 v[120:123], v195 offset:2048\n"
        "s_and_b32 s55, s86, 3\n"
        "s_lshl_b32 s55, s55, 6\n"
        "v_add_u32 v193, s55, v8\n"
        "v_add_u32 v194, s55, v9\n"
        "ds_read2_b32 v[124:125], v193 offset1:64\n"
        "ds_read_b128 v[180:183], v194\n"
        "ds_read_b128 v[188:191], v194 offset:256\n"
        "s_waitcnt lgkmcnt(6)\n"
        "s_cmp_lt_u32 s86, 4\n"
        "s_cbranch_scc0 Lv65_%=\n"
        "v_mfma_f32_16x16x32_bf16 v[176:179], a[64:67], v[96:99], 0\n"
        "v_mfma_f32_16x16x32_bf16 v[176:179], a[68:71], v[100:103], v[176:179]\n"
        "v_mfma_f32_16x16x32_bf16 v[176:179], a[72:75], v[104:107], v[176:179]\n"
        "v_mfma_f32_16x16x32_bf16 v[176:179], a[76:79], v[108:111], v[176:179]\n"
        "s_branch Lw66_%=\n"
        "Lv65_%=:\n"
        "v_mfma_f32_16x16x32_bf16 v[176:179], v[96:99], a[64:67], 0\n"
        "v_mfma_f32_16x16x32_bf16 v[176:179], v[100:103], a[68:71], v[176:179]\n"
        "v_mfma_f32_16x16x32_bf16 v[176:179], v[104:107], a[72:75], v[176:179]\n"
        "v_mfma_f32_16x16x32_bf16 v[176:179], v[108:111], a[76:79], v[176:179]\n"
        "Lw66_%=:\n"
        "s_waitcnt lgkmcnt(0)\n"
        "v_pk_add_f32 v[112:113], v[112:113], v[116:117]\n"
        "v_pk_add_f32 v[114:115], v[114:115], v[118:119]\n"
        "v_pk_add_f32 v[112:113], v[112:113], v[120:121]\n"
        "v_pk_add_f32 v[114:115], v[114:115], v[122:123]\n"
        "s_nop 3\n"
        "v_pk_add_f32 v[176:177], v[176:177], v[112:113]\n"
        "v_pk_add_f32 v[178:179], v[178:179], v[114:115]\n"
        "s_cmp_lt_u32 s86, 4\n"
        "s_cbranch_scc0 Lv63_%=\n"
        "v_pk_fma_f32 v[176:177], v[124:125], v[236:237], v[176:177] op_sel_hi:[0,1,1] neg_lo:[1,0,0] neg_hi:[1,0,0]\n"
        "v_pk_fma_f32 v[178:179], v[124:125], v[238:239], v[178:179] op_sel_hi:[0,1,1] neg_lo:[1,0,0] neg_hi:[1,0,0]\n"
        "v_pk_mul_f32 v[176:177], v[124:125], v[176:177] op_sel:[1,0] op_sel_hi:[1,1]\n"
        "v_pk_mul_f32 v[178:179], v[124:125], v[178:179] op_sel:[1,0] op_sel_hi:[1,1]\n"
        "v_pk_mul_f32 v[184:185], v[176:177], v[176:177]\n"
        "v_pk_fma_f32 v[184:185], v[178:179], v[178:179], v[184:185]\n"
        "v_add_f32 v184, v184, v185\n"
        "v_mov_b32 v185, v184\n"
        "s_nop 1\n"
        "v_permlane16_swap_b32 v184, v185\n"
        "v_add_f32 v184, v184, v185\n"
        "v_mov_b32 v185, v184\n"
        "s_nop 1\n"
        "v_permlane32_swap_b32 v184, v185\n"
        "v_add_f32 v184, v184, v185\n"
        "s_branch Lw64_%=\n"
        "Lv63_%=:\n"
        "v_pk_fma_f32 v[176:177], v[180:181], v[236:237], v[176:177] op_sel_hi:[1,0,1] neg_lo:[1,0,0] neg_hi:[1,0,0]\n"
        "v_pk_fma_f32 v[178:179], v[182:183], v[236:237], v[178:179] op_sel_hi:[1,0,1] neg_lo:[1,0,0] neg_hi:[1,0,0]\n"
        "v_pk_mul_f32 v[176:177], v[188:189], v[176:177]\n"
        "v_pk_mul_f32 v[178:179], v[190:191], v[178:179]\n"
        "v_max3_f32 v184, |v176|, |v177|, |v178|\n"
        "v_max_f32 v184, v184, |v179|\n"
        "Lw64_%=:\n"
        "s_nop 1\n"
        "v_cvt_pk_bf16_f32 v186, v176, v177\n"
        "v_cvt_pk_bf16_f32 v187, v178, v179\n"
        "v_min_u32 v186, -2, v186\n"
        "v_min_u32 v187, -2, v187\n"
        "v_mov_b32 v188, v186\n"
        "v_mov_b32 v189, v187\n"
        "s_nop 0\n"
        "v_permlane16_swap_b32 v186, v188\n"
        "v_permlane16_swap_b32 v187, v189\n"
        "s_mov_b32 exec_lo, 0xffff\n"
        "s_mov_b32 exec_hi, 0xffff\n"
        "buffer_store_dwordx4 v[186:189], v226, %[srd], s74 offen sc1\n"
        "s_mov_b64 exec, -1\n"
        "v_max_u32_dpp v184, v184, v184 row_ror:8 row_mask:0xf bank_mask:0xf\n"
        "s_nop 1\n"
        "v_max_u32_dpp v184, v184, v184 row_ror:4 row_mask:0xf bank_mask:0xf\n"
        "s_nop 1\n"
        "v_max_u32_dpp v184, v184, v184 row_ror:2 row_mask:0xf bank_mask:0xf\n"
        "s_nop 1\n"
        "v_max_u32_dpp v184, v184, v184 row_ror:1 row_mask:0xf bank_mask:0xf\n"
        "v_mov_b32 v185, v184\n"
        "s_nop 1\n"
        "v_permlane16_swap_b32 v184, v185\n"
        "v_max_u32 v184, v184, v185\n"
        "v_mov_b32 v185, v184\n"
        "s_nop 1\n"
        "v_permlane32_swap_b32 v184, v185\n"
        "v_max_u32 v184, v184, v185\n"
        "v_min_u32 v184, -2, v184\n"
        "s_and_b32 s54, s76, 0x7f80\n"
        "s_add_u32 s54, s54, s74\n"
        "s_add_u32 s54, s54, 0x20000\n"
        "v_lshrrev_b32 v185, 2, v0\n"
        "s_mov_b32 exec_hi, 0\n"
        "buffer_store_dword v184, v185, %[srd], s54 offen sc1\n"
        "s_mov_b64 exec, -1\n"
        "Lkvs2_%=:\n"
        "s_waitcnt lgkmcnt(0)\n"
        "v_mul_f32 v241, 0x3eb8aa3b, v241\n"
        "v_mul_f32 v243, 0x3eb8aa3b, v243\n"
        "v_mul_f32 v245, 0x3eb8aa3b, v245\n"
        "v_mul_f32 v247, 0x3eb8aa3b, v247\n"
        "v_pk_fma_f32 v[160:161], v[240:241], v[232:233], v[160:161] op_sel_hi:[0,1,1] neg_lo:[1,0,0] neg_hi:[1,0,0]\n"
        "v_pk_fma_f32 v[162:163], v[240:241], v[234:235], v[162:163] op_sel_hi:[0,1,1] neg_lo:[1,0,0] neg_hi:[1,0,0]\n"
        "v_pk_fma_f32 v[164:165], v[242:243], v[232:233], v[164:165] op_sel_hi:[0,1,1] neg_lo:[1,0,0] neg_hi:[1,0,0]\n"
        "v_pk_fma_f32 v[166:167], v[242:243], v[234:235], v[166:167] op_sel_hi:[0,1,1] neg_lo:[1,0,0] neg_hi:[1,0,0]\n"
        "v_pk_fma_f32 v[168:169], v[244:245], v[232:233], v[168:169] op_sel_hi:[0,1,1] neg_lo:[1,0,0] neg_hi:[1,0,0]\n"
        "v_pk_fma_f32 v[170:171], v[244:245], v[234:235], v[170:171] op_sel_hi:[0,1,1] neg_lo:[1,0,0] neg_hi:[1,0,0]\n"
        "v_pk_fma_f32 v[172:173], v[246:247], v[232:233], v[172:173] op_sel_hi:[0,1,1] neg_lo:[1,0,0] neg_hi:[1,0,0]\n"
        "v_pk_fma_f32 v[174:175], v[246:247], v[234:235], v[174:175] op_sel_hi:[0,1,1] neg_lo:[1,0,0] neg_hi:[1,0,0]\n"
        "v_pk_mul_f32 v[160:161], v[240:241], v[160:161] op_sel:[1,0] op_sel_hi:[1,1]\n"
        "v_pk_mul_f32 v[162:163], v[240:241], v[162:163] op_sel:[1,0] op_sel_hi:[1,1]\n"
        "v_pk_mul_f32 v[164:165], v[242:243], v[164:165] op_sel:[1,0] op_sel_hi:[1,1]\n"
        "v_pk_mul_f32 v[166:167], v[242:243], v[166:167] op_sel:[1,0] op_sel_hi:[1,1]\n"
        "v_pk_mul_f32 v[168:169], v[244:245], v[168:169] op_sel:[1,0] op_sel_hi:[1,1]\n"
        "v_pk_mul_f32 v[170:171], v[244:245], v[170:171] op_sel:[1,0] op_sel_hi:[1,1]\n"
        "v_pk_mul_f32 v[172:173], v[246:247], v[172:173] op_sel:[1,0] op_sel_hi:[1,1]\n"
        "v_pk_mul_f32 v[174:175], v[246:247], v[174:175] op_sel:[1,0] op_sel_hi:[1,1]\n"
        "s_branch Lseok_%=\n"
        "Lend_%=:\n"
            : "+{s[64:79]}"(sb), "+{s[80:87]}"(sb2), "+{v[96:111]}"(pcv), "+{v[224:231]}"(cb1)
            : "{v[0:15]}"(cb0), [srd] "s"(srw), [sro] "s"(sro), [sq] "s"(sq), [sp] "s"(sp), "{s49}"(rfl(lb)), "{s52}"(rfl(lb + L_SM)), "{s46}"(rfl(b * 4 + w)),
              "{s40}"(rfl(c * 128)), "{s41}"(rfl(pco)), "{s42}"(rfl(8 * pld)), "{s43}"(rfl((w - kw) & 3)),
              "{s44}"(rfl(wg < 160 ? OFF_WT + (4 * pg + w) * 16384 : 0u)), "{s39}"(rfl(OFF_CS + (4 * pg + w) * 256 + pq * 64)), "{s35}"(rfl((4 * pq - 2 * (pg & 7)) * 1024))
            : "v16","v17","v18","v19","v20","v21","v22","v23","v24","v25","v26","v27","v28","v29","v30","v31","v32","v33","v34","v35","v36","v37","v38","v39","v40","v41","v42","v43","v44","v45","v46","v47","v48","v49","v50","v51","v52","v53","v54","v55","v56","v57","v58","v59","v60","v61","v62","v63","v64","v65","v66","v67","v68","v69","v70","v71","v72","v73","v74","v75","v76","v77","v78","v79","v80","v81","v82","v83","v84","v85","v86","v87","v88","v89","v90","v91","v92","v93","v94","v95","v112","v113","v114","v115","v116","v117","v118","v119","v120","v121","v122","v123","v124","v125","v126","v127","v128","v129","v130","v131","v132","v133","v134","v135","v136","v137","v138","v139","v140","v141","v142","v143","v144","v145","v146","v147","v148","v149","v150","v151","v152","v153","v154","v155","v156","v157","v158","v159","v160","v161","v162","v163","v164","v165","v166","v167","v168","v169","v170","v171","v172","v173","v174","v175","v176","v177","v178","v179","v180","v181","v182","v183","v184","v185","v186","v187","v188","v189","v190","v191","v192","v193","v194","v195","v196","v197","v198","v199","v200","v201","v202","v203","v204","v205","v206","v207","v208","v209","v210","v211","v212","v213","v214","v215","v216","v217","v218","v219","v220","v221","v222","v223","v232","v233","v234","v235","v236","v237","v238","v239","v240","v241","v242","v243","v244","v245","v246","v247","v248","v249","v250","v251","v252","v253","v254","v255", "s28", "s29", "s30", "s31", "s34", "s36", "s37", "s38", "s47", "s48", "s50", "s51", "s57", "s94", "s95", STMP, AGC);
    }
}

extern "C" void kernel_launch(void* const* d_in, const int* in_sizes, int n_in, void* d_out, int out_size, void* d_ws, size_t ws_size, hipStream_t stream) {
    Params p;
    p.x = (const u16*)d_in[0]; p.wq = (const u16*)d_in[1]; p.wk = (const u16*)d_in[2]; p.wv = (const u16*)d_in[3]; p.woA = (const u16*)d_in[4];
    p.wqkv = (const u16*)d_in[6]; p.woB = (const u16*)d_in[7]; p.out = (u16*)d_out; p.ws = (char*)d_ws;
    fused_abab<<<256, 256, 0, stream>>>(p);
}
```
